```python
import torch

def _softmax(x, axis=-1):
    return torch.softmax(x, dim=axis)
def _gelu(x):
    return torch.nn.functional.gelu(x, approximate="tanh")
def _silu(x):
    return torch.nn.functional.silu(x)
def _sigmoid(x):
    return torch.sigmoid(x)
def _layernorm(x, axis=-1, eps=1e-5):
    m = x.mean(dim=axis, keepdim=True)
    v = x.var(dim=axis, keepdim=True, unbiased=False)
    return (x - m) / torch.sqrt(v + eps)
def _scatter_add(tgt, idx, src, axis=0):
    return tgt.clone().index_add_(axis, idx, src)
def _segment_sum(src, seg, n):
    out = torch.zeros((n,) + src.shape[1:], dtype=src.dtype)
    return out.index_add_(0, seg, src)
def _pad(x, pw):
    flat = tuple(v for pair in reversed(pw) for v in pair)
    return torch.nn.functional.pad(x, flat)

kernel_name = 'gated_mix__linear_attn_block__dense_activation_mish__prompt'

def setup_inputs():

    _w = (lambda a: (lambda t: t.to(torch.bfloat16) if t.is_floating_point() else t)(torch.as_tensor(a)))
    def _A_setup():
        B, S, D = 1, 2048, 576
        return {
            "x": _w((lambda *s: torch.randn(*s, dtype=torch.bfloat16) * 0.05)(B, S, D)),
            "wqkv": _w((lambda *s: torch.randn(*s, dtype=torch.bfloat16) * 0.8)(D, 3 * D)),
            "wo": _w((lambda *s: torch.randn(*s, dtype=torch.bfloat16) * 0.05)(D, D)),
        }
    _A_in = _A_setup()

    def _B_setup():
        B, S, D, F = 1, 2048, 576, 4608
        return {
            "x": _w((lambda *s: torch.randn(*s, dtype=torch.bfloat16) * 0.05)(B, S, D)),
            "w1": _w((lambda *s: torch.randn(*s, dtype=torch.bfloat16) * 12.8)(D, F) * (D ** -0.5)),
            "b1": _w((lambda *s: torch.randn(*s, dtype=torch.bfloat16) * 0.2)(F)),
            "w2": _w((lambda *s: torch.randn(*s, dtype=torch.bfloat16) * 0.05)(F, D) * (F ** -0.5)),
        }
    _B_in = _B_setup()

    return {
        "A_x": _A_in["x"],
        "A_wqkv": _A_in["wqkv"],
        "A_wo": _A_in["wo"],
        "B_x": _B_in["x"],
        "B_w1": _B_in["w1"],
        "B_b1": _B_in["b1"],
        "B_w2": _B_in["w2"],
        "gate_w": _w((lambda *s: torch.randn(*s, dtype=torch.bfloat16) * 0.05)(576) * 16.0),
    }

def reference(A_x, A_wqkv, A_wo, B_x, B_w1, B_b1, B_w2, gate_w):
    def _A_ref(x, wqkv, wo):
        B, S, D = x.shape
        H = 32; Dh = D // H
        qkv = (lambda x, *d: x.permute(*d))((x @ wqkv).reshape(B, S, 3, H, Dh), 2, 0, 3, 1, 4)
        q, k, v = qkv[0], qkv[1], qkv[2]
        phi = lambda t: torch.where(t > 0, t + 1, torch.exp(t)) + 1e-6
        pq, pk = phi(q), phi(k)
        kv = torch.einsum("bhsd,bhse->bhde", pk, v)
        z = torch.einsum("bhsd->bhd", pk)
        num = torch.einsum("bhqd,bhde->bhqe", pq, kv)
        den = torch.einsum("bhqd,bhd->bhq", pq, z)[..., None] + 1e-6
        o = num / den
        return (lambda x, *d: x.permute(*d))(o, 0, 2, 1, 3).reshape(B, S, D) @ wo

    def _B_ref(x, w1, b1, w2):
        h = torch.einsum("bsd,df->bsf", x, w1) + b1
        sp = torch.log1p(torch.exp(-torch.abs(h))) + torch.where(h > 0, h, torch.zeros_like(h))
        act = h * torch.tanh(sp)
        return torch.einsum("bsf,fd->bsd", act, w2)

    x = A_x
    g = _sigmoid((x * gate_w).sum(-1, keepdims=True))
    a = _A_ref(x, A_wqkv, A_wo)
    b = _B_ref(x, B_w1, B_b1, B_w2)
    return g * a + (1 - g) * b
```

```cpp
#include <hip/hip_runtime.h>
#include <hip/hip_bf16.h>
typedef __hip_bfloat16 bf16;
constexpr int S=2048, D=576, F=4608, H=32, DH=18;
__device__ inline float b2f(bf16 v){ return __bfloat162float(v);}
template<typename TA>
__global__ void gemm_naive(const TA* A, const bf16* B, float* C, int M, int N, int K){
  __shared__ float As[16][17], Bs[16][17];
  int tx=threadIdx.x, ty=threadIdx.y; int row=blockIdx.y*16+ty, col=blockIdx.x*16+tx; float acc=0;
  for(int k0=0;k0<K;k0+=16){ As[ty][tx]=(float)A[(long)row*K+k0+tx]; Bs[ty][tx]=b2f(B[(long)(k0+ty)*N+col]); __syncthreads();
    for(int k=0;k<16;k++) acc+=As[ty][k]*Bs[k][tx]; __syncthreads(); }
  C[(long)row*N+col]=acc;
}
__device__ inline float phi(float t){ return (t>0? t+1.f : __expf(t)) + 1e-6f; }
__global__ void kv_kernel(const float* qkv, float* kv, float* z){
  int h=blockIdx.x; int t=threadIdx.x; if(t>=18*19) return; int d=t/19, e=t%19; float acc=0;
  for(int s=0;s<S;s++){ float pk=phi(qkv[(long)s*3*D+D+h*DH+d]); float v=(e<18)? qkv[(long)s*3*D+2*D+h*DH+e]:1.f; acc+=pk*v; }
  if(e<18) kv[(h*DH+d)*DH+e]=acc; else z[h*DH+d]=acc;
}
__global__ void o_kernel(const float* qkv,const float* kv,const float* z,float* o){
  int s=blockIdx.x, t=threadIdx.x; int h=t/DH,e=t%DH; float num=0,den=0;
  for(int d=0;d<DH;d++){ float pq=phi(qkv[(long)s*3*D+h*DH+d]); num+=pq*kv[(h*DH+d)*DH+e]; den+=pq*z[h*DH+d]; }
  o[(long)s*D+t]=num/(den+1e-6f);
}
__global__ void mish_kernel(float* hbuf,const bf16* b1){ long i=(long)blockIdx.x*256+threadIdx.x; float h=hbuf[i]+b2f(b1[i%F]);
  float sp=log1pf(expf(-fabsf(h)))+(h>0?h:0.f); hbuf[i]=h*tanhf(sp); }
__global__ void final_kernel(const bf16* x,const bf16* gw,const float* a,const float* b,bf16* out){ int s=blockIdx.x,t=threadIdx.x; __shared__ float red[576];
  red[t]=b2f(x[(long)s*D+t])*b2f(gw[t]); __syncthreads(); if(t==0){float sum=0; for(int i=0;i<D;i++) sum+=red[i]; red[0]=1.f/(1.f+expf(-sum));} __syncthreads(); float g=red[0];
  out[(long)s*D+t]=__float2bfloat16(g*a[(long)s*D+t]+(1.f-g)*b[(long)s*D+t]); }
extern "C" void kernel_launch(void* const* d_in, const int* in_sizes, int n_in, void* d_out, int out_size, void* d_ws, size_t ws_size, hipStream_t stream){
  const bf16* x=(const bf16*)d_in[0]; const bf16* wqkv=(const bf16*)d_in[1]; const bf16* wo=(const bf16*)d_in[2];
  const bf16* w1=(const bf16*)d_in[4]; const bf16* b1=(const bf16*)d_in[5]; const bf16* w2=(const bf16*)d_in[6]; const bf16* gw=(const bf16*)d_in[7];
  float* ws=(float*)d_ws; float* qkv=ws; float* hb=qkv+(long)S*3*D; float* o=hb+(long)S*F; float* a=o+(long)S*D; float* b=a+(long)S*D; float* kv=b+(long)S*D; float* z=kv+H*DH*DH;
  dim3 blk(16,16);
  gemm_naive<bf16><<<dim3(3*D/16,S/16),blk,0,stream>>>(x,wqkv,qkv,S,3*D,D);
  kv_kernel<<<H,384,0,stream>>>(qkv,kv,z);
  o_kernel<<<S,576,0,stream>>>(qkv,kv,z,o);
  gemm_naive<float><<<dim3(D/16,S/16),blk,0,stream>>>(o,wo,a,S,D,D);
  gemm_naive<bf16><<<dim3(F/16,S/16),blk,0,stream>>>(x,w1,hb,S,F,D);
  mish_kernel<<<(long)S*F/256,256,0,stream>>>(hb,b1);
  gemm_naive<float><<<dim3(D/16,S/16),blk,0,stream>>>(hb,w2,b,S,D,F);
  final_kernel<<<S,576,0,stream>>>(x,gw,a,b,(bf16*)d_out);
}
```

```cpp
#include <hip/hip_runtime.h>
#include <hip/hip_bf16.h>
typedef unsigned u32;
typedef u32 u32x4 __attribute__((ext_vector_type(4)));
typedef u32 u32x2 __attribute__((ext_vector_type(2)));
typedef float f32x2 __attribute__((ext_vector_type(2)));
typedef __bf16 bf16x2 __attribute__((ext_vector_type(2)));

__global__ __launch_bounds__(256, 1) void gm_main(const void* x, const void* wqkv, const void* wo, const void* w1, const void* b1,
                                                  const void* w2, const void* gw, void* ws, void* out) {
  const u32 wid = __builtin_amdgcn_readfirstlane(threadIdx.x >> 6);
  const u32 bid = blockIdx.x;
  auto ka = __builtin_amdgcn_kernarg_segment_ptr();
  asm volatile(
    "s_load_dwordx16 s[20:35], %[ka], 0\n"
    "s_load_dwordx2 s[12:13], %[ka], 64\n"
    "v_mbcnt_lo_u32_b32 v124, -1, 0\n"
    "v_mbcnt_hi_u32_b32 v124, -1, v124\n"
    "v_lshlrev_b32 v241, 4, v124\n"
    "s_mov_b32 s66, %[wid]\n"
    "s_lshl_b32 s64, s66, 10\n"
    "s_and_b32 s68, %[bid], 3\n"
    "s_bfe_u32 s69, %[bid], 0x20003\n"
    "s_lshr_b32 s67, %[bid], 5\n"
    "s_and_b32 s96, %[bid], 4\n"
    "s_lshl_b32 s96, s96, 1\n"
    "s_or_b32 s67, s67, s96\n"
    "s_lshr_b32 s96, s69, 1\n"
    "s_lshl_b32 s71, s68, 1\n"
    "s_add_u32 s71, s71, s96\n"
    "s_mov_b32 s80, 0x3fb8aa3b\n"
    "s_bitcmp1_b32 s69, 0\n"
    "s_cbranch_scc1 LroleQ_%=\n"
    "s_nop 1\n"
    "v_bfe_u32 v125, v124, 2, 3\n"
    "v_lshrrev_b32 v127, 2, v125\n"
    "v_lshl_add_u32 v125, s66, 3, v125\n"
    "v_lshrrev_b32 v126, 5, v124\n"
    "v_lshrrev_b32 v128, 2, v125\n"
    "v_xor_b32 v128, v128, v124\n"
    "v_and_b32 v128, 3, v128\n"
    "v_lshl_add_u32 v128, v126, 2, v128\n"
    "v_lshlrev_b32 v128, 4, v128\n"
    "v_mul_u32_u24 v129, 1152, v125\n"
    "v_add_u32 v214, v129, v128\n"
    "s_mul_i32 s96, s66, 55296\n"
    "s_mul_i32 s93, s66, 5120\n"
    "v_add_u32 v130, 0, v124\n"
    "v_mul_u32_u24 v131, 3277, v130\n"
    "v_lshrrev_b32 v131, 16, v131\n"
    "v_mul_u32_u24 v219, 20, v131\n"
    "v_sub_u32 v130, v130, v219\n"
    "v_mul_u32_u24 v131, 3456, v131\n"
    "v_and_b32 v219, 7, v130\n"
    "v_lshlrev_b32 v219, 4, v219\n"
    "v_cmp_lt_u32 vcc, 15, v130\n"
    "v_mov_b32 v128, 128\n"
    "s_nop 0\n"
    "v_cndmask_b32_e64 v219, v219, v128, vcc\n"
    "v_add_u32 v219, v219, v131\n"
    "v_subrev_u32 v131, 8, v130\n"
    "v_cmp_gt_u32 vcc, 8, v131\n"
    "v_cmp_lt_u32_e64 s[18:19], 16, v130\n"
    "s_nop 0\n"
    "s_or_b64 vcc, vcc, s[18:19]\n"
    "v_mov_b32 v128, 2304\n"
    "v_mov_b32 v131, 1152\n"
    "v_cndmask_b32_e64 v128, v131, v128, vcc\n"
    "v_add3_u32 v219, v219, v128, s96\n"
    "v_add_u32 v130, 64, v124\n"
    "v_mul_u32_u24 v131, 3277, v130\n"
    "v_lshrrev_b32 v131, 16, v131\n"
    "v_mul_u32_u24 v220, 20, v131\n"
    "v_sub_u32 v130, v130, v220\n"
    "v_mul_u32_u24 v131, 3456, v131\n"
    "v_and_b32 v220, 7, v130\n"
    "v_lshlrev_b32 v220, 4, v220\n"
    "v_cmp_lt_u32 vcc, 15, v130\n"
    "v_mov_b32 v128, 128\n"
    "s_nop 0\n"
    "v_cndmask_b32_e64 v220, v220, v128, vcc\n"
    "v_add_u32 v220, v220, v131\n"
    "v_subrev_u32 v131, 8, v130\n"
    "v_cmp_gt_u32 vcc, 8, v131\n"
    "v_cmp_lt_u32_e64 s[18:19], 16, v130\n"
    "s_nop 0\n"
    "s_or_b64 vcc, vcc, s[18:19]\n"
    "v_mov_b32 v128, 2304\n"
    "v_mov_b32 v131, 1152\n"
    "v_cndmask_b32_e64 v128, v131, v128, vcc\n"
    "v_add3_u32 v220, v220, v128, s96\n"
    "v_add_u32 v130, 128, v124\n"
    "v_mul_u32_u24 v131, 3277, v130\n"
    "v_lshrrev_b32 v131, 16, v131\n"
    "v_mul_u32_u24 v221, 20, v131\n"
    "v_sub_u32 v130, v130, v221\n"
    "v_mul_u32_u24 v131, 3456, v131\n"
    "v_and_b32 v221, 7, v130\n"
    "v_lshlrev_b32 v221, 4, v221\n"
    "v_cmp_lt_u32 vcc, 15, v130\n"
    "v_mov_b32 v128, 128\n"
    "s_nop 0\n"
    "v_cndmask_b32_e64 v221, v221, v128, vcc\n"
    "v_add_u32 v221, v221, v131\n"
    "v_subrev_u32 v131, 8, v130\n"
    "v_cmp_gt_u32 vcc, 8, v131\n"
    "v_cmp_lt_u32_e64 s[18:19], 16, v130\n"
    "s_nop 0\n"
    "s_or_b64 vcc, vcc, s[18:19]\n"
    "v_mov_b32 v128, 2304\n"
    "v_mov_b32 v131, 1152\n"
    "v_cndmask_b32_e64 v128, v131, v128, vcc\n"
    "v_add3_u32 v221, v221, v128, s96\n"
    "v_add_u32 v130, 192, v124\n"
    "v_mul_u32_u24 v131, 3277, v130\n"
    "v_lshrrev_b32 v131, 16, v131\n"
    "v_mul_u32_u24 v222, 20, v131\n"
    "v_sub_u32 v130, v130, v222\n"
    "v_mul_u32_u24 v131, 3456, v131\n"
    "v_and_b32 v222, 7, v130\n"
    "v_lshlrev_b32 v222, 4, v222\n"
    "v_cmp_lt_u32 vcc, 15, v130\n"
    "v_mov_b32 v128, 128\n"
    "s_nop 0\n"
    "v_cndmask_b32_e64 v222, v222, v128, vcc\n"
    "v_add_u32 v222, v222, v131\n"
    "v_subrev_u32 v131, 8, v130\n"
    "v_cmp_gt_u32 vcc, 8, v131\n"
    "v_cmp_lt_u32_e64 s[18:19], 16, v130\n"
    "s_nop 0\n"
    "s_or_b64 vcc, vcc, s[18:19]\n"
    "v_mov_b32 v128, 2304\n"
    "v_mov_b32 v131, 1152\n"
    "v_cndmask_b32_e64 v128, v131, v128, vcc\n"
    "v_add3_u32 v222, v222, v128, s96\n"
    "v_add_u32 v130, 256, v124\n"
    "v_mul_u32_u24 v131, 3277, v130\n"
    "v_lshrrev_b32 v131, 16, v131\n"
    "v_mul_u32_u24 v223, 20, v131\n"
    "v_sub_u32 v130, v130, v223\n"
    "v_mul_u32_u24 v131, 3456, v131\n"
    "v_and_b32 v223, 7, v130\n"
    "v_lshlrev_b32 v223, 4, v223\n"
    "v_cmp_lt_u32 vcc, 15, v130\n"
    "v_mov_b32 v128, 128\n"
    "s_nop 0\n"
    "v_cndmask_b32_e64 v223, v223, v128, vcc\n"
    "v_add_u32 v223, v223, v131\n"
    "v_subrev_u32 v131, 8, v130\n"
    "v_cmp_gt_u32 vcc, 8, v131\n"
    "v_cmp_lt_u32_e64 s[18:19], 16, v130\n"
    "s_nop 0\n"
    "s_or_b64 vcc, vcc, s[18:19]\n"
    "v_mov_b32 v128, 2304\n"
    "v_mov_b32 v131, 1152\n"
    "v_cndmask_b32_e64 v128, v131, v128, vcc\n"
    "v_add3_u32 v223, v223, v128, s96\n"
    "v_and_b32 v130, 15, v124\n"
    "v_and_b32 v128, 3, v124\n"
    "v_bfe_u32 v131, v124, 4, 1\n"
    "v_lshlrev_b32 v128, 3, v128\n"
    "v_lshl_add_u32 v128, v131, 5, v128\n"
    "v_bfe_u32 v131, v124, 2, 2\n"
    "v_lshl_add_u32 v236, v131, 6, v128\n"
    "v_lshl_add_u32 v131, v126, 3, v131\n"
    "v_mul_u32_u24 v131, 320, v131\n"
    "v_add_u32 v224, v131, v128\n"
    "v_mul_u32_u24 v131, 320, v130\n"
    "v_lshrrev_b32 v128, 4, v124\n"
    "v_lshl_add_u32 v131, v128, 2, v131\n"
    "s_add_u32 s97, s93, 16672\n"
    "v_add_u32 v225, s97, v131\n"
    "v_bfe_u32 v131, v124, 3, 2\n"
    "v_lshlrev_b32 v131, 10, v131\n"
    "v_and_b32 v128, 7, v124\n"
    "v_lshl_add_u32 v131, v128, 6, v131\n"
    "v_bfe_u32 v128, v124, 2, 2\n"
    "v_xor_b32 v128, v128, v126\n"
    "v_lshl_add_u32 v131, v128, 4, v131\n"
    "v_lshl_add_u32 v215, s66, 12, v131\n"
    "s_and_b32 s97, s68, 1\n"
    "s_lshl_b32 s97, s97, 11\n"
    "v_xor_b32 v215, s97, v215\n"
    "v_xor_b32 v216, 32, v215\n"
    "v_xor_b32 v217, 0x1000, v215\n"
    "v_xor_b32 v218, 0x1000, v216\n"
    "v_lshl_add_u32 v131, s66, 4, v130\n"
    "v_lshlrev_b32 v131, 1, v131\n"
    "v_cmp_eq_u32 vcc, 0, v124\n"
    "v_mov_b32 v242, 268435456\n"
    "s_nop 0\n"
    "v_cndmask_b32_e64 v242, v242, 0, vcc\n"
    "s_mul_i32 s96, s67, 147456\n"
    "s_mul_i32 s97, s71, 144\n"
    "s_waitcnt lgkmcnt(0)\n"
    "s_add_u32 s36, s20, s96\n"
    "s_addc_u32 s37, s21, 0\n"
    "s_and_b32 s37, s37, 0xffff\n"
    "s_mov_b32 s38, 147456\n"
    "s_mov_b32 s39, 0x20000\n"
    "s_add_u32 m0, s64, 0\n"
    "s_mov_b32 s65, 0\n"
    "buffer_load_dwordx4 v214, s[36:39], s65 offen offset:0 lds\n"
    "s_add_u32 m0, s64, 4096\n"
    "s_mov_b32 s65, 36864\n"
    "buffer_load_dwordx4 v214, s[36:39], s65 offen offset:0 lds\n"
    "s_add_u32 m0, s64, 8192\n"
    "s_mov_b32 s65, 73728\n"
    "buffer_load_dwordx4 v214, s[36:39], s65 offen offset:0 lds\n"
    "s_add_u32 m0, s64, 12288\n"
    "s_mov_b32 s65, 110592\n"
    "buffer_load_dwordx4 v214, s[36:39], s65 offen offset:0 lds\n"
    "s_add_u32 s40, s22, s97\n"
    "s_addc_u32 s41, s23, 0\n"
    "s_and_b32 s41, s41, 0xffff\n"
    "s_mov_b32 s42, 1989648\n"
    "s_mov_b32 s43, 0x20000\n"
    "s_add_u32 m0, s93, 16384\n"
    "s_mov_b32 s65, 0\n"
    "buffer_load_dwordx4 v219, s[40:43], s65 offen offset:0 lds\n"
    "s_add_u32 m0, s93, 17408\n"
    "s_mov_b32 s65, 0\n"
    "buffer_load_dwordx4 v220, s[40:43], s65 offen offset:0 lds\n"
    "s_add_u32 m0, s93, 18432\n"
    "s_mov_b32 s65, 0\n"
    "buffer_load_dwordx4 v221, s[40:43], s65 offen offset:0 lds\n"
    "s_add_u32 m0, s93, 19456\n"
    "s_mov_b32 s65, 0\n"
    "buffer_load_dwordx4 v222, s[40:43], s65 offen offset:0 lds\n"
    "s_add_u32 m0, s93, 20480\n"
    "s_mov_b32 s65, 0\n"
    "buffer_load_dwordx4 v223, s[40:43], s65 offen offset:0 lds\n"
    "s_mov_b32 s60, s32\n"
    "s_and_b32 s61, s33, 0xffff\n"
    "s_mov_b32 s62, 1152\n"
    "s_mov_b32 s63, 0x20000\n"
    "buffer_load_ushort v226, v131, s[60:63], 0 offen offset:0\n"
    "buffer_load_ushort v227, v131, s[60:63], 0 offen offset:128\n"
    "buffer_load_ushort v228, v131, s[60:63], 0 offen offset:256\n"
    "buffer_load_ushort v229, v131, s[60:63], 0 offen offset:384\n"
    "buffer_load_ushort v230, v131, s[60:63], 0 offen offset:512\n"
    "buffer_load_ushort v231, v131, s[60:63], 0 offen offset:640\n"
    "buffer_load_ushort v232, v131, s[60:63], 0 offen offset:768\n"
    "buffer_load_ushort v233, v131, s[60:63], 0 offen offset:896\n"
    "buffer_load_ushort v206, v131, s[60:63], 0 offen offset:1024\n"
    "s_mov_b32 s56, s34\n"
    "s_and_b32 s57, s35, 0xffff\n"
    "s_mov_b32 s58, 50331648\n"
    "s_mov_b32 s59, 0x20000\n"
    "s_mov_b32 s96, 98304\n"
    "buffer_load_dword v210, off, s[56:59], s96 sc1\n"
    "s_add_u32 m0, s64, 36864\n"
    "s_mov_b32 s65, 128\n"
    "buffer_load_dwordx4 v214, s[36:39], s65 offen offset:0 lds\n"
    "s_add_u32 m0, s64, 40960\n"
    "s_mov_b32 s65, 36992\n"
    "buffer_load_dwordx4 v214, s[36:39], s65 offen offset:0 lds\n"
    "s_add_u32 m0, s64, 45056\n"
    "s_mov_b32 s65, 73856\n"
    "buffer_load_dwordx4 v214, s[36:39], s65 offen offset:0 lds\n"
    "s_add_u32 m0, s64, 49152\n"
    "s_mov_b32 s65, 110720\n"
    "buffer_load_dwordx4 v214, s[36:39], s65 offen offset:0 lds\n"
    "s_add_u32 m0, s93, 53248\n"
    "s_mov_b32 s65, 221184\n"
    "buffer_load_dwordx4 v219, s[40:43], s65 offen offset:0 lds\n"
    "s_add_u32 m0, s93, 54272\n"
    "s_mov_b32 s65, 221184\n"
    "buffer_load_dwordx4 v220, s[40:43], s65 offen offset:0 lds\n"
    "s_add_u32 m0, s93, 55296\n"
    "s_mov_b32 s65, 221184\n"
    "buffer_load_dwordx4 v221, s[40:43], s65 offen offset:0 lds\n"
    "s_add_u32 m0, s93, 56320\n"
    "s_mov_b32 s65, 221184\n"
    "buffer_load_dwordx4 v222, s[40:43], s65 offen offset:0 lds\n"
    "s_add_u32 m0, s93, 57344\n"
    "s_mov_b32 s65, 221184\n"
    "buffer_load_dwordx4 v223, s[40:43], s65 offen offset:0 lds\n"
    "s_waitcnt vmcnt(18)\n"
    "v_lshl_or_b32 v226, v226, 16, v226\n"
    "ds_write_b32 v225, v226 offset:0\n"
    "s_waitcnt lgkmcnt(0)\n"
    "s_barrier\n"
    "v_add_u32 v224, 16384, v224\n"
    "v_add_u32 v215, 0, v215\n"
    "ds_read_b128 a[0:3], v215 offset:0\n"
    "v_add_u32 v216, 0, v216\n"
    "ds_read_b128 a[8:11], v216 offset:0\n"
    "ds_read_b128 a[16:19], v215 offset:512\n"
    "ds_read_b128 a[24:27], v216 offset:512\n"
    "ds_read_b64_tr_b16 v[134:135], v224 offset:0\n"
    "ds_read_b64_tr_b16 v[136:137], v224 offset:1280\n"
    "ds_read_b64_tr_b16 v[138:139], v224 offset:64\n"
    "ds_read_b64_tr_b16 v[140:141], v224 offset:1344\n"
    "ds_read_b64_tr_b16 v[142:143], v224 offset:128\n"
    "ds_read_b64_tr_b16 v[144:145], v224 offset:1408\n"
    "ds_read_b64_tr_b16 v[146:147], v224 offset:192\n"
    "ds_read_b64_tr_b16 v[148:149], v224 offset:1472\n"
    "s_waitcnt lgkmcnt(6)\n"
    "v_mfma_f32_32x32x16_bf16 v[64:79], a[0:3], v[134:137], 0\n"
    "v_add_u32 v217, 0, v217\n"
    "ds_read_b128 a[4:7], v217 offset:0\n"
    "ds_read_b64_tr_b16 v[198:199], v224 offset:256\n"
    "ds_read_b64_tr_b16 v[200:201], v224 offset:1536\n"
    "s_add_u32 m0, s64, 73728\n"
    "s_mov_b32 s65, 256\n"
    "buffer_load_dwordx4 v214, s[36:39], s65 offen offset:0 lds\n"
    "s_waitcnt lgkmcnt(7)\n"
    "v_mfma_f32_32x32x16_bf16 v[80:95], a[0:3], v[138:141], 0\n"
    "v_add_u32 v218, 0, v218\n"
    "ds_read_b128 a[12:15], v218 offset:0\n"
    "ds_read_b64_tr_b16 v[202:203], v224 offset:5120\n"
    "ds_read_b64_tr_b16 v[204:205], v224 offset:6400\n"
    "s_add_u32 m0, s64, 77824\n"
    "s_mov_b32 s65, 37120\n"
    "buffer_load_dwordx4 v214, s[36:39], s65 offen offset:0 lds\n"
    "s_waitcnt vmcnt(19)\n"
    "v_lshl_or_b32 v227, v227, 16, v227\n"
    "s_waitcnt lgkmcnt(8)\n"
    "v_mfma_f32_32x32x16_bf16 v[150:165], a[0:3], v[142:145], 0\n"
    "ds_read_b128 a[20:23], v217 offset:512\n"
    "ds_read_b64_tr_b16 v[134:135], v224 offset:5184\n"
    "ds_read_b64_tr_b16 v[136:137], v224 offset:6464\n"
    "s_add_u32 m0, s64, 81920\n"
    "s_mov_b32 s65, 73984\n"
    "buffer_load_dwordx4 v214, s[36:39], s65 offen offset:0 lds\n"
    "s_waitcnt lgkmcnt(9)\n"
    "v_mfma_f32_32x32x16_bf16 v[166:181], a[0:3], v[146:149], 0\n"
    "ds_read_b128 a[28:31], v218 offset:512\n"
    "ds_read_b64_tr_b16 v[138:139], v224 offset:5248\n"
    "ds_read_b64_tr_b16 v[140:141], v224 offset:6528\n"
    "s_add_u32 m0, s64, 86016\n"
    "s_mov_b32 s65, 110848\n"
    "buffer_load_dwordx4 v214, s[36:39], s65 offen offset:0 lds\n"
    "s_waitcnt vmcnt(20)\n"
    "v_lshl_or_b32 v228, v228, 16, v228\n"
    "s_waitcnt lgkmcnt(9)\n"
    "v_mfma_f32_32x32x16_bf16 v[182:197], a[0:3], v[198:201], 0\n"
    "ds_read_b64_tr_b16 v[142:143], v224 offset:5312\n"
    "ds_read_b64_tr_b16 v[144:145], v224 offset:6592\n"
    "s_add_u32 m0, s93, 90112\n"
    "s_mov_b32 s65, 442368\n"
    "buffer_load_dwordx4 v219, s[40:43], s65 offen offset:0 lds\n"
    "s_waitcnt lgkmcnt(8)\n"
    "v_mfma_f32_32x32x16_bf16 v[64:79], a[8:11], v[202:205], v[64:79]\n"
    "ds_read_b64_tr_b16 v[146:147], v224 offset:5376\n"
    "ds_read_b64_tr_b16 v[148:149], v224 offset:6656\n"
    "s_add_u32 m0, s93, 91136\n"
    "s_mov_b32 s65, 442368\n"
    "buffer_load_dwordx4 v220, s[40:43], s65 offen offset:0 lds\n"
    "s_waitcnt vmcnt(21)\n"
    "v_lshl_or_b32 v229, v229, 16, v229\n"
    "s_waitcnt lgkmcnt(7)\n"
    "v_mfma_f32_32x32x16_bf16 v[80:95], a[8:11], v[134:137], v[80:95]\n"
    "ds_read_b64_tr_b16 v[198:199], v224 offset:10240\n"
    "ds_read_b64_tr_b16 v[200:201], v224 offset:11520\n"
    "s_add_u32 m0, s93, 92160\n"
    "s_mov_b32 s65, 442368\n"
    "buffer_load_dwordx4 v221, s[40:43], s65 offen offset:0 lds\n"
    "s_waitcnt lgkmcnt(6)\n"
    "v_mfma_f32_32x32x16_bf16 v[150:165], a[8:11], v[138:141], v[150:165]\n"
    "ds_read_b64_tr_b16 v[202:203], v224 offset:10304\n"
    "ds_read_b64_tr_b16 v[204:205], v224 offset:11584\n"
    "s_add_u32 m0, s93, 93184\n"
    "s_mov_b32 s65, 442368\n"
    "buffer_load_dwordx4 v222, s[40:43], s65 offen offset:0 lds\n"
    "s_waitcnt vmcnt(22)\n"
    "v_lshl_or_b32 v230, v230, 16, v230\n"
    "s_waitcnt lgkmcnt(6)\n"
    "v_mfma_f32_32x32x16_bf16 v[166:181], a[8:11], v[142:145], v[166:181]\n"
    "ds_read_b64_tr_b16 v[134:135], v224 offset:10368\n"
    "ds_read_b64_tr_b16 v[136:137], v224 offset:11648\n"
    "s_add_u32 m0, s93, 94208\n"
    "s_mov_b32 s65, 442368\n"
    "buffer_load_dwordx4 v223, s[40:43], s65 offen offset:0 lds\n"
    "s_waitcnt lgkmcnt(6)\n"
    "v_mfma_f32_32x32x16_bf16 v[182:197], a[8:11], v[146:149], v[182:197]\n"
    "ds_read_b64_tr_b16 v[138:139], v224 offset:10432\n"
    "ds_read_b64_tr_b16 v[140:141], v224 offset:11712\n"
    "s_add_u32 m0, s64, 110592\n"
    "s_mov_b32 s65, 384\n"
    "buffer_load_dwordx4 v214, s[36:39], s65 offen offset:0 lds\n"
    "s_waitcnt lgkmcnt(6)\n"
    "v_mfma_f32_32x32x16_bf16 v[64:79], a[16:19], v[198:201], v[64:79]\n"
    "ds_read_b64_tr_b16 v[142:143], v224 offset:10496\n"
    "ds_read_b64_tr_b16 v[144:145], v224 offset:11776\n"
    "s_add_u32 m0, s64, 114688\n"
    "s_mov_b32 s65, 37248\n"
    "buffer_load_dwordx4 v214, s[36:39], s65 offen offset:0 lds\n"
    "s_waitcnt vmcnt(24)\n"
    "v_lshl_or_b32 v231, v231, 16, v231\n"
    "s_waitcnt lgkmcnt(6)\n"
    "v_mfma_f32_32x32x16_bf16 v[80:95], a[16:19], v[202:205], v[80:95]\n"
    "ds_read_b64_tr_b16 v[146:147], v224 offset:15360\n"
    "ds_read_b64_tr_b16 v[148:149], v224 offset:16640\n"
    "s_add_u32 m0, s64, 118784\n"
    "s_mov_b32 s65, 74112\n"
    "buffer_load_dwordx4 v214, s[36:39], s65 offen offset:0 lds\n"
    "s_waitcnt lgkmcnt(6)\n"
    "v_mfma_f32_32x32x16_bf16 v[150:165], a[16:19], v[134:137], v[150:165]\n"
    "ds_read_b64_tr_b16 v[198:199], v224 offset:15424\n"
    "ds_read_b64_tr_b16 v[200:201], v224 offset:16704\n"
    "s_add_u32 m0, s64, 122880\n"
    "s_mov_b32 s65, 110976\n"
    "buffer_load_dwordx4 v214, s[36:39], s65 offen offset:0 lds\n"
    "s_waitcnt vmcnt(25)\n"
    "v_lshl_or_b32 v232, v232, 16, v232\n"
    "s_waitcnt lgkmcnt(6)\n"
    "v_mfma_f32_32x32x16_bf16 v[166:181], a[16:19], v[138:141], v[166:181]\n"
    "ds_read_b64_tr_b16 v[202:203], v224 offset:15488\n"
    "ds_read_b64_tr_b16 v[204:205], v224 offset:16768\n"
    "s_add_u32 m0, s93, 126976\n"
    "s_mov_b32 s65, 663552\n"
    "buffer_load_dwordx4 v219, s[40:43], s65 offen offset:0 lds\n"
    "v_add_u32 v225, 36864, v225\n"
    "s_waitcnt vmcnt(14)\n"
    "ds_write_b32 v225, v227 offset:0\n"
    "s_waitcnt lgkmcnt(7)\n"
    "v_mfma_f32_32x32x16_bf16 v[182:197], a[16:19], v[142:145], v[182:197]\n"
    "ds_read_b64_tr_b16 v[134:135], v224 offset:15552\n"
    "ds_read_b64_tr_b16 v[136:137], v224 offset:16832\n"
    "s_add_u32 m0, s93, 128000\n"
    "s_mov_b32 s65, 663552\n"
    "buffer_load_dwordx4 v220, s[40:43], s65 offen offset:0 lds\n"
    "v_lshl_or_b32 v233, v233, 16, v233\n"
    "s_waitcnt lgkmcnt(7)\n"
    "v_mfma_f32_32x32x16_bf16 v[64:79], a[24:27], v[146:149], v[64:79]\n"
    "ds_read_b64_tr_b16 v[138:139], v224 offset:15616\n"
    "ds_read_b64_tr_b16 v[140:141], v224 offset:16896\n"
    "s_add_u32 m0, s93, 129024\n"
    "s_mov_b32 s65, 663552\n"
    "buffer_load_dwordx4 v221, s[40:43], s65 offen offset:0 lds\n"
    "s_waitcnt lgkmcnt(7)\n"
    "v_mfma_f32_32x32x16_bf16 v[80:95], a[24:27], v[198:201], v[80:95]\n"
    "s_waitcnt lgkmcnt(4)\n"
    "s_barrier\n"
    "v_add_u32 v224, 36864, v224\n"
    "v_add_u32 v215, 36864, v215\n"
    "ds_read_b128 a[32:35], v215 offset:0\n"
    "ds_read_b64_tr_b16 v[142:143], v224 offset:0\n"
    "ds_read_b64_tr_b16 v[144:145], v224 offset:1280\n"
    "v_lshl_or_b32 v206, v206, 16, v206\n"
    "v_mfma_f32_32x32x16_bf16 v[150:165], a[24:27], v[202:205], v[150:165]\n"
    "v_add_u32 v216, 36864, v216\n"
    "ds_read_b128 a[40:43], v216 offset:0\n"
    "ds_read_b64_tr_b16 v[146:147], v224 offset:64\n"
    "ds_read_b64_tr_b16 v[148:149], v224 offset:1344\n"
    "s_add_u32 m0, s93, 130048\n"
    "s_mov_b32 s65, 663552\n"
    "buffer_load_dwordx4 v222, s[40:43], s65 offen offset:0 lds\n"
    "s_waitcnt lgkmcnt(8)\n"
    "v_mfma_f32_32x32x16_bf16 v[166:181], a[24:27], v[134:137], v[166:181]\n"
    "ds_read_b128 a[48:51], v215 offset:512\n"
    "ds_read_b64_tr_b16 v[198:199], v224 offset:128\n"
    "ds_read_b64_tr_b16 v[200:201], v224 offset:1408\n"
    "s_mul_i32 s97, s68, 18\n"
    "s_lshr_b32 s96, s69, 1\n"
    "s_mul_i32 s96, s96, 9\n"
    "s_waitcnt lgkmcnt(9)\n"
    "v_mfma_f32_32x32x16_bf16 v[182:197], a[24:27], v[138:141], v[182:197]\n"
    "ds_read_b128 a[56:59], v216 offset:512\n"
    "ds_read_b64_tr_b16 v[202:203], v224 offset:192\n"
    "ds_read_b64_tr_b16 v[204:205], v224 offset:1472\n"
    "s_add_u32 s97, s97, s96\n"
    "s_lshl_b32 s97, s97, 7\n"
    "s_waitcnt lgkmcnt(9)\n"
    "v_mfma_f32_32x32x16_bf16 v[64:79], a[32:35], v[142:145], v[64:79]\n"
    "v_add_u32 v217, 36864, v217\n"
    "ds_read_b128 a[36:39], v217 offset:0\n"
    "ds_read_b64_tr_b16 v[134:135], v224 offset:256\n"
    "ds_read_b64_tr_b16 v[136:137], v224 offset:1536\n"
    "s_add_u32 s44, s26, s97\n"
    "s_addc_u32 s45, s27, 0\n"
    "s_waitcnt lgkmcnt(9)\n"
    "v_mfma_f32_32x32x16_bf16 v[80:95], a[32:35], v[146:149], v[80:95]\n"
    "v_add_u32 v218, 36864, v218\n"
    "ds_read_b128 a[44:47], v218 offset:0\n"
    "ds_read_b64_tr_b16 v[138:139], v224 offset:5120\n"
    "ds_read_b64_tr_b16 v[140:141], v224 offset:6400\n"
    "s_and_b32 s45, s45, 0xffff\n"
    "s_mov_b32 s46, 5299840\n"
    "s_waitcnt lgkmcnt(9)\n"
    "v_mfma_f32_32x32x16_bf16 v[150:165], a[32:35], v[198:201], v[150:165]\n"
    "ds_read_b128 a[52:55], v217 offset:512\n"
    "ds_read_b64_tr_b16 v[142:143], v224 offset:5184\n"
    "ds_read_b64_tr_b16 v[144:145], v224 offset:6464\n"
    "s_mov_b32 s47, 0x20000\n"
    "v_mul_u32_u24 v239, 9216, v125\n"
    "v_and_b32 v128, 3, v124\n"
    "s_waitcnt lgkmcnt(9)\n"
    "v_mfma_f32_32x32x16_bf16 v[166:181], a[32:35], v[202:205], v[166:181]\n"
    "ds_read_b128 a[60:63], v218 offset:512\n"
    "ds_read_b64_tr_b16 v[146:147], v224 offset:5248\n"
    "ds_read_b64_tr_b16 v[148:149], v224 offset:6528\n"
    "v_lshl_add_u32 v239, v128, 4, v239\n"
    "v_lshl_add_u32 v239, v126, 6, v239\n"
    "s_sub_u32 s96, 9216, s97\n"
    "s_waitcnt lgkmcnt(9)\n"
    "v_mfma_f32_32x32x16_bf16 v[182:197], a[32:35], v[134:137], v[182:197]\n"
    "ds_read_b64_tr_b16 v[198:199], v224 offset:5312\n"
    "ds_read_b64_tr_b16 v[200:201], v224 offset:6592\n"
    "s_add_u32 s60, s28, s97\n"
    "s_addc_u32 s61, s29, 0\n"
    "s_and_b32 s61, s61, 0xffff\n"
    "s_mov_b32 s62, s96\n"
    "s_waitcnt lgkmcnt(8)\n"
    "v_mfma_f32_32x32x16_bf16 v[64:79], a[40:43], v[138:141], v[64:79]\n"
    "ds_read_b64_tr_b16 v[202:203], v224 offset:5376\n"
    "ds_read_b64_tr_b16 v[204:205], v224 offset:6656\n"
    "v_lshl_add_u32 v209, s66, 6, v124\n"
    "v_lshlrev_b32 v131, 1, v209\n"
    "buffer_load_ushort v207, v131, s[60:63], 0 offen offset:0\n"
    "s_waitcnt lgkmcnt(7)\n"
    "v_mfma_f32_32x32x16_bf16 v[80:95], a[40:43], v[142:145], v[80:95]\n"
    "ds_read_b64_tr_b16 v[134:135], v224 offset:10240\n"
    "ds_read_b64_tr_b16 v[136:137], v224 offset:11520\n"
    "s_add_u32 m0, s93, 131072\n"
    "s_mov_b32 s65, 663552\n"
    "buffer_load_dwordx4 v223, s[40:43], s65 offen offset:0 lds\n"
    "s_waitcnt lgkmcnt(6)\n"
    "v_mfma_f32_32x32x16_bf16 v[150:165], a[40:43], v[146:149], v[150:165]\n"
    "ds_read_b64_tr_b16 v[138:139], v224 offset:10304\n"
    "ds_read_b64_tr_b16 v[140:141], v224 offset:11584\n"
    "buffer_load_ushort v208, v131, s[60:63], 0 offen offset:512\n"
    "s_mul_i32 s96, s68, 2304\n"
    "s_waitcnt lgkmcnt(6)\n"
    "v_mfma_f32_32x32x16_bf16 v[166:181], a[40:43], v[198:201], v[166:181]\n"
    "ds_read_b64_tr_b16 v[142:143], v224 offset:10368\n"
    "ds_read_b64_tr_b16 v[144:145], v224 offset:11648\n"
    "s_add_u32 s60, s26, s96\n"
    "s_addc_u32 s61, s27, 0\n"
    "s_and_b32 s61, s61, 0xffff\n"
    "s_mov_b32 s62, 5301504\n"
    "s_waitcnt lgkmcnt(6)\n"
    "v_mfma_f32_32x32x16_bf16 v[182:197], a[40:43], v[202:205], v[182:197]\n"
    "ds_read_b64_tr_b16 v[146:147], v224 offset:10432\n"
    "ds_read_b64_tr_b16 v[148:149], v224 offset:11712\n"
    "s_mov_b32 s63, 0x20000\n"
    "s_mov_b32 s52, s24\n"
    "s_and_b32 s53, s25, 0xffff\n"
    "s_mov_b32 s54, 663552\n"
    "s_waitcnt lgkmcnt(6)\n"
    "v_mfma_f32_32x32x16_bf16 v[64:79], a[48:51], v[134:137], v[64:79]\n"
    "ds_read_b64_tr_b16 v[198:199], v224 offset:10496\n"
    "ds_read_b64_tr_b16 v[200:201], v224 offset:11776\n"
    "s_mov_b32 s55, 0x20000\n"
    "s_mul_i32 s96, s68, 1327104\n"
    "s_add_u32 s48, s30, s96\n"
    "s_addc_u32 s49, s31, 0\n"
    "s_waitcnt lgkmcnt(6)\n"
    "v_mfma_f32_32x32x16_bf16 v[80:95], a[48:51], v[138:141], v[80:95]\n"
    "ds_read_b64_tr_b16 v[202:203], v224 offset:15360\n"
    "ds_read_b64_tr_b16 v[204:205], v224 offset:16640\n"
    "s_and_b32 s49, s49, 0xffff\n"
    "s_mov_b32 s50, 1327104\n"
    "s_mov_b32 s51, 0x20000\n"
    "s_and_b32 s13, s13, 0xffff\n"
    "s_waitcnt lgkmcnt(6)\n"
    "v_mfma_f32_32x32x16_bf16 v[150:165], a[48:51], v[142:145], v[150:165]\n"
    "ds_read_b64_tr_b16 v[134:135], v224 offset:15424\n"
    "ds_read_b64_tr_b16 v[136:137], v224 offset:16704\n"
    "s_mov_b32 s14, 2359296\n"
    "s_mov_b32 s15, 0x20000\n"
    "v_readfirstlane_b32 s70, v210\n"
    "s_nop 0\n"
    "s_add_u32 s70, s70, 32\n"
    "s_waitcnt lgkmcnt(6)\n"
    "v_mfma_f32_32x32x16_bf16 v[166:181], a[48:51], v[146:149], v[166:181]\n"
    "ds_read_b64_tr_b16 v[138:139], v224 offset:15488\n"
    "ds_read_b64_tr_b16 v[140:141], v224 offset:16768\n"
    "v_add_u32 v225, 36864, v225\n"
    "s_waitcnt vmcnt(11)\n"
    "ds_write_b32 v225, v228 offset:0\n"
    "s_xor_b32 s21, s70, 1540483477\n"
    "s_waitcnt lgkmcnt(7)\n"
    "v_mfma_f32_32x32x16_bf16 v[182:197], a[48:51], v[198:201], v[182:197]\n"
    "ds_read_b64_tr_b16 v[142:143], v224 offset:15552\n"
    "ds_read_b64_tr_b16 v[144:145], v224 offset:16832\n"
    "s_lshl_b32 s96, s67, 2\n"
    "s_add_u32 s96, s96, s68\n"
    "s_lshl_b32 s22, s96, 2\n"
    "s_add_u32 s22, s22, s66\n"
    "s_waitcnt lgkmcnt(7)\n"
    "v_mfma_f32_32x32x16_bf16 v[64:79], a[56:59], v[202:205], v[64:79]\n"
    "ds_read_b64_tr_b16 v[146:147], v224 offset:15616\n"
    "ds_read_b64_tr_b16 v[148:149], v224 offset:16896\n"
    "s_lshl_b32 s22, s22, 8\n"
    "s_add_u32 s22, s22, 32768\n"
    "s_lshl_b32 s97, s69, 6\n"
    "s_add_u32 s89, s22, s97\n"
    "s_waitcnt lgkmcnt(7)\n"
    "v_mfma_f32_32x32x16_bf16 v[80:95], a[56:59], v[134:137], v[80:95]\n"
    "s_waitcnt lgkmcnt(4)\n"
    "s_barrier\n"
    "v_add_u32 v224, 36864, v224\n"
    "v_add_u32 v215, 36864, v215\n"
    "ds_read_b128 a[64:67], v215 offset:0\n"
    "ds_read_b64_tr_b16 v[198:199], v224 offset:0\n"
    "ds_read_b64_tr_b16 v[200:201], v224 offset:1280\n"
    "v_mfma_f32_32x32x16_bf16 v[150:165], a[56:59], v[138:141], v[150:165]\n"
    "v_add_u32 v216, 36864, v216\n"
    "ds_read_b128 a[72:75], v216 offset:0\n"
    "ds_read_b64_tr_b16 v[202:203], v224 offset:64\n"
    "ds_read_b64_tr_b16 v[204:205], v224 offset:1344\n"
    "s_add_u32 m0, s64, 0\n"
    "s_mov_b32 s65, 512\n"
    "buffer_load_dwordx4 v214, s[36:39], s65 offen offset:0 lds\n"
    "s_waitcnt lgkmcnt(8)\n"
    "v_mfma_f32_32x32x16_bf16 v[166:181], a[56:59], v[142:145], v[166:181]\n"
    "ds_read_b128 a[80:83], v215 offset:512\n"
    "ds_read_b64_tr_b16 v[134:135], v224 offset:128\n"
    "ds_read_b64_tr_b16 v[136:137], v224 offset:1408\n"
    "s_waitcnt lgkmcnt(9)\n"
    "v_mfma_f32_32x32x16_bf16 v[182:197], a[56:59], v[146:149], v[182:197]\n"
    "ds_read_b128 a[88:91], v216 offset:512\n"
    "ds_read_b64_tr_b16 v[138:139], v224 offset:192\n"
    "ds_read_b64_tr_b16 v[140:141], v224 offset:1472\n"
    "s_add_u32 m0, s64, 4096\n"
    "s_mov_b32 s65, 37376\n"
    "buffer_load_dwordx4 v214, s[36:39], s65 offen offset:0 lds\n"
    "s_waitcnt lgkmcnt(9)\n"
    "v_mfma_f32_32x32x16_bf16 v[64:79], a[64:67], v[198:201], v[64:79]\n"
    "v_add_u32 v217, 36864, v217\n"
    "ds_read_b128 a[68:71], v217 offset:0\n"
    "ds_read_b64_tr_b16 v[142:143], v224 offset:256\n"
    "ds_read_b64_tr_b16 v[144:145], v224 offset:1536\n"
    "s_waitcnt lgkmcnt(9)\n"
    "v_mfma_f32_32x32x16_bf16 v[80:95], a[64:67], v[202:205], v[80:95]\n"
    "v_add_u32 v218, 36864, v218\n"
    "ds_read_b128 a[76:79], v218 offset:0\n"
    "ds_read_b64_tr_b16 v[146:147], v224 offset:5120\n"
    "ds_read_b64_tr_b16 v[148:149], v224 offset:6400\n"
    "s_add_u32 m0, s64, 8192\n"
    "s_mov_b32 s65, 74240\n"
    "buffer_load_dwordx4 v214, s[36:39], s65 offen offset:0 lds\n"
    "s_waitcnt lgkmcnt(9)\n"
    "v_mfma_f32_32x32x16_bf16 v[150:165], a[64:67], v[134:137], v[150:165]\n"
    "ds_read_b128 a[84:87], v217 offset:512\n"
    "ds_read_b64_tr_b16 v[198:199], v224 offset:5184\n"
    "ds_read_b64_tr_b16 v[200:201], v224 offset:6464\n"
    "s_getpc_b64 s[18:19]\n"
    "LpcK_%=:\n"
    "v_lshlrev_b32 v128, 7, v124\n"
    "v_lshl_add_u32 v128, s66, 13, v128\n"
    "v_min_u32 v131, LceK_%=-LpcK_%=-4, v128\n"
    "global_load_dword v234, v131, s[18:19]\n"
    "v_add_u32 v128, 0x8000, v128\n"
    "v_min_u32 v131, LceK_%=-LpcK_%=-4, v128\n"
    "global_load_dword v234, v131, s[18:19]\n"
    "v_add_u32 v128, 0x8000, v128\n"
    "s_waitcnt lgkmcnt(9)\n"
    "v_mfma_f32_32x32x16_bf16 v[166:181], a[64:67], v[138:141], v[166:181]\n"
    "ds_read_b128 a[92:95], v218 offset:512\n"
    "ds_read_b64_tr_b16 v[202:203], v224 offset:5248\n"
    "ds_read_b64_tr_b16 v[204:205], v224 offset:6528\n"
    "s_add_u32 m0, s64, 12288\n"
    "s_mov_b32 s65, 111104\n"
    "buffer_load_dwordx4 v214, s[36:39], s65 offen offset:0 lds\n"
    "s_waitcnt lgkmcnt(9)\n"
    "v_mfma_f32_32x32x16_bf16 v[182:197], a[64:67], v[142:145], v[182:197]\n"
    "ds_read_b64_tr_b16 v[134:135], v224 offset:5312\n"
    "ds_read_b64_tr_b16 v[136:137], v224 offset:6592\n"
    "s_waitcnt lgkmcnt(8)\n"
    "v_mfma_f32_32x32x16_bf16 v[64:79], a[72:75], v[146:149], v[64:79]\n"
    "ds_read_b64_tr_b16 v[138:139], v224 offset:5376\n"
    "ds_read_b64_tr_b16 v[140:141], v224 offset:6656\n"
    "s_add_u32 m0, s93, 16384\n"
    "s_mov_b32 s65, 884736\n"
    "buffer_load_dwordx4 v219, s[40:43], s65 offen offset:0 lds\n"
    "s_waitcnt lgkmcnt(7)\n"
    "v_mfma_f32_32x32x16_bf16 v[80:95], a[72:75], v[198:201], v[80:95]\n"
    "ds_read_b64_tr_b16 v[142:143], v224 offset:10240\n"
    "ds_read_b64_tr_b16 v[144:145], v224 offset:11520\n"
    "v_and_b32 v128, 3, v124\n"
    "s_waitcnt lgkmcnt(6)\n"
    "v_mfma_f32_32x32x16_bf16 v[150:165], a[72:75], v[202:205], v[150:165]\n"
    "ds_read_b64_tr_b16 v[146:147], v224 offset:10304\n"
    "ds_read_b64_tr_b16 v[148:149], v224 offset:11584\n"
    "s_add_u32 m0, s93, 17408\n"
    "s_mov_b32 s65, 884736\n"
    "buffer_load_dwordx4 v220, s[40:43], s65 offen offset:0 lds\n"
    "s_waitcnt lgkmcnt(6)\n"
    "v_mfma_f32_32x32x16_bf16 v[166:181], a[72:75], v[134:137], v[166:181]\n"
    "ds_read_b64_tr_b16 v[198:199], v224 offset:10368\n"
    "ds_read_b64_tr_b16 v[200:201], v224 offset:11648\n"
    "v_lshlrev_b32 v127, 1, v127\n"
    "v_xor_b32 v127, v127, v128\n"
    "v_lshl_add_u32 v240, v127, 4, v129\n"
    "s_waitcnt lgkmcnt(6)\n"
    "v_mfma_f32_32x32x16_bf16 v[182:197], a[72:75], v[138:141], v[182:197]\n"
    "ds_read_b64_tr_b16 v[202:203], v224 offset:10432\n"
    "ds_read_b64_tr_b16 v[204:205], v224 offset:11712\n"
    "s_add_u32 m0, s93, 18432\n"
    "s_mov_b32 s65, 884736\n"
    "buffer_load_dwordx4 v221, s[40:43], s65 offen offset:0 lds\n"
    "s_waitcnt lgkmcnt(6)\n"
    "v_mfma_f32_32x32x16_bf16 v[64:79], a[80:83], v[142:145], v[64:79]\n"
    "ds_read_b64_tr_b16 v[134:135], v224 offset:10496\n"
    "ds_read_b64_tr_b16 v[136:137], v224 offset:11776\n"
    "s_lshr_b32 s96, s69, 1\n"
    "v_xor_b32 v127, s96, v126\n"
    "v_lshl_add_u32 v240, v127, 6, v240\n"
    "s_waitcnt lgkmcnt(6)\n"
    "v_mfma_f32_32x32x16_bf16 v[80:95], a[80:83], v[146:149], v[80:95]\n"
    "ds_read_b64_tr_b16 v[138:139], v224 offset:15360\n"
    "ds_read_b64_tr_b16 v[140:141], v224 offset:16640\n"
    "s_add_u32 m0, s93, 19456\n"
    "s_mov_b32 s65, 884736\n"
    "buffer_load_dwordx4 v222, s[40:43], s65 offen offset:0 lds\n"
    "s_waitcnt lgkmcnt(6)\n"
    "v_mfma_f32_32x32x16_bf16 v[150:165], a[80:83], v[198:201], v[150:165]\n"
    "ds_read_b64_tr_b16 v[142:143], v224 offset:15424\n"
    "ds_read_b64_tr_b16 v[144:145], v224 offset:16704\n"
    "v_lshl_add_u32 v236, v126, 10, v236\n"
    "s_and_b32 s96, s66, 1\n"
    "s_lshl_b32 s97, s96, 9\n"
    "s_waitcnt lgkmcnt(6)\n"
    "v_mfma_f32_32x32x16_bf16 v[166:181], a[80:83], v[202:205], v[166:181]\n"
    "ds_read_b64_tr_b16 v[146:147], v224 offset:15488\n"
    "ds_read_b64_tr_b16 v[148:149], v224 offset:16768\n"
    "s_add_u32 m0, s93, 20480\n"
    "s_mov_b32 s65, 884736\n"
    "buffer_load_dwordx4 v223, s[40:43], s65 offen offset:0 lds\n"
    "v_add_u32 v225, 36864, v225\n"
    "s_waitcnt vmcnt(12)\n"
    "ds_write_b32 v225, v229 offset:0\n"
    "s_waitcnt lgkmcnt(7)\n"
    "v_mfma_f32_32x32x16_bf16 v[182:197], a[80:83], v[134:137], v[182:197]\n"
    "ds_read_b64_tr_b16 v[198:199], v224 offset:15552\n"
    "ds_read_b64_tr_b16 v[200:201], v224 offset:16832\n"
    "v_add_u32 v236, s97, v236\n"
    "v_add_u32 v237, 0x10000, v236\n"
    "v_add_u32 v238, 0x20000, v236\n"
    "s_waitcnt lgkmcnt(7)\n"
    "v_mfma_f32_32x32x16_bf16 v[64:79], a[88:91], v[138:141], v[64:79]\n"
    "ds_read_b64_tr_b16 v[202:203], v224 offset:15616\n"
    "ds_read_b64_tr_b16 v[204:205], v224 offset:16896\n"
    "s_lshl_b32 s97, s96, 7\n"
    "s_add_u32 s97, s97, 147968\n"
    "v_lshl_add_u32 v244, v126, 4, s97\n"
    "s_waitcnt lgkmcnt(7)\n"
    "v_mfma_f32_32x32x16_bf16 v[80:95], a[88:91], v[142:145], v[80:95]\n"
    "s_waitcnt lgkmcnt(4)\n"
    "s_barrier\n"
    "v_add_u32 v224, 36864, v224\n"
    "v_add_u32 v215, 36864, v215\n"
    "ds_read_b128 a[96:99], v215 offset:0\n"
    "ds_read_b64_tr_b16 v[134:135], v224 offset:0\n"
    "ds_read_b64_tr_b16 v[136:137], v224 offset:1280\n"
    "v_mfma_f32_32x32x16_bf16 v[150:165], a[88:91], v[146:149], v[150:165]\n"
    "v_add_u32 v216, 36864, v216\n"
    "ds_read_b128 a[104:107], v216 offset:0\n"
    "ds_read_b64_tr_b16 v[138:139], v224 offset:64\n"
    "ds_read_b64_tr_b16 v[140:141], v224 offset:1344\n"
    "s_add_u32 m0, s64, 36864\n"
    "s_mov_b32 s65, 640\n"
    "buffer_load_dwordx4 v214, s[36:39], s65 offen offset:0 lds\n"
    "s_waitcnt lgkmcnt(8)\n"
    "v_mfma_f32_32x32x16_bf16 v[166:181], a[88:91], v[198:201], v[166:181]\n"
    "ds_read_b128 a[112:115], v215 offset:512\n"
    "ds_read_b64_tr_b16 v[142:143], v224 offset:128\n"
    "ds_read_b64_tr_b16 v[144:145], v224 offset:1408\n"
    "s_and_b32 s99, s67, 7\n"
    "s_lshl_b32 s99, s99, 2\n"
    "s_add_u32 s99, s99, s69\n"
    "s_lshl_b32 s99, s99, 2\n"
    "s_add_u32 s99, s99, s66\n"
    "s_lshl_b32 s99, s99, 1\n"
    "s_lshr_b32 s65, s67, 3\n"
    "s_add_u32 s99, s99, s65\n"
    "s_mul_i32 s99, s99, 41\n"
    "s_add_u32 s99, s99, 0\n"
    "v_min_u32 v132, 40, v124\n"
    "v_add_u32 v132, s99, v132\n"
    "v_mul_u32_u24 v133, 3641, v132\n"
    "v_lshrrev_b32 v133, 16, v133\n"
    "v_lshlrev_b32 v132, 7, v132\n"
    "v_mul_u32_u24 v133, 6912, v133\n"
    "v_add_u32 v132, v132, v133\n"
    "buffer_load_dword v234, v132, s[60:63], 0 offen\n"
    "s_mov_b32 s94, 0\n"
    "s_waitcnt lgkmcnt(9)\n"
    "v_mfma_f32_32x32x16_bf16 v[182:197], a[88:91], v[202:205], v[182:197]\n"
    "ds_read_b128 a[120:123], v216 offset:512\n"
    "ds_read_b64_tr_b16 v[146:147], v224 offset:192\n"
    "ds_read_b64_tr_b16 v[148:149], v224 offset:1472\n"
    "s_add_u32 m0, s64, 40960\n"
    "s_mov_b32 s65, 37504\n"
    "buffer_load_dwordx4 v214, s[36:39], s65 offen offset:0 lds\n"
    "s_waitcnt lgkmcnt(9)\n"
    "v_mfma_f32_32x32x16_bf16 v[64:79], a[96:99], v[134:137], v[64:79]\n"
    "v_add_u32 v217, 36864, v217\n"
    "ds_read_b128 a[100:103], v217 offset:0\n"
    "ds_read_b64_tr_b16 v[198:199], v224 offset:256\n"
    "ds_read_b64_tr_b16 v[200:201], v224 offset:1536\n"
    "s_mov_b32 s95, -1\n"
    "s_waitcnt lgkmcnt(9)\n"
    "v_mfma_f32_32x32x16_bf16 v[80:95], a[96:99], v[138:141], v[80:95]\n"
    "v_add_u32 v218, 36864, v218\n"
    "ds_read_b128 a[108:111], v218 offset:0\n"
    "ds_read_b64_tr_b16 v[202:203], v224 offset:5120\n"
    "ds_read_b64_tr_b16 v[204:205], v224 offset:6400\n"
    "s_add_u32 m0, s64, 45056\n"
    "s_mov_b32 s65, 74368\n"
    "buffer_load_dwordx4 v214, s[36:39], s65 offen offset:0 lds\n"
    "s_waitcnt lgkmcnt(9)\n"
    "v_mfma_f32_32x32x16_bf16 v[150:165], a[96:99], v[142:145], v[150:165]\n"
    "ds_read_b128 a[116:119], v217 offset:512\n"
    "ds_read_b64_tr_b16 v[134:135], v224 offset:5184\n"
    "ds_read_b64_tr_b16 v[136:137], v224 offset:6464\n"
    "v_lshlrev_b32 v209, 2, v209\n"
    "v_add_u32 v209, 147968, v209\n"
    "s_waitcnt lgkmcnt(9)\n"
    "v_mfma_f32_32x32x16_bf16 v[166:181], a[96:99], v[146:149], v[166:181]\n"
    "ds_read_b128 a[124:127], v218 offset:512\n"
    "ds_read_b64_tr_b16 v[138:139], v224 offset:5248\n"
    "ds_read_b64_tr_b16 v[140:141], v224 offset:6528\n"
    "s_add_u32 m0, s64, 49152\n"
    "s_mov_b32 s65, 111232\n"
    "buffer_load_dwordx4 v214, s[36:39], s65 offen offset:0 lds\n"
    "v_lshlrev_b32 v207, 16, v207\n"
    "s_waitcnt lgkmcnt(9)\n"
    "v_mfma_f32_32x32x16_bf16 v[182:197], a[96:99], v[198:201], v[182:197]\n"
    "ds_read_b64_tr_b16 v[142:143], v224 offset:5312\n"
    "ds_read_b64_tr_b16 v[144:145], v224 offset:6592\n"
    "s_waitcnt vmcnt(16)\n"
    "v_lshlrev_b32 v208, 16, v208\n"
    "ds_write_b32 v209, v207 offset:0\n"
    "s_waitcnt lgkmcnt(9)\n"
    "v_mfma_f32_32x32x16_bf16 v[64:79], a[104:107], v[202:205], v[64:79]\n"
    "ds_read_b64_tr_b16 v[146:147], v224 offset:5376\n"
    "ds_read_b64_tr_b16 v[148:149], v224 offset:6656\n"
    "s_add_u32 m0, s93, 53248\n"
    "s_mov_b32 s65, 1105920\n"
    "buffer_load_dwordx4 v219, s[40:43], s65 offen offset:0 lds\n"
    "s_waitcnt lgkmcnt(8)\n"
    "v_mfma_f32_32x32x16_bf16 v[80:95], a[104:107], v[134:137], v[80:95]\n"
    "ds_read_b64_tr_b16 v[198:199], v224 offset:10240\n"
    "ds_read_b64_tr_b16 v[200:201], v224 offset:11520\n"
    "ds_write_b32 v209, v208 offset:1024\n"
    "s_lshl_b32 s96, s67, 2\n"
    "s_waitcnt lgkmcnt(8)\n"
    "v_mfma_f32_32x32x16_bf16 v[150:165], a[104:107], v[138:141], v[150:165]\n"
    "ds_read_b64_tr_b16 v[202:203], v224 offset:10304\n"
    "ds_read_b64_tr_b16 v[204:205], v224 offset:11584\n"
    "s_add_u32 m0, s93, 54272\n"
    "s_mov_b32 s65, 1105920\n"
    "buffer_load_dwordx4 v220, s[40:43], s65 offen offset:0 lds\n"
    "s_waitcnt lgkmcnt(8)\n"
    "v_mfma_f32_32x32x16_bf16 v[166:181], a[104:107], v[142:145], v[166:181]\n"
    "ds_read_b64_tr_b16 v[134:135], v224 offset:10368\n"
    "ds_read_b64_tr_b16 v[136:137], v224 offset:11648\n"
    "s_add_u32 s96, s96, s68\n"
    "s_lshr_b32 s97, s68, 1\n"
    "s_lshl_b32 s91, s69, 1\n"
    "s_waitcnt lgkmcnt(7)\n"
    "v_mfma_f32_32x32x16_bf16 v[182:197], a[104:107], v[146:149], v[182:197]\n"
    "ds_read_b64_tr_b16 v[138:139], v224 offset:10432\n"
    "ds_read_b64_tr_b16 v[140:141], v224 offset:11712\n"
    "s_add_u32 m0, s93, 55296\n"
    "s_mov_b32 s65, 1105920\n"
    "buffer_load_dwordx4 v221, s[40:43], s65 offen offset:0 lds\n"
    "s_waitcnt lgkmcnt(7)\n"
    "v_mfma_f32_32x32x16_bf16 v[64:79], a[112:115], v[198:201], v[64:79]\n"
    "ds_read_b64_tr_b16 v[142:143], v224 offset:10496\n"
    "ds_read_b64_tr_b16 v[144:145], v224 offset:11776\n"
    "s_add_u32 s91, s91, s97\n"
    "s_lshl_b32 s91, s91, 7\n"
    "s_xor_b32 s92, s91, 128\n"
    "s_waitcnt lgkmcnt(6)\n"
    "v_mfma_f32_32x32x16_bf16 v[80:95], a[112:115], v[202:205], v[80:95]\n"
    "ds_read_b64_tr_b16 v[146:147], v224 offset:15360\n"
    "ds_read_b64_tr_b16 v[148:149], v224 offset:16640\n"
    "s_add_u32 m0, s93, 56320\n"
    "s_mov_b32 s65, 1105920\n"
    "buffer_load_dwordx4 v222, s[40:43], s65 offen offset:0 lds\n"
    "s_waitcnt lgkmcnt(6)\n"
    "v_mfma_f32_32x32x16_bf16 v[150:165], a[112:115], v[134:137], v[150:165]\n"
    "ds_read_b64_tr_b16 v[198:199], v224 offset:15424\n"
    "ds_read_b64_tr_b16 v[200:201], v224 offset:16704\n"
    "s_xor_b32 s97, s69, 0\n"
    "s_lshl_b32 s85, s96, 2\n"
    "s_add_u32 s85, s85, s97\n"
    "s_waitcnt lgkmcnt(6)\n"
    "v_mfma_f32_32x32x16_bf16 v[166:181], a[112:115], v[138:141], v[166:181]\n"
    "ds_read_b64_tr_b16 v[202:203], v224 offset:15488\n"
    "ds_read_b64_tr_b16 v[204:205], v224 offset:16768\n"
    "s_add_u32 m0, s93, 57344\n"
    "s_mov_b32 s65, 1105920\n"
    "buffer_load_dwordx4 v223, s[40:43], s65 offen offset:0 lds\n"
    "v_add_u32 v225, 4294856704, v225\n"
    "s_waitcnt vmcnt(10)\n"
    "ds_write_b32 v225, v230 offset:0\n"
    "s_waitcnt lgkmcnt(7)\n"
    "v_mfma_f32_32x32x16_bf16 v[182:197], a[112:115], v[142:145], v[182:197]\n"
    "ds_read_b64_tr_b16 v[134:135], v224 offset:15552\n"
    "ds_read_b64_tr_b16 v[136:137], v224 offset:16832\n"
    "s_mul_i32 s85, s85, 81920\n"
    "s_lshl_b32 s72, s66, 12\n"
    "s_add_u32 s72, s72, s85\n"
    "s_waitcnt lgkmcnt(7)\n"
    "v_mfma_f32_32x32x16_bf16 v[64:79], a[120:123], v[146:149], v[64:79]\n"
    "ds_read_b64_tr_b16 v[138:139], v224 offset:15616\n"
    "ds_read_b64_tr_b16 v[140:141], v224 offset:16896\n"
    "s_add_u32 s72, s72, 1048576\n"
    "s_lshr_b32 s85, s97, 1\n"
    "s_mul_i32 s85, s85, 9\n"
    "s_waitcnt lgkmcnt(7)\n"
    "v_mfma_f32_32x32x16_bf16 v[80:95], a[120:123], v[198:201], v[80:95]\n"
    "s_waitcnt lgkmcnt(4)\n"
    "s_barrier\n"
    "v_add_u32 v224, 4294856704, v224\n"
    "v_add_u32 v215, 4294856704, v215\n"
    "ds_read_b128 a[128:131], v215 offset:0\n"
    "ds_read_b64_tr_b16 v[142:143], v224 offset:0\n"
    "ds_read_b64_tr_b16 v[144:145], v224 offset:1280\n"
    "v_mfma_f32_32x32x16_bf16 v[150:165], a[120:123], v[202:205], v[150:165]\n"
    "v_add_u32 v216, 4294856704, v216\n"
    "ds_read_b128 a[136:139], v216 offset:0\n"
    "ds_read_b64_tr_b16 v[146:147], v224 offset:64\n"
    "ds_read_b64_tr_b16 v[148:149], v224 offset:1344\n"
    "s_add_u32 m0, s64, 73728\n"
    "s_mov_b32 s65, 768\n"
    "buffer_load_dwordx4 v214, s[36:39], s65 offen offset:0 lds\n"
    "s_and_b32 s97, s97, 1\n"
    "s_waitcnt lgkmcnt(8)\n"
    "v_mfma_f32_32x32x16_bf16 v[166:181], a[120:123], v[134:137], v[166:181]\n"
    "ds_read_b128 a[144:147], v215 offset:512\n"
    "ds_read_b64_tr_b16 v[198:199], v224 offset:128\n"
    "ds_read_b64_tr_b16 v[200:201], v224 offset:1408\n"
    "s_lshl_b32 s97, s97, 2\n"
    "s_add_u32 s85, s85, s97\n"
    "s_waitcnt lgkmcnt(9)\n"
    "v_mfma_f32_32x32x16_bf16 v[182:197], a[120:123], v[138:141], v[182:197]\n"
    "ds_read_b128 a[152:155], v216 offset:512\n"
    "ds_read_b64_tr_b16 v[202:203], v224 offset:192\n"
    "ds_read_b64_tr_b16 v[204:205], v224 offset:1472\n"
    "s_add_u32 m0, s64, 77824\n"
    "s_mov_b32 s65, 37632\n"
    "buffer_load_dwordx4 v214, s[36:39], s65 offen offset:0 lds\n"
    "s_waitcnt lgkmcnt(9)\n"
    "v_mfma_f32_32x32x16_bf16 v[64:79], a[128:131], v[142:145], v[64:79]\n"
    "v_add_u32 v217, 4294856704, v217\n"
    "ds_read_b128 a[132:135], v217 offset:0\n"
    "ds_read_b64_tr_b16 v[134:135], v224 offset:256\n"
    "ds_read_b64_tr_b16 v[136:137], v224 offset:1536\n"
    "s_mul_i32 s85, s85, 73728\n"
    "s_waitcnt lgkmcnt(9)\n"
    "v_mfma_f32_32x32x16_bf16 v[80:95], a[128:131], v[146:149], v[80:95]\n"
    "v_add_u32 v218, 4294856704, v218\n"
    "ds_read_b128 a[140:143], v218 offset:0\n"
    "ds_read_b64_tr_b16 v[138:139], v224 offset:5120\n"
    "ds_read_b64_tr_b16 v[140:141], v224 offset:6400\n"
    "s_add_u32 m0, s64, 81920\n"
    "s_mov_b32 s65, 74496\n"
    "buffer_load_dwordx4 v214, s[36:39], s65 offen offset:0 lds\n"
    "s_waitcnt lgkmcnt(9)\n"
    "v_mfma_f32_32x32x16_bf16 v[150:165], a[128:131], v[198:201], v[150:165]\n"
    "ds_read_b128 a[148:151], v217 offset:512\n"
    "ds_read_b64_tr_b16 v[142:143], v224 offset:5184\n"
    "ds_read_b64_tr_b16 v[144:145], v224 offset:6464\n"
    "s_add_u32 s81, s85, 1024\n"
    "s_add_u32 s76, s85, s91\n"
    "s_waitcnt lgkmcnt(9)\n"
    "v_mfma_f32_32x32x16_bf16 v[166:181], a[128:131], v[202:205], v[166:181]\n"
    "ds_read_b128 a[156:159], v218 offset:512\n"
    "ds_read_b64_tr_b16 v[146:147], v224 offset:5248\n"
    "ds_read_b64_tr_b16 v[148:149], v224 offset:6528\n"
    "s_add_u32 m0, s64, 86016\n"
    "s_mov_b32 s65, 111360\n"
    "buffer_load_dwordx4 v214, s[36:39], s65 offen offset:0 lds\n"
    "s_add_u32 s24, s85, s92\n"
    "s_waitcnt lgkmcnt(9)\n"
    "v_mfma_f32_32x32x16_bf16 v[182:197], a[128:131], v[134:137], v[182:197]\n"
    "ds_read_b64_tr_b16 v[198:199], v224 offset:5312\n"
    "ds_read_b64_tr_b16 v[200:201], v224 offset:6592\n"
    "s_xor_b32 s97, s69, 1\n"
    "s_lshl_b32 s85, s96, 2\n"
    "s_waitcnt lgkmcnt(8)\n"
    "v_mfma_f32_32x32x16_bf16 v[64:79], a[136:139], v[138:141], v[64:79]\n"
    "ds_read_b64_tr_b16 v[202:203], v224 offset:5376\n"
    "ds_read_b64_tr_b16 v[204:205], v224 offset:6656\n"
    "s_add_u32 m0, s93, 90112\n"
    "s_mov_b32 s65, 1327104\n"
    "buffer_load_dwordx4 v219, s[40:43], s65 offen offset:0 lds\n"
    "s_add_u32 s85, s85, s97\n"
    "s_waitcnt lgkmcnt(7)\n"
    "v_mfma_f32_32x32x16_bf16 v[80:95], a[136:139], v[142:145], v[80:95]\n"
    "ds_read_b64_tr_b16 v[134:135], v224 offset:10240\n"
    "ds_read_b64_tr_b16 v[136:137], v224 offset:11520\n"
    "s_mul_i32 s85, s85, 81920\n"
    "s_lshl_b32 s73, s66, 12\n"
    "s_waitcnt lgkmcnt(6)\n"
    "v_mfma_f32_32x32x16_bf16 v[150:165], a[136:139], v[146:149], v[150:165]\n"
    "ds_read_b64_tr_b16 v[138:139], v224 offset:10304\n"
    "ds_read_b64_tr_b16 v[140:141], v224 offset:11584\n"
    "s_add_u32 m0, s93, 91136\n"
    "s_mov_b32 s65, 1327104\n"
    "buffer_load_dwordx4 v220, s[40:43], s65 offen offset:0 lds\n"
    "s_add_u32 s73, s73, s85\n"
    "s_waitcnt lgkmcnt(6)\n"
    "v_mfma_f32_32x32x16_bf16 v[166:181], a[136:139], v[198:201], v[166:181]\n"
    "ds_read_b64_tr_b16 v[142:143], v224 offset:10368\n"
    "ds_read_b64_tr_b16 v[144:145], v224 offset:11648\n"
    "s_add_u32 s73, s73, 1048576\n"
    "s_lshr_b32 s85, s97, 1\n"
    "s_mul_i32 s85, s85, 9\n"
    "s_waitcnt lgkmcnt(6)\n"
    "v_mfma_f32_32x32x16_bf16 v[182:197], a[136:139], v[202:205], v[182:197]\n"
    "ds_read_b64_tr_b16 v[146:147], v224 offset:10432\n"
    "ds_read_b64_tr_b16 v[148:149], v224 offset:11712\n"
    "s_add_u32 m0, s93, 92160\n"
    "s_mov_b32 s65, 1327104\n"
    "buffer_load_dwordx4 v221, s[40:43], s65 offen offset:0 lds\n"
    "s_waitcnt lgkmcnt(6)\n"
    "v_mfma_f32_32x32x16_bf16 v[64:79], a[144:147], v[134:137], v[64:79]\n"
    "ds_read_b64_tr_b16 v[198:199], v224 offset:10496\n"
    "ds_read_b64_tr_b16 v[200:201], v224 offset:11776\n"
    "s_and_b32 s97, s97, 1\n"
    "s_lshl_b32 s97, s97, 2\n"
    "s_add_u32 s85, s85, s97\n"
    "s_waitcnt lgkmcnt(6)\n"
    "v_mfma_f32_32x32x16_bf16 v[80:95], a[144:147], v[138:141], v[80:95]\n"
    "ds_read_b64_tr_b16 v[202:203], v224 offset:15360\n"
    "ds_read_b64_tr_b16 v[204:205], v224 offset:16640\n"
    "s_add_u32 m0, s93, 93184\n"
    "s_mov_b32 s65, 1327104\n"
    "buffer_load_dwordx4 v222, s[40:43], s65 offen offset:0 lds\n"
    "s_waitcnt lgkmcnt(6)\n"
    "v_mfma_f32_32x32x16_bf16 v[150:165], a[144:147], v[142:145], v[150:165]\n"
    "ds_read_b64_tr_b16 v[134:135], v224 offset:15424\n"
    "ds_read_b64_tr_b16 v[136:137], v224 offset:16704\n"
    "s_mul_i32 s85, s85, 73728\n"
    "s_add_u32 s82, s85, 1024\n"
    "s_add_u32 s77, s85, s91\n"
    "s_waitcnt lgkmcnt(6)\n"
    "v_mfma_f32_32x32x16_bf16 v[166:181], a[144:147], v[146:149], v[166:181]\n"
    "ds_read_b64_tr_b16 v[138:139], v224 offset:15488\n"
    "ds_read_b64_tr_b16 v[140:141], v224 offset:16768\n"
    "s_add_u32 m0, s93, 94208\n"
    "s_mov_b32 s65, 1327104\n"
    "buffer_load_dwordx4 v223, s[40:43], s65 offen offset:0 lds\n"
    "v_add_u32 v225, 36864, v225\n"
    "s_waitcnt vmcnt(9)\n"
    "ds_write_b32 v225, v231 offset:0\n"
    "s_waitcnt lgkmcnt(7)\n"
    "v_mfma_f32_32x32x16_bf16 v[182:197], a[144:147], v[198:201], v[182:197]\n"
    "ds_read_b64_tr_b16 v[142:143], v224 offset:15552\n"
    "ds_read_b64_tr_b16 v[144:145], v224 offset:16832\n"
    "s_add_u32 s25, s85, s92\n"
    "s_xor_b32 s97, s69, 2\n"
    "s_lshl_b32 s85, s96, 2\n"
    "s_waitcnt lgkmcnt(7)\n"
    "v_mfma_f32_32x32x16_bf16 v[64:79], a[152:155], v[202:205], v[64:79]\n"
    "ds_read_b64_tr_b16 v[146:147], v224 offset:15616\n"
    "ds_read_b64_tr_b16 v[148:149], v224 offset:16896\n"
    "s_add_u32 s85, s85, s97\n"
    "s_mul_i32 s85, s85, 81920\n"
    "s_lshl_b32 s74, s66, 12\n"
    "s_waitcnt lgkmcnt(7)\n"
    "v_mfma_f32_32x32x16_bf16 v[80:95], a[152:155], v[134:137], v[80:95]\n"
    "s_waitcnt lgkmcnt(4)\n"
    "s_barrier\n"
    "v_add_u32 v224, 36864, v224\n"
    "v_add_u32 v215, 36864, v215\n"
    "ds_read_b128 a[160:163], v215 offset:0\n"
    "ds_read_b64_tr_b16 v[198:199], v224 offset:0\n"
    "ds_read_b64_tr_b16 v[200:201], v224 offset:1280\n"
    "v_mfma_f32_32x32x16_bf16 v[150:165], a[152:155], v[138:141], v[150:165]\n"
    "v_add_u32 v216, 36864, v216\n"
    "ds_read_b128 a[168:171], v216 offset:0\n"
    "ds_read_b64_tr_b16 v[202:203], v224 offset:64\n"
    "ds_read_b64_tr_b16 v[204:205], v224 offset:1344\n"
    "s_add_u32 m0, s64, 110592\n"
    "s_mov_b32 s65, 896\n"
    "buffer_load_dwordx4 v214, s[36:39], s65 offen offset:0 lds\n"
    "s_waitcnt lgkmcnt(8)\n"
    "v_mfma_f32_32x32x16_bf16 v[166:181], a[152:155], v[142:145], v[166:181]\n"
    "ds_read_b128 a[176:179], v215 offset:512\n"
    "ds_read_b64_tr_b16 v[134:135], v224 offset:128\n"
    "ds_read_b64_tr_b16 v[136:137], v224 offset:1408\n"
    "s_add_u32 s74, s74, s85\n"
    "s_add_u32 s74, s74, 1048576\n"
    "s_waitcnt lgkmcnt(9)\n"
    "v_mfma_f32_32x32x16_bf16 v[182:197], a[152:155], v[146:149], v[182:197]\n"
    "ds_read_b128 a[184:187], v216 offset:512\n"
    "ds_read_b64_tr_b16 v[138:139], v224 offset:192\n"
    "ds_read_b64_tr_b16 v[140:141], v224 offset:1472\n"
    "s_add_u32 m0, s64, 114688\n"
    "s_mov_b32 s65, 37760\n"
    "buffer_load_dwordx4 v214, s[36:39], s65 offen offset:0 lds\n"
    "s_lshr_b32 s85, s97, 1\n"
    "s_waitcnt lgkmcnt(9)\n"
    "v_mfma_f32_32x32x16_bf16 v[64:79], a[160:163], v[198:201], v[64:79]\n"
    "v_add_u32 v217, 36864, v217\n"
    "ds_read_b128 a[164:167], v217 offset:0\n"
    "ds_read_b64_tr_b16 v[142:143], v224 offset:256\n"
    "ds_read_b64_tr_b16 v[144:145], v224 offset:1536\n"
    "s_mul_i32 s85, s85, 9\n"
    "s_waitcnt lgkmcnt(9)\n"
    "v_mfma_f32_32x32x16_bf16 v[80:95], a[160:163], v[202:205], v[80:95]\n"
    "v_add_u32 v218, 36864, v218\n"
    "ds_read_b128 a[172:175], v218 offset:0\n"
    "ds_read_b64_tr_b16 v[146:147], v224 offset:5120\n"
    "ds_read_b64_tr_b16 v[148:149], v224 offset:6400\n"
    "s_add_u32 m0, s64, 118784\n"
    "s_mov_b32 s65, 74624\n"
    "buffer_load_dwordx4 v214, s[36:39], s65 offen offset:0 lds\n"
    "s_waitcnt lgkmcnt(9)\n"
    "v_mfma_f32_32x32x16_bf16 v[150:165], a[160:163], v[134:137], v[150:165]\n"
    "ds_read_b128 a[180:183], v217 offset:512\n"
    "ds_read_b64_tr_b16 v[198:199], v224 offset:5184\n"
    "ds_read_b64_tr_b16 v[200:201], v224 offset:6464\n"
    "s_and_b32 s97, s97, 1\n"
    "s_lshl_b32 s97, s97, 2\n"
    "s_waitcnt lgkmcnt(9)\n"
    "v_mfma_f32_32x32x16_bf16 v[166:181], a[160:163], v[138:141], v[166:181]\n"
    "ds_read_b128 a[188:191], v218 offset:512\n"
    "ds_read_b64_tr_b16 v[202:203], v224 offset:5248\n"
    "ds_read_b64_tr_b16 v[204:205], v224 offset:6528\n"
    "s_add_u32 m0, s64, 122880\n"
    "s_mov_b32 s65, 111488\n"
    "buffer_load_dwordx4 v214, s[36:39], s65 offen offset:0 lds\n"
    "s_waitcnt lgkmcnt(9)\n"
    "v_mfma_f32_32x32x16_bf16 v[182:197], a[160:163], v[142:145], v[182:197]\n"
    "ds_read_b64_tr_b16 v[134:135], v224 offset:5312\n"
    "ds_read_b64_tr_b16 v[136:137], v224 offset:6592\n"
    "s_add_u32 s85, s85, s97\n"
    "s_mul_i32 s85, s85, 73728\n"
    "s_add_u32 s78, s85, s91\n"
    "s_waitcnt lgkmcnt(8)\n"
    "v_mfma_f32_32x32x16_bf16 v[64:79], a[168:171], v[146:149], v[64:79]\n"
    "ds_read_b64_tr_b16 v[138:139], v224 offset:5376\n"
    "ds_read_b64_tr_b16 v[140:141], v224 offset:6656\n"
    "s_add_u32 m0, s93, 126976\n"
    "s_mov_b32 s65, 1548288\n"
    "buffer_load_dwordx4 v219, s[40:43], s65 offen offset:0 lds\n"
    "s_waitcnt lgkmcnt(7)\n"
    "v_mfma_f32_32x32x16_bf16 v[80:95], a[168:171], v[198:201], v[80:95]\n"
    "ds_read_b64_tr_b16 v[142:143], v224 offset:10240\n"
    "ds_read_b64_tr_b16 v[144:145], v224 offset:11520\n"
    "s_add_u32 s26, s85, s92\n"
    "s_xor_b32 s97, s69, 3\n"
    "s_lshl_b32 s85, s96, 2\n"
    "s_waitcnt lgkmcnt(6)\n"
    "v_mfma_f32_32x32x16_bf16 v[150:165], a[168:171], v[202:205], v[150:165]\n"
    "ds_read_b64_tr_b16 v[146:147], v224 offset:10304\n"
    "ds_read_b64_tr_b16 v[148:149], v224 offset:11584\n"
    "s_add_u32 m0, s93, 128000\n"
    "s_mov_b32 s65, 1548288\n"
    "buffer_load_dwordx4 v220, s[40:43], s65 offen offset:0 lds\n"
    "s_add_u32 s85, s85, s97\n"
    "s_waitcnt lgkmcnt(6)\n"
    "v_mfma_f32_32x32x16_bf16 v[166:181], a[168:171], v[134:137], v[166:181]\n"
    "ds_read_b64_tr_b16 v[198:199], v224 offset:10368\n"
    "ds_read_b64_tr_b16 v[200:201], v224 offset:11648\n"
    "s_mul_i32 s85, s85, 81920\n"
    "s_lshl_b32 s75, s66, 12\n"
    "s_waitcnt lgkmcnt(6)\n"
    "v_mfma_f32_32x32x16_bf16 v[182:197], a[168:171], v[138:141], v[182:197]\n"
    "ds_read_b64_tr_b16 v[202:203], v224 offset:10432\n"
    "ds_read_b64_tr_b16 v[204:205], v224 offset:11712\n"
    "s_add_u32 m0, s93, 129024\n"
    "s_mov_b32 s65, 1548288\n"
    "buffer_load_dwordx4 v221, s[40:43], s65 offen offset:0 lds\n"
    "s_add_u32 s75, s75, s85\n"
    "s_waitcnt lgkmcnt(6)\n"
    "v_mfma_f32_32x32x16_bf16 v[64:79], a[176:179], v[142:145], v[64:79]\n"
    "ds_read_b64_tr_b16 v[134:135], v224 offset:10496\n"
    "ds_read_b64_tr_b16 v[136:137], v224 offset:11776\n"
    "s_add_u32 s75, s75, 1048576\n"
    "s_lshr_b32 s85, s97, 1\n"
    "s_waitcnt lgkmcnt(6)\n"
    "v_mfma_f32_32x32x16_bf16 v[80:95], a[176:179], v[146:149], v[80:95]\n"
    "ds_read_b64_tr_b16 v[138:139], v224 offset:15360\n"
    "ds_read_b64_tr_b16 v[140:141], v224 offset:16640\n"
    "s_add_u32 m0, s93, 130048\n"
    "s_mov_b32 s65, 1548288\n"
    "buffer_load_dwordx4 v222, s[40:43], s65 offen offset:0 lds\n"
    "s_mul_i32 s85, s85, 9\n"
    "s_waitcnt lgkmcnt(6)\n"
    "v_mfma_f32_32x32x16_bf16 v[150:165], a[176:179], v[198:201], v[150:165]\n"
    "ds_read_b64_tr_b16 v[142:143], v224 offset:15424\n"
    "ds_read_b64_tr_b16 v[144:145], v224 offset:16704\n"
    "s_and_b32 s97, s97, 1\n"
    "s_lshl_b32 s97, s97, 2\n"
    "s_add_u32 s85, s85, s97\n"
    "s_waitcnt lgkmcnt(6)\n"
    "v_mfma_f32_32x32x16_bf16 v[166:181], a[176:179], v[202:205], v[166:181]\n"
    "ds_read_b64_tr_b16 v[146:147], v224 offset:15488\n"
    "ds_read_b64_tr_b16 v[148:149], v224 offset:16768\n"
    "s_add_u32 m0, s93, 131072\n"
    "s_mov_b32 s65, 1548288\n"
    "buffer_load_dwordx4 v223, s[40:43], s65 offen offset:0 lds\n"
    "v_add_u32 v225, 36864, v225\n"
    "s_waitcnt vmcnt(9)\n"
    "ds_write_b32 v225, v232 offset:0\n"
    "s_waitcnt lgkmcnt(7)\n"
    "v_mfma_f32_32x32x16_bf16 v[182:197], a[176:179], v[134:137], v[182:197]\n"
    "ds_read_b64_tr_b16 v[198:199], v224 offset:15552\n"
    "ds_read_b64_tr_b16 v[200:201], v224 offset:16832\n"
    "s_mul_i32 s85, s85, 73728\n"
    "s_add_u32 s79, s85, s91\n"
    "s_add_u32 s27, s85, s92\n"
    "s_waitcnt lgkmcnt(7)\n"
    "v_mfma_f32_32x32x16_bf16 v[64:79], a[184:187], v[138:141], v[64:79]\n"
    "ds_read_b64_tr_b16 v[202:203], v224 offset:15616\n"
    "ds_read_b64_tr_b16 v[204:205], v224 offset:16896\n"
    "s_mul_i32 s97, s68, 165888\n"
    "s_add_u32 s83, s97, s91\n"
    "s_waitcnt lgkmcnt(7)\n"
    "v_mfma_f32_32x32x16_bf16 v[80:95], a[184:187], v[142:145], v[80:95]\n"
    "s_waitcnt lgkmcnt(4)\n"
    "s_barrier\n"
    "v_add_u32 v224, 36864, v224\n"
    "v_add_u32 v215, 36864, v215\n"
    "ds_read_b128 a[192:195], v215 offset:0\n"
    "ds_read_b64_tr_b16 v[134:135], v224 offset:0\n"
    "ds_read_b64_tr_b16 v[136:137], v224 offset:1280\n"
    "s_add_u32 s28, s97, s92\n"
    "v_mfma_f32_32x32x16_bf16 v[150:165], a[184:187], v[146:149], v[150:165]\n"
    "v_add_u32 v216, 36864, v216\n"
    "ds_read_b128 a[200:203], v216 offset:0\n"
    "ds_read_b64_tr_b16 v[138:139], v224 offset:64\n"
    "ds_read_b64_tr_b16 v[140:141], v224 offset:1344\n"
    "s_add_u32 m0, s64, 0\n"
    "s_mov_b32 s65, 1024\n"
    "buffer_load_dwordx4 v214, s[36:39], s65 offen offset:0 lds\n"
    "s_waitcnt lgkmcnt(8)\n"
    "v_mfma_f32_32x32x16_bf16 v[166:181], a[184:187], v[198:201], v[166:181]\n"
    "ds_read_b128 a[208:211], v215 offset:512\n"
    "ds_read_b64_tr_b16 v[142:143], v224 offset:128\n"
    "ds_read_b64_tr_b16 v[144:145], v224 offset:1408\n"
    "s_add_u32 s84, s97, 1024\n"
    "s_lshl_b32 s88, s67, 3\n"
    "s_waitcnt lgkmcnt(9)\n"
    "v_mfma_f32_32x32x16_bf16 v[182:197], a[184:187], v[202:205], v[182:197]\n"
    "ds_read_b128 a[216:219], v216 offset:512\n"
    "ds_read_b64_tr_b16 v[146:147], v224 offset:192\n"
    "ds_read_b64_tr_b16 v[148:149], v224 offset:1472\n"
    "s_add_u32 m0, s64, 4096\n"
    "s_mov_b32 s65, 37888\n"
    "buffer_load_dwordx4 v214, s[36:39], s65 offen offset:0 lds\n"
    "s_waitcnt lgkmcnt(9)\n"
    "v_mfma_f32_32x32x16_bf16 v[64:79], a[192:195], v[134:137], v[64:79]\n"
    "v_add_u32 v217, 36864, v217\n"
    "ds_read_b128 a[196:199], v217 offset:0\n"
    "ds_read_b64_tr_b16 v[198:199], v224 offset:256\n"
    "ds_read_b64_tr_b16 v[200:201], v224 offset:1536\n"
    "s_add_u32 s97, s88, s71\n"
    "s_lshl_b32 s97, s97, 2\n"
    "s_waitcnt lgkmcnt(9)\n"
    "v_mfma_f32_32x32x16_bf16 v[80:95], a[192:195], v[138:141], v[80:95]\n"
    "v_add_u32 v218, 36864, v218\n"
    "ds_read_b128 a[204:207], v218 offset:0\n"
    "ds_read_b64_tr_b16 v[202:203], v224 offset:5120\n"
    "ds_read_b64_tr_b16 v[204:205], v224 offset:6400\n"
    "s_add_u32 m0, s64, 8192\n"
    "s_mov_b32 s65, 74752\n"
    "buffer_load_dwordx4 v214, s[36:39], s65 offen offset:0 lds\n"
    "s_waitcnt lgkmcnt(9)\n"
    "v_mfma_f32_32x32x16_bf16 v[150:165], a[192:195], v[142:145], v[150:165]\n"
    "ds_read_b128 a[212:215], v217 offset:512\n"
    "ds_read_b64_tr_b16 v[134:135], v224 offset:5184\n"
    "ds_read_b64_tr_b16 v[136:137], v224 offset:6464\n"
    "s_add_u32 s97, s97, s66\n"
    "s_mul_i32 s97, s97, 6144\n"
    "s_waitcnt lgkmcnt(9)\n"
    "v_mfma_f32_32x32x16_bf16 v[166:181], a[192:195], v[146:149], v[166:181]\n"
    "ds_read_b128 a[220:223], v218 offset:512\n"
    "ds_read_b64_tr_b16 v[138:139], v224 offset:5248\n"
    "ds_read_b64_tr_b16 v[140:141], v224 offset:6528\n"
    "s_add_u32 m0, s64, 12288\n"
    "s_mov_b32 s65, 111616\n"
    "buffer_load_dwordx4 v214, s[36:39], s65 offen offset:0 lds\n"
    "s_waitcnt lgkmcnt(9)\n"
    "v_mfma_f32_32x32x16_bf16 v[182:197], a[192:195], v[198:201], v[182:197]\n"
    "ds_read_b64_tr_b16 v[142:143], v224 offset:5312\n"
    "ds_read_b64_tr_b16 v[144:145], v224 offset:6592\n"
    "s_add_u32 s85, s97, 22020096\n"
    "s_lshl_b32 s97, s68, 1\n"
    "s_add_u32 s88, s88, s97\n"
    "s_waitcnt lgkmcnt(8)\n"
    "v_mfma_f32_32x32x16_bf16 v[64:79], a[200:203], v[202:205], v[64:79]\n"
    "ds_read_b64_tr_b16 v[146:147], v224 offset:5376\n"
    "ds_read_b64_tr_b16 v[148:149], v224 offset:6656\n"
    "s_add_u32 m0, s93, 16384\n"
    "s_mov_b32 s65, 1769472\n"
    "buffer_load_dwordx4 v219, s[40:43], s65 offen offset:0 lds\n"
    "s_waitcnt lgkmcnt(7)\n"
    "v_mfma_f32_32x32x16_bf16 v[80:95], a[200:203], v[134:137], v[80:95]\n"
    "ds_read_b64_tr_b16 v[198:199], v224 offset:10240\n"
    "ds_read_b64_tr_b16 v[200:201], v224 offset:11520\n"
    "s_and_b32 s97, s66, 1\n"
    "s_add_u32 s88, s88, s97\n"
    "s_lshl_b32 s88, s88, 2\n"
    "s_waitcnt lgkmcnt(6)\n"
    "v_mfma_f32_32x32x16_bf16 v[150:165], a[200:203], v[138:141], v[150:165]\n"
    "ds_read_b64_tr_b16 v[202:203], v224 offset:10304\n"
    "ds_read_b64_tr_b16 v[204:205], v224 offset:11584\n"
    "s_add_u32 m0, s93, 17408\n"
    "s_mov_b32 s65, 1769472\n"
    "buffer_load_dwordx4 v220, s[40:43], s65 offen offset:0 lds\n"
    "s_waitcnt lgkmcnt(6)\n"
    "v_mfma_f32_32x32x16_bf16 v[166:181], a[200:203], v[142:145], v[166:181]\n"
    "ds_read_b64_tr_b16 v[134:135], v224 offset:10368\n"
    "ds_read_b64_tr_b16 v[136:137], v224 offset:11648\n"
    "s_xor_b32 s97, s66, 0\n"
    "s_add_u32 s97, s97, s88\n"
    "s_mul_i32 s97, s97, 6144\n"
    "s_waitcnt lgkmcnt(6)\n"
    "v_mfma_f32_32x32x16_bf16 v[182:197], a[200:203], v[146:149], v[182:197]\n"
    "ds_read_b64_tr_b16 v[138:139], v224 offset:10432\n"
    "ds_read_b64_tr_b16 v[140:141], v224 offset:11712\n"
    "s_add_u32 m0, s93, 18432\n"
    "s_mov_b32 s65, 1769472\n"
    "buffer_load_dwordx4 v221, s[40:43], s65 offen offset:0 lds\n"
    "s_waitcnt lgkmcnt(6)\n"
    "v_mfma_f32_32x32x16_bf16 v[64:79], a[208:211], v[198:201], v[64:79]\n"
    "ds_read_b64_tr_b16 v[142:143], v224 offset:10496\n"
    "ds_read_b64_tr_b16 v[144:145], v224 offset:11776\n"
    "s_add_u32 s86, s97, 22020096\n"
    "s_xor_b32 s97, s66, 1\n"
    "s_add_u32 s97, s97, s88\n"
    "s_waitcnt lgkmcnt(6)\n"
    "v_mfma_f32_32x32x16_bf16 v[80:95], a[208:211], v[202:205], v[80:95]\n"
    "ds_read_b64_tr_b16 v[146:147], v224 offset:15360\n"
    "ds_read_b64_tr_b16 v[148:149], v224 offset:16640\n"
    "s_add_u32 m0, s93, 19456\n"
    "s_mov_b32 s65, 1769472\n"
    "buffer_load_dwordx4 v222, s[40:43], s65 offen offset:0 lds\n"
    "s_mul_i32 s97, s97, 6144\n"
    "s_waitcnt lgkmcnt(6)\n"
    "v_mfma_f32_32x32x16_bf16 v[150:165], a[208:211], v[134:137], v[150:165]\n"
    "ds_read_b64_tr_b16 v[198:199], v224 offset:15424\n"
    "ds_read_b64_tr_b16 v[200:201], v224 offset:16704\n"
    "s_add_u32 s87, s97, 22020096\n"
    "s_lshl_b32 s97, s71, 4\n"
    "s_waitcnt lgkmcnt(6)\n"
    "v_mfma_f32_32x32x16_bf16 v[166:181], a[208:211], v[138:141], v[166:181]\n"
    "ds_read_b64_tr_b16 v[202:203], v224 offset:15488\n"
    "ds_read_b64_tr_b16 v[204:205], v224 offset:16768\n"
    "s_add_u32 m0, s93, 20480\n"
    "s_mov_b32 s65, 1769472\n"
    "buffer_load_dwordx4 v223, s[40:43], s65 offen offset:0 lds\n"
    "v_add_u32 v225, 36864, v225\n"
    "s_waitcnt vmcnt(9)\n"
    "ds_write_b32 v225, v233 offset:0\n"
    "s_add_u32 s97, s97, s67\n"
    "s_waitcnt lgkmcnt(7)\n"
    "v_mfma_f32_32x32x16_bf16 v[182:197], a[208:211], v[142:145], v[182:197]\n"
    "ds_read_b64_tr_b16 v[134:135], v224 offset:15552\n"
    "ds_read_b64_tr_b16 v[136:137], v224 offset:16832\n"
    "s_lshl_b32 s90, s97, 15\n"
    "s_lshl_b32 s88, s66, 13\n"
    "s_waitcnt lgkmcnt(7)\n"
    "v_mfma_f32_32x32x16_bf16 v[64:79], a[216:219], v[146:149], v[64:79]\n"
    "ds_read_b64_tr_b16 v[138:139], v224 offset:15616\n"
    "ds_read_b64_tr_b16 v[140:141], v224 offset:16896\n"
    "s_add_u32 s88, s88, s90\n"
    "s_add_u32 s88, s88, 25165824\n"
    "s_lshl_b32 s97, s97, 2\n"
    "s_waitcnt lgkmcnt(7)\n"
    "v_mfma_f32_32x32x16_bf16 v[80:95], a[216:219], v[198:201], v[80:95]\n"
    "s_waitcnt lgkmcnt(4)\n"
    "s_barrier\n"
    "v_add_u32 v224, 36864, v224\n"
    "v_add_u32 v215, 36864, v215\n"
    "ds_read_b128 a[224:227], v215 offset:0\n"
    "ds_read_b64_tr_b16 v[142:143], v224 offset:0\n"
    "ds_read_b64_tr_b16 v[144:145], v224 offset:1280\n"
    "s_add_u32 s97, s97, s66\n"
    "v_mfma_f32_32x32x16_bf16 v[150:165], a[216:219], v[202:205], v[150:165]\n"
    "v_add_u32 v216, 36864, v216\n"
    "ds_read_b128 a[232:235], v216 offset:0\n"
    "ds_read_b64_tr_b16 v[146:147], v224 offset:64\n"
    "ds_read_b64_tr_b16 v[148:149], v224 offset:1344\n"
    "s_add_u32 m0, s64, 36864\n"
    "s_mov_b32 s65, 0\n"
    "buffer_load_dwordx4 v239, s[44:47], s65 offen offset:0 lds\n"
    "s_waitcnt lgkmcnt(8)\n"
    "v_mfma_f32_32x32x16_bf16 v[166:181], a[216:219], v[134:137], v[166:181]\n"
    "ds_read_b128 a[240:243], v215 offset:512\n"
    "ds_read_b64_tr_b16 v[198:199], v224 offset:128\n"
    "ds_read_b64_tr_b16 v[200:201], v224 offset:1408\n"
    "s_and_b32 s99, s67, 7\n"
    "s_lshl_b32 s99, s99, 2\n"
    "s_add_u32 s99, s99, s69\n"
    "s_lshl_b32 s99, s99, 2\n"
    "s_add_u32 s99, s99, s66\n"
    "s_lshl_b32 s99, s99, 1\n"
    "s_lshr_b32 s65, s67, 3\n"
    "s_add_u32 s99, s99, s65\n"
    "s_mul_i32 s99, s99, 41\n"
    "s_add_u32 s99, s99, 0\n"
    "v_min_u32 v132, 40, v124\n"
    "v_add_u32 v132, s99, v132\n"
    "v_lshlrev_b32 v132, 7, v132\n"
    "buffer_load_dword v234, v132, s[48:51], 0 offen\n"
    "s_waitcnt lgkmcnt(9)\n"
    "v_mfma_f32_32x32x16_bf16 v[182:197], a[216:219], v[138:141], v[182:197]\n"
    "ds_read_b128 a[248:251], v216 offset:512\n"
    "ds_read_b64_tr_b16 v[202:203], v224 offset:192\n"
    "ds_read_b64_tr_b16 v[204:205], v224 offset:1472\n"
    "s_add_u32 m0, s64, 40960\n"
    "s_mov_b32 s65, 294912\n"
    "buffer_load_dwordx4 v239, s[44:47], s65 offen offset:0 lds\n"
    "s_lshl_b32 s90, s97, 6\n"
    "s_waitcnt lgkmcnt(9)\n"
    "v_mfma_f32_32x32x16_bf16 v[64:79], a[224:227], v[142:145], v[64:79]\n"
    "v_add_u32 v217, 36864, v217\n"
    "ds_read_b128 a[228:231], v217 offset:0\n"
    "ds_read_b64_tr_b16 v[134:135], v224 offset:256\n"
    "ds_read_b64_tr_b16 v[136:137], v224 offset:1536\n"
    "s_lshl_b32 s96, s67, 2\n"
    "s_waitcnt lgkmcnt(9)\n"
    "v_mfma_f32_32x32x16_bf16 v[80:95], a[224:227], v[146:149], v[80:95]\n"
    "v_add_u32 v218, 36864, v218\n"
    "ds_read_b128 a[236:239], v218 offset:0\n"
    "ds_read_b64_tr_b16 v[138:139], v224 offset:5120\n"
    "ds_read_b64_tr_b16 v[140:141], v224 offset:6400\n"
    "s_add_u32 m0, s64, 45056\n"
    "s_mov_b32 s65, 589824\n"
    "buffer_load_dwordx4 v239, s[44:47], s65 offen offset:0 lds\n"
    "s_waitcnt lgkmcnt(9)\n"
    "v_mfma_f32_32x32x16_bf16 v[150:165], a[224:227], v[198:201], v[150:165]\n"
    "ds_read_b128 a[244:247], v217 offset:512\n"
    "ds_read_b64_tr_b16 v[142:143], v224 offset:5184\n"
    "ds_read_b64_tr_b16 v[144:145], v224 offset:6464\n"
    "s_add_u32 s97, s96, s69\n"
    "s_lshl_b32 s97, s97, 2\n"
    "s_waitcnt lgkmcnt(9)\n"
    "v_mfma_f32_32x32x16_bf16 v[166:181], a[224:227], v[202:205], v[166:181]\n"
    "ds_read_b128 a[252:255], v218 offset:512\n"
    "ds_read_b64_tr_b16 v[146:147], v224 offset:5248\n"
    "ds_read_b64_tr_b16 v[148:149], v224 offset:6528\n"
    "s_add_u32 m0, s64, 49152\n"
    "s_mov_b32 s65, 884736\n"
    "buffer_load_dwordx4 v239, s[44:47], s65 offen offset:0 lds\n"
    "s_waitcnt lgkmcnt(9)\n"
    "v_mfma_f32_32x32x16_bf16 v[182:197], a[224:227], v[134:137], v[182:197]\n"
    "ds_read_b64_tr_b16 v[198:199], v224 offset:5312\n"
    "ds_read_b64_tr_b16 v[200:201], v224 offset:6592\n"
    "s_and_b32 s99, s67, 7\n"
    "s_lshl_b32 s99, s99, 2\n"
    "s_add_u32 s99, s99, s69\n"
    "s_lshl_b32 s99, s99, 2\n"
    "s_add_u32 s99, s99, s66\n"
    "s_lshl_b32 s99, s99, 1\n"
    "s_lshr_b32 s65, s67, 3\n"
    "s_add_u32 s99, s99, s65\n"
    "s_mul_i32 s99, s99, 6\n"
    "v_min_u32 v132, 5, v124\n"
    "v_add_u32 v132, s99, v132\n"
    "v_lshlrev_b32 v132, 7, v132\n"
    "s_mul_i32 s99, s68, 165888\n"
    "buffer_load_dword v234, v132, s[52:55], s99 offen\n"
    "s_add_u32 s97, s97, s68\n"
    "s_waitcnt lgkmcnt(8)\n"
    "v_mfma_f32_32x32x16_bf16 v[64:79], a[232:235], v[138:141], v[64:79]\n"
    "ds_read_b64_tr_b16 v[202:203], v224 offset:5376\n"
    "ds_read_b64_tr_b16 v[204:205], v224 offset:6656\n"
    "s_add_u32 m0, s64, 53248\n"
    "s_mov_b32 s65, 1179648\n"
    "buffer_load_dwordx4 v239, s[44:47], s65 offen offset:0 lds\n"
    "s_waitcnt lgkmcnt(7)\n"
    "v_mfma_f32_32x32x16_bf16 v[80:95], a[232:235], v[142:145], v[80:95]\n"
    "ds_read_b64_tr_b16 v[134:135], v224 offset:10240\n"
    "ds_read_b64_tr_b16 v[136:137], v224 offset:11520\n"
    "s_lshl_b32 s97, s97, 2\n"
    "s_add_u32 s97, s97, s66\n"
    "s_lshl_b32 s97, s97, 13\n"
    "s_waitcnt lgkmcnt(6)\n"
    "v_mfma_f32_32x32x16_bf16 v[150:165], a[232:235], v[146:149], v[150:165]\n"
    "ds_read_b64_tr_b16 v[138:139], v224 offset:10304\n"
    "ds_read_b64_tr_b16 v[140:141], v224 offset:11584\n"
    "s_add_u32 m0, s64, 57344\n"
    "s_mov_b32 s65, 1474560\n"
    "buffer_load_dwordx4 v239, s[44:47], s65 offen offset:0 lds\n"
    "s_waitcnt lgkmcnt(6)\n"
    "v_mfma_f32_32x32x16_bf16 v[166:181], a[232:235], v[198:201], v[166:181]\n"
    "ds_read_b64_tr_b16 v[142:143], v224 offset:10368\n"
    "ds_read_b64_tr_b16 v[144:145], v224 offset:11648\n"
    "s_add_u32 s91, s97, 29360128\n"
    "s_add_u32 s96, s96, s66\n"
    "s_lshl_b32 s96, s96, 2\n"
    "s_waitcnt lgkmcnt(6)\n"
    "v_mfma_f32_32x32x16_bf16 v[182:197], a[232:235], v[202:205], v[182:197]\n"
    "ds_read_b64_tr_b16 v[146:147], v224 offset:10432\n"
    "ds_read_b64_tr_b16 v[148:149], v224 offset:11712\n"
    "s_add_u32 m0, s64, 61440\n"
    "s_mov_b32 s65, 1769472\n"
    "buffer_load_dwordx4 v239, s[44:47], s65 offen offset:0 lds\n"
    "s_waitcnt lgkmcnt(6)\n"
    "v_mfma_f32_32x32x16_bf16 v[64:79], a[240:243], v[134:137], v[64:79]\n"
    "ds_read_b64_tr_b16 v[198:199], v224 offset:10496\n"
    "ds_read_b64_tr_b16 v[200:201], v224 offset:11776\n"
    "s_add_u32 s96, s96, s68\n"
    "s_lshl_b32 s96, s96, 1\n"
    "s_lshr_b32 s97, s69, 1\n"
    "s_waitcnt lgkmcnt(6)\n"
    "v_mfma_f32_32x32x16_bf16 v[80:95], a[240:243], v[138:141], v[80:95]\n"
    "ds_read_b64_tr_b16 v[202:203], v224 offset:15360\n"
    "ds_read_b64_tr_b16 v[204:205], v224 offset:16640\n"
    "s_add_u32 m0, s64, 65536\n"
    "s_mov_b32 s65, 2064384\n"
    "buffer_load_dwordx4 v239, s[44:47], s65 offen offset:0 lds\n"
    "s_waitcnt lgkmcnt(6)\n"
    "v_mfma_f32_32x32x16_bf16 v[150:165], a[240:243], v[142:145], v[150:165]\n"
    "ds_read_b64_tr_b16 v[134:135], v224 offset:15424\n"
    "ds_read_b64_tr_b16 v[136:137], v224 offset:16704\n"
    "s_add_u32 s96, s96, s97\n"
    "s_lshl_b32 s96, s96, 12\n"
    "s_add_u32 s92, s96, 37748736\n"
    "s_waitcnt lgkmcnt(6)\n"
    "v_mfma_f32_32x32x16_bf16 v[166:181], a[240:243], v[146:149], v[166:181]\n"
    "ds_read_b64_tr_b16 v[138:139], v224 offset:15488\n"
    "ds_read_b64_tr_b16 v[140:141], v224 offset:16768\n"
    "s_add_u32 m0, s64, 69632\n"
    "s_mov_b32 s65, 2359296\n"
    "buffer_load_dwordx4 v239, s[44:47], s65 offen offset:0 lds\n"
    "v_add_u32 v225, 4294856704, v225\n"
    "s_waitcnt vmcnt(11)\n"
    "ds_write_b32 v225, v206 offset:0\n"
    "s_waitcnt lgkmcnt(7)\n"
    "v_mfma_f32_32x32x16_bf16 v[182:197], a[240:243], v[198:201], v[182:197]\n"
    "ds_read_b64_tr_b16 v[142:143], v224 offset:15552\n"
    "ds_read_b64_tr_b16 v[144:145], v224 offset:16832\n"
    "s_waitcnt lgkmcnt(7)\n"
    "v_mfma_f32_32x32x16_bf16 v[64:79], a[248:251], v[202:205], v[64:79]\n"
    "ds_read_b64_tr_b16 v[146:147], v224 offset:15616\n"
    "ds_read_b64_tr_b16 v[148:149], v224 offset:16896\n"
    "s_waitcnt lgkmcnt(7)\n"
    "v_mfma_f32_32x32x16_bf16 v[80:95], a[248:251], v[134:137], v[80:95]\n"
    "s_waitcnt lgkmcnt(4)\n"
    "s_barrier\n"
    "v_add_u32 v224, 4294856704, v224\n"
    "v_add_u32 v215, 4294856704, v215\n"
    "ds_read_b128 v[0:3], v215 offset:0\n"
    "ds_read_b64_tr_b16 v[198:199], v224 offset:0\n"
    "ds_read_b64_tr_b16 v[200:201], v224 offset:1280\n"
    "v_mfma_f32_32x32x16_bf16 v[150:165], a[248:251], v[138:141], v[150:165]\n"
    "v_add_u32 v216, 4294856704, v216\n"
    "ds_read_b128 v[8:11], v216 offset:0\n"
    "ds_read_b64_tr_b16 v[202:203], v224 offset:64\n"
    "ds_read_b64_tr_b16 v[204:205], v224 offset:1344\n"
    "s_add_u32 m0, s64, 73728\n"
    "s_mov_b32 s65, 2654208\n"
    "buffer_load_dwordx4 v239, s[44:47], s65 offen offset:0 lds\n"
    "s_waitcnt lgkmcnt(8)\n"
    "v_mfma_f32_32x32x16_bf16 v[166:181], a[248:251], v[142:145], v[166:181]\n"
    "ds_read_b128 v[16:19], v215 offset:512\n"
    "ds_read_b64_tr_b16 v[134:135], v224 offset:128\n"
    "ds_read_b64_tr_b16 v[136:137], v224 offset:1408\n"
    "s_waitcnt lgkmcnt(9)\n"
    "v_mfma_f32_32x32x16_bf16 v[182:197], a[248:251], v[146:149], v[182:197]\n"
    "ds_read_b128 v[24:27], v216 offset:512\n"
    "ds_read_b64_tr_b16 v[138:139], v224 offset:192\n"
    "ds_read_b64_tr_b16 v[140:141], v224 offset:1472\n"
    "s_add_u32 m0, s64, 77824\n"
    "s_mov_b32 s65, 2949120\n"
    "buffer_load_dwordx4 v239, s[44:47], s65 offen offset:0 lds\n"
    "s_waitcnt lgkmcnt(9)\n"
    "v_mfma_f32_32x32x16_bf16 v[64:79], v[0:3], v[198:201], v[64:79]\n"
    "v_add_u32 v217, 4294856704, v217\n"
    "ds_read_b128 v[4:7], v217 offset:0\n"
    "ds_read_b64_tr_b16 v[142:143], v224 offset:256\n"
    "ds_read_b64_tr_b16 v[144:145], v224 offset:1536\n"
    "s_waitcnt lgkmcnt(9)\n"
    "v_mfma_f32_32x32x16_bf16 v[80:95], v[0:3], v[202:205], v[80:95]\n"
    "v_add_u32 v218, 4294856704, v218\n"
    "ds_read_b128 v[12:15], v218 offset:0\n"
    "ds_read_b64_tr_b16 v[146:147], v224 offset:5120\n"
    "ds_read_b64_tr_b16 v[148:149], v224 offset:6400\n"
    "s_add_u32 m0, s64, 81920\n"
    "s_mov_b32 s65, 3244032\n"
    "buffer_load_dwordx4 v239, s[44:47], s65 offen offset:0 lds\n"
    "s_waitcnt lgkmcnt(9)\n"
    "v_mfma_f32_32x32x16_bf16 v[150:165], v[0:3], v[134:137], v[150:165]\n"
    "ds_read_b128 v[20:23], v217 offset:512\n"
    "ds_read_b64_tr_b16 v[198:199], v224 offset:5184\n"
    "ds_read_b64_tr_b16 v[200:201], v224 offset:6464\n"
    "s_waitcnt lgkmcnt(9)\n"
    "v_mfma_f32_32x32x16_bf16 v[166:181], v[0:3], v[138:141], v[166:181]\n"
    "ds_read_b128 v[28:31], v218 offset:512\n"
    "ds_read_b64_tr_b16 v[202:203], v224 offset:5248\n"
    "ds_read_b64_tr_b16 v[204:205], v224 offset:6528\n"
    "s_waitcnt lgkmcnt(9)\n"
    "v_mfma_f32_32x32x16_bf16 v[182:197], v[0:3], v[142:145], v[182:197]\n"
    "ds_read_b64_tr_b16 v[134:135], v224 offset:5312\n"
    "ds_read_b64_tr_b16 v[136:137], v224 offset:6592\n"
    "s_add_u32 m0, s64, 86016\n"
    "s_mov_b32 s65, 3538944\n"
    "buffer_load_dwordx4 v239, s[44:47], s65 offen offset:0 lds\n"
    "s_waitcnt lgkmcnt(8)\n"
    "v_mfma_f32_32x32x16_bf16 v[64:79], v[8:11], v[146:149], v[64:79]\n"
    "ds_read_b64_tr_b16 v[138:139], v224 offset:5376\n"
    "ds_read_b64_tr_b16 v[140:141], v224 offset:6656\n"
    "s_waitcnt lgkmcnt(7)\n"
    "v_mfma_f32_32x32x16_bf16 v[80:95], v[8:11], v[198:201], v[80:95]\n"
    "ds_read_b64_tr_b16 v[142:143], v224 offset:10240\n"
    "ds_read_b64_tr_b16 v[144:145], v224 offset:11520\n"
    "s_add_u32 m0, s64, 90112\n"
    "s_mov_b32 s65, 3833856\n"
    "buffer_load_dwordx4 v239, s[44:47], s65 offen offset:0 lds\n"
    "s_waitcnt lgkmcnt(6)\n"
    "v_mfma_f32_32x32x16_bf16 v[150:165], v[8:11], v[202:205], v[150:165]\n"
    "ds_read_b64_tr_b16 v[146:147], v224 offset:10304\n"
    "ds_read_b64_tr_b16 v[148:149], v224 offset:11584\n"
    "s_waitcnt lgkmcnt(6)\n"
    "v_mfma_f32_32x32x16_bf16 v[166:181], v[8:11], v[134:137], v[166:181]\n"
    "ds_read_b64_tr_b16 v[198:199], v224 offset:10368\n"
    "ds_read_b64_tr_b16 v[200:201], v224 offset:11648\n"
    "s_add_u32 m0, s64, 94208\n"
    "s_mov_b32 s65, 4128768\n"
    "buffer_load_dwordx4 v239, s[44:47], s65 offen offset:0 lds\n"
    "s_waitcnt lgkmcnt(6)\n"
    "v_mfma_f32_32x32x16_bf16 v[182:197], v[8:11], v[138:141], v[182:197]\n"
    "ds_read_b64_tr_b16 v[202:203], v224 offset:10432\n"
    "ds_read_b64_tr_b16 v[204:205], v224 offset:11712\n"
    "ds_read_b128 v[96:99], v244 offset:0\n"
    "s_waitcnt lgkmcnt(7)\n"
    "v_mfma_f32_32x32x16_bf16 v[64:79], v[16:19], v[142:145], v[64:79]\n"
    "ds_read_b64_tr_b16 v[134:135], v224 offset:10496\n"
    "ds_read_b64_tr_b16 v[136:137], v224 offset:11776\n"
    "ds_read_b128 v[100:103], v244 offset:32\n"
    "s_waitcnt lgkmcnt(8)\n"
    "v_mfma_f32_32x32x16_bf16 v[80:95], v[16:19], v[146:149], v[80:95]\n"
    "ds_read_b64_tr_b16 v[138:139], v224 offset:15360\n"
    "ds_read_b64_tr_b16 v[140:141], v224 offset:16640\n"
    "s_add_u32 m0, s64, 98304\n"
    "s_mov_b32 s65, 4423680\n"
    "buffer_load_dwordx4 v239, s[44:47], s65 offen offset:0 lds\n"
    "s_waitcnt lgkmcnt(8)\n"
    "v_mfma_f32_32x32x16_bf16 v[150:165], v[16:19], v[198:201], v[150:165]\n"
    "ds_read_b64_tr_b16 v[142:143], v224 offset:15424\n"
    "ds_read_b64_tr_b16 v[144:145], v224 offset:16704\n"
    "ds_read_b128 v[104:107], v244 offset:64\n"
    "s_waitcnt lgkmcnt(9)\n"
    "v_mfma_f32_32x32x16_bf16 v[166:181], v[16:19], v[202:205], v[166:181]\n"
    "ds_read_b64_tr_b16 v[146:147], v224 offset:15488\n"
    "ds_read_b64_tr_b16 v[148:149], v224 offset:16768\n"
    "s_add_u32 m0, s64, 102400\n"
    "s_mov_b32 s65, 4718592\n"
    "buffer_load_dwordx4 v239, s[44:47], s65 offen offset:0 lds\n"
    "s_waitcnt lgkmcnt(8)\n"
    "v_mfma_f32_32x32x16_bf16 v[182:197], v[16:19], v[134:137], v[182:197]\n"
    "ds_read_b64_tr_b16 v[198:199], v224 offset:15552\n"
    "ds_read_b64_tr_b16 v[200:201], v224 offset:16832\n"
    "ds_read_b128 v[108:111], v244 offset:96\n"
    "s_waitcnt lgkmcnt(8)\n"
    "v_mfma_f32_32x32x16_bf16 v[64:79], v[24:27], v[138:141], v[64:79]\n"
    "ds_read_b64_tr_b16 v[202:203], v224 offset:15616\n"
    "ds_read_b64_tr_b16 v[204:205], v224 offset:16896\n"
    "s_add_u32 m0, s64, 106496\n"
    "s_mov_b32 s65, 5013504\n"
    "buffer_load_dwordx4 v239, s[44:47], s65 offen offset:0 lds\n"
    "s_waitcnt lgkmcnt(8)\n"
    "v_mfma_f32_32x32x16_bf16 v[80:95], v[24:27], v[142:145], v[80:95]\n"
    "s_waitcnt lgkmcnt(5)\n"
    "v_mfma_f32_32x32x16_bf16 v[150:165], v[24:27], v[146:149], v[150:165]\n"
    "s_waitcnt lgkmcnt(3)\n"
    "v_mfma_f32_32x32x16_bf16 v[166:181], v[24:27], v[198:201], v[166:181]\n"
    "s_waitcnt vmcnt(12)\n"
    "s_barrier\n"
    "ds_read_b64_tr_b16 v[112:113], v236 offset:36864\n"
    "ds_read_b64_tr_b16 v[114:115], v236 offset:37120\n"
    "s_waitcnt lgkmcnt(2)\n"
    "v_mfma_f32_32x32x16_bf16 v[182:197], v[24:27], v[202:205], v[182:197]\n"
    "ds_read_b64_tr_b16 v[116:117], v236 offset:38912\n"
    "ds_read_b64_tr_b16 v[118:119], v236 offset:39168\n"
    "s_add_u32 m0, s64, 143360\n"
    "s_mov_b32 s65, 128\n"
    "buffer_load_dwordx4 v239, s[44:47], s65 offen offset:0 lds\n"
    "s_waitcnt lgkmcnt(2)\n"
    "v_mfma_f32_32x32x16_bf16 v[32:47], v[112:115], a[0:3], v[96:111]\n"
    "ds_read_b64_tr_b16 v[120:121], v236 offset:40960\n"
    "v_mfma_f32_32x32x16_bf16 v[48:63], v[112:115], a[4:7], v[96:111]\n"
    "ds_read_b64_tr_b16 v[122:123], v236 offset:41216\n"
    "s_waitcnt lgkmcnt(2)\n"
    "v_mfma_f32_32x32x16_bf16 v[32:47], v[116:119], a[8:11], v[32:47]\n"
    "ds_read_b64_tr_b16 v[112:113], v236 offset:43008\n"
    "v_lshrrev_b32 v131, 9, v241\n"
    "s_lshl_b32 s96, s66, 7\n"
    "s_add_u32 s96, s96, 147456\n"
    "v_lshl_add_u32 v131, v131, 4, s96\n"
    "v_mfma_f32_32x32x16_bf16 v[48:63], v[116:119], a[12:15], v[48:63]\n"
    "ds_read_b64_tr_b16 v[114:115], v236 offset:43264\n"
    "s_waitcnt lgkmcnt(2)\n"
    "v_mfma_f32_32x32x16_bf16 v[32:47], v[120:123], a[16:19], v[32:47]\n"
    "ds_read_b64_tr_b16 v[116:117], v236 offset:45056\n"
    "s_mov_b32 exec_lo, 0x10000\n"
    "s_mov_b32 exec_hi, 0x10000\n"
    "ds_write_b128 v131, v[182:185] offset:0\n"
    "ds_write_b128 v131, v[186:189] offset:32\n"
    "ds_write_b128 v131, v[190:193] offset:64\n"
    "ds_write_b128 v131, v[194:197] offset:96\n"
    "s_mov_b64 exec, -1\n"
    "s_nop 0\n"
    "v_mfma_f32_32x32x16_bf16 v[48:63], v[120:123], a[20:23], v[48:63]\n"
    "ds_read_b64_tr_b16 v[118:119], v236 offset:45312\n"
    "s_waitcnt lgkmcnt(6)\n"
    "v_mfma_f32_32x32x16_bf16 v[32:47], v[112:115], a[24:27], v[32:47]\n"
    "ds_read_b64_tr_b16 v[120:121], v236 offset:47104\n"
    "v_and_b32 v129, 0x1f0, v241\n"
    "v_lshrrev_b32 v129, 2, v129\n"
    "s_lshl_b32 s96, s66, 7\n"
    "s_add_u32 s96, s96, 147456\n"
    "v_add_u32 v129, s96, v129\n"
    "v_xor_b32 v130, 0x80, v129\n"
    "v_mfma_f32_32x32x16_bf16 v[48:63], v[112:115], a[28:31], v[48:63]\n"
    "ds_read_b64_tr_b16 v[122:123], v236 offset:47360\n"
    "v_lshl_add_u32 v222, s66, 11, v241\n"
    "v_add_u32 v224, 150016, v222\n"
    "v_add_u32 v222, 110592, v222\n"
    "v_bfe_u32 v223, v241, 4, 5\n"
    "v_mov_b32 v218, 0xff800000\n"
    "v_mov_b32 v219, 0x7f800000\n"
    "v_mov_b32 v220, 0x358637bd\n"
    "v_cmp_gt_u32 vcc, 8, v223\n"
    "s_nop 1\n"
    "v_cndmask_b32_e64 v218, v218, 0, vcc\n"
    "v_cndmask_b32_e64 v220, 0, v220, vcc\n"
    "v_cndmask_b32_e64 v221, 0, 1.0, vcc\n"
    "s_waitcnt lgkmcnt(2)\n"
    "v_mfma_f32_32x32x16_bf16 v[32:47], v[116:119], a[32:35], v[32:47]\n"
    "ds_read_b64_tr_b16 v[112:113], v236 offset:49152\n"
    "v_cmp_lt_u32 vcc, 15, v223\n"
    "s_nop 1\n"
    "v_cndmask_b32_e64 v218, v218, 1.0, vcc\n"
    "v_cndmask_b32_e64 v219, v219, 1.0, vcc\n"
    "v_mul_f32 v124, s80, v64\n"
    "v_exp_f32_e64 v124, v124 clamp\n"
    "v_mul_f32 v125, s80, v65\n"
    "v_max_f32 v64, 0, v64\n"
    "v_exp_f32_e64 v125, v125 clamp\n"
    "v_mul_f32 v126, s80, v66\n"
    "v_add_f32 v64, v64, v124\n"
    "v_mfma_f32_32x32x16_bf16 v[48:63], v[116:119], a[36:39], v[48:63]\n"
    "ds_read_b64_tr_b16 v[114:115], v236 offset:49408\n"
    "v_max_f32 v65, 0, v65\n"
    "v_exp_f32_e64 v126, v126 clamp\n"
    "v_mul_f32 v127, s80, v67\n"
    "v_add_f32 v64, 0x358637bd, v64\n"
    "v_add_f32 v65, v65, v125\n"
    "v_max_f32 v66, 0, v66\n"
    "v_exp_f32_e64 v127, v127 clamp\n"
    "v_mul_f32 v128, s80, v68\n"
    "s_waitcnt lgkmcnt(2)\n"
    "v_mfma_f32_32x32x16_bf16 v[32:47], v[120:123], a[40:43], v[32:47]\n"
    "ds_read_b64_tr_b16 v[116:117], v236 offset:51200\n"
    "v_add_f32 v65, 0x358637bd, v65\n"
    "v_add_f32 v66, v66, v126\n"
    "v_max_f32 v67, 0, v67\n"
    "v_exp_f32_e64 v128, v128 clamp\n"
    "v_mul_f32 v124, s80, v69\n"
    "v_cvt_pk_bf16_f32 v64, v64, v65\n"
    "v_add_f32 v66, 0x358637bd, v66\n"
    "v_add_f32 v67, v67, v127\n"
    "v_max_f32 v68, 0, v68\n"
    "v_exp_f32_e64 v124, v124 clamp\n"
    "v_mfma_f32_32x32x16_bf16 v[48:63], v[120:123], a[44:47], v[48:63]\n"
    "ds_read_b64_tr_b16 v[118:119], v236 offset:51456\n"
    "v_mul_f32 v125, s80, v70\n"
    "v_add_f32 v67, 0x358637bd, v67\n"
    "v_add_f32 v68, v68, v128\n"
    "v_max_f32 v69, 0, v69\n"
    "v_exp_f32_e64 v125, v125 clamp\n"
    "v_mul_f32 v126, s80, v71\n"
    "v_cvt_pk_bf16_f32 v65, v66, v67\n"
    "v_add_f32 v68, 0x358637bd, v68\n"
    "v_add_f32 v69, v69, v124\n"
    "s_waitcnt lgkmcnt(2)\n"
    "v_mfma_f32_32x32x16_bf16 v[32:47], v[112:115], a[48:51], v[32:47]\n"
    "ds_read_b64_tr_b16 v[120:121], v236 offset:53248\n"
    "v_max_f32 v70, 0, v70\n"
    "v_exp_f32_e64 v126, v126 clamp\n"
    "v_mul_f32 v127, s80, v72\n"
    "v_add_f32 v69, 0x358637bd, v69\n"
    "v_add_f32 v70, v70, v125\n"
    "v_max_f32 v71, 0, v71\n"
    "v_exp_f32_e64 v127, v127 clamp\n"
    "v_mul_f32 v128, s80, v73\n"
    "v_cvt_pk_bf16_f32 v66, v68, v69\n"
    "v_add_f32 v70, 0x358637bd, v70\n"
    "v_add_f32 v71, v71, v126\n"
    "v_mfma_f32_32x32x16_bf16 v[48:63], v[112:115], a[52:55], v[48:63]\n"
    "ds_read_b64_tr_b16 v[122:123], v236 offset:53504\n"
    "ds_read_b128 v[96:99], v244 offset:256\n"
    "v_max_f32 v72, 0, v72\n"
    "v_exp_f32_e64 v128, v128 clamp\n"
    "v_mul_f32 v124, s80, v74\n"
    "v_add_f32 v71, 0x358637bd, v71\n"
    "v_add_f32 v72, v72, v127\n"
    "v_max_f32 v73, 0, v73\n"
    "s_waitcnt lgkmcnt(3)\n"
    "v_mfma_f32_32x32x16_bf16 v[32:47], v[116:119], a[56:59], v[32:47]\n"
    "ds_read_b64_tr_b16 v[112:113], v236 offset:55296\n"
    "v_exp_f32_e64 v124, v124 clamp\n"
    "v_mul_f32 v125, s80, v75\n"
    "v_cvt_pk_bf16_f32 v67, v70, v71\n"
    "v_add_f32 v72, 0x358637bd, v72\n"
    "v_add_f32 v73, v73, v128\n"
    "v_max_f32 v74, 0, v74\n"
    "v_exp_f32_e64 v125, v125 clamp\n"
    "v_mul_f32 v126, s80, v76\n"
    "v_add_f32 v73, 0x358637bd, v73\n"
    "v_add_f32 v74, v74, v124\n"
    "v_max_f32 v75, 0, v75\n"
    "v_mfma_f32_32x32x16_bf16 v[48:63], v[116:119], a[60:63], v[48:63]\n"
    "ds_read_b64_tr_b16 v[114:115], v236 offset:55552\n"
    "v_exp_f32_e64 v126, v126 clamp\n"
    "v_mul_f32 v127, s80, v77\n"
    "v_cvt_pk_bf16_f32 v68, v72, v73\n"
    "v_add_f32 v74, 0x358637bd, v74\n"
    "v_add_f32 v75, v75, v125\n"
    "v_max_f32 v76, 0, v76\n"
    "v_exp_f32_e64 v127, v127 clamp\n"
    "v_mul_f32 v128, s80, v78\n"
    "s_waitcnt lgkmcnt(3)\n"
    "v_mfma_f32_32x32x16_bf16 v[32:47], v[120:123], a[64:67], v[32:47]\n"
    "ds_read_b64_tr_b16 v[116:117], v236 offset:57344\n"
    "v_add_f32 v75, 0x358637bd, v75\n"
    "v_add_f32 v76, v76, v126\n"
    "v_max_f32 v77, 0, v77\n"
    "v_exp_f32_e64 v128, v128 clamp\n"
    "v_mul_f32 v124, s80, v79\n"
    "v_cvt_pk_bf16_f32 v69, v74, v75\n"
    "v_add_f32 v76, 0x358637bd, v76\n"
    "v_add_f32 v77, v77, v127\n"
    "v_max_f32 v78, 0, v78\n"
    "v_exp_f32_e64 v124, v124 clamp\n"
    "v_add_f32 v77, 0x358637bd, v77\n"
    "v_mfma_f32_32x32x16_bf16 v[48:63], v[120:123], a[68:71], v[48:63]\n"
    "ds_read_b64_tr_b16 v[118:119], v236 offset:57600\n"
    "v_add_f32 v78, v78, v128\n"
    "v_max_f32 v79, 0, v79\n"
    "v_cvt_pk_bf16_f32 v70, v76, v77\n"
    "v_add_f32 v78, 0x358637bd, v78\n"
    "v_add_f32 v79, v79, v124\n"
    "v_add_f32 v79, 0x358637bd, v79\n"
    "v_cvt_pk_bf16_f32 v71, v78, v79\n"
    "v_mul_f32 v124, s80, v80\n"
    "v_exp_f32_e64 v124, v124 clamp\n"
    "s_waitcnt lgkmcnt(2)\n"
    "v_mfma_f32_32x32x16_bf16 v[32:47], v[112:115], a[72:75], v[32:47]\n"
    "ds_read_b64_tr_b16 v[120:121], v236 offset:59392\n"
    "v_mul_f32 v125, s80, v81\n"
    "v_max_f32 v80, 0, v80\n"
    "v_exp_f32_e64 v125, v125 clamp\n"
    "v_mul_f32 v126, s80, v82\n"
    "v_add_f32 v80, v80, v124\n"
    "v_max_f32 v81, 0, v81\n"
    "v_exp_f32_e64 v126, v126 clamp\n"
    "v_mul_f32 v127, s80, v83\n"
    "v_add_f32 v80, 0x358637bd, v80\n"
    "v_add_f32 v81, v81, v125\n"
    "v_mfma_f32_32x32x16_bf16 v[48:63], v[112:115], a[76:79], v[48:63]\n"
    "ds_read_b64_tr_b16 v[122:123], v236 offset:59648\n"
    "v_max_f32 v82, 0, v82\n"
    "v_exp_f32_e64 v127, v127 clamp\n"
    "v_mul_f32 v128, s80, v84\n"
    "v_add_f32 v81, 0x358637bd, v81\n"
    "v_add_f32 v82, v82, v126\n"
    "v_max_f32 v83, 0, v83\n"
    "v_exp_f32_e64 v128, v128 clamp\n"
    "v_mul_f32 v124, s80, v85\n"
    "v_cvt_pk_bf16_f32 v80, v80, v81\n"
    "s_waitcnt lgkmcnt(2)\n"
    "v_mfma_f32_32x32x16_bf16 v[32:47], v[116:119], a[80:83], v[32:47]\n"
    "s_waitcnt vmcnt(7)\n"
    "s_barrier\n"
    "ds_read_b64_tr_b16 v[112:113], v236 offset:61440\n"
    "v_add_f32 v82, 0x358637bd, v82\n"
    "v_add_f32 v83, v83, v127\n"
    "v_max_f32 v84, 0, v84\n"
    "v_exp_f32_e64 v124, v124 clamp\n"
    "s_xor_b32 s96, s69, 1\n"
    "s_lshl_b32 s96, s96, 6\n"
    "s_add_u32 s96, s96, s22\n"
    "v_mov_b32 v243, s96\n"
    "buffer_load_dwordx2 v[234:235], v243, s[56:59], 0 offen offset:0 sc1\n"
    "v_mfma_f32_32x32x16_bf16 v[48:63], v[116:119], a[84:87], v[48:63]\n"
    "ds_read_b64_tr_b16 v[114:115], v236 offset:61696\n"
    "s_add_u32 m0, s64, 0\n"
    "s_mov_b32 s65, 295040\n"
    "buffer_load_dwordx4 v239, s[44:47], s65 offen offset:0 lds\n"
    "v_mul_f32 v125, s80, v86\n"
    "ds_read_b32 v245, v129 offset:0\n"
    "ds_read_b32 v246, v130 offset:0\n"
    "s_waitcnt lgkmcnt(4)\n"
    "v_mfma_f32_32x32x16_bf16 v[32:47], v[120:123], a[88:91], v[32:47]\n"
    "ds_read_b64_tr_b16 v[116:117], v236 offset:63488\n"
    "v_add_f32 v83, 0x358637bd, v83\n"
    "v_add_f32 v84, v84, v128\n"
    "v_max_f32 v85, 0, v85\n"
    "v_exp_f32_e64 v125, v125 clamp\n"
    "v_mul_f32 v126, s80, v87\n"
    "v_cvt_pk_bf16_f32 v81, v82, v83\n"
    "v_add_f32 v84, 0x358637bd, v84\n"
    "v_add_f32 v85, v85, v124\n"
    "v_max_f32 v86, 0, v86\n"
    "v_exp_f32_e64 v126, v126 clamp\n"
    "s_waitcnt lgkmcnt(2)\n"
    "v_mul_f32 v249, 0xbfb8aa3b, v245\n"
    "v_mul_f32 v245, s80, v245\n"
    "v_mfma_f32_32x32x16_bf16 v[48:63], v[120:123], a[92:95], v[48:63]\n"
    "ds_read_b64_tr_b16 v[118:119], v236 offset:63744\n"
    "s_add_u32 m0, s64, 4096\n"
    "s_mov_b32 s65, 589952\n"
    "buffer_load_dwordx4 v239, s[44:47], s65 offen offset:0 lds\n"
    "v_mul_f32 v127, s80, v88\n"
    "s_waitcnt lgkmcnt(2)\n"
    "v_mul_f32 v246, s80, v246\n"
    "v_exp_f32 v249, v249\n"
    "v_mfma_f32_32x32x16_bf16 v[32:47], v[112:115], a[96:99], v[32:47]\n"
    "ds_read_b64_tr_b16 v[120:121], v237 offset:0\n"
    "v_add_f32 v85, 0x358637bd, v85\n"
    "v_add_f32 v86, v86, v125\n"
    "v_max_f32 v87, 0, v87\n"
    "v_exp_f32_e64 v127, v127 clamp\n"
    "v_mul_f32 v128, s80, v89\n"
    "v_cvt_pk_bf16_f32 v82, v84, v85\n"
    "v_add_f32 v86, 0x358637bd, v86\n"
    "v_add_f32 v87, v87, v126\n"
    "v_max_f32 v88, 0, v88\n"
    "v_exp_f32_e64 v128, v128 clamp\n"
    "v_mul_f32 v124, s80, v90\n"
    "v_exp_f32 v245, v245\n"
    "v_mfma_f32_32x32x16_bf16 v[48:63], v[112:115], a[100:103], v[48:63]\n"
    "ds_read_b64_tr_b16 v[122:123], v237 offset:256\n"
    "ds_read_b128 v[100:103], v244 offset:288\n"
    "v_add_f32 v87, 0x358637bd, v87\n"
    "v_add_f32 v88, v88, v127\n"
    "v_max_f32 v89, 0, v89\n"
    "v_exp_f32_e64 v124, v124 clamp\n"
    "v_mul_f32 v125, s80, v91\n"
    "v_cvt_pk_bf16_f32 v83, v86, v87\n"
    "v_add_f32 v88, 0x358637bd, v88\n"
    "v_exp_f32 v246, v246\n"
    "s_waitcnt lgkmcnt(3)\n"
    "v_mfma_f32_32x32x16_bf16 v[32:47], v[116:119], a[104:107], v[32:47]\n"
    "ds_read_b64_tr_b16 v[112:113], v237 offset:2048\n"
    "s_add_u32 m0, s64, 8192\n"
    "s_mov_b32 s65, 884864\n"
    "buffer_load_dwordx4 v239, s[44:47], s65 offen offset:0 lds\n"
    "v_add_f32 v249, 1.0, v249\n"
    "v_mfma_f32_32x32x16_bf16 v[48:63], v[116:119], a[108:111], v[48:63]\n"
    "ds_read_b64_tr_b16 v[114:115], v237 offset:2304\n"
    "v_add_f32 v89, v89, v128\n"
    "v_max_f32 v90, 0, v90\n"
    "v_exp_f32_e64 v125, v125 clamp\n"
    "v_mul_f32 v126, s80, v92\n"
    "v_add_f32 v89, 0x358637bd, v89\n"
    "v_add_f32 v90, v90, v124\n"
    "v_max_f32 v91, 0, v91\n"
    "v_exp_f32_e64 v126, v126 clamp\n"
    "v_mul_f32 v127, s80, v93\n"
    "v_add_f32 v245, 1.0, v245\n"
    "v_add_f32 v246, 1.0, v246\n"
    "s_waitcnt lgkmcnt(3)\n"
    "v_mfma_f32_32x32x16_bf16 v[32:47], v[120:123], a[112:115], v[32:47]\n"
    "ds_read_b64_tr_b16 v[116:117], v237 offset:4096\n"
    "s_add_u32 m0, s64, 12288\n"
    "s_mov_b32 s65, 1179776\n"
    "buffer_load_dwordx4 v239, s[44:47], s65 offen offset:0 lds\n"
    "v_rcp_f32 v249, v249\n"
    "v_mfma_f32_32x32x16_bf16 v[48:63], v[120:123], a[116:119], v[48:63]\n"
    "ds_read_b64_tr_b16 v[118:119], v237 offset:4352\n"
    "v_cvt_pk_bf16_f32 v84, v88, v89\n"
    "v_add_f32 v90, 0x358637bd, v90\n"
    "v_add_f32 v91, v91, v125\n"
    "v_max_f32 v92, 0, v92\n"
    "v_exp_f32_e64 v127, v127 clamp\n"
    "v_mul_f32 v128, s80, v94\n"
    "v_add_f32 v91, 0x358637bd, v91\n"
    "v_add_f32 v92, v92, v126\n"
    "v_max_f32 v93, 0, v93\n"
    "v_rcp_f32 v245, v245\n"
    "s_waitcnt lgkmcnt(2)\n"
    "v_mfma_f32_32x32x16_bf16 v[32:47], v[112:115], a[120:123], v[32:47]\n"
    "ds_read_b64_tr_b16 v[120:121], v237 offset:6144\n"
    "v_exp_f32_e64 v128, v128 clamp\n"
    "v_mul_f32 v124, s80, v95\n"
    "v_cvt_pk_bf16_f32 v85, v90, v91\n"
    "v_add_f32 v92, 0x358637bd, v92\n"
    "v_add_f32 v93, v93, v127\n"
    "v_max_f32 v94, 0, v94\n"
    "v_exp_f32_e64 v124, v124 clamp\n"
    "v_add_f32 v93, 0x358637bd, v93\n"
    "v_add_f32 v94, v94, v128\n"
    "v_max_f32 v95, 0, v95\n"
    "v_cvt_pk_bf16_f32 v86, v92, v93\n"
    "v_rcp_f32 v246, v246\n"
    "v_mfma_f32_32x32x16_bf16 v[48:63], v[112:115], a[124:127], v[48:63]\n"
    "ds_read_b64_tr_b16 v[122:123], v237 offset:6400\n"
    "s_add_u32 m0, s64, 16384\n"
    "s_mov_b32 s65, 1474688\n"
    "buffer_load_dwordx4 v239, s[44:47], s65 offen offset:0 lds\n"
    "v_add_f32 v94, 0x358637bd, v94\n"
    "v_mul_f32 v247, -2.0, v245\n"
    "v_mul_f32 v248, -2.0, v246\n"
    "s_waitcnt lgkmcnt(2)\n"
    "v_mfma_f32_32x32x16_bf16 v[32:47], v[116:119], a[128:131], v[32:47]\n"
    "ds_read_b64_tr_b16 v[112:113], v237 offset:8192\n"
    "v_add_f32 v95, v95, v124\n"
    "v_add_f32 v95, 0x358637bd, v95\n"
    "v_cvt_pk_bf16_f32 v87, v94, v95\n"
    "v_cvt_pk_bf16_f32 v150, v150, v151\n"
    "v_cvt_pk_bf16_f32 v151, v152, v153\n"
    "v_cvt_pk_bf16_f32 v152, v154, v155\n"
    "v_cvt_pk_bf16_f32 v153, v156, v157\n"
    "v_cvt_pk_bf16_f32 v154, v158, v159\n"
    "v_cvt_pk_bf16_f32 v155, v160, v161\n"
    "v_cvt_pk_bf16_f32 v156, v162, v163\n"
    "v_cvt_pk_bf16_f32 v157, v164, v165\n"
    "v_cvt_pk_bf16_f32 v166, v166, v167\n"
    "v_mfma_f32_32x32x16_bf16 v[48:63], v[116:119], a[132:135], v[48:63]\n"
    "ds_read_b64_tr_b16 v[114:115], v237 offset:8448\n"
    "s_add_u32 m0, s64, 20480\n"
    "s_mov_b32 s65, 1769600\n"
    "buffer_load_dwordx4 v239, s[44:47], s65 offen offset:0 lds\n"
    "v_cvt_pk_bf16_f32 v167, v168, v169\n"
    "s_waitcnt lgkmcnt(2)\n"
    "v_mfma_f32_32x32x16_bf16 v[32:47], v[120:123], a[136:139], v[32:47]\n"
    "ds_read_b64_tr_b16 v[116:117], v237 offset:10240\n"
    "v_cvt_pk_bf16_f32 v168, v170, v171\n"
    "v_cvt_pk_bf16_f32 v169, v172, v173\n"
    "v_cvt_pk_bf16_f32 v170, v174, v175\n"
    "v_cvt_pk_bf16_f32 v171, v176, v177\n"
    "v_cvt_pk_bf16_f32 v172, v178, v179\n"
    "v_cvt_pk_bf16_f32 v173, v180, v181\n"
    "v_mul_f32 v124, s80, v182\n"
    "v_exp_f32_e64 v124, v124 clamp\n"
    "v_mul_f32 v125, s80, v183\n"
    "v_med3_f32 v182, v182, v218, v219\n"
    "v_exp_f32_e64 v125, v125 clamp\n"
    "v_mfma_f32_32x32x16_bf16 v[48:63], v[120:123], a[140:143], v[48:63]\n"
    "ds_read_b64_tr_b16 v[118:119], v237 offset:10496\n"
    "v_mul_f32 v126, s80, v184\n"
    "v_fma_f32 v182, v124, v221, v182\n"
    "v_med3_f32 v183, v183, v218, v219\n"
    "v_exp_f32_e64 v126, v126 clamp\n"
    "v_mul_f32 v127, s80, v185\n"
    "v_add_f32 v182, v220, v182\n"
    "v_fma_f32 v183, v125, v221, v183\n"
    "v_med3_f32 v184, v184, v218, v219\n"
    "s_waitcnt lgkmcnt(2)\n"
    "v_mfma_f32_32x32x16_bf16 v[32:47], v[112:115], a[144:147], v[32:47]\n"
    "ds_read_b64_tr_b16 v[120:121], v237 offset:12288\n"
    "s_add_u32 m0, s64, 24576\n"
    "s_mov_b32 s65, 2064512\n"
    "buffer_load_dwordx4 v239, s[44:47], s65 offen offset:0 lds\n"
    "v_exp_f32_e64 v127, v127 clamp\n"
    "v_mfma_f32_32x32x16_bf16 v[48:63], v[112:115], a[148:151], v[48:63]\n"
    "ds_read_b64_tr_b16 v[122:123], v237 offset:12544\n"
    "v_mul_f32 v128, s80, v186\n"
    "v_add_f32 v183, v220, v183\n"
    "v_fma_f32 v184, v126, v221, v184\n"
    "v_med3_f32 v185, v185, v218, v219\n"
    "v_exp_f32_e64 v128, v128 clamp\n"
    "v_mul_f32 v124, s80, v187\n"
    "v_cvt_pk_bf16_f32 v182, v182, v183\n"
    "v_add_f32 v184, v220, v184\n"
    "v_fma_f32 v185, v127, v221, v185\n"
    "s_waitcnt lgkmcnt(2)\n"
    "v_mfma_f32_32x32x16_bf16 v[32:47], v[116:119], a[152:155], v[32:47]\n"
    "ds_read_b64_tr_b16 v[112:113], v237 offset:14336\n"
    "s_add_u32 m0, s64, 28672\n"
    "s_mov_b32 s65, 2359424\n"
    "buffer_load_dwordx4 v239, s[44:47], s65 offen offset:0 lds\n"
    "v_med3_f32 v186, v186, v218, v219\n"
    "v_mfma_f32_32x32x16_bf16 v[48:63], v[116:119], a[156:159], v[48:63]\n"
    "ds_read_b64_tr_b16 v[114:115], v237 offset:14592\n"
    "v_exp_f32_e64 v124, v124 clamp\n"
    "v_mul_f32 v125, s80, v188\n"
    "v_add_f32 v185, v220, v185\n"
    "v_fma_f32 v186, v128, v221, v186\n"
    "v_med3_f32 v187, v187, v218, v219\n"
    "v_exp_f32_e64 v125, v125 clamp\n"
    "v_mul_f32 v126, s80, v189\n"
    "v_cvt_pk_bf16_f32 v183, v184, v185\n"
    "s_waitcnt lgkmcnt(2)\n"
    "v_mfma_f32_32x32x16_bf16 v[32:47], v[120:123], a[160:163], v[32:47]\n"
    "ds_read_b64_tr_b16 v[116:117], v237 offset:16384\n"
    "v_add_f32 v186, v220, v186\n"
    "v_fma_f32 v187, v124, v221, v187\n"
    "v_med3_f32 v188, v188, v218, v219\n"
    "v_exp_f32_e64 v126, v126 clamp\n"
    "v_mul_f32 v127, s80, v190\n"
    "v_add_f32 v187, v220, v187\n"
    "v_fma_f32 v188, v125, v221, v188\n"
    "v_med3_f32 v189, v189, v218, v219\n"
    "v_exp_f32_e64 v127, v127 clamp\n"
    "v_mul_f32 v128, s80, v191\n"
    "v_mfma_f32_32x32x16_bf16 v[48:63], v[120:123], a[164:167], v[48:63]\n"
    "ds_read_b64_tr_b16 v[118:119], v237 offset:16640\n"
    "s_add_u32 m0, s64, 32768\n"
    "s_mov_b32 s65, 2654336\n"
    "buffer_load_dwordx4 v239, s[44:47], s65 offen offset:0 lds\n"
    "v_cvt_pk_bf16_f32 v184, v186, v187\n"
    "s_waitcnt lgkmcnt(2)\n"
    "v_mfma_f32_32x32x16_bf16 v[32:47], v[112:115], a[168:171], v[32:47]\n"
    "ds_read_b64_tr_b16 v[120:121], v237 offset:18432\n"
    "ds_read_b128 v[104:107], v244 offset:320\n"
    "v_add_f32 v188, v220, v188\n"
    "v_fma_f32 v189, v126, v221, v189\n"
    "v_med3_f32 v190, v190, v218, v219\n"
    "v_exp_f32_e64 v128, v128 clamp\n"
    "v_mul_f32 v124, s80, v192\n"
    "v_add_f32 v189, v220, v189\n"
    "v_fma_f32 v190, v127, v221, v190\n"
    "v_med3_f32 v191, v191, v218, v219\n"
    "v_exp_f32_e64 v124, v124 clamp\n"
    "v_mfma_f32_32x32x16_bf16 v[48:63], v[112:115], a[172:175], v[48:63]\n"
    "ds_read_b64_tr_b16 v[122:123], v237 offset:18688\n"
    "v_mul_f32 v125, s80, v193\n"
    "v_cvt_pk_bf16_f32 v185, v188, v189\n"
    "v_add_f32 v190, v220, v190\n"
    "v_fma_f32 v191, v128, v221, v191\n"
    "v_med3_f32 v192, v192, v218, v219\n"
    "v_exp_f32_e64 v125, v125 clamp\n"
    "v_mul_f32 v126, s80, v194\n"
    "v_add_f32 v191, v220, v191\n"
    "v_fma_f32 v192, v124, v221, v192\n"
    "s_waitcnt lgkmcnt(3)\n"
    "v_mfma_f32_32x32x16_bf16 v[32:47], v[116:119], a[176:179], v[32:47]\n"
    "s_waitcnt vmcnt(11)\n"
    "s_barrier\n"
    "ds_read_b64_tr_b16 v[112:113], v237 offset:20480\n"
    "v_med3_f32 v193, v193, v218, v219\n"
    "v_exp_f32_e64 v126, v126 clamp\n"
    "v_mul_f32 v127, s80, v195\n"
    "v_cvt_pk_bf16_f32 v186, v190, v191\n"
    "v_mfma_f32_32x32x16_bf16 v[48:63], v[116:119], a[180:183], v[48:63]\n"
    "ds_read_b64_tr_b16 v[114:115], v237 offset:20736\n"
    "s_add_u32 m0, s64, 36864\n"
    "s_mov_b32 s65, 2949248\n"
    "buffer_load_dwordx4 v239, s[44:47], s65 offen offset:0 lds\n"
    "v_add_f32 v192, v220, v192\n"
    "s_waitcnt lgkmcnt(2)\n"
    "v_mfma_f32_32x32x16_bf16 v[32:47], v[120:123], a[184:187], v[32:47]\n"
    "ds_read_b64_tr_b16 v[116:117], v237 offset:22528\n"
    "v_fma_f32 v193, v125, v221, v193\n"
    "v_med3_f32 v194, v194, v218, v219\n"
    "v_exp_f32_e64 v127, v127 clamp\n"
    "v_mul_f32 v128, s80, v196\n"
    "v_add_f32 v193, v220, v193\n"
    "v_fma_f32 v194, v126, v221, v194\n"
    "v_med3_f32 v195, v195, v218, v219\n"
    "v_exp_f32_e64 v128, v128 clamp\n"
    "v_mul_f32 v124, s80, v197\n"
    "v_cvt_pk_bf16_f32 v187, v192, v193\n"
    "v_mfma_f32_32x32x16_bf16 v[48:63], v[120:123], a[188:191], v[48:63]\n"
    "ds_read_b64_tr_b16 v[118:119], v237 offset:22784\n"
    "v_add_f32 v194, v220, v194\n"
    "v_fma_f32 v195, v127, v221, v195\n"
    "v_med3_f32 v196, v196, v218, v219\n"
    "v_exp_f32_e64 v124, v124 clamp\n"
    "v_add_f32 v195, v220, v195\n"
    "v_fma_f32 v196, v128, v221, v196\n"
    "v_med3_f32 v197, v197, v218, v219\n"
    "v_cvt_pk_bf16_f32 v188, v194, v195\n"
    "v_add_f32 v196, v220, v196\n"
    "s_waitcnt lgkmcnt(2)\n"
    "v_mfma_f32_32x32x16_bf16 v[32:47], v[112:115], a[192:195], v[32:47]\n"
    "ds_read_b64_tr_b16 v[120:121], v237 offset:24576\n"
    "s_add_u32 m0, s64, 40960\n"
    "s_mov_b32 s65, 3244160\n"
    "buffer_load_dwordx4 v239, s[44:47], s65 offen offset:0 lds\n"
    "v_fma_f32 v197, v124, v221, v197\n"
    "v_mfma_f32_32x32x16_bf16 v[48:63], v[112:115], a[196:199], v[48:63]\n"
    "ds_read_b64_tr_b16 v[122:123], v237 offset:24832\n"
    "v_add_f32 v197, v220, v197\n"
    "v_cvt_pk_bf16_f32 v189, v196, v197\n"
    "s_lshl_b32 s96, s66, 2\n"
    "s_lshr_b32 s96, 16400, s96\n"
    "s_and_b32 s96, s96, 15\n"
    "s_lshl_b32 s97, s96, 13\n"
    "s_add_u32 s97, s97, 110592\n"
    "s_cmp_eq_u32 s96, 4\n"
    "s_cselect_b32 s97, 150016, s97\n"
    "v_add_u32 v214, s97, v241\n"
    "s_waitcnt lgkmcnt(2)\n"
    "v_mfma_f32_32x32x16_bf16 v[32:47], v[116:119], a[200:203], v[32:47]\n"
    "ds_read_b64_tr_b16 v[112:113], v237 offset:26624\n"
    "s_lshl_b32 s96, s66, 2\n"
    "s_lshr_b32 s96, 13346, s96\n"
    "s_and_b32 s96, s96, 15\n"
    "s_lshl_b32 s97, s96, 13\n"
    "s_add_u32 s97, s97, 110592\n"
    "s_cmp_eq_u32 s96, 4\n"
    "s_cselect_b32 s97, 150016, s97\n"
    "v_add_u32 v215, s97, v241\n"
    "s_lshl_b32 s96, s66, 2\n"
    "s_lshr_b32 s96, 16656, s96\n"
    "s_and_b32 s96, s96, 15\n"
    "s_lshl_b32 s97, s96, 13\n"
    "s_add_u32 s97, s97, 110592\n"
    "s_cmp_eq_u32 s96, 4\n"
    "s_cselect_b32 s97, 150016, s97\n"
    "v_add_u32 v216, s97, v241\n"
    "s_waitcnt vmcnt(11)\n"
    "v_subrev_u32 v234, s70, v234\n"
    "v_cmp_eq_u32_e64 s[16:17], s21, v235\n"
    "v_cmp_gt_u32 vcc, 16, v234\n"
    "s_nop 0\n"
    "s_and_b64 vcc, vcc, s[16:17]\n"
    "s_cmp_eq_u64 vcc, -1\n"
    "s_cbranch_scc0 LslowK0_%=\n"
    "LbackK0_%=:\n"
    "v_mfma_f32_32x32x16_bf16 v[48:63], v[116:119], a[204:207], v[48:63]\n"
    "ds_read_b64_tr_b16 v[114:115], v237 offset:26880\n"
    "s_lshl_b32 s96, s66, 2\n"
    "s_lshr_b32 s96, 17459, s96\n"
    "s_and_b32 s96, s96, 15\n"
    "s_lshl_b32 s97, s96, 13\n"
    "s_add_u32 s97, s97, 110592\n"
    "s_cmp_eq_u32 s96, 4\n"
    "s_cselect_b32 s97, 150016, s97\n"
    "v_add_u32 v217, s97, v241\n"
    "s_waitcnt lgkmcnt(2)\n"
    "v_mfma_f32_32x32x16_bf16 v[32:47], v[120:123], a[208:211], v[32:47]\n"
    "ds_read_b64_tr_b16 v[116:117], v237 offset:28672\n"
    "s_add_u32 m0, s64, 45056\n"
    "s_mov_b32 s65, 3539072\n"
    "buffer_load_dwordx4 v239, s[44:47], s65 offen offset:0 lds\n"
    "v_mfma_f32_32x32x16_bf16 v[48:63], v[120:123], a[212:215], v[48:63]\n"
    "ds_read_b64_tr_b16 v[118:119], v237 offset:28928\n"
    "ds_write_b128 v222, v[64:67] offset:0\n"
    "s_waitcnt lgkmcnt(3)\n"
    "v_mfma_f32_32x32x16_bf16 v[32:47], v[112:115], a[216:219], v[32:47]\n"
    "ds_read_b64_tr_b16 v[120:121], v237 offset:30720\n"
    "ds_write_b128 v222, v[68:71] offset:1024\n"
    "ds_write_b128 v222, v[80:83] offset:8192\n"
    "v_mfma_f32_32x32x16_bf16 v[48:63], v[112:115], a[220:223], v[48:63]\n"
    "ds_read_b64_tr_b16 v[122:123], v237 offset:30976\n"
    "s_add_u32 m0, s64, 49152\n"
    "s_mov_b32 s65, 3833984\n"
    "buffer_load_dwordx4 v239, s[44:47], s65 offen offset:0 lds\n"
    "s_waitcnt lgkmcnt(5)\n"
    "v_mfma_f32_32x32x16_bf16 v[32:47], v[116:119], a[224:227], v[32:47]\n"
    "ds_read_b64_tr_b16 v[112:113], v237 offset:32768\n"
    "ds_read_b128 v[108:111], v244 offset:352\n"
    "ds_write_b128 v222, v[84:87] offset:9216\n"
    "v_mfma_f32_32x32x16_bf16 v[48:63], v[116:119], a[228:231], v[48:63]\n"
    "ds_read_b64_tr_b16 v[114:115], v237 offset:33024\n"
    "ds_write_b128 v222, v[150:153] offset:16384\n"
    "s_waitcnt lgkmcnt(5)\n"
    "v_mfma_f32_32x32x16_bf16 v[32:47], v[120:123], a[232:235], v[32:47]\n"
    "ds_read_b64_tr_b16 v[116:117], v237 offset:34816\n"
    "ds_write_b128 v222, v[154:157] offset:17408\n"
    "ds_write_b128 v222, v[166:169] offset:24576\n"
    "v_mfma_f32_32x32x16_bf16 v[48:63], v[120:123], a[236:239], v[48:63]\n"
    "ds_read_b64_tr_b16 v[118:119], v237 offset:35072\n"
    "s_add_u32 m0, s64, 53248\n"
    "s_mov_b32 s65, 4128896\n"
    "buffer_load_dwordx4 v239, s[44:47], s65 offen offset:0 lds\n"
    "s_waitcnt lgkmcnt(5)\n"
    "v_mfma_f32_32x32x16_bf16 v[32:47], v[112:115], a[240:243], v[32:47]\n"
    "ds_read_b64_tr_b16 v[120:121], v237 offset:36864\n"
    "ds_write_b128 v222, v[170:173] offset:25600\n"
    "ds_write_b128 v224, v[182:185] offset:0\n"
    "v_mfma_f32_32x32x16_bf16 v[48:63], v[112:115], a[244:247], v[48:63]\n"
    "ds_read_b64_tr_b16 v[122:123], v237 offset:37120\n"
    "ds_write_b128 v224, v[186:189] offset:1024\n"
    "s_waitcnt lgkmcnt(5)\n"
    "v_mfma_f32_32x32x16_bf16 v[32:47], v[116:119], a[248:251], v[32:47]\n"
    "ds_read_b64_tr_b16 v[112:113], v237 offset:38912\n"
    "s_add_u32 m0, s64, 57344\n"
    "s_mov_b32 s65, 4423808\n"
    "buffer_load_dwordx4 v239, s[44:47], s65 offen offset:0 lds\n"
    "v_mfma_f32_32x32x16_bf16 v[48:63], v[116:119], a[252:255], v[48:63]\n"
    "ds_read_b64_tr_b16 v[114:115], v237 offset:39168\n"
    "s_waitcnt lgkmcnt(3)\n"
    "v_mfma_f32_32x32x16_bf16 v[32:47], v[120:123], v[0:3], v[32:47]\n"
    "ds_read_b64_tr_b16 v[116:117], v237 offset:40960\n"
    "v_mfma_f32_32x32x16_bf16 v[48:63], v[120:123], v[4:7], v[48:63]\n"
    "ds_read_b64_tr_b16 v[118:119], v237 offset:41216\n"
    "s_waitcnt lgkmcnt(2)\n"
    "v_mfma_f32_32x32x16_bf16 v[32:47], v[112:115], v[8:11], v[32:47]\n"
    "ds_read_b64_tr_b16 v[120:121], v237 offset:43008\n"
    "s_waitcnt vmcnt(10)\n"
    "s_barrier\n"
    "ds_read_b128 v[150:153], v214 offset:0\n"
    "ds_read_b128 v[154:157], v215 offset:0\n"
    "v_mfma_f32_32x32x16_bf16 v[48:63], v[112:115], v[12:15], v[48:63]\n"
    "ds_read_b64_tr_b16 v[122:123], v237 offset:43264\n"
    "s_add_u32 m0, s64, 61440\n"
    "s_mov_b32 s65, 4718720\n"
    "buffer_load_dwordx4 v239, s[44:47], s65 offen offset:0 lds\n"
    "s_waitcnt lgkmcnt(4)\n"
    "v_mfma_f32_32x32x16_bf16 v[32:47], v[116:119], v[16:19], v[32:47]\n"
    "ds_read_b128 v[158:161], v216 offset:0\n"
    "ds_read_b128 v[162:165], v217 offset:0\n"
    "v_mfma_f32_32x32x16_bf16 v[48:63], v[116:119], v[20:23], v[48:63]\n"
    "s_waitcnt lgkmcnt(2)\n"
    "v_mfma_f32_32x32x16_bf16 v[32:47], v[120:123], v[24:27], v[32:47]\n"
    "ds_read_b128 v[198:201], v214 offset:1024\n"
    "ds_read_b128 v[202:205], v215 offset:1024\n"
    "v_mfma_f32_32x32x16_bf16 v[48:63], v[120:123], v[28:31], v[48:63]\n"
    "v_mfma_f32_32x32x16_bf16 v[166:181], v[150:153], v[154:157], 0\n"
    "ds_read_b128 v[206:209], v216 offset:1024\n"
    "ds_read_b128 v[210:213], v217 offset:1024\n"
    "s_waitcnt lgkmcnt(4)\n"
    "v_mfma_f32_32x32x16_bf16 v[182:197], v[158:161], v[162:165], 0\n"
    "ds_read_b128 v[150:153], v214 offset:2048\n"
    "ds_read_b128 v[154:157], v215 offset:2048\n"
    "s_add_u32 m0, s64, 65536\n"
    "s_mov_b32 s65, 5013632\n"
    "buffer_load_dwordx4 v239, s[44:47], s65 offen offset:0 lds\n"
    "s_waitcnt lgkmcnt(4)\n"
    "v_mfma_f32_32x32x16_bf16 v[166:181], v[198:201], v[202:205], v[166:181]\n"
    "ds_read_b128 v[158:161], v216 offset:2048\n"
    "ds_read_b128 v[162:165], v217 offset:2048\n"
    "s_waitcnt lgkmcnt(4)\n"
    "v_mfma_f32_32x32x16_bf16 v[182:197], v[206:209], v[210:213], v[182:197]\n"
    "ds_read_b128 v[198:201], v214 offset:3072\n"
    "ds_read_b128 v[202:205], v215 offset:3072\n"
    "v_mul_f32 v124, s80, v32\n"
    "v_exp_f32 v124, v124\n"
    "v_mul_f32 v125, s80, v33\n"
    "v_add_f32 v124, 1.0, v124\n"
    "s_waitcnt lgkmcnt(4)\n"
    "v_mfma_f32_32x32x16_bf16 v[166:181], v[150:153], v[154:157], v[166:181]\n"
    "ds_read_b128 v[206:209], v216 offset:3072\n"
    "ds_read_b128 v[210:213], v217 offset:3072\n"
    "v_exp_f32 v125, v125\n"
    "v_mul_f32 v126, s80, v34\n"
    "v_fma_f32 v124, v124, v124, 1.0\n"
    "s_waitcnt lgkmcnt(4)\n"
    "v_mfma_f32_32x32x16_bf16 v[182:197], v[158:161], v[162:165], v[182:197]\n"
    "ds_read_b128 v[150:153], v214 offset:4096\n"
    "ds_read_b128 v[154:157], v215 offset:4096\n"
    "v_add_f32 v125, 1.0, v125\n"
    "v_exp_f32 v126, v126\n"
    "v_mul_f32 v127, s80, v35\n"
    "s_waitcnt lgkmcnt(4)\n"
    "v_mfma_f32_32x32x16_bf16 v[166:181], v[198:201], v[202:205], v[166:181]\n"
    "ds_read_b128 v[158:161], v216 offset:4096\n"
    "ds_read_b128 v[162:165], v217 offset:4096\n"
    "v_rcp_f32 v124, v124\n"
    "v_fma_f32 v125, v125, v125, 1.0\n"
    "v_add_f32 v126, 1.0, v126\n"
    "v_exp_f32 v127, v127\n"
    "s_waitcnt lgkmcnt(4)\n"
    "v_mfma_f32_32x32x16_bf16 v[182:197], v[206:209], v[210:213], v[182:197]\n"
    "ds_read_b128 v[198:201], v214 offset:5120\n"
    "ds_read_b128 v[202:205], v215 offset:5120\n"
    "v_mul_f32 v128, s80, v36\n"
    "v_fma_f32 v124, v124, v247, v245\n"
    "v_rcp_f32 v125, v125\n"
    "s_waitcnt lgkmcnt(4)\n"
    "v_mfma_f32_32x32x16_bf16 v[166:181], v[150:153], v[154:157], v[166:181]\n"
    "ds_read_b128 v[206:209], v216 offset:5120\n"
    "ds_read_b128 v[210:213], v217 offset:5120\n"
    "s_add_u32 m0, s64, 69632\n"
    "s_mov_b32 s65, 256\n"
    "buffer_load_dwordx4 v239, s[44:47], s65 offen offset:0 lds\n"
    "s_waitcnt lgkmcnt(4)\n"
    "v_mfma_f32_32x32x16_bf16 v[182:197], v[158:161], v[162:165], v[182:197]\n"
    "ds_read_b128 v[150:153], v214 offset:6144\n"
    "ds_read_b128 v[154:157], v215 offset:6144\n"
    "v_fma_f32 v126, v126, v126, 1.0\n"
    "v_add_f32 v127, 1.0, v127\n"
    "v_exp_f32 v128, v128\n"
    "v_mul_f32 v129, s80, v37\n"
    "s_waitcnt lgkmcnt(4)\n"
    "v_mfma_f32_32x32x16_bf16 v[166:181], v[198:201], v[202:205], v[166:181]\n"
    "ds_read_b128 v[158:161], v216 offset:6144\n"
    "ds_read_b128 v[162:165], v217 offset:6144\n"
    "v_mul_f32 v32, v124, v32\n"
    "v_fma_f32 v125, v125, v247, v245\n"
    "v_rcp_f32 v126, v126\n"
    "s_waitcnt lgkmcnt(4)\n"
    "v_mfma_f32_32x32x16_bf16 v[182:197], v[206:209], v[210:213], v[182:197]\n"
    "ds_read_b128 v[198:201], v214 offset:7168\n"
    "ds_read_b128 v[202:205], v215 offset:7168\n"
    "v_fma_f32 v127, v127, v127, 1.0\n"
    "v_add_f32 v128, 1.0, v128\n"
    "v_exp_f32 v129, v129\n"
    "v_mul_f32 v130, s80, v38\n"
    "s_waitcnt lgkmcnt(4)\n"
    "v_mfma_f32_32x32x16_bf16 v[166:181], v[150:153], v[154:157], v[166:181]\n"
    "ds_read_b128 v[206:209], v216 offset:7168\n"
    "ds_read_b128 v[210:213], v217 offset:7168\n"
    "v_mul_f32 v33, v125, v33\n"
    "v_fma_f32 v126, v126, v247, v245\n"
    "v_rcp_f32 v127, v127\n"
    "s_waitcnt lgkmcnt(4)\n"
    "v_mfma_f32_32x32x16_bf16 v[182:197], v[158:161], v[162:165], v[182:197]\n"
    "v_fma_f32 v128, v128, v128, 1.0\n"
    "v_add_f32 v129, 1.0, v129\n"
    "v_exp_f32 v130, v130\n"
    "v_mul_f32 v124, s80, v39\n"
    "v_cvt_pk_bf16_f32 v134, v32, v33\n"
    "v_mul_f32 v34, v126, v34\n"
    "s_waitcnt lgkmcnt(2)\n"
    "v_mfma_f32_32x32x16_bf16 v[166:181], v[198:201], v[202:205], v[166:181]\n"
    "ds_read_b64_tr_b16 v[112:113], v238 offset:12288\n"
    "ds_read_b64_tr_b16 v[114:115], v238 offset:12544\n"
    "v_fma_f32 v127, v127, v247, v245\n"
    "v_rcp_f32 v128, v128\n"
    "v_fma_f32 v129, v129, v129, 1.0\n"
    "v_add_f32 v130, 1.0, v130\n"
    "s_waitcnt lgkmcnt(2)\n"
    "v_mfma_f32_32x32x16_bf16 v[182:197], v[206:209], v[210:213], v[182:197]\n"
    "ds_read_b64_tr_b16 v[116:117], v238 offset:14336\n"
    "ds_read_b64_tr_b16 v[118:119], v238 offset:14592\n"
    "s_add_u32 m0, s64, 73728\n"
    "s_mov_b32 s65, 295168\n"
    "buffer_load_dwordx4 v239, s[44:47], s65 offen offset:0 lds\n"
    "s_waitcnt lgkmcnt(2)\n"
    "v_mfma_f32_32x32x16_bf16 v[64:79], v[112:115], a[0:3], v[96:111]\n"
    "ds_read_b64_tr_b16 v[120:121], v236 offset:0\n"
    "v_exp_f32 v124, v124\n"
    "v_mul_f32 v125, s80, v40\n"
    "v_mul_f32 v35, v127, v35\n"
    "v_fma_f32 v128, v128, v247, v245\n"
    "v_rcp_f32 v129, v129\n"
    "v_fma_f32 v130, v130, v130, 1.0\n"
    "v_mfma_f32_32x32x16_bf16 v[80:95], v[112:115], a[4:7], v[96:111]\n"
    "ds_read_b64_tr_b16 v[122:123], v236 offset:256\n"
    "s_nop 5\n"
    "ds_read_b128 v[96:99], v244 offset:512\n"
    "v_add_f32 v124, 1.0, v124\n"
    "v_exp_f32 v125, v125\n"
    "v_mul_f32 v126, s80, v41\n"
    "s_waitcnt lgkmcnt(3)\n"
    "v_mfma_f32_32x32x16_bf16 v[64:79], v[116:119], a[8:11], v[64:79]\n"
    "ds_read_b64_tr_b16 v[112:113], v236 offset:2048\n"
    "v_cvt_pk_bf16_f32 v135, v34, v35\n"
    "v_mul_f32 v36, v128, v36\n"
    "v_fma_f32 v129, v129, v247, v245\n"
    "v_rcp_f32 v130, v130\n"
    "v_fma_f32 v124, v124, v124, 1.0\n"
    "v_add_f32 v125, 1.0, v125\n"
    "v_mfma_f32_32x32x16_bf16 v[80:95], v[116:119], a[12:15], v[80:95]\n"
    "ds_read_b64_tr_b16 v[114:115], v236 offset:2304\n"
    "s_add_u32 s62, s88, 0\n"
    "buffer_store_dwordx4 v[166:169], v241, s[56:59], s62 offen offset:0 sc1\n"
    "s_waitcnt lgkmcnt(3)\n"
    "v_mfma_f32_32x32x16_bf16 v[64:79], v[120:123], a[16:19], v[64:79]\n"
    "ds_read_b64_tr_b16 v[116:117], v236 offset:4096\n"
    "buffer_store_dwordx4 v[170:173], v241, s[56:59], s62 offen offset:1024 sc1\n"
    "v_exp_f32 v126, v126\n"
    "v_mul_f32 v127, s80, v42\n"
    "v_mul_f32 v37, v129, v37\n"
    "v_mfma_f32_32x32x16_bf16 v[80:95], v[120:123], a[20:23], v[80:95]\n"
    "ds_read_b64_tr_b16 v[118:119], v236 offset:4352\n"
    "v_fma_f32 v130, v130, v247, v245\n"
    "v_rcp_f32 v124, v124\n"
    "v_fma_f32 v125, v125, v125, 1.0\n"
    "v_add_f32 v126, 1.0, v126\n"
    "s_waitcnt lgkmcnt(2)\n"
    "v_mfma_f32_32x32x16_bf16 v[64:79], v[112:115], a[24:27], v[64:79]\n"
    "ds_read_b64_tr_b16 v[120:121], v236 offset:6144\n"
    "s_add_u32 m0, s64, 77824\n"
    "s_mov_b32 s65, 590080\n"
    "buffer_load_dwordx4 v239, s[44:47], s65 offen offset:0 lds\n"
    "buffer_store_dwordx4 v[174:177], v241, s[56:59], s62 offen offset:2048 sc1\n"
    "v_exp_f32 v127, v127\n"
    "v_mfma_f32_32x32x16_bf16 v[80:95], v[112:115], a[28:31], v[80:95]\n"
    "ds_read_b64_tr_b16 v[122:123], v236 offset:6400\n"
    "v_mul_f32 v128, s80, v43\n"
    "v_cvt_pk_bf16_f32 v136, v36, v37\n"
    "v_mul_f32 v38, v130, v38\n"
    "v_fma_f32 v124, v124, v247, v245\n"
    "v_rcp_f32 v125, v125\n"
    "s_waitcnt lgkmcnt(2)\n"
    "v_mfma_f32_32x32x16_bf16 v[64:79], v[116:119], a[32:35], v[64:79]\n"
    "ds_read_b64_tr_b16 v[112:113], v236 offset:8192\n"
    "buffer_store_dwordx4 v[178:181], v241, s[56:59], s62 offen offset:3072 sc1\n"
    "v_fma_f32 v126, v126, v126, 1.0\n"
    "v_add_f32 v127, 1.0, v127\n"
    "v_mfma_f32_32x32x16_bf16 v[80:95], v[116:119], a[36:39], v[80:95]\n"
    "ds_read_b64_tr_b16 v[114:115], v236 offset:8448\n"
    "s_add_u32 s62, s88, 4096\n"
    "buffer_store_dwordx4 v[182:185], v241, s[56:59], s62 offen offset:0 sc1\n"
    "s_waitcnt lgkmcnt(2)\n"
    "v_mfma_f32_32x32x16_bf16 v[64:79], v[120:123], a[40:43], v[64:79]\n"
    "ds_read_b64_tr_b16 v[116:117], v236 offset:10240\n"
    "buffer_store_dwordx4 v[186:189], v241, s[56:59], s62 offen offset:1024 sc1\n"
    "v_exp_f32 v128, v128\n"
    "v_mul_f32 v129, s80, v44\n"
    "v_mul_f32 v39, v124, v39\n"
    "v_mfma_f32_32x32x16_bf16 v[80:95], v[120:123], a[44:47], v[80:95]\n"
    "ds_read_b64_tr_b16 v[118:119], v236 offset:10496\n"
    "buffer_store_dwordx4 v[190:193], v241, s[56:59], s62 offen offset:2048 sc1\n"
    "v_fma_f32 v125, v125, v247, v245\n"
    "s_waitcnt lgkmcnt(2)\n"
    "v_mfma_f32_32x32x16_bf16 v[64:79], v[112:115], a[48:51], v[64:79]\n"
    "ds_read_b64_tr_b16 v[120:121], v236 offset:12288\n"
    "buffer_store_dwordx4 v[194:197], v241, s[56:59], s62 offen offset:3072 sc1\n"
    "v_rcp_f32 v126, v126\n"
    "v_fma_f32 v127, v127, v127, 1.0\n"
    "v_add_f32 v128, 1.0, v128\n"
    "v_mfma_f32_32x32x16_bf16 v[80:95], v[112:115], a[52:55], v[80:95]\n"
    "ds_read_b64_tr_b16 v[122:123], v236 offset:12544\n"
    "s_add_u32 m0, s64, 81920\n"
    "s_mov_b32 s65, 884992\n"
    "buffer_load_dwordx4 v239, s[44:47], s65 offen offset:0 lds\n"
    "s_waitcnt lgkmcnt(2)\n"
    "v_mfma_f32_32x32x16_bf16 v[64:79], v[116:119], a[56:59], v[64:79]\n"
    "ds_read_b64_tr_b16 v[112:113], v236 offset:14336\n"
    "v_exp_f32 v129, v129\n"
    "v_mul_f32 v130, s80, v45\n"
    "v_cvt_pk_bf16_f32 v137, v38, v39\n"
    "v_mul_f32 v40, v125, v40\n"
    "v_fma_f32 v126, v126, v247, v245\n"
    "v_rcp_f32 v127, v127\n"
    "v_mfma_f32_32x32x16_bf16 v[80:95], v[116:119], a[60:63], v[80:95]\n"
    "ds_read_b64_tr_b16 v[114:115], v236 offset:14592\n"
    "ds_read_b128 v[100:103], v244 offset:544\n"
    "v_fma_f32 v128, v128, v128, 1.0\n"
    "v_add_f32 v129, 1.0, v129\n"
    "v_exp_f32 v130, v130\n"
    "s_waitcnt lgkmcnt(3)\n"
    "v_mfma_f32_32x32x16_bf16 v[64:79], v[120:123], a[64:67], v[64:79]\n"
    "ds_read_b64_tr_b16 v[116:117], v236 offset:16384\n"
    "v_mul_f32 v124, s80, v46\n"
    "v_mul_f32 v41, v126, v41\n"
    "v_fma_f32 v127, v127, v247, v245\n"
    "v_rcp_f32 v128, v128\n"
    "v_fma_f32 v129, v129, v129, 1.0\n"
    "v_add_f32 v130, 1.0, v130\n"
    "v_mfma_f32_32x32x16_bf16 v[80:95], v[120:123], a[68:71], v[80:95]\n"
    "ds_read_b64_tr_b16 v[118:119], v236 offset:16640\n"
    "v_exp_f32 v124, v124\n"
    "v_mul_f32 v125, s80, v47\n"
    "v_cvt_pk_bf16_f32 v138, v40, v41\n"
    "v_mul_f32 v42, v127, v42\n"
    "v_fma_f32 v128, v128, v247, v245\n"
    "s_waitcnt lgkmcnt(3)\n"
    "v_mfma_f32_32x32x16_bf16 v[64:79], v[112:115], a[72:75], v[64:79]\n"
    "ds_read_b64_tr_b16 v[120:121], v236 offset:18432\n"
    "v_rcp_f32 v129, v129\n"
    "v_fma_f32 v130, v130, v130, 1.0\n"
    "v_add_f32 v124, 1.0, v124\n"
    "v_exp_f32 v125, v125\n"
    "v_mul_f32 v43, v128, v43\n"
    "v_fma_f32 v129, v129, v247, v245\n"
    "v_mfma_f32_32x32x16_bf16 v[80:95], v[112:115], a[76:79], v[80:95]\n"
    "ds_read_b64_tr_b16 v[122:123], v236 offset:18688\n"
    "v_rcp_f32 v130, v130\n"
    "v_fma_f32 v124, v124, v124, 1.0\n"
    "v_add_f32 v125, 1.0, v125\n"
    "v_cvt_pk_bf16_f32 v139, v42, v43\n"
    "s_waitcnt lgkmcnt(2)\n"
    "v_mfma_f32_32x32x16_bf16 v[64:79], v[116:119], a[80:83], v[64:79]\n"
    "s_waitcnt vmcnt(18)\n"
    "s_barrier\n"
    "ds_read_b64_tr_b16 v[112:113], v236 offset:20480\n"
    "v_mul_f32 v44, v129, v44\n"
    "v_fma_f32 v130, v130, v247, v245\n"
    "v_rcp_f32 v124, v124\n"
    "v_mfma_f32_32x32x16_bf16 v[80:95], v[116:119], a[84:87], v[80:95]\n"
    "ds_read_b64_tr_b16 v[114:115], v236 offset:20736\n"
    "s_add_u32 m0, s64, 86016\n"
    "s_mov_b32 s65, 1179904\n"
    "buffer_load_dwordx4 v239, s[44:47], s65 offen offset:0 lds\n"
    "s_waitcnt lgkmcnt(2)\n"
    "v_mfma_f32_32x32x16_bf16 v[64:79], v[120:123], a[88:91], v[64:79]\n"
    "ds_read_b64_tr_b16 v[116:117], v236 offset:22528\n"
    "v_fma_f32 v125, v125, v125, 1.0\n"
    "v_mul_f32 v45, v130, v45\n"
    "v_fma_f32 v124, v124, v247, v245\n"
    "v_rcp_f32 v125, v125\n"
    "v_cvt_pk_bf16_f32 v140, v44, v45\n"
    "v_mul_f32 v46, v124, v46\n"
    "v_mfma_f32_32x32x16_bf16 v[80:95], v[120:123], a[92:95], v[80:95]\n"
    "ds_read_b64_tr_b16 v[118:119], v236 offset:22784\n"
    "v_fma_f32 v125, v125, v247, v245\n"
    "v_mul_f32 v47, v125, v47\n"
    "v_cvt_pk_bf16_f32 v141, v46, v47\n"
    "v_permlane32_swap_b32 v134, v138\n"
    "v_permlane32_swap_b32 v135, v139\n"
    "v_permlane32_swap_b32 v136, v140\n"
    "s_waitcnt lgkmcnt(2)\n"
    "v_mfma_f32_32x32x16_bf16 v[64:79], v[112:115], a[96:99], v[64:79]\n"
    "ds_read_b64_tr_b16 v[120:121], v236 offset:24576\n"
    "s_add_u32 m0, s64, 90112\n"
    "s_mov_b32 s65, 1474816\n"
    "buffer_load_dwordx4 v239, s[44:47], s65 offen offset:0 lds\n"
    "v_permlane32_swap_b32 v137, v141\n"
    "v_mfma_f32_32x32x16_bf16 v[80:95], v[112:115], a[100:103], v[80:95]\n"
    "ds_read_b64_tr_b16 v[122:123], v236 offset:24832\n"
    "v_permlane16_swap_b32 v134, v138\n"
    "v_permlane16_swap_b32 v135, v139\n"
    "v_permlane16_swap_b32 v136, v140\n"
    "v_permlane16_swap_b32 v137, v141\n"
    "s_waitcnt lgkmcnt(2)\n"
    "v_mfma_f32_32x32x16_bf16 v[64:79], v[116:119], a[104:107], v[64:79]\n"
    "ds_read_b64_tr_b16 v[112:113], v236 offset:26624\n"
    "s_add_u32 s60, s72, 0\n"
    "buffer_store_dwordx4 v[134:137], v241, s[56:59], s60 offen offset:0 sc1\n"
    "buffer_store_dwordx4 v[138:141], v241, s[56:59], s60 offen offset:1024 sc1\n"
    "v_mul_f32 v124, s80, v48\n"
    "v_mfma_f32_32x32x16_bf16 v[80:95], v[116:119], a[108:111], v[80:95]\n"
    "ds_read_b64_tr_b16 v[114:115], v236 offset:26880\n"
    "v_exp_f32 v124, v124\n"
    "v_mul_f32 v125, s80, v49\n"
    "v_add_f32 v124, 1.0, v124\n"
    "v_exp_f32 v125, v125\n"
    "v_mul_f32 v126, s80, v50\n"
    "s_waitcnt lgkmcnt(2)\n"
    "v_mfma_f32_32x32x16_bf16 v[64:79], v[120:123], a[112:115], v[64:79]\n"
    "ds_read_b64_tr_b16 v[116:117], v236 offset:28672\n"
    "s_add_u32 m0, s64, 94208\n"
    "s_mov_b32 s65, 1769728\n"
    "buffer_load_dwordx4 v239, s[44:47], s65 offen offset:0 lds\n"
    "v_mfma_f32_32x32x16_bf16 v[80:95], v[120:123], a[116:119], v[80:95]\n"
    "ds_read_b64_tr_b16 v[118:119], v236 offset:28928\n"
    "v_fma_f32 v124, v124, v124, 1.0\n"
    "v_add_f32 v125, 1.0, v125\n"
    "v_exp_f32 v126, v126\n"
    "v_mul_f32 v127, s80, v51\n"
    "s_waitcnt lgkmcnt(2)\n"
    "v_mfma_f32_32x32x16_bf16 v[64:79], v[112:115], a[120:123], v[64:79]\n"
    "ds_read_b64_tr_b16 v[120:121], v236 offset:30720\n"
    "v_rcp_f32 v124, v124\n"
    "v_fma_f32 v125, v125, v125, 1.0\n"
    "v_add_f32 v126, 1.0, v126\n"
    "v_exp_f32 v127, v127\n"
    "v_mul_f32 v128, s80, v52\n"
    "v_fma_f32 v124, v124, v248, v246\n"
    "v_mfma_f32_32x32x16_bf16 v[80:95], v[112:115], a[124:127], v[80:95]\n"
    "ds_read_b64_tr_b16 v[122:123], v236 offset:30976\n"
    "ds_read_b128 v[104:107], v244 offset:576\n"
    "v_rcp_f32 v125, v125\n"
    "v_fma_f32 v126, v126, v126, 1.0\n"
    "v_add_f32 v127, 1.0, v127\n"
    "s_waitcnt lgkmcnt(3)\n"
    "v_mfma_f32_32x32x16_bf16 v[64:79], v[116:119], a[128:131], v[64:79]\n"
    "ds_read_b64_tr_b16 v[112:113], v236 offset:32768\n"
    "s_add_u32 m0, s64, 98304\n"
    "s_mov_b32 s65, 2064640\n"
    "buffer_load_dwordx4 v239, s[44:47], s65 offen offset:0 lds\n"
    "v_exp_f32 v128, v128\n"
    "v_mfma_f32_32x32x16_bf16 v[80:95], v[116:119], a[132:135], v[80:95]\n"
    "ds_read_b64_tr_b16 v[114:115], v236 offset:33024\n"
    "v_mul_f32 v129, s80, v53\n"
    "v_mul_f32 v48, v124, v48\n"
    "v_fma_f32 v125, v125, v248, v246\n"
    "v_rcp_f32 v126, v126\n"
    "s_waitcnt lgkmcnt(3)\n"
    "v_mfma_f32_32x32x16_bf16 v[64:79], v[120:123], a[136:139], v[64:79]\n"
    "ds_read_b64_tr_b16 v[116:117], v236 offset:34816\n"
    "v_fma_f32 v127, v127, v127, 1.0\n"
    "v_add_f32 v128, 1.0, v128\n"
    "v_exp_f32 v129, v129\n"
    "v_mul_f32 v130, s80, v54\n"
    "v_mul_f32 v49, v125, v49\n"
    "v_fma_f32 v126, v126, v248, v246\n"
    "v_mfma_f32_32x32x16_bf16 v[80:95], v[120:123], a[140:143], v[80:95]\n"
    "ds_read_b64_tr_b16 v[118:119], v236 offset:35072\n"
    "v_rcp_f32 v127, v127\n"
    "v_fma_f32 v128, v128, v128, 1.0\n"
    "v_add_f32 v129, 1.0, v129\n"
    "v_exp_f32 v130, v130\n"
    "s_waitcnt lgkmcnt(2)\n"
    "v_mfma_f32_32x32x16_bf16 v[64:79], v[112:115], a[144:147], v[64:79]\n"
    "ds_read_b64_tr_b16 v[120:121], v236 offset:36864\n"
    "s_add_u32 m0, s64, 102400\n"
    "s_mov_b32 s65, 2359552\n"
    "buffer_load_dwordx4 v239, s[44:47], s65 offen offset:0 lds\n"
    "v_mul_f32 v124, s80, v55\n"
    "v_mfma_f32_32x32x16_bf16 v[80:95], v[112:115], a[148:151], v[80:95]\n"
    "ds_read_b64_tr_b16 v[122:123], v236 offset:37120\n"
    "v_cvt_pk_bf16_f32 v142, v48, v49\n"
    "v_mul_f32 v50, v126, v50\n"
    "v_fma_f32 v127, v127, v248, v246\n"
    "v_rcp_f32 v128, v128\n"
    "s_waitcnt lgkmcnt(2)\n"
    "v_mfma_f32_32x32x16_bf16 v[64:79], v[116:119], a[152:155], v[64:79]\n"
    "ds_read_b64_tr_b16 v[112:113], v236 offset:38912\n"
    "v_fma_f32 v129, v129, v129, 1.0\n"
    "v_add_f32 v130, 1.0, v130\n"
    "v_exp_f32 v124, v124\n"
    "v_mul_f32 v125, s80, v56\n"
    "v_mul_f32 v51, v127, v51\n"
    "v_fma_f32 v128, v128, v248, v246\n"
    "v_mfma_f32_32x32x16_bf16 v[80:95], v[116:119], a[156:159], v[80:95]\n"
    "ds_read_b64_tr_b16 v[114:115], v236 offset:39168\n"
    "v_rcp_f32 v129, v129\n"
    "v_fma_f32 v130, v130, v130, 1.0\n"
    "v_add_f32 v124, 1.0, v124\n"
    "v_exp_f32 v125, v125\n"
    "s_waitcnt lgkmcnt(2)\n"
    "v_mfma_f32_32x32x16_bf16 v[64:79], v[120:123], a[160:163], v[64:79]\n"
    "ds_read_b64_tr_b16 v[116:117], v236 offset:40960\n"
    "s_add_u32 m0, s64, 106496\n"
    "s_mov_b32 s65, 2654464\n"
    "buffer_load_dwordx4 v239, s[44:47], s65 offen offset:0 lds\n"
    "v_mul_f32 v126, s80, v57\n"
    "v_mfma_f32_32x32x16_bf16 v[80:95], v[120:123], a[164:167], v[80:95]\n"
    "ds_read_b64_tr_b16 v[118:119], v236 offset:41216\n"
    "v_cvt_pk_bf16_f32 v143, v50, v51\n"
    "v_mul_f32 v52, v128, v52\n"
    "v_fma_f32 v129, v129, v248, v246\n"
    "v_rcp_f32 v130, v130\n"
    "v_fma_f32 v124, v124, v124, 1.0\n"
    "s_waitcnt lgkmcnt(2)\n"
    "v_mfma_f32_32x32x16_bf16 v[64:79], v[112:115], a[168:171], v[64:79]\n"
    "ds_read_b64_tr_b16 v[120:121], v236 offset:43008\n"
    "v_add_f32 v125, 1.0, v125\n"
    "v_exp_f32 v126, v126\n"
    "v_mul_f32 v127, s80, v58\n"
    "v_mul_f32 v53, v129, v53\n"
    "v_fma_f32 v130, v130, v248, v246\n"
    "v_mfma_f32_32x32x16_bf16 v[80:95], v[112:115], a[172:175], v[80:95]\n"
    "ds_read_b64_tr_b16 v[122:123], v236 offset:43264\n"
    "v_rcp_f32 v124, v124\n"
    "v_fma_f32 v125, v125, v125, 1.0\n"
    "v_add_f32 v126, 1.0, v126\n"
    "v_exp_f32 v127, v127\n"
    "v_mul_f32 v128, s80, v59\n"
    "s_waitcnt lgkmcnt(2)\n"
    "v_mfma_f32_32x32x16_bf16 v[64:79], v[116:119], a[176:179], v[64:79]\n"
    "s_waitcnt vmcnt(20)\n"
    "s_barrier\n"
    "ds_read_b64_tr_b16 v[112:113], v236 offset:45056\n"
    "v_cvt_pk_bf16_f32 v144, v52, v53\n"
    "v_mul_f32 v54, v130, v54\n"
    "v_fma_f32 v124, v124, v248, v246\n"
    "v_mfma_f32_32x32x16_bf16 v[80:95], v[116:119], a[180:183], v[80:95]\n"
    "ds_read_b64_tr_b16 v[114:115], v236 offset:45312\n"
    "s_add_u32 m0, s64, 110592\n"
    "s_mov_b32 s65, 2949376\n"
    "buffer_load_dwordx4 v239, s[44:47], s65 offen offset:0 lds\n"
    "s_waitcnt lgkmcnt(2)\n"
    "v_mfma_f32_32x32x16_bf16 v[64:79], v[120:123], a[184:187], v[64:79]\n"
    "ds_read_b64_tr_b16 v[116:117], v236 offset:47104\n"
    "s_add_u32 m0, s64, 114688\n"
    "s_mov_b32 s65, 3244288\n"
    "buffer_load_dwordx4 v239, s[44:47], s65 offen offset:0 lds\n"
    "v_rcp_f32 v125, v125\n"
    "v_mfma_f32_32x32x16_bf16 v[80:95], v[120:123], a[188:191], v[80:95]\n"
    "ds_read_b64_tr_b16 v[118:119], v236 offset:47360\n"
    "v_fma_f32 v126, v126, v126, 1.0\n"
    "v_add_f32 v127, 1.0, v127\n"
    "v_exp_f32 v128, v128\n"
    "v_mul_f32 v129, s80, v60\n"
    "s_waitcnt lgkmcnt(2)\n"
    "v_mfma_f32_32x32x16_bf16 v[64:79], v[112:115], a[192:195], v[64:79]\n"
    "ds_read_b64_tr_b16 v[120:121], v236 offset:49152\n"
    "s_add_u32 m0, s64, 118784\n"
    "s_mov_b32 s65, 3539200\n"
    "buffer_load_dwordx4 v239, s[44:47], s65 offen offset:0 lds\n"
    "v_mfma_f32_32x32x16_bf16 v[80:95], v[112:115], a[196:199], v[80:95]\n"
    "ds_read_b64_tr_b16 v[122:123], v236 offset:49408\n"
    "v_mul_f32 v55, v124, v55\n"
    "v_fma_f32 v125, v125, v248, v246\n"
    "v_rcp_f32 v126, v126\n"
    "v_fma_f32 v127, v127, v127, 1.0\n"
    "v_add_f32 v128, 1.0, v128\n"
    "s_waitcnt lgkmcnt(2)\n"
    "v_mfma_f32_32x32x16_bf16 v[64:79], v[116:119], a[200:203], v[64:79]\n"
    "ds_read_b64_tr_b16 v[112:113], v236 offset:51200\n"
    "s_add_u32 m0, s64, 122880\n"
    "s_mov_b32 s65, 3834112\n"
    "buffer_load_dwordx4 v239, s[44:47], s65 offen offset:0 lds\n"
    "v_mfma_f32_32x32x16_bf16 v[80:95], v[116:119], a[204:207], v[80:95]\n"
    "ds_read_b64_tr_b16 v[114:115], v236 offset:51456\n"
    "ds_read_b128 v[108:111], v244 offset:608\n"
    "v_exp_f32 v129, v129\n"
    "v_mul_f32 v130, s80, v61\n"
    "v_cvt_pk_bf16_f32 v145, v54, v55\n"
    "v_mul_f32 v56, v125, v56\n"
    "s_waitcnt lgkmcnt(3)\n"
    "v_mfma_f32_32x32x16_bf16 v[64:79], v[120:123], a[208:211], v[64:79]\n"
    "ds_read_b64_tr_b16 v[116:117], v236 offset:53248\n"
    "s_add_u32 m0, s64, 126976\n"
    "s_mov_b32 s65, 4129024\n"
    "buffer_load_dwordx4 v239, s[44:47], s65 offen offset:0 lds\n"
    "s_waitcnt vmcnt(14)\n"
    "s_add_u32 s99, s70, 1\n"
    "v_mov_b32 v133, s21\n"
    "v_mov_b32 v132, s99\n"
    "buffer_store_dwordx2 v[132:133], v242, s[56:59], s90 offen offset:0 sc1\n"
    "v_fma_f32 v126, v126, v248, v246\n"
    "v_mfma_f32_32x32x16_bf16 v[80:95], v[120:123], a[212:215], v[80:95]\n"
    "ds_read_b64_tr_b16 v[118:119], v236 offset:53504\n"
    "v_rcp_f32 v127, v127\n"
    "v_fma_f32 v128, v128, v128, 1.0\n"
    "v_add_f32 v129, 1.0, v129\n"
    "v_exp_f32 v130, v130\n"
    "s_waitcnt lgkmcnt(3)\n"
    "v_mfma_f32_32x32x16_bf16 v[64:79], v[112:115], a[216:219], v[64:79]\n"
    "ds_read_b64_tr_b16 v[120:121], v236 offset:55296\n"
    "s_add_u32 m0, s64, 131072\n"
    "s_mov_b32 s65, 4423936\n"
    "buffer_load_dwordx4 v239, s[44:47], s65 offen offset:0 lds\n"
    "v_mfma_f32_32x32x16_bf16 v[80:95], v[112:115], a[220:223], v[80:95]\n"
    "ds_read_b64_tr_b16 v[122:123], v236 offset:55552\n"
    "v_mul_f32 v124, s80, v62\n"
    "v_mul_f32 v57, v126, v57\n"
    "v_fma_f32 v127, v127, v248, v246\n"
    "v_rcp_f32 v128, v128\n"
    "v_fma_f32 v129, v129, v129, 1.0\n"
    "s_waitcnt lgkmcnt(2)\n"
    "v_mfma_f32_32x32x16_bf16 v[64:79], v[116:119], a[224:227], v[64:79]\n"
    "ds_read_b64_tr_b16 v[112:113], v236 offset:57344\n"
    "s_add_u32 m0, s64, 135168\n"
    "s_mov_b32 s65, 4718848\n"
    "buffer_load_dwordx4 v239, s[44:47], s65 offen offset:0 lds\n"
    "v_mfma_f32_32x32x16_bf16 v[80:95], v[116:119], a[228:231], v[80:95]\n"
    "ds_read_b64_tr_b16 v[114:115], v236 offset:57600\n"
    "v_add_f32 v130, 1.0, v130\n"
    "v_exp_f32 v124, v124\n"
    "v_mul_f32 v125, s80, v63\n"
    "v_cvt_pk_bf16_f32 v146, v56, v57\n"
    "v_mul_f32 v58, v127, v58\n"
    "s_waitcnt lgkmcnt(2)\n"
    "v_mfma_f32_32x32x16_bf16 v[64:79], v[120:123], a[232:235], v[64:79]\n"
    "ds_read_b64_tr_b16 v[116:117], v236 offset:59392\n"
    "s_add_u32 m0, s64, 139264\n"
    "s_mov_b32 s65, 5013760\n"
    "buffer_load_dwordx4 v239, s[44:47], s65 offen offset:0 lds\n"
    "v_fma_f32 v128, v128, v248, v246\n"
    "v_mfma_f32_32x32x16_bf16 v[80:95], v[120:123], a[236:239], v[80:95]\n"
    "ds_read_b64_tr_b16 v[118:119], v236 offset:59648\n"
    "v_rcp_f32 v129, v129\n"
    "v_fma_f32 v130, v130, v130, 1.0\n"
    "v_add_f32 v124, 1.0, v124\n"
    "v_exp_f32 v125, v125\n"
    "s_waitcnt lgkmcnt(2)\n"
    "v_mfma_f32_32x32x16_bf16 v[64:79], v[112:115], a[240:243], v[64:79]\n"
    "ds_read_b64_tr_b16 v[120:121], v236 offset:61440\n"
    "s_add_u32 m0, s64, 143360\n"
    "s_mov_b32 s65, 384\n"
    "buffer_load_dwordx4 v239, s[44:47], s65 offen offset:0 lds\n"
    "v_mfma_f32_32x32x16_bf16 v[80:95], v[112:115], a[244:247], v[80:95]\n"
    "ds_read_b64_tr_b16 v[122:123], v236 offset:61696\n"
    "v_mul_f32 v59, v128, v59\n"
    "v_fma_f32 v129, v129, v248, v246\n"
    "v_rcp_f32 v130, v130\n"
    "v_fma_f32 v124, v124, v124, 1.0\n"
    "v_add_f32 v125, 1.0, v125\n"
    "s_waitcnt lgkmcnt(2)\n"
    "v_mfma_f32_32x32x16_bf16 v[64:79], v[116:119], a[248:251], v[64:79]\n"
    "ds_read_b64_tr_b16 v[112:113], v236 offset:63488\n"
    "s_add_u32 m0, s64, 0\n"
    "s_mov_b32 s65, 295296\n"
    "buffer_load_dwordx4 v239, s[44:47], s65 offen offset:0 lds\n"
    "v_cvt_pk_bf16_f32 v147, v58, v59\n"
    "v_mfma_f32_32x32x16_bf16 v[80:95], v[116:119], a[252:255], v[80:95]\n"
    "ds_read_b64_tr_b16 v[114:115], v236 offset:63744\n"
    "v_mul_f32 v60, v129, v60\n"
    "v_fma_f32 v130, v130, v248, v246\n"
    "v_rcp_f32 v124, v124\n"
    "v_fma_f32 v125, v125, v125, 1.0\n"
    "s_waitcnt lgkmcnt(2)\n"
    "v_mfma_f32_32x32x16_bf16 v[64:79], v[120:123], v[0:3], v[64:79]\n"
    "ds_read_b64_tr_b16 v[116:117], v237 offset:0\n"
    "s_add_u32 m0, s64, 4096\n"
    "s_mov_b32 s65, 590208\n"
    "buffer_load_dwordx4 v239, s[44:47], s65 offen offset:0 lds\n"
    "v_mul_f32 v61, v130, v61\n"
    "v_mfma_f32_32x32x16_bf16 v[80:95], v[120:123], v[4:7], v[80:95]\n"
    "ds_read_b64_tr_b16 v[118:119], v237 offset:256\n"
    "v_fma_f32 v124, v124, v248, v246\n"
    "v_rcp_f32 v125, v125\n"
    "v_cvt_pk_bf16_f32 v148, v60, v61\n"
    "v_mul_f32 v62, v124, v62\n"
    "v_fma_f32 v125, v125, v248, v246\n"
    "s_waitcnt lgkmcnt(2)\n"
    "v_mfma_f32_32x32x16_bf16 v[64:79], v[112:115], v[8:11], v[64:79]\n"
    "ds_read_b64_tr_b16 v[120:121], v237 offset:2048\n"
    "s_add_u32 m0, s64, 8192\n"
    "s_mov_b32 s65, 885120\n"
    "buffer_load_dwordx4 v239, s[44:47], s65 offen offset:0 lds\n"
    "v_mfma_f32_32x32x16_bf16 v[80:95], v[112:115], v[12:15], v[80:95]\n"
    "ds_read_b64_tr_b16 v[122:123], v237 offset:2304\n"
    "v_mul_f32 v63, v125, v63\n"
    "v_cvt_pk_bf16_f32 v149, v62, v63\n"
    "v_permlane32_swap_b32 v142, v146\n"
    "v_permlane32_swap_b32 v143, v147\n"
    "v_permlane32_swap_b32 v144, v148\n"
    "v_permlane32_swap_b32 v145, v149\n"
    "s_waitcnt lgkmcnt(2)\n"
    "v_mfma_f32_32x32x16_bf16 v[64:79], v[116:119], v[16:19], v[64:79]\n"
    "s_waitcnt vmcnt(19)\n"
    "s_barrier\n"
    "ds_read_b64_tr_b16 v[112:113], v237 offset:4096\n"
    "v_permlane16_swap_b32 v142, v146\n"
    "v_permlane16_swap_b32 v143, v147\n"
    "v_permlane16_swap_b32 v144, v148\n"
    "v_mfma_f32_32x32x16_bf16 v[80:95], v[116:119], v[20:23], v[80:95]\n"
    "ds_read_b64_tr_b16 v[114:115], v237 offset:4352\n"
    "s_add_u32 m0, s64, 12288\n"
    "s_mov_b32 s65, 1180032\n"
    "buffer_load_dwordx4 v239, s[44:47], s65 offen offset:0 lds\n"
    "v_permlane16_swap_b32 v145, v149\n"
    "s_waitcnt lgkmcnt(2)\n"
    "v_mfma_f32_32x32x16_bf16 v[64:79], v[120:123], v[24:27], v[64:79]\n"
    "ds_read_b64_tr_b16 v[116:117], v237 offset:6144\n"
    "s_add_u32 s60, s72, 0\n"
    "buffer_store_dwordx4 v[142:145], v241, s[56:59], s60 offen offset:2048 sc1\n"
    "buffer_store_dwordx4 v[146:149], v241, s[56:59], s60 offen offset:3072 sc1\n"
    "v_mfma_f32_32x32x16_bf16 v[80:95], v[120:123], v[28:31], v[80:95]\n"
    "ds_read_b64_tr_b16 v[118:119], v237 offset:6400\n"
    "s_add_u32 m0, s64, 16384\n"
    "s_mov_b32 s65, 1474944\n"
    "buffer_load_dwordx4 v239, s[44:47], s65 offen offset:0 lds\n"
    "s_waitcnt lgkmcnt(2)\n"
    "v_mfma_f32_32x32x16_bf16 v[32:47], v[112:115], a[0:3], v[96:111]\n"
    "ds_read_b64_tr_b16 v[120:121], v237 offset:8192\n"
    "v_mfma_f32_32x32x16_bf16 v[48:63], v[112:115], a[4:7], v[96:111]\n"
    "ds_read_b64_tr_b16 v[122:123], v237 offset:8448\n"
    "s_waitcnt lgkmcnt(2)\n"
    "v_mfma_f32_32x32x16_bf16 v[32:47], v[116:119], a[8:11], v[32:47]\n"
    "ds_read_b64_tr_b16 v[112:113], v237 offset:10240\n"
    "s_add_u32 m0, s64, 20480\n"
    "s_mov_b32 s65, 1769856\n"
    "buffer_load_dwordx4 v239, s[44:47], s65 offen offset:0 lds\n"
    "v_mfma_f32_32x32x16_bf16 v[48:63], v[116:119], a[12:15], v[48:63]\n"
    "ds_read_b64_tr_b16 v[114:115], v237 offset:10496\n"
    "v_mul_f32 v124, s80, v64\n"
    "v_exp_f32 v124, v124\n"
    "v_mul_f32 v125, s80, v65\n"
    "v_add_f32 v124, 1.0, v124\n"
    "v_exp_f32 v125, v125\n"
    "s_waitcnt lgkmcnt(2)\n"
    "v_mfma_f32_32x32x16_bf16 v[32:47], v[120:123], a[16:19], v[32:47]\n"
    "ds_read_b64_tr_b16 v[116:117], v237 offset:12288\n"
    "v_mul_f32 v126, s80, v66\n"
    "v_fma_f32 v124, v124, v124, 1.0\n"
    "v_add_f32 v125, 1.0, v125\n"
    "v_exp_f32 v126, v126\n"
    "v_mul_f32 v127, s80, v67\n"
    "v_rcp_f32 v124, v124\n"
    "v_mfma_f32_32x32x16_bf16 v[48:63], v[120:123], a[20:23], v[48:63]\n"
    "ds_read_b64_tr_b16 v[118:119], v237 offset:12544\n"
    "s_add_u32 m0, s64, 24576\n"
    "s_mov_b32 s65, 2064768\n"
    "buffer_load_dwordx4 v239, s[44:47], s65 offen offset:0 lds\n"
    "s_waitcnt lgkmcnt(2)\n"
    "v_mfma_f32_32x32x16_bf16 v[32:47], v[112:115], a[24:27], v[32:47]\n"
    "ds_read_b64_tr_b16 v[120:121], v237 offset:14336\n"
    "v_fma_f32 v125, v125, v125, 1.0\n"
    "v_add_f32 v126, 1.0, v126\n"
    "v_exp_f32 v127, v127\n"
    "v_mul_f32 v128, s80, v68\n"
    "v_fma_f32 v124, v124, v247, v245\n"
    "v_rcp_f32 v125, v125\n"
    "v_mfma_f32_32x32x16_bf16 v[48:63], v[112:115], a[28:31], v[48:63]\n"
    "ds_read_b64_tr_b16 v[122:123], v237 offset:14592\n"
    "v_fma_f32 v126, v126, v126, 1.0\n"
    "v_add_f32 v127, 1.0, v127\n"
    "v_exp_f32 v128, v128\n"
    "v_mul_f32 v129, s80, v69\n"
    "v_mul_f32 v64, v124, v64\n"
    "s_waitcnt lgkmcnt(2)\n"
    "v_mfma_f32_32x32x16_bf16 v[32:47], v[116:119], a[32:35], v[32:47]\n"
    "ds_read_b64_tr_b16 v[112:113], v237 offset:16384\n"
    "s_add_u32 m0, s64, 28672\n"
    "s_mov_b32 s65, 2359680\n"
    "buffer_load_dwordx4 v239, s[44:47], s65 offen offset:0 lds\n"
    "v_mfma_f32_32x32x16_bf16 v[48:63], v[116:119], a[36:39], v[48:63]\n"
    "ds_read_b64_tr_b16 v[114:115], v237 offset:16640\n"
    "v_fma_f32 v125, v125, v247, v245\n"
    "v_rcp_f32 v126, v126\n"
    "v_fma_f32 v127, v127, v127, 1.0\n"
    "v_add_f32 v128, 1.0, v128\n"
    "v_exp_f32 v129, v129\n"
    "s_waitcnt lgkmcnt(2)\n"
    "v_mfma_f32_32x32x16_bf16 v[32:47], v[120:123], a[40:43], v[32:47]\n"
    "ds_read_b64_tr_b16 v[116:117], v237 offset:18432\n"
    "v_mul_f32 v130, s80, v70\n"
    "v_mul_f32 v65, v125, v65\n"
    "v_fma_f32 v126, v126, v247, v245\n"
    "v_rcp_f32 v127, v127\n"
    "v_fma_f32 v128, v128, v128, 1.0\n"
    "v_add_f32 v129, 1.0, v129\n"
    "v_mfma_f32_32x32x16_bf16 v[48:63], v[120:123], a[44:47], v[48:63]\n"
    "ds_read_b64_tr_b16 v[118:119], v237 offset:18688\n"
    "s_add_u32 m0, s64, 32768\n"
    "s_mov_b32 s65, 2654592\n"
    "buffer_load_dwordx4 v239, s[44:47], s65 offen offset:0 lds\n"
    "v_exp_f32 v130, v130\n"
    "s_waitcnt lgkmcnt(2)\n"
    "v_mfma_f32_32x32x16_bf16 v[32:47], v[112:115], a[48:51], v[32:47]\n"
    "ds_read_b64_tr_b16 v[120:121], v237 offset:20480\n"
    "v_mul_f32 v124, s80, v71\n"
    "v_cvt_pk_bf16_f32 v150, v64, v65\n"
    "v_mul_f32 v66, v126, v66\n"
    "v_fma_f32 v127, v127, v247, v245\n"
    "v_rcp_f32 v128, v128\n"
    "v_fma_f32 v129, v129, v129, 1.0\n"
    "v_mfma_f32_32x32x16_bf16 v[48:63], v[112:115], a[52:55], v[48:63]\n"
    "ds_read_b64_tr_b16 v[122:123], v237 offset:20736\n"
    "v_add_f32 v130, 1.0, v130\n"
    "v_exp_f32 v124, v124\n"
    "v_mul_f32 v125, s80, v72\n"
    "v_mul_f32 v67, v127, v67\n"
    "v_fma_f32 v128, v128, v247, v245\n"
    "s_waitcnt lgkmcnt(2)\n"
    "v_mfma_f32_32x32x16_bf16 v[32:47], v[116:119], a[56:59], v[32:47]\n"
    "ds_read_b64_tr_b16 v[112:113], v237 offset:22528\n"
    "s_add_u32 m0, s64, 36864\n"
    "s_mov_b32 s65, 2949504\n"
    "buffer_load_dwordx4 v239, s[44:47], s65 offen offset:0 lds\n"
    "v_mfma_f32_32x32x16_bf16 v[48:63], v[116:119], a[60:63], v[48:63]\n"
    "ds_read_b64_tr_b16 v[114:115], v237 offset:22784\n"
    "ds_read_b128 v[96:99], v244 offset:768\n"
    "v_rcp_f32 v129, v129\n"
    "v_fma_f32 v130, v130, v130, 1.0\n"
    "v_add_f32 v124, 1.0, v124\n"
    "v_exp_f32 v125, v125\n"
    "s_waitcnt lgkmcnt(3)\n"
    "v_mfma_f32_32x32x16_bf16 v[32:47], v[120:123], a[64:67], v[32:47]\n"
    "ds_read_b64_tr_b16 v[116:117], v237 offset:24576\n"
    "v_mul_f32 v126, s80, v73\n"
    "v_cvt_pk_bf16_f32 v151, v66, v67\n"
    "v_mul_f32 v68, v128, v68\n"
    "v_fma_f32 v129, v129, v247, v245\n"
    "v_rcp_f32 v130, v130\n"
    "v_fma_f32 v124, v124, v124, 1.0\n"
    "v_add_f32 v125, 1.0, v125\n"
    "v_mfma_f32_32x32x16_bf16 v[48:63], v[120:123], a[68:71], v[48:63]\n"
    "ds_read_b64_tr_b16 v[118:119], v237 offset:24832\n"
    "s_add_u32 m0, s64, 40960\n"
    "s_mov_b32 s65, 3244416\n"
    "buffer_load_dwordx4 v239, s[44:47], s65 offen offset:0 lds\n"
    "s_waitcnt lgkmcnt(3)\n"
    "v_mfma_f32_32x32x16_bf16 v[32:47], v[112:115], a[72:75], v[32:47]\n"
    "ds_read_b64_tr_b16 v[120:121], v237 offset:26624\n"
    "v_exp_f32 v126, v126\n"
    "v_mul_f32 v127, s80, v74\n"
    "v_mul_f32 v69, v129, v69\n"
    "v_fma_f32 v130, v130, v247, v245\n"
    "v_rcp_f32 v124, v124\n"
    "v_fma_f32 v125, v125, v125, 1.0\n"
    "v_mfma_f32_32x32x16_bf16 v[48:63], v[112:115], a[76:79], v[48:63]\n"
    "ds_read_b64_tr_b16 v[122:123], v237 offset:26880\n"
    "v_add_f32 v126, 1.0, v126\n"
    "v_exp_f32 v127, v127\n"
    "v_mul_f32 v128, s80, v75\n"
    "v_cvt_pk_bf16_f32 v152, v68, v69\n"
    "v_mul_f32 v70, v130, v70\n"
    "s_waitcnt lgkmcnt(2)\n"
    "v_mfma_f32_32x32x16_bf16 v[32:47], v[116:119], a[80:83], v[32:47]\n"
    "s_waitcnt vmcnt(21)\n"
    "s_barrier\n"
    "ds_read_b64_tr_b16 v[112:113], v237 offset:28672\n"
    "v_fma_f32 v124, v124, v247, v245\n"
    "v_rcp_f32 v125, v125\n"
    "v_fma_f32 v126, v126, v126, 1.0\n"
    "v_mfma_f32_32x32x16_bf16 v[48:63], v[116:119], a[84:87], v[48:63]\n"
    "ds_read_b64_tr_b16 v[114:115], v237 offset:28928\n"
    "s_add_u32 m0, s64, 45056\n"
    "s_mov_b32 s65, 3539328\n"
    "buffer_load_dwordx4 v239, s[44:47], s65 offen offset:0 lds\n"
    "s_waitcnt lgkmcnt(2)\n"
    "v_mfma_f32_32x32x16_bf16 v[32:47], v[120:123], a[88:91], v[32:47]\n"
    "ds_read_b64_tr_b16 v[116:117], v237 offset:30720\n"
    "v_add_f32 v127, 1.0, v127\n"
    "v_exp_f32 v128, v128\n"
    "v_mul_f32 v129, s80, v76\n"
    "v_mul_f32 v71, v124, v71\n"
    "v_fma_f32 v125, v125, v247, v245\n"
    "v_rcp_f32 v126, v126\n"
    "v_mfma_f32_32x32x16_bf16 v[48:63], v[120:123], a[92:95], v[48:63]\n"
    "ds_read_b64_tr_b16 v[118:119], v237 offset:30976\n"
    "v_fma_f32 v127, v127, v127, 1.0\n"
    "v_add_f32 v128, 1.0, v128\n"
    "v_exp_f32 v129, v129\n"
    "v_mul_f32 v130, s80, v77\n"
    "v_cvt_pk_bf16_f32 v153, v70, v71\n"
    "v_mul_f32 v72, v125, v72\n"
    "s_waitcnt lgkmcnt(2)\n"
    "v_mfma_f32_32x32x16_bf16 v[32:47], v[112:115], a[96:99], v[32:47]\n"
    "ds_read_b64_tr_b16 v[120:121], v237 offset:32768\n"
    "s_add_u32 m0, s64, 49152\n"
    "s_mov_b32 s65, 3834240\n"
    "buffer_load_dwordx4 v239, s[44:47], s65 offen offset:0 lds\n"
    "v_mfma_f32_32x32x16_bf16 v[48:63], v[112:115], a[100:103], v[48:63]\n"
    "ds_read_b64_tr_b16 v[122:123], v237 offset:33024\n"
    "v_fma_f32 v126, v126, v247, v245\n"
    "v_rcp_f32 v127, v127\n"
    "v_fma_f32 v128, v128, v128, 1.0\n"
    "v_add_f32 v129, 1.0, v129\n"
    "v_exp_f32 v130, v130\n"
    "s_waitcnt lgkmcnt(2)\n"
    "v_mfma_f32_32x32x16_bf16 v[32:47], v[116:119], a[104:107], v[32:47]\n"
    "ds_read_b64_tr_b16 v[112:113], v237 offset:34816\n"
    "v_mul_f32 v124, s80, v78\n"
    "v_mul_f32 v73, v126, v73\n"
    "v_fma_f32 v127, v127, v247, v245\n"
    "v_rcp_f32 v128, v128\n"
    "v_fma_f32 v129, v129, v129, 1.0\n"
    "v_add_f32 v130, 1.0, v130\n"
    "v_mfma_f32_32x32x16_bf16 v[48:63], v[116:119], a[108:111], v[48:63]\n"
    "ds_read_b64_tr_b16 v[114:115], v237 offset:35072\n"
    "v_exp_f32 v124, v124\n"
    "v_mul_f32 v125, s80, v79\n"
    "v_cvt_pk_bf16_f32 v154, v72, v73\n"
    "v_mul_f32 v74, v127, v74\n"
    "v_fma_f32 v128, v128, v247, v245\n"
    "s_waitcnt lgkmcnt(2)\n"
    "v_mfma_f32_32x32x16_bf16 v[32:47], v[120:123], a[112:115], v[32:47]\n"
    "ds_read_b64_tr_b16 v[116:117], v237 offset:36864\n"
    "s_add_u32 m0, s64, 53248\n"
    "s_mov_b32 s65, 4129152\n"
    "buffer_load_dwordx4 v239, s[44:47], s65 offen offset:0 lds\n"
    "v_rcp_f32 v129, v129\n"
    "v_mfma_f32_32x32x16_bf16 v[48:63], v[120:123], a[116:119], v[48:63]\n"
    "ds_read_b64_tr_b16 v[118:119], v237 offset:37120\n"
    "v_fma_f32 v130, v130, v130, 1.0\n"
    "v_add_f32 v124, 1.0, v124\n"
    "v_exp_f32 v125, v125\n"
    "v_mul_f32 v75, v128, v75\n"
    "v_fma_f32 v129, v129, v247, v245\n"
    "s_waitcnt lgkmcnt(2)\n"
    "v_mfma_f32_32x32x16_bf16 v[32:47], v[112:115], a[120:123], v[32:47]\n"
    "ds_read_b64_tr_b16 v[120:121], v237 offset:38912\n"
    "ds_read_b128 v[100:103], v244 offset:800\n"
    "v_rcp_f32 v130, v130\n"
    "v_fma_f32 v124, v124, v124, 1.0\n"
    "v_add_f32 v125, 1.0, v125\n"
    "v_cvt_pk_bf16_f32 v155, v74, v75\n"
    "v_mul_f32 v76, v129, v76\n"
    "v_mfma_f32_32x32x16_bf16 v[48:63], v[112:115], a[124:127], v[48:63]\n"
    "ds_read_b64_tr_b16 v[122:123], v237 offset:39168\n"
    "v_fma_f32 v130, v130, v247, v245\n"
    "v_rcp_f32 v124, v124\n"
    "v_fma_f32 v125, v125, v125, 1.0\n"
    "v_mul_f32 v77, v130, v77\n"
    "v_fma_f32 v124, v124, v247, v245\n"
    "s_waitcnt lgkmcnt(3)\n"
    "v_mfma_f32_32x32x16_bf16 v[32:47], v[116:119], a[128:131], v[32:47]\n"
    "ds_read_b64_tr_b16 v[112:113], v237 offset:40960\n"
    "s_add_u32 m0, s64, 57344\n"
    "s_mov_b32 s65, 4424064\n"
    "buffer_load_dwordx4 v239, s[44:47], s65 offen offset:0 lds\n"
    "v_mfma_f32_32x32x16_bf16 v[48:63], v[116:119], a[132:135], v[48:63]\n"
    "ds_read_b64_tr_b16 v[114:115], v237 offset:41216\n"
    "v_rcp_f32 v125, v125\n"
    "v_cvt_pk_bf16_f32 v156, v76, v77\n"
    "v_mul_f32 v78, v124, v78\n"
    "v_fma_f32 v125, v125, v247, v245\n"
    "v_mul_f32 v79, v125, v79\n"
    "v_cvt_pk_bf16_f32 v157, v78, v79\n"
    "s_waitcnt lgkmcnt(2)\n"
    "v_mfma_f32_32x32x16_bf16 v[32:47], v[120:123], a[136:139], v[32:47]\n"
    "ds_read_b64_tr_b16 v[116:117], v237 offset:43008\n"
    "v_permlane32_swap_b32 v150, v154\n"
    "v_permlane32_swap_b32 v151, v155\n"
    "v_permlane32_swap_b32 v152, v156\n"
    "v_permlane32_swap_b32 v153, v157\n"
    "v_permlane16_swap_b32 v150, v154\n"
    "v_permlane16_swap_b32 v151, v155\n"
    "v_permlane16_swap_b32 v152, v156\n"
    "v_permlane16_swap_b32 v153, v157\n"
    "v_mfma_f32_32x32x16_bf16 v[48:63], v[120:123], a[140:143], v[48:63]\n"
    "ds_read_b64_tr_b16 v[118:119], v237 offset:43264\n"
    "s_add_u32 s60, s72, 16384\n"
    "buffer_store_dwordx4 v[150:153], v241, s[56:59], s60 offen offset:0 sc1\n"
    "buffer_store_dwordx4 v[154:157], v241, s[56:59], s60 offen offset:1024 sc1\n"
    "s_waitcnt lgkmcnt(2)\n"
    "v_mfma_f32_32x32x16_bf16 v[32:47], v[112:115], a[144:147], v[32:47]\n"
    "ds_read_b64_tr_b16 v[120:121], v237 offset:45056\n"
    "s_add_u32 m0, s64, 61440\n"
    "s_mov_b32 s65, 4718976\n"
    "buffer_load_dwordx4 v239, s[44:47], s65 offen offset:0 lds\n"
    "v_mfma_f32_32x32x16_bf16 v[48:63], v[112:115], a[148:151], v[48:63]\n"
    "ds_read_b64_tr_b16 v[122:123], v237 offset:45312\n"
    "v_mul_f32 v124, s80, v80\n"
    "v_exp_f32 v124, v124\n"
    "v_mul_f32 v125, s80, v81\n"
    "v_add_f32 v124, 1.0, v124\n"
    "v_exp_f32 v125, v125\n"
    "s_waitcnt lgkmcnt(2)\n"
    "v_mfma_f32_32x32x16_bf16 v[32:47], v[116:119], a[152:155], v[32:47]\n"
    "ds_read_b64_tr_b16 v[112:113], v237 offset:47104\n"
    "s_waitcnt vmcnt(14)\n"
    "s_add_u32 s99, s70, 2\n"
    "v_mov_b32 v133, s21\n"
    "v_mov_b32 v132, s99\n"
    "buffer_store_dwordx2 v[132:133], v242, s[56:59], s89 offen offset:0 sc1\n"
    "v_mfma_f32_32x32x16_bf16 v[48:63], v[116:119], a[156:159], v[48:63]\n"
    "ds_read_b64_tr_b16 v[114:115], v237 offset:47360\n"
    "v_mul_f32 v126, s80, v82\n"
    "v_fma_f32 v124, v124, v124, 1.0\n"
    "v_add_f32 v125, 1.0, v125\n"
    "v_exp_f32 v126, v126\n"
    "v_mul_f32 v127, s80, v83\n"
    "s_waitcnt lgkmcnt(2)\n"
    "v_mfma_f32_32x32x16_bf16 v[32:47], v[120:123], a[160:163], v[32:47]\n"
    "ds_read_b64_tr_b16 v[116:117], v237 offset:49152\n"
    "s_add_u32 m0, s64, 65536\n"
    "s_mov_b32 s65, 5013888\n"
    "buffer_load_dwordx4 v239, s[44:47], s65 offen offset:0 lds\n"
    "v_mfma_f32_32x32x16_bf16 v[48:63], v[120:123], a[164:167], v[48:63]\n"
    "ds_read_b64_tr_b16 v[118:119], v237 offset:49408\n"
    "v_rcp_f32 v124, v124\n"
    "v_fma_f32 v125, v125, v125, 1.0\n"
    "v_add_f32 v126, 1.0, v126\n"
    "v_exp_f32 v127, v127\n"
    "v_mul_f32 v128, s80, v84\n"
    "s_waitcnt lgkmcnt(2)\n"
    "v_mfma_f32_32x32x16_bf16 v[32:47], v[112:115], a[168:171], v[32:47]\n"
    "ds_read_b64_tr_b16 v[120:121], v237 offset:51200\n"
    "v_fma_f32 v124, v124, v248, v246\n"
    "v_rcp_f32 v125, v125\n"
    "v_fma_f32 v126, v126, v126, 1.0\n"
    "v_add_f32 v127, 1.0, v127\n"
    "v_exp_f32 v128, v128\n"
    "v_mul_f32 v129, s80, v85\n"
    "v_mfma_f32_32x32x16_bf16 v[48:63], v[112:115], a[172:175], v[48:63]\n"
    "ds_read_b64_tr_b16 v[122:123], v237 offset:51456\n"
    "ds_read_b128 v[104:107], v244 offset:832\n"
    "v_mul_f32 v80, v124, v80\n"
    "v_fma_f32 v125, v125, v248, v246\n"
    "v_rcp_f32 v126, v126\n"
    "v_fma_f32 v127, v127, v127, 1.0\n"
    "s_waitcnt lgkmcnt(3)\n"
    "v_mfma_f32_32x32x16_bf16 v[32:47], v[116:119], a[176:179], v[32:47]\n"
    "s_barrier\n"
    "ds_read_b64_tr_b16 v[112:113], v237 offset:53248\n"
    "v_add_f32 v128, 1.0, v128\n"
    "v_exp_f32 v129, v129\n"
    "v_mfma_f32_32x32x16_bf16 v[48:63], v[116:119], a[180:183], v[48:63]\n"
    "ds_read_b64_tr_b16 v[114:115], v237 offset:53504\n"
    "s_add_u32 m0, s64, 69632\n"
    "s_add_u32 s65, s81, 0\n"
    "buffer_load_dwordx4 v240, s[48:51], s65 offen offset:0 lds\n"
    "v_mul_f32 v130, s80, v86\n"
    "s_waitcnt lgkmcnt(3)\n"
    "v_mfma_f32_32x32x16_bf16 v[32:47], v[120:123], a[184:187], v[32:47]\n"
    "ds_read_b64_tr_b16 v[116:117], v237 offset:55296\n"
    "v_mul_f32 v81, v125, v81\n"
    "v_fma_f32 v126, v126, v248, v246\n"
    "v_rcp_f32 v127, v127\n"
    "v_fma_f32 v128, v128, v128, 1.0\n"
    "v_add_f32 v129, 1.0, v129\n"
    "v_exp_f32 v130, v130\n"
    "v_mfma_f32_32x32x16_bf16 v[48:63], v[120:123], a[188:191], v[48:63]\n"
    "ds_read_b64_tr_b16 v[118:119], v237 offset:55552\n"
    "v_mul_f32 v124, s80, v87\n"
    "v_cvt_pk_bf16_f32 v158, v80, v81\n"
    "v_mul_f32 v82, v126, v82\n"
    "v_fma_f32 v127, v127, v248, v246\n"
    "v_rcp_f32 v128, v128\n"
    "s_waitcnt lgkmcnt(2)\n"
    "v_mfma_f32_32x32x16_bf16 v[32:47], v[112:115], a[192:195], v[32:47]\n"
    "ds_read_b64_tr_b16 v[120:121], v237 offset:57344\n"
    "s_add_u32 m0, s64, 73728\n"
    "s_add_u32 s65, s81, 36864\n"
    "buffer_load_dwordx4 v240, s[48:51], s65 offen offset:0 lds\n"
    "v_fma_f32 v129, v129, v129, 1.0\n"
    "v_mfma_f32_32x32x16_bf16 v[48:63], v[112:115], a[196:199], v[48:63]\n"
    "ds_read_b64_tr_b16 v[122:123], v237 offset:57600\n"
    "v_add_f32 v130, 1.0, v130\n"
    "v_exp_f32 v124, v124\n"
    "v_mul_f32 v125, s80, v88\n"
    "v_mul_f32 v83, v127, v83\n"
    "v_fma_f32 v128, v128, v248, v246\n"
    "s_waitcnt lgkmcnt(2)\n"
    "v_mfma_f32_32x32x16_bf16 v[32:47], v[116:119], a[200:203], v[32:47]\n"
    "ds_read_b64_tr_b16 v[112:113], v237 offset:59392\n"
    "v_rcp_f32 v129, v129\n"
    "v_fma_f32 v130, v130, v130, 1.0\n"
    "v_add_f32 v124, 1.0, v124\n"
    "v_exp_f32 v125, v125\n"
    "v_mul_f32 v126, s80, v89\n"
    "v_cvt_pk_bf16_f32 v159, v82, v83\n"
    "v_mfma_f32_32x32x16_bf16 v[48:63], v[116:119], a[204:207], v[48:63]\n"
    "ds_read_b64_tr_b16 v[114:115], v237 offset:59648\n"
    "v_mul_f32 v84, v128, v84\n"
    "v_fma_f32 v129, v129, v248, v246\n"
    "v_rcp_f32 v130, v130\n"
    "v_fma_f32 v124, v124, v124, 1.0\n"
    "v_add_f32 v125, 1.0, v125\n"
    "s_waitcnt lgkmcnt(2)\n"
    "v_mfma_f32_32x32x16_bf16 v[32:47], v[120:123], a[208:211], v[32:47]\n"
    "ds_read_b64_tr_b16 v[116:117], v237 offset:61440\n"
    "s_add_u32 m0, s64, 77824\n"
    "s_add_u32 s65, s76, 0\n"
    "buffer_load_dwordx4 v240, s[48:51], s65 offen offset:0 lds\n"
    "v_mfma_f32_32x32x16_bf16 v[48:63], v[120:123], a[212:215], v[48:63]\n"
    "ds_read_b64_tr_b16 v[118:119], v237 offset:61696\n"
    "v_exp_f32 v126, v126\n"
    "v_mul_f32 v127, s80, v90\n"
    "v_mul_f32 v85, v129, v85\n"
    "v_fma_f32 v130, v130, v248, v246\n"
    "v_rcp_f32 v124, v124\n"
    "s_waitcnt lgkmcnt(2)\n"
    "v_mfma_f32_32x32x16_bf16 v[32:47], v[112:115], a[216:219], v[32:47]\n"
    "ds_read_b64_tr_b16 v[120:121], v237 offset:63488\n"
    "v_fma_f32 v125, v125, v125, 1.0\n"
    "v_add_f32 v126, 1.0, v126\n"
    "v_exp_f32 v127, v127\n"
    "v_mul_f32 v128, s80, v91\n"
    "v_cvt_pk_bf16_f32 v160, v84, v85\n"
    "v_mul_f32 v86, v130, v86\n"
    "v_fma_f32 v124, v124, v248, v246\n"
    "v_mfma_f32_32x32x16_bf16 v[48:63], v[112:115], a[220:223], v[48:63]\n"
    "ds_read_b64_tr_b16 v[122:123], v237 offset:63744\n"
    "v_rcp_f32 v125, v125\n"
    "v_fma_f32 v126, v126, v126, 1.0\n"
    "v_add_f32 v127, 1.0, v127\n"
    "v_exp_f32 v128, v128\n"
    "s_waitcnt lgkmcnt(2)\n"
    "v_mfma_f32_32x32x16_bf16 v[32:47], v[116:119], a[224:227], v[32:47]\n"
    "ds_read_b64_tr_b16 v[112:113], v238 offset:0\n"
    "s_add_u32 m0, s64, 81920\n"
    "s_add_u32 s65, s76, 36864\n"
    "buffer_load_dwordx4 v240, s[48:51], s65 offen offset:0 lds\n"
    "v_mul_f32 v129, s80, v92\n"
    "v_mfma_f32_32x32x16_bf16 v[48:63], v[116:119], a[228:231], v[48:63]\n"
    "ds_read_b64_tr_b16 v[114:115], v238 offset:256\n"
    "ds_read_b128 v[108:111], v244 offset:864\n"
    "v_mul_f32 v87, v124, v87\n"
    "v_fma_f32 v125, v125, v248, v246\n"
    "v_rcp_f32 v126, v126\n"
    "v_fma_f32 v127, v127, v127, 1.0\n"
    "s_waitcnt lgkmcnt(3)\n"
    "v_mfma_f32_32x32x16_bf16 v[32:47], v[120:123], a[232:235], v[32:47]\n"
    "ds_read_b64_tr_b16 v[116:117], v238 offset:2048\n"
    "v_add_f32 v128, 1.0, v128\n"
    "v_exp_f32 v129, v129\n"
    "v_mul_f32 v130, s80, v93\n"
    "v_cvt_pk_bf16_f32 v161, v86, v87\n"
    "v_mul_f32 v88, v125, v88\n"
    "v_fma_f32 v126, v126, v248, v246\n"
    "v_mfma_f32_32x32x16_bf16 v[48:63], v[120:123], a[236:239], v[48:63]\n"
    "ds_read_b64_tr_b16 v[118:119], v238 offset:2304\n"
    "v_rcp_f32 v127, v127\n"
    "v_fma_f32 v128, v128, v128, 1.0\n"
    "v_add_f32 v129, 1.0, v129\n"
    "v_exp_f32 v130, v130\n"
    "v_mul_f32 v124, s80, v94\n"
    "s_waitcnt lgkmcnt(3)\n"
    "v_mfma_f32_32x32x16_bf16 v[32:47], v[112:115], a[240:243], v[32:47]\n"
    "ds_read_b64_tr_b16 v[120:121], v238 offset:4096\n"
    "s_add_u32 m0, s64, 86016\n"
    "s_add_u32 s65, s24, 0\n"
    "buffer_load_dwordx4 v240, s[48:51], s65 offen offset:0 lds\n"
    "v_mul_f32 v89, v126, v89\n"
    "v_mfma_f32_32x32x16_bf16 v[48:63], v[112:115], a[244:247], v[48:63]\n"
    "ds_read_b64_tr_b16 v[122:123], v238 offset:4352\n"
    "v_fma_f32 v127, v127, v248, v246\n"
    "v_rcp_f32 v128, v128\n"
    "v_fma_f32 v129, v129, v129, 1.0\n"
    "v_add_f32 v130, 1.0, v130\n"
    "s_waitcnt lgkmcnt(2)\n"
    "v_mfma_f32_32x32x16_bf16 v[32:47], v[116:119], a[248:251], v[32:47]\n"
    "ds_read_b64_tr_b16 v[112:113], v238 offset:6144\n"
    "v_exp_f32 v124, v124\n"
    "v_mul_f32 v125, s80, v95\n"
    "v_cvt_pk_bf16_f32 v162, v88, v89\n"
    "v_mul_f32 v90, v127, v90\n"
    "v_fma_f32 v128, v128, v248, v246\n"
    "v_rcp_f32 v129, v129\n"
    "v_fma_f32 v130, v130, v130, 1.0\n"
    "v_mfma_f32_32x32x16_bf16 v[48:63], v[116:119], a[252:255], v[48:63]\n"
    "ds_read_b64_tr_b16 v[114:115], v238 offset:6400\n"
    "v_add_f32 v124, 1.0, v124\n"
    "v_exp_f32 v125, v125\n"
    "v_mul_f32 v91, v128, v91\n"
    "v_fma_f32 v129, v129, v248, v246\n"
    "v_rcp_f32 v130, v130\n"
    "s_waitcnt lgkmcnt(2)\n"
    "v_mfma_f32_32x32x16_bf16 v[32:47], v[120:123], v[0:3], v[32:47]\n"
    "ds_read_b64_tr_b16 v[116:117], v238 offset:8192\n"
    "s_add_u32 m0, s64, 90112\n"
    "s_add_u32 s65, s24, 36864\n"
    "buffer_load_dwordx4 v240, s[48:51], s65 offen offset:0 lds\n"
    "v_mfma_f32_32x32x16_bf16 v[48:63], v[120:123], v[4:7], v[48:63]\n"
    "ds_read_b64_tr_b16 v[118:119], v238 offset:8448\n"
    "v_fma_f32 v124, v124, v124, 1.0\n"
    "v_add_f32 v125, 1.0, v125\n"
    "v_cvt_pk_bf16_f32 v163, v90, v91\n"
    "v_mul_f32 v92, v129, v92\n"
    "v_fma_f32 v130, v130, v248, v246\n"
    "s_waitcnt lgkmcnt(2)\n"
    "v_mfma_f32_32x32x16_bf16 v[32:47], v[112:115], v[8:11], v[32:47]\n"
    "ds_read_b64_tr_b16 v[120:121], v238 offset:10240\n"
    "v_rcp_f32 v124, v124\n"
    "v_fma_f32 v125, v125, v125, 1.0\n"
    "v_mul_f32 v93, v130, v93\n"
    "v_fma_f32 v124, v124, v248, v246\n"
    "v_rcp_f32 v125, v125\n"
    "v_cvt_pk_bf16_f32 v164, v92, v93\n"
    "v_mul_f32 v94, v124, v94\n"
    "v_mfma_f32_32x32x16_bf16 v[48:63], v[112:115], v[12:15], v[48:63]\n"
    "ds_read_b64_tr_b16 v[122:123], v238 offset:10496\n"
    "v_fma_f32 v125, v125, v248, v246\n"
    "v_mul_f32 v95, v125, v95\n"
    "v_cvt_pk_bf16_f32 v165, v94, v95\n"
    "v_permlane32_swap_b32 v158, v162\n"
    "v_permlane32_swap_b32 v159, v163\n"
    "v_permlane32_swap_b32 v160, v164\n"
    "s_waitcnt lgkmcnt(2)\n"
    "v_mfma_f32_32x32x16_bf16 v[32:47], v[116:119], v[16:19], v[32:47]\n"
    "s_waitcnt vmcnt(21)\n"
    "s_barrier\n"
    "ds_read_b64_tr_b16 v[112:113], v238 offset:12288\n"
    "v_permlane32_swap_b32 v161, v165\n"
    "v_permlane16_swap_b32 v158, v162\n"
    "v_permlane16_swap_b32 v159, v163\n"
    "v_permlane16_swap_b32 v160, v164\n"
    "v_mfma_f32_32x32x16_bf16 v[48:63], v[116:119], v[20:23], v[48:63]\n"
    "ds_read_b64_tr_b16 v[114:115], v238 offset:12544\n"
    "s_add_u32 m0, s64, 94208\n"
    "s_add_u32 s65, s81, 73728\n"
    "buffer_load_dwordx4 v240, s[48:51], s65 offen offset:0 lds\n"
    "s_waitcnt lgkmcnt(2)\n"
    "v_mfma_f32_32x32x16_bf16 v[32:47], v[120:123], v[24:27], v[32:47]\n"
    "ds_read_b64_tr_b16 v[116:117], v238 offset:14336\n"
    "v_permlane16_swap_b32 v161, v165\n"
    "s_add_u32 s60, s72, 16384\n"
    "buffer_store_dwordx4 v[158:161], v241, s[56:59], s60 offen offset:2048 sc1\n"
    "buffer_store_dwordx4 v[162:165], v241, s[56:59], s60 offen offset:3072 sc1\n"
    "v_mfma_f32_32x32x16_bf16 v[48:63], v[120:123], v[28:31], v[48:63]\n"
    "ds_read_b64_tr_b16 v[118:119], v238 offset:14592\n"
    "s_waitcnt lgkmcnt(2)\n"
    "v_mfma_f32_32x32x16_bf16 v[64:79], v[112:115], a[0:3], v[96:111]\n"
    "ds_read_b64_tr_b16 v[120:121], v236 offset:0\n"
    "s_add_u32 m0, s64, 98304\n"
    "s_add_u32 s65, s81, 110592\n"
    "buffer_load_dwordx4 v240, s[48:51], s65 offen offset:0 lds\n"
    "v_mfma_f32_32x32x16_bf16 v[80:95], v[112:115], a[4:7], v[96:111]\n"
    "ds_read_b64_tr_b16 v[122:123], v236 offset:256\n"
    "s_waitcnt lgkmcnt(2)\n"
    "v_mfma_f32_32x32x16_bf16 v[64:79], v[116:119], a[8:11], v[64:79]\n"
    "ds_read_b64_tr_b16 v[112:113], v236 offset:2048\n"
    "v_lshrrev_b32 v250, 9, v241\n"
    "v_mfma_f32_32x32x16_bf16 v[80:95], v[116:119], a[12:15], v[80:95]\n"
    "ds_read_b64_tr_b16 v[114:115], v236 offset:2304\n"
    "v_lshlrev_b32 v250, 11, v250\n"
    "v_mul_f32 v124, s80, v32\n"
    "v_exp_f32 v124, v124\n"
    "v_mul_f32 v125, s80, v33\n"
    "v_add_f32 v124, 1.0, v124\n"
    "s_waitcnt lgkmcnt(2)\n"
    "v_mfma_f32_32x32x16_bf16 v[64:79], v[120:123], a[16:19], v[64:79]\n"
    "ds_read_b64_tr_b16 v[116:117], v236 offset:4096\n"
    "s_add_u32 m0, s64, 102400\n"
    "s_add_u32 s65, s76, 73728\n"
    "buffer_load_dwordx4 v240, s[48:51], s65 offen offset:0 lds\n"
    "v_mfma_f32_32x32x16_bf16 v[80:95], v[120:123], a[20:23], v[80:95]\n"
    "ds_read_b64_tr_b16 v[118:119], v236 offset:4352\n"
    "v_exp_f32 v125, v125\n"
    "v_mul_f32 v126, s80, v34\n"
    "v_fma_f32 v124, v124, v124, 1.0\n"
    "v_add_f32 v125, 1.0, v125\n"
    "v_exp_f32 v126, v126\n"
    "s_waitcnt lgkmcnt(2)\n"
    "v_mfma_f32_32x32x16_bf16 v[64:79], v[112:115], a[24:27], v[64:79]\n"
    "ds_read_b64_tr_b16 v[120:121], v236 offset:6144\n"
    "v_bfe_u32 v251, v241, 6, 3\n"
    "v_mul_f32 v127, s80, v35\n"
    "v_rcp_f32 v124, v124\n"
    "v_fma_f32 v125, v125, v125, 1.0\n"
    "v_add_f32 v126, 1.0, v126\n"
    "v_exp_f32 v127, v127\n"
    "v_mfma_f32_32x32x16_bf16 v[80:95], v[112:115], a[28:31], v[80:95]\n"
    "ds_read_b64_tr_b16 v[122:123], v236 offset:6400\n"
    "v_lshl_add_u32 v250, v251, 6, v250\n"
    "v_mul_f32 v128, s80, v36\n"
    "v_fma_f32 v124, v124, v247, v245\n"
    "v_rcp_f32 v125, v125\n"
    "s_waitcnt lgkmcnt(2)\n"
    "v_mfma_f32_32x32x16_bf16 v[64:79], v[116:119], a[32:35], v[64:79]\n"
    "ds_read_b64_tr_b16 v[112:113], v236 offset:8192\n"
    "s_add_u32 m0, s64, 106496\n"
    "s_add_u32 s65, s76, 110592\n"
    "buffer_load_dwordx4 v240, s[48:51], s65 offen offset:0 lds\n"
    "v_fma_f32 v126, v126, v126, 1.0\n"
    "v_mfma_f32_32x32x16_bf16 v[80:95], v[116:119], a[36:39], v[80:95]\n"
    "ds_read_b64_tr_b16 v[114:115], v236 offset:8448\n"
    "v_bfe_u32 v251, v241, 8, 1\n"
    "v_add_f32 v127, 1.0, v127\n"
    "v_exp_f32 v128, v128\n"
    "v_mul_f32 v129, s80, v37\n"
    "s_waitcnt lgkmcnt(2)\n"
    "v_mfma_f32_32x32x16_bf16 v[64:79], v[120:123], a[40:43], v[64:79]\n"
    "ds_read_b64_tr_b16 v[116:117], v236 offset:10240\n"
    "v_lshl_add_u32 v250, v251, 5, v250\n"
    "v_mul_f32 v32, v124, v32\n"
    "v_fma_f32 v125, v125, v247, v245\n"
    "v_rcp_f32 v126, v126\n"
    "v_fma_f32 v127, v127, v127, 1.0\n"
    "v_add_f32 v128, 1.0, v128\n"
    "v_mfma_f32_32x32x16_bf16 v[80:95], v[120:123], a[44:47], v[80:95]\n"
    "ds_read_b64_tr_b16 v[118:119], v236 offset:10496\n"
    "v_exp_f32 v129, v129\n"
    "v_mul_f32 v130, s80, v38\n"
    "v_mul_f32 v33, v125, v33\n"
    "v_fma_f32 v126, v126, v247, v245\n"
    "v_rcp_f32 v127, v127\n"
    "s_waitcnt lgkmcnt(2)\n"
    "v_mfma_f32_32x32x16_bf16 v[64:79], v[112:115], a[48:51], v[64:79]\n"
    "ds_read_b64_tr_b16 v[120:121], v236 offset:12288\n"
    "s_add_u32 m0, s64, 110592\n"
    "s_add_u32 s65, s24, 73728\n"
    "buffer_load_dwordx4 v240, s[48:51], s65 offen offset:0 lds\n"
    "v_fma_f32 v128, v128, v128, 1.0\n"
    "v_bfe_u32 v243, v241, 4, 2\n"
    "v_max_u32 v243, 1, v243\n"
    "v_xor_b32 v243, s69, v243\n"
    "v_lshl_add_u32 v243, v243, 6, s22\n"
    "buffer_load_dwordx2 v[234:235], v243, s[56:59], 0 offen offset:0 sc1\n"
    "v_mfma_f32_32x32x16_bf16 v[80:95], v[112:115], a[52:55], v[80:95]\n"
    "ds_read_b64_tr_b16 v[122:123], v236 offset:12544\n"
    "v_bfe_u32 v251, v241, 4, 2\n"
    "v_add_f32 v129, 1.0, v129\n"
    "v_exp_f32 v130, v130\n"
    "v_mul_f32 v124, s80, v39\n"
    "v_cvt_pk_bf16_f32 v166, v32, v33\n"
    "s_waitcnt lgkmcnt(2)\n"
    "v_mfma_f32_32x32x16_bf16 v[64:79], v[116:119], a[56:59], v[64:79]\n"
    "ds_read_b64_tr_b16 v[112:113], v236 offset:14336\n"
    "v_lshl_add_u32 v250, v251, 3, v250\n"
    "v_mul_f32 v34, v126, v34\n"
    "v_fma_f32 v127, v127, v247, v245\n"
    "v_rcp_f32 v128, v128\n"
    "v_fma_f32 v129, v129, v129, 1.0\n"
    "v_add_f32 v130, 1.0, v130\n"
    "v_mfma_f32_32x32x16_bf16 v[80:95], v[116:119], a[60:63], v[80:95]\n"
    "ds_read_b64_tr_b16 v[114:115], v236 offset:14592\n"
    "v_exp_f32 v124, v124\n"
    "v_mul_f32 v125, s80, v40\n"
    "v_mul_f32 v35, v127, v35\n"
    "v_fma_f32 v128, v128, v247, v245\n"
    "v_rcp_f32 v129, v129\n"
    "s_waitcnt lgkmcnt(2)\n"
    "v_mfma_f32_32x32x16_bf16 v[64:79], v[120:123], a[64:67], v[64:79]\n"
    "ds_read_b64_tr_b16 v[116:117], v236 offset:16384\n"
    "s_add_u32 m0, s64, 114688\n"
    "s_add_u32 s65, s24, 110592\n"
    "buffer_load_dwordx4 v240, s[48:51], s65 offen offset:0 lds\n"
    "v_mfma_f32_32x32x16_bf16 v[80:95], v[120:123], a[68:71], v[80:95]\n"
    "ds_read_b64_tr_b16 v[118:119], v236 offset:16640\n"
    "v_fma_f32 v130, v130, v130, 1.0\n"
    "v_add_f32 v124, 1.0, v124\n"
    "v_exp_f32 v125, v125\n"
    "v_mul_f32 v126, s80, v41\n"
    "v_cvt_pk_bf16_f32 v167, v34, v35\n"
    "s_waitcnt lgkmcnt(2)\n"
    "v_mfma_f32_32x32x16_bf16 v[64:79], v[112:115], a[72:75], v[64:79]\n"
    "ds_read_b64_tr_b16 v[120:121], v236 offset:18432\n"
    "s_and_b32 s96, s66, 1\n"
    "s_lshl_b32 s96, s96, 12\n"
    "v_add_u32 v250, s96, v250\n"
    "v_mul_f32 v36, v128, v36\n"
    "v_fma_f32 v129, v129, v247, v245\n"
    "v_rcp_f32 v130, v130\n"
    "v_mfma_f32_32x32x16_bf16 v[80:95], v[112:115], a[76:79], v[80:95]\n"
    "ds_read_b64_tr_b16 v[122:123], v236 offset:18688\n"
    "v_fma_f32 v124, v124, v124, 1.0\n"
    "v_add_f32 v125, 1.0, v125\n"
    "v_exp_f32 v126, v126\n"
    "v_mul_f32 v127, s80, v42\n"
    "v_mul_f32 v37, v129, v37\n"
    "s_waitcnt lgkmcnt(2)\n"
    "v_mfma_f32_32x32x16_bf16 v[64:79], v[116:119], a[80:83], v[64:79]\n"
    "s_waitcnt vmcnt(24)\n"
    "s_barrier\n"
    "ds_read_b64_tr_b16 v[112:113], v236 offset:20480\n"
    "v_fma_f32 v130, v130, v247, v245\n"
    "v_rcp_f32 v124, v124\n"
    "v_mfma_f32_32x32x16_bf16 v[80:95], v[116:119], a[84:87], v[80:95]\n"
    "ds_read_b64_tr_b16 v[114:115], v236 offset:20736\n"
    "s_add_u32 m0, s64, 118784\n"
    "s_add_u32 s65, s81, 147456\n"
    "buffer_load_dwordx4 v240, s[48:51], s65 offen offset:0 lds\n"
    "v_fma_f32 v125, v125, v125, 1.0\n"
    "s_waitcnt lgkmcnt(2)\n"
    "v_mfma_f32_32x32x16_bf16 v[64:79], v[120:123], a[88:91], v[64:79]\n"
    "ds_read_b64_tr_b16 v[116:117], v236 offset:22528\n"
    "v_xor_b32 v251, 32, v250\n"
    "v_add_f32 v126, 1.0, v126\n"
    "v_exp_f32 v127, v127\n"
    "v_mul_f32 v128, s80, v43\n"
    "v_cvt_pk_bf16_f32 v168, v36, v37\n"
    "v_mul_f32 v38, v130, v38\n"
    "v_mfma_f32_32x32x16_bf16 v[80:95], v[120:123], a[92:95], v[80:95]\n"
    "ds_read_b64_tr_b16 v[118:119], v236 offset:22784\n"
    "v_add_u32 v252, 65536, v250\n"
    "v_fma_f32 v124, v124, v247, v245\n"
    "v_rcp_f32 v125, v125\n"
    "v_fma_f32 v126, v126, v126, 1.0\n"
    "v_add_f32 v127, 1.0, v127\n"
    "s_waitcnt lgkmcnt(2)\n"
    "v_mfma_f32_32x32x16_bf16 v[64:79], v[112:115], a[96:99], v[64:79]\n"
    "ds_read_b64_tr_b16 v[120:121], v236 offset:24576\n"
    "s_add_u32 m0, s64, 122880\n"
    "s_add_u32 s65, s81, 184320\n"
    "buffer_load_dwordx4 v240, s[48:51], s65 offen offset:0 lds\n"
    "v_mfma_f32_32x32x16_bf16 v[80:95], v[112:115], a[100:103], v[80:95]\n"
    "ds_read_b64_tr_b16 v[122:123], v236 offset:24832\n"
    "v_add_u32 v253, 65536, v251\n"
    "v_exp_f32 v128, v128\n"
    "v_mul_f32 v129, s80, v44\n"
    "v_mul_f32 v39, v124, v39\n"
    "v_fma_f32 v125, v125, v247, v245\n"
    "s_waitcnt lgkmcnt(2)\n"
    "v_mfma_f32_32x32x16_bf16 v[64:79], v[116:119], a[104:107], v[64:79]\n"
    "ds_read_b64_tr_b16 v[112:113], v236 offset:26624\n"
    "v_add_u32 v254, 131072, v250\n"
    "v_rcp_f32 v126, v126\n"
    "v_fma_f32 v127, v127, v127, 1.0\n"
    "v_add_f32 v128, 1.0, v128\n"
    "v_exp_f32 v129, v129\n"
    "v_mul_f32 v130, s80, v45\n"
    "v_mfma_f32_32x32x16_bf16 v[80:95], v[116:119], a[108:111], v[80:95]\n"
    "ds_read_b64_tr_b16 v[114:115], v236 offset:26880\n"
    "v_cvt_pk_bf16_f32 v169, v38, v39\n"
    "v_mul_f32 v40, v125, v40\n"
    "v_fma_f32 v126, v126, v247, v245\n"
    "v_rcp_f32 v127, v127\n"
    "v_fma_f32 v128, v128, v128, 1.0\n"
    "s_waitcnt lgkmcnt(2)\n"
    "v_mfma_f32_32x32x16_bf16 v[64:79], v[120:123], a[112:115], v[64:79]\n"
    "ds_read_b64_tr_b16 v[116:117], v236 offset:28672\n"
    "s_add_u32 m0, s64, 126976\n"
    "s_add_u32 s65, s76, 147456\n"
    "buffer_load_dwordx4 v240, s[48:51], s65 offen offset:0 lds\n"
    "v_add_f32 v129, 1.0, v129\n"
    "v_mfma_f32_32x32x16_bf16 v[80:95], v[120:123], a[116:119], v[80:95]\n"
    "ds_read_b64_tr_b16 v[118:119], v236 offset:28928\n"
    "v_add_u32 v255, 131072, v251\n"
    "v_exp_f32 v130, v130\n"
    "v_mul_f32 v124, s80, v46\n"
    "v_mul_f32 v41, v126, v41\n"
    "v_fma_f32 v127, v127, v247, v245\n"
    "s_waitcnt lgkmcnt(2)\n"
    "v_mfma_f32_32x32x16_bf16 v[64:79], v[112:115], a[120:123], v[64:79]\n"
    "ds_read_b64_tr_b16 v[120:121], v236 offset:30720\n"
    "v_rcp_f32 v128, v128\n"
    "v_fma_f32 v129, v129, v129, 1.0\n"
    "v_add_f32 v130, 1.0, v130\n"
    "v_exp_f32 v124, v124\n"
    "v_mul_f32 v125, s80, v47\n"
    "v_mfma_f32_32x32x16_bf16 v[80:95], v[112:115], a[124:127], v[80:95]\n"
    "ds_read_b64_tr_b16 v[122:123], v236 offset:30976\n"
    "v_cvt_pk_bf16_f32 v170, v40, v41\n"
    "v_mul_f32 v42, v127, v42\n"
    "v_fma_f32 v128, v128, v247, v245\n"
    "v_rcp_f32 v129, v129\n"
    "v_fma_f32 v130, v130, v130, 1.0\n"
    "v_add_f32 v124, 1.0, v124\n"
    "s_waitcnt lgkmcnt(2)\n"
    "v_mfma_f32_32x32x16_bf16 v[64:79], v[116:119], a[128:131], v[64:79]\n"
    "ds_read_b64_tr_b16 v[112:113], v236 offset:32768\n"
    "s_add_u32 m0, s64, 131072\n"
    "s_add_u32 s65, s76, 184320\n"
    "buffer_load_dwordx4 v240, s[48:51], s65 offen offset:0 lds\n"
    "v_mfma_f32_32x32x16_bf16 v[80:95], v[116:119], a[132:135], v[80:95]\n"
    "ds_read_b64_tr_b16 v[114:115], v236 offset:33024\n"
    "v_exp_f32 v125, v125\n"
    "v_mul_f32 v43, v128, v43\n"
    "v_fma_f32 v129, v129, v247, v245\n"
    "v_rcp_f32 v130, v130\n"
    "v_fma_f32 v124, v124, v124, 1.0\n"
    "s_waitcnt lgkmcnt(2)\n"
    "v_mfma_f32_32x32x16_bf16 v[64:79], v[120:123], a[136:139], v[64:79]\n"
    "ds_read_b64_tr_b16 v[116:117], v236 offset:34816\n"
    "v_add_f32 v125, 1.0, v125\n"
    "v_cvt_pk_bf16_f32 v171, v42, v43\n"
    "v_mul_f32 v44, v129, v44\n"
    "v_fma_f32 v130, v130, v247, v245\n"
    "v_rcp_f32 v124, v124\n"
    "v_fma_f32 v125, v125, v125, 1.0\n"
    "v_mfma_f32_32x32x16_bf16 v[80:95], v[120:123], a[140:143], v[80:95]\n"
    "ds_read_b64_tr_b16 v[118:119], v236 offset:35072\n"
    "v_mul_f32 v45, v130, v45\n"
    "v_fma_f32 v124, v124, v247, v245\n"
    "v_rcp_f32 v125, v125\n"
    "v_cvt_pk_bf16_f32 v172, v44, v45\n"
    "v_mul_f32 v46, v124, v46\n"
    "v_fma_f32 v125, v125, v247, v245\n"
    "s_waitcnt lgkmcnt(2)\n"
    "v_mfma_f32_32x32x16_bf16 v[64:79], v[112:115], a[144:147], v[64:79]\n"
    "ds_read_b64_tr_b16 v[120:121], v236 offset:36864\n"
    "s_add_u32 m0, s64, 135168\n"
    "s_add_u32 s65, s24, 147456\n"
    "buffer_load_dwordx4 v240, s[48:51], s65 offen offset:0 lds\n"
    "v_mfma_f32_32x32x16_bf16 v[80:95], v[112:115], a[148:151], v[80:95]\n"
    "ds_read_b64_tr_b16 v[122:123], v236 offset:37120\n"
    "v_mul_f32 v47, v125, v47\n"
    "v_cvt_pk_bf16_f32 v173, v46, v47\n"
    "v_permlane32_swap_b32 v166, v170\n"
    "v_permlane32_swap_b32 v167, v171\n"
    "v_permlane32_swap_b32 v168, v172\n"
    "v_permlane32_swap_b32 v169, v173\n"
    "v_permlane16_swap_b32 v166, v170\n"
    "s_waitcnt lgkmcnt(2)\n"
    "v_mfma_f32_32x32x16_bf16 v[64:79], v[116:119], a[152:155], v[64:79]\n"
    "ds_read_b64_tr_b16 v[112:113], v236 offset:38912\n"
    "s_waitcnt vmcnt(11)\n"
    "s_add_u32 s99, s70, 3\n"
    "v_mov_b32 v133, s21\n"
    "v_mov_b32 v132, s99\n"
    "buffer_store_dwordx2 v[132:133], v242, s[56:59], s89 offen offset:0 sc1\n"
    "v_mfma_f32_32x32x16_bf16 v[80:95], v[116:119], a[156:159], v[80:95]\n"
    "ds_read_b64_tr_b16 v[114:115], v236 offset:39168\n"
    "v_permlane16_swap_b32 v167, v171\n"
    "v_permlane16_swap_b32 v168, v172\n"
    "v_permlane16_swap_b32 v169, v173\n"
    "s_add_u32 s60, s72, 32768\n"
    "buffer_store_dwordx4 v[166:169], v241, s[56:59], s60 offen offset:0 sc1\n"
    "s_waitcnt lgkmcnt(2)\n"
    "v_mfma_f32_32x32x16_bf16 v[64:79], v[120:123], a[160:163], v[64:79]\n"
    "ds_read_b64_tr_b16 v[116:117], v236 offset:40960\n"
    "s_add_u32 m0, s64, 139264\n"
    "s_add_u32 s65, s24, 184320\n"
    "buffer_load_dwordx4 v240, s[48:51], s65 offen offset:0 lds\n"
    "v_mfma_f32_32x32x16_bf16 v[80:95], v[120:123], a[164:167], v[80:95]\n"
    "ds_read_b64_tr_b16 v[118:119], v236 offset:41216\n"
    "buffer_store_dwordx4 v[170:173], v241, s[56:59], s60 offen offset:1024 sc1\n"
    "v_mul_f32 v124, s80, v48\n"
    "v_exp_f32 v124, v124\n"
    "s_waitcnt lgkmcnt(2)\n"
    "v_mfma_f32_32x32x16_bf16 v[64:79], v[112:115], a[168:171], v[64:79]\n"
    "ds_read_b64_tr_b16 v[120:121], v236 offset:43008\n"
    "v_mul_f32 v125, s80, v49\n"
    "v_add_f32 v124, 1.0, v124\n"
    "v_exp_f32 v125, v125\n"
    "v_mul_f32 v126, s80, v50\n"
    "v_fma_f32 v124, v124, v124, 1.0\n"
    "v_add_f32 v125, 1.0, v125\n"
    "v_exp_f32 v126, v126\n"
    "s_waitcnt vmcnt(10)\n"
    "v_subrev_u32 v234, s70, v234\n"
    "v_cmp_eq_u32_e64 s[16:17], s21, v235\n"
    "v_subrev_u32 v234, 2, v234\n"
    "v_cmp_gt_u32 vcc, 16, v234\n"
    "s_nop 0\n"
    "s_and_b64 vcc, vcc, s[16:17]\n"
    "s_cmp_eq_u64 vcc, -1\n"
    "s_cbranch_scc0 LslowK1_%=\n"
    "LbackK1_%=:\n"
    "v_mfma_f32_32x32x16_bf16 v[80:95], v[112:115], a[172:175], v[80:95]\n"
    "ds_read_b64_tr_b16 v[122:123], v236 offset:43264\n"
    "v_mul_f32 v127, s80, v51\n"
    "v_rcp_f32 v124, v124\n"
    "v_fma_f32 v125, v125, v125, 1.0\n"
    "v_add_f32 v126, 1.0, v126\n"
    "s_waitcnt lgkmcnt(2)\n"
    "v_mfma_f32_32x32x16_bf16 v[64:79], v[116:119], a[176:179], v[64:79]\n"
    "s_barrier\n"
    "ds_read_b64_tr_b16 v[112:113], v236 offset:45056\n"
    "v_exp_f32 v127, v127\n"
    "v_mul_f32 v128, s80, v52\n"
    "v_fma_f32 v124, v124, v248, v246\n"
    "v_mfma_f32_32x32x16_bf16 v[80:95], v[116:119], a[180:183], v[80:95]\n"
    "ds_read_b64_tr_b16 v[114:115], v236 offset:45312\n"
    "s_add_u32 m0, s64, 0\n"
    "s_add_u32 s65, s82, 0\n"
    "buffer_load_dwordx4 v240, s[48:51], s65 offen offset:0 lds\n"
    "s_waitcnt lgkmcnt(2)\n"
    "v_mfma_f32_32x32x16_bf16 v[64:79], v[120:123], a[184:187], v[64:79]\n"
    "ds_read_b64_tr_b16 v[116:117], v236 offset:47104\n"
    "v_rcp_f32 v125, v125\n"
    "v_fma_f32 v126, v126, v126, 1.0\n"
    "v_add_f32 v127, 1.0, v127\n"
    "v_exp_f32 v128, v128\n"
    "v_mul_f32 v129, s80, v53\n"
    "v_mul_f32 v48, v124, v48\n"
    "v_mfma_f32_32x32x16_bf16 v[80:95], v[120:123], a[188:191], v[80:95]\n"
    "ds_read_b64_tr_b16 v[118:119], v236 offset:47360\n"
    "v_fma_f32 v125, v125, v248, v246\n"
    "v_rcp_f32 v126, v126\n"
    "v_fma_f32 v127, v127, v127, 1.0\n"
    "v_add_f32 v128, 1.0, v128\n"
    "v_exp_f32 v129, v129\n"
    "s_waitcnt lgkmcnt(2)\n"
    "v_mfma_f32_32x32x16_bf16 v[64:79], v[112:115], a[192:195], v[64:79]\n"
    "ds_read_b64_tr_b16 v[120:121], v236 offset:49152\n"
    "v_mul_f32 v130, s80, v54\n"
    "v_mul_f32 v49, v125, v49\n"
    "v_fma_f32 v126, v126, v248, v246\n"
    "v_rcp_f32 v127, v127\n"
    "v_fma_f32 v128, v128, v128, 1.0\n"
    "v_add_f32 v129, 1.0, v129\n"
    "v_mfma_f32_32x32x16_bf16 v[80:95], v[112:115], a[196:199], v[80:95]\n"
    "ds_read_b64_tr_b16 v[122:123], v236 offset:49408\n"
    "v_exp_f32 v130, v130\n"
    "v_mul_f32 v124, s80, v55\n"
    "v_cvt_pk_bf16_f32 v174, v48, v49\n"
    "v_mul_f32 v50, v126, v50\n"
    "v_fma_f32 v127, v127, v248, v246\n"
    "s_waitcnt lgkmcnt(2)\n"
    "v_mfma_f32_32x32x16_bf16 v[64:79], v[116:119], a[200:203], v[64:79]\n"
    "ds_read_b64_tr_b16 v[112:113], v236 offset:51200\n"
    "s_add_u32 m0, s64, 4096\n"
    "s_add_u32 s65, s82, 36864\n"
    "buffer_load_dwordx4 v240, s[48:51], s65 offen offset:0 lds\n"
    "v_rcp_f32 v128, v128\n"
    "v_mfma_f32_32x32x16_bf16 v[80:95], v[116:119], a[204:207], v[80:95]\n"
    "ds_read_b64_tr_b16 v[114:115], v236 offset:51456\n"
    "v_fma_f32 v129, v129, v129, 1.0\n"
    "v_add_f32 v130, 1.0, v130\n"
    "v_exp_f32 v124, v124\n"
    "v_mul_f32 v125, s80, v56\n"
    "v_mul_f32 v51, v127, v51\n"
    "s_waitcnt lgkmcnt(2)\n"
    "v_mfma_f32_32x32x16_bf16 v[64:79], v[120:123], a[208:211], v[64:79]\n"
    "ds_read_b64_tr_b16 v[116:117], v236 offset:53248\n"
    "v_fma_f32 v128, v128, v248, v246\n"
    "v_rcp_f32 v129, v129\n"
    "v_fma_f32 v130, v130, v130, 1.0\n"
    "v_add_f32 v124, 1.0, v124\n"
    "v_exp_f32 v125, v125\n"
    "v_mfma_f32_32x32x16_bf16 v[80:95], v[120:123], a[212:215], v[80:95]\n"
    "ds_read_b64_tr_b16 v[118:119], v236 offset:53504\n"
    "v_mul_f32 v126, s80, v57\n"
    "v_cvt_pk_bf16_f32 v175, v50, v51\n"
    "v_mul_f32 v52, v128, v52\n"
    "v_fma_f32 v129, v129, v248, v246\n"
    "v_rcp_f32 v130, v130\n"
    "v_fma_f32 v124, v124, v124, 1.0\n"
    "s_waitcnt lgkmcnt(2)\n"
    "v_mfma_f32_32x32x16_bf16 v[64:79], v[112:115], a[216:219], v[64:79]\n"
    "ds_read_b64_tr_b16 v[120:121], v236 offset:55296\n"
    "v_add_f32 v125, 1.0, v125\n"
    "v_exp_f32 v126, v126\n"
    "v_mul_f32 v127, s80, v58\n"
    "v_mul_f32 v53, v129, v53\n"
    "v_fma_f32 v130, v130, v248, v246\n"
    "v_rcp_f32 v124, v124\n"
    "v_mfma_f32_32x32x16_bf16 v[80:95], v[112:115], a[220:223], v[80:95]\n"
    "ds_read_b64_tr_b16 v[122:123], v236 offset:55552\n"
    "v_fma_f32 v125, v125, v125, 1.0\n"
    "v_add_f32 v126, 1.0, v126\n"
    "v_exp_f32 v127, v127\n"
    "v_mul_f32 v128, s80, v59\n"
    "v_cvt_pk_bf16_f32 v176, v52, v53\n"
    "s_waitcnt lgkmcnt(2)\n"
    "v_mfma_f32_32x32x16_bf16 v[64:79], v[116:119], a[224:227], v[64:79]\n"
    "ds_read_b64_tr_b16 v[112:113], v236 offset:57344\n"
    "s_add_u32 m0, s64, 8192\n"
    "s_add_u32 s65, s77, 0\n"
    "buffer_load_dwordx4 v240, s[48:51], s65 offen offset:0 lds\n"
    "v_mfma_f32_32x32x16_bf16 v[80:95], v[116:119], a[228:231], v[80:95]\n"
    "ds_read_b64_tr_b16 v[114:115], v236 offset:57600\n"
    "v_mul_f32 v54, v130, v54\n"
    "v_fma_f32 v124, v124, v248, v246\n"
    "v_rcp_f32 v125, v125\n"
    "v_fma_f32 v126, v126, v126, 1.0\n"
    "v_add_f32 v127, 1.0, v127\n"
    "s_waitcnt lgkmcnt(2)\n"
    "v_mfma_f32_32x32x16_bf16 v[64:79], v[120:123], a[232:235], v[64:79]\n"
    "ds_read_b64_tr_b16 v[116:117], v236 offset:59392\n"
    "v_exp_f32 v128, v128\n"
    "v_mul_f32 v129, s80, v60\n"
    "v_mul_f32 v55, v124, v55\n"
    "v_fma_f32 v125, v125, v248, v246\n"
    "v_rcp_f32 v126, v126\n"
    "v_fma_f32 v127, v127, v127, 1.0\n"
    "v_add_f32 v128, 1.0, v128\n"
    "v_mfma_f32_32x32x16_bf16 v[80:95], v[120:123], a[236:239], v[80:95]\n"
    "ds_read_b64_tr_b16 v[118:119], v236 offset:59648\n"
    "v_exp_f32 v129, v129\n"
    "v_mul_f32 v130, s80, v61\n"
    "v_cvt_pk_bf16_f32 v177, v54, v55\n"
    "v_mul_f32 v56, v125, v56\n"
    "v_fma_f32 v126, v126, v248, v246\n"
    "s_waitcnt lgkmcnt(2)\n"
    "v_mfma_f32_32x32x16_bf16 v[64:79], v[112:115], a[240:243], v[64:79]\n"
    "ds_read_b64_tr_b16 v[120:121], v236 offset:61440\n"
    "v_rcp_f32 v127, v127\n"
    "v_fma_f32 v128, v128, v128, 1.0\n"
    "v_add_f32 v129, 1.0, v129\n"
    "v_exp_f32 v130, v130\n"
    "v_mul_f32 v124, s80, v62\n"
    "v_mul_f32 v57, v126, v57\n"
    "v_mfma_f32_32x32x16_bf16 v[80:95], v[112:115], a[244:247], v[80:95]\n"
    "ds_read_b64_tr_b16 v[122:123], v236 offset:61696\n"
    "v_fma_f32 v127, v127, v248, v246\n"
    "v_rcp_f32 v128, v128\n"
    "v_fma_f32 v129, v129, v129, 1.0\n"
    "v_add_f32 v130, 1.0, v130\n"
    "v_exp_f32 v124, v124\n"
    "s_waitcnt lgkmcnt(2)\n"
    "v_mfma_f32_32x32x16_bf16 v[64:79], v[116:119], a[248:251], v[64:79]\n"
    "ds_read_b64_tr_b16 v[112:113], v236 offset:63488\n"
    "s_add_u32 m0, s64, 12288\n"
    "s_add_u32 s65, s77, 36864\n"
    "buffer_load_dwordx4 v240, s[48:51], s65 offen offset:0 lds\n"
    "v_mfma_f32_32x32x16_bf16 v[80:95], v[116:119], a[252:255], v[80:95]\n"
    "ds_read_b64_tr_b16 v[114:115], v236 offset:63744\n"
    "v_mul_f32 v125, s80, v63\n"
    "v_cvt_pk_bf16_f32 v178, v56, v57\n"
    "v_mul_f32 v58, v127, v58\n"
    "v_fma_f32 v128, v128, v248, v246\n"
    "v_rcp_f32 v129, v129\n"
    "s_waitcnt lgkmcnt(2)\n"
    "v_mfma_f32_32x32x16_bf16 v[64:79], v[120:123], v[0:3], v[64:79]\n"
    "ds_read_b64_tr_b16 v[116:117], v237 offset:0\n"
    "v_fma_f32 v130, v130, v130, 1.0\n"
    "v_add_f32 v124, 1.0, v124\n"
    "v_exp_f32 v125, v125\n"
    "v_mul_f32 v59, v128, v59\n"
    "v_fma_f32 v129, v129, v248, v246\n"
    "v_rcp_f32 v130, v130\n"
    "v_mfma_f32_32x32x16_bf16 v[80:95], v[120:123], v[4:7], v[80:95]\n"
    "ds_read_b64_tr_b16 v[118:119], v237 offset:256\n"
    "v_fma_f32 v124, v124, v124, 1.0\n"
    "v_add_f32 v125, 1.0, v125\n"
    "v_cvt_pk_bf16_f32 v179, v58, v59\n"
    "v_mul_f32 v60, v129, v60\n"
    "v_fma_f32 v130, v130, v248, v246\n"
    "v_rcp_f32 v124, v124\n"
    "s_waitcnt lgkmcnt(2)\n"
    "v_mfma_f32_32x32x16_bf16 v[64:79], v[112:115], v[8:11], v[64:79]\n"
    "ds_read_b64_tr_b16 v[120:121], v237 offset:2048\n"
    "v_fma_f32 v125, v125, v125, 1.0\n"
    "v_mul_f32 v61, v130, v61\n"
    "v_fma_f32 v124, v124, v248, v246\n"
    "v_rcp_f32 v125, v125\n"
    "v_cvt_pk_bf16_f32 v180, v60, v61\n"
    "v_mul_f32 v62, v124, v62\n"
    "v_mfma_f32_32x32x16_bf16 v[80:95], v[112:115], v[12:15], v[80:95]\n"
    "ds_read_b64_tr_b16 v[122:123], v237 offset:2304\n"
    "v_fma_f32 v125, v125, v248, v246\n"
    "v_mul_f32 v63, v125, v63\n"
    "v_cvt_pk_bf16_f32 v181, v62, v63\n"
    "v_permlane32_swap_b32 v174, v178\n"
    "v_permlane32_swap_b32 v175, v179\n"
    "v_permlane32_swap_b32 v176, v180\n"
    "s_waitcnt lgkmcnt(2)\n"
    "v_mfma_f32_32x32x16_bf16 v[64:79], v[116:119], v[16:19], v[64:79]\n"
    "s_barrier\n"
    "ds_read_b64_tr_b16 v[112:113], v252 offset:4096\n"
    "v_permlane32_swap_b32 v177, v181\n"
    "v_permlane16_swap_b32 v174, v178\n"
    "v_permlane16_swap_b32 v175, v179\n"
    "v_permlane16_swap_b32 v176, v180\n"
    "v_mfma_f32_32x32x16_bf16 v[80:95], v[116:119], v[20:23], v[80:95]\n"
    "ds_read_b64_tr_b16 v[114:115], v252 offset:5120\n"
    "s_add_u32 m0, s64, 16384\n"
    "s_add_u32 s65, s25, 0\n"
    "buffer_load_dwordx4 v240, s[48:51], s65 offen offset:0 lds\n"
    "s_waitcnt lgkmcnt(2)\n"
    "v_mfma_f32_32x32x16_bf16 v[64:79], v[120:123], v[24:27], v[64:79]\n"
    "ds_read_b64_tr_b16 v[116:117], v253 offset:4096\n"
    "v_permlane16_swap_b32 v177, v181\n"
    "s_add_u32 s60, s72, 32768\n"
    "buffer_store_dwordx4 v[174:177], v241, s[56:59], s60 offen offset:2048 sc1\n"
    "buffer_store_dwordx4 v[178:181], v241, s[56:59], s60 offen offset:3072 sc1\n"
    "v_mfma_f32_32x32x16_bf16 v[80:95], v[120:123], v[28:31], v[80:95]\n"
    "ds_read_b64_tr_b16 v[118:119], v253 offset:5120\n"
    "s_waitcnt lgkmcnt(2)\n"
    "v_mfma_f32_16x16x32_bf16 a[192:195], v[112:115], v[134:137], 0\n"
    "ds_read_b64_tr_b16 v[120:121], v252 offset:4608\n"
    "v_mfma_f32_16x16x32_bf16 a[196:199], v[112:115], v[138:141], 0\n"
    "ds_read_b64_tr_b16 v[122:123], v252 offset:5632\n"
    "v_mfma_f32_16x16x32_bf16 a[200:203], v[112:115], v[142:145], 0\n"
    "v_mfma_f32_16x16x32_bf16 a[204:207], v[112:115], v[146:149], 0\n"
    "s_waitcnt lgkmcnt(2)\n"
    "v_mfma_f32_16x16x32_bf16 a[208:211], v[116:119], v[134:137], 0\n"
    "ds_read_b64_tr_b16 v[112:113], v253 offset:4608\n"
    "s_add_u32 m0, s64, 20480\n"
    "v_mfma_f32_16x16x32_bf16 a[212:215], v[116:119], v[138:141], 0\n"
    "ds_read_b64_tr_b16 v[114:115], v253 offset:5632\n"
    "s_add_u32 s65, s25, 36864\n"
    "v_mfma_f32_16x16x32_bf16 a[216:219], v[116:119], v[142:145], 0\n"
    "buffer_load_dwordx4 v240, s[48:51], s65 offen offset:0 lds\n"
    "s_add_u32 s61, s73, 0\n"
    "buffer_load_dwordx4 v[0:3], v241, s[56:59], s61 offen offset:0 sc1\n"
    "v_mfma_f32_16x16x32_bf16 a[220:223], v[116:119], v[146:149], 0\n"
    "v_mul_f32 v124, s80, v64\n"
    "v_exp_f32 v124, v124\n"
    "s_waitcnt lgkmcnt(2)\n"
    "v_mfma_f32_16x16x32_bf16 a[224:227], v[120:123], v[134:137], 0\n"
    "ds_read_b64_tr_b16 v[116:117], v252 offset:12288\n"
    "v_mul_f32 v125, s80, v65\n"
    "v_add_f32 v124, 1.0, v124\n"
    "v_mfma_f32_16x16x32_bf16 a[228:231], v[120:123], v[138:141], 0\n"
    "ds_read_b64_tr_b16 v[118:119], v252 offset:13312\n"
    "v_exp_f32 v125, v125\n"
    "v_mfma_f32_16x16x32_bf16 a[232:235], v[120:123], v[142:145], 0\n"
    "v_mul_f32 v126, s80, v66\n"
    "v_fma_f32 v124, v124, v124, 1.0\n"
    "v_add_f32 v125, 1.0, v125\n"
    "v_mfma_f32_16x16x32_bf16 a[236:239], v[120:123], v[146:149], 0\n"
    "s_add_u32 m0, s64, 24576\n"
    "v_exp_f32 v126, v126\n"
    "s_add_u32 s61, s73, 0\n"
    "buffer_load_dwordx4 v[4:7], v241, s[56:59], s61 offen offset:1024 sc1\n"
    "s_waitcnt lgkmcnt(2)\n"
    "v_mfma_f32_16x16x32_bf16 a[240:243], v[112:115], v[134:137], 0\n"
    "ds_read_b64_tr_b16 v[120:121], v253 offset:12288\n"
    "s_add_u32 s65, s82, 73728\n"
    "v_mfma_f32_16x16x32_bf16 a[244:247], v[112:115], v[138:141], 0\n"
    "ds_read_b64_tr_b16 v[122:123], v253 offset:13312\n"
    "buffer_load_dwordx4 v240, s[48:51], s65 offen offset:0 lds\n"
    "v_mfma_f32_16x16x32_bf16 a[248:251], v[112:115], v[142:145], 0\n"
    "v_mul_f32 v127, s80, v67\n"
    "v_rcp_f32 v124, v124\n"
    "v_fma_f32 v125, v125, v125, 1.0\n"
    "v_mfma_f32_16x16x32_bf16 a[252:255], v[112:115], v[146:149], 0\n"
    "v_add_f32 v126, 1.0, v126\n"
    "v_exp_f32 v127, v127\n"
    "s_waitcnt lgkmcnt(2)\n"
    "v_mfma_f32_16x16x32_bf16 a[64:67], v[116:119], v[134:137], 0\n"
    "ds_read_b64_tr_b16 v[112:113], v252 offset:12800\n"
    "v_mul_f32 v128, s80, v68\n"
    "s_add_u32 s61, s73, 0\n"
    "buffer_load_dwordx4 v[8:11], v241, s[56:59], s61 offen offset:2048 sc1\n"
    "v_mfma_f32_16x16x32_bf16 a[68:71], v[116:119], v[138:141], 0\n"
    "ds_read_b64_tr_b16 v[114:115], v252 offset:13824\n"
    "v_fma_f32 v124, v124, v247, v245\n"
    "v_rcp_f32 v125, v125\n"
    "v_mfma_f32_16x16x32_bf16 a[72:75], v[116:119], v[142:145], 0\n"
    "s_add_u32 m0, s64, 28672\n"
    "v_fma_f32 v126, v126, v126, 1.0\n"
    "v_add_f32 v127, 1.0, v127\n"
    "v_mfma_f32_16x16x32_bf16 a[76:79], v[116:119], v[146:149], 0\n"
    "s_add_u32 s65, s82, 110592\n"
    "s_waitcnt lgkmcnt(2)\n"
    "v_mfma_f32_16x16x32_bf16 a[80:83], v[120:123], v[134:137], 0\n"
    "ds_read_b64_tr_b16 v[116:117], v253 offset:12800\n"
    "buffer_load_dwordx4 v240, s[48:51], s65 offen offset:0 lds\n"
    "v_bfe_u32 v243, v241, 4, 2\n"
    "v_max_u32 v243, 1, v243\n"
    "v_xor_b32 v243, s69, v243\n"
    "v_lshl_add_u32 v243, v243, 6, s22\n"
    "buffer_load_dwordx2 v[234:235], v243, s[56:59], 0 offen offset:0 sc1\n"
    "v_mfma_f32_16x16x32_bf16 a[84:87], v[120:123], v[138:141], 0\n"
    "ds_read_b64_tr_b16 v[118:119], v253 offset:13824\n"
    "v_exp_f32 v128, v128\n"
    "v_mul_f32 v129, s80, v69\n"
    "s_add_u32 s61, s73, 0\n"
    "buffer_load_dwordx4 v[12:15], v241, s[56:59], s61 offen offset:3072 sc1\n"
    "v_mfma_f32_16x16x32_bf16 a[88:91], v[120:123], v[142:145], 0\n"
    "v_mul_f32 v64, v124, v64\n"
    "v_fma_f32 v125, v125, v247, v245\n"
    "v_rcp_f32 v126, v126\n"
    "v_mfma_f32_16x16x32_bf16 a[92:95], v[120:123], v[146:149], 0\n"
    "v_fma_f32 v127, v127, v127, 1.0\n"
    "v_add_f32 v128, 1.0, v128\n"
    "s_waitcnt lgkmcnt(2)\n"
    "v_mfma_f32_16x16x32_bf16 a[96:99], v[112:115], v[134:137], 0\n"
    "ds_read_b64_tr_b16 v[120:121], v252 offset:20480\n"
    "v_exp_f32 v129, v129\n"
    "v_mfma_f32_16x16x32_bf16 a[100:103], v[112:115], v[138:141], 0\n"
    "ds_read_b64_tr_b16 v[122:123], v252 offset:21504\n"
    "s_add_u32 m0, s64, 32768\n"
    "v_mul_f32 v130, s80, v70\n"
    "v_mfma_f32_16x16x32_bf16 a[104:107], v[112:115], v[142:145], 0\n"
    "s_add_u32 s65, s77, 73728\n"
    "v_mul_f32 v65, v125, v65\n"
    "v_fma_f32 v126, v126, v247, v245\n"
    "v_mfma_f32_16x16x32_bf16 a[108:111], v[112:115], v[146:149], 0\n"
    "buffer_load_dwordx4 v240, s[48:51], s65 offen offset:0 lds\n"
    "s_waitcnt lgkmcnt(2)\n"
    "v_mfma_f32_16x16x32_bf16 a[112:115], v[116:119], v[134:137], 0\n"
    "ds_read_b64_tr_b16 v[112:113], v253 offset:20480\n"
    "v_rcp_f32 v127, v127\n"
    "v_mfma_f32_16x16x32_bf16 a[116:119], v[116:119], v[138:141], 0\n"
    "ds_read_b64_tr_b16 v[114:115], v253 offset:21504\n"
    "v_fma_f32 v128, v128, v128, 1.0\n"
    "v_add_f32 v129, 1.0, v129\n"
    "v_mfma_f32_16x16x32_bf16 a[120:123], v[116:119], v[142:145], 0\n"
    "v_exp_f32 v130, v130\n"
    "v_mul_f32 v124, s80, v71\n"
    "v_cvt_pk_bf16_f32 v182, v64, v65\n"
    "v_mul_f32 v66, v126, v66\n"
    "v_mfma_f32_16x16x32_bf16 a[124:127], v[116:119], v[146:149], 0\n"
    "v_fma_f32 v127, v127, v247, v245\n"
    "s_waitcnt lgkmcnt(2)\n"
    "v_mfma_f32_16x16x32_bf16 a[128:131], v[120:123], v[134:137], 0\n"
    "ds_read_b64_tr_b16 v[116:117], v252 offset:20992\n"
    "s_add_u32 m0, s64, 36864\n"
    "v_rcp_f32 v128, v128\n"
    "v_mfma_f32_16x16x32_bf16 a[132:135], v[120:123], v[138:141], 0\n"
    "ds_read_b64_tr_b16 v[118:119], v252 offset:22016\n"
    "s_add_u32 s65, s77, 110592\n"
    "v_mfma_f32_16x16x32_bf16 a[136:139], v[120:123], v[142:145], 0\n"
    "buffer_load_dwordx4 v240, s[48:51], s65 offen offset:0 lds\n"
    "v_mfma_f32_16x16x32_bf16 a[140:143], v[120:123], v[146:149], 0\n"
    "v_fma_f32 v129, v129, v129, 1.0\n"
    "v_add_f32 v130, 1.0, v130\n"
    "s_waitcnt lgkmcnt(2)\n"
    "v_mfma_f32_16x16x32_bf16 a[144:147], v[112:115], v[134:137], 0\n"
    "ds_read_b64_tr_b16 v[120:121], v253 offset:20992\n"
    "v_exp_f32 v124, v124\n"
    "v_mfma_f32_16x16x32_bf16 a[148:151], v[112:115], v[138:141], 0\n"
    "ds_read_b64_tr_b16 v[122:123], v253 offset:22016\n"
    "v_mul_f32 v125, s80, v72\n"
    "v_mul_f32 v67, v127, v67\n"
    "v_mfma_f32_16x16x32_bf16 a[152:155], v[112:115], v[142:145], 0\n"
    "v_fma_f32 v128, v128, v247, v245\n"
    "v_rcp_f32 v129, v129\n"
    "v_fma_f32 v130, v130, v130, 1.0\n"
    "v_mfma_f32_16x16x32_bf16 a[156:159], v[112:115], v[146:149], 0\n"
    "v_add_f32 v124, 1.0, v124\n"
    "v_exp_f32 v125, v125\n"
    "s_waitcnt lgkmcnt(2)\n"
    "v_mfma_f32_16x16x32_bf16 a[160:163], v[116:119], v[134:137], 0\n"
    "s_waitcnt vmcnt(26)\n"
    "s_barrier\n"
    "ds_read_b64_tr_b16 v[112:113], v252 offset:28672\n"
    "v_mfma_f32_16x16x32_bf16 a[164:167], v[116:119], v[138:141], 0\n"
    "ds_read_b64_tr_b16 v[114:115], v252 offset:29696\n"
    "s_add_u32 m0, s64, 40960\n"
    "v_mfma_f32_16x16x32_bf16 a[168:171], v[116:119], v[142:145], 0\n"
    "s_add_u32 s65, s25, 73728\n"
    "v_mul_f32 v126, s80, v73\n"
    "v_cvt_pk_bf16_f32 v183, v66, v67\n"
    "v_mul_f32 v68, v128, v68\n"
    "v_mfma_f32_16x16x32_bf16 a[172:175], v[116:119], v[146:149], 0\n"
    "buffer_load_dwordx4 v240, s[48:51], s65 offen offset:0 lds\n"
    "s_waitcnt lgkmcnt(2)\n"
    "v_mfma_f32_16x16x32_bf16 a[176:179], v[120:123], v[134:137], 0\n"
    "ds_read_b64_tr_b16 v[116:117], v253 offset:28672\n"
    "v_fma_f32 v129, v129, v247, v245\n"
    "v_rcp_f32 v130, v130\n"
    "v_mfma_f32_16x16x32_bf16 a[180:183], v[120:123], v[138:141], 0\n"
    "ds_read_b64_tr_b16 v[118:119], v253 offset:29696\n"
    "v_fma_f32 v124, v124, v124, 1.0\n"
    "v_mfma_f32_16x16x32_bf16 a[184:187], v[120:123], v[142:145], 0\n"
    "v_add_f32 v125, 1.0, v125\n"
    "v_exp_f32 v126, v126\n"
    "v_mul_f32 v127, s80, v74\n"
    "v_mfma_f32_16x16x32_bf16 a[188:191], v[120:123], v[146:149], 0\n"
    "v_mul_f32 v69, v129, v69\n"
    "v_fma_f32 v130, v130, v247, v245\n"
    "s_waitcnt lgkmcnt(2)\n"
    "v_mfma_f32_16x16x32_bf16 a[192:195], v[112:115], v[150:153], a[192:195]\n"
    "ds_read_b64_tr_b16 v[120:121], v252 offset:29184\n"
    "s_add_u32 m0, s64, 45056\n"
    "v_mfma_f32_16x16x32_bf16 a[196:199], v[112:115], v[154:157], a[196:199]\n"
    "ds_read_b64_tr_b16 v[122:123], v252 offset:30208\n"
    "s_add_u32 s65, s25, 110592\n"
    "v_rcp_f32 v124, v124\n"
    "v_mfma_f32_16x16x32_bf16 a[200:203], v[112:115], v[158:161], a[200:203]\n"
    "buffer_load_dwordx4 v240, s[48:51], s65 offen offset:0 lds\n"
    "v_mfma_f32_16x16x32_bf16 a[204:207], v[112:115], v[162:165], a[204:207]\n"
    "v_fma_f32 v125, v125, v125, 1.0\n"
    "v_add_f32 v126, 1.0, v126\n"
    "s_waitcnt lgkmcnt(2)\n"
    "v_mfma_f32_16x16x32_bf16 a[208:211], v[116:119], v[150:153], a[208:211]\n"
    "ds_read_b64_tr_b16 v[112:113], v253 offset:29184\n"
    "v_exp_f32 v127, v127\n"
    "v_mfma_f32_16x16x32_bf16 a[212:215], v[116:119], v[154:157], a[212:215]\n"
    "ds_read_b64_tr_b16 v[114:115], v253 offset:30208\n"
    "v_mul_f32 v128, s80, v75\n"
    "v_cvt_pk_bf16_f32 v184, v68, v69\n"
    "v_mfma_f32_16x16x32_bf16 a[216:219], v[116:119], v[158:161], a[216:219]\n"
    "v_mul_f32 v70, v130, v70\n"
    "v_fma_f32 v124, v124, v247, v245\n"
    "v_rcp_f32 v125, v125\n"
    "s_add_u32 s61, s73, 16384\n"
    "buffer_load_dwordx4 v[134:137], v241, s[56:59], s61 offen offset:0 sc1\n"
    "v_mfma_f32_16x16x32_bf16 a[220:223], v[116:119], v[162:165], a[220:223]\n"
    "v_fma_f32 v126, v126, v126, 1.0\n"
    "v_add_f32 v127, 1.0, v127\n"
    "s_waitcnt lgkmcnt(2)\n"
    "v_mfma_f32_16x16x32_bf16 a[224:227], v[120:123], v[150:153], a[224:227]\n"
    "ds_read_b64_tr_b16 v[116:117], v252 offset:36864\n"
    "s_add_u32 m0, s64, 49152\n"
    "v_mfma_f32_16x16x32_bf16 a[228:231], v[120:123], v[154:157], a[228:231]\n"
    "ds_read_b64_tr_b16 v[118:119], v252 offset:37888\n"
    "s_add_u32 s65, s78, 0\n"
    "v_exp_f32 v128, v128\n"
    "v_mfma_f32_16x16x32_bf16 a[232:235], v[120:123], v[158:161], a[232:235]\n"
    "buffer_load_dwordx4 v240, s[48:51], s65 offen offset:0 lds\n"
    "v_mfma_f32_16x16x32_bf16 a[236:239], v[120:123], v[162:165], a[236:239]\n"
    "v_mul_f32 v129, s80, v76\n"
    "v_mul_f32 v71, v124, v71\n"
    "s_add_u32 s61, s73, 16384\n"
    "buffer_load_dwordx4 v[138:141], v241, s[56:59], s61 offen offset:1024 sc1\n"
    "s_waitcnt lgkmcnt(2)\n"
    "v_mfma_f32_16x16x32_bf16 a[240:243], v[112:115], v[150:153], a[240:243]\n"
    "ds_read_b64_tr_b16 v[120:121], v253 offset:36864\n"
    "v_fma_f32 v125, v125, v247, v245\n"
    "v_mfma_f32_16x16x32_bf16 a[244:247], v[112:115], v[154:157], a[244:247]\n"
    "ds_read_b64_tr_b16 v[122:123], v253 offset:37888\n"
    "v_rcp_f32 v126, v126\n"
    "v_fma_f32 v127, v127, v127, 1.0\n"
    "v_mfma_f32_16x16x32_bf16 a[248:251], v[112:115], v[158:161], a[248:251]\n"
    "v_add_f32 v128, 1.0, v128\n"
    "v_exp_f32 v129, v129\n"
    "v_mul_f32 v130, s80, v77\n"
    "v_mfma_f32_16x16x32_bf16 a[252:255], v[112:115], v[162:165], a[252:255]\n"
    "v_cvt_pk_bf16_f32 v185, v70, v71\n"
    "v_mul_f32 v72, v125, v72\n"
    "s_waitcnt lgkmcnt(2)\n"
    "v_mfma_f32_16x16x32_bf16 a[64:67], v[116:119], v[150:153], a[64:67]\n"
    "ds_read_b64_tr_b16 v[112:113], v252 offset:37376\n"
    "s_add_u32 m0, s64, 53248\n"
    "v_fma_f32 v126, v126, v247, v245\n"
    "s_add_u32 s61, s73, 16384\n"
    "buffer_load_dwordx4 v[142:145], v241, s[56:59], s61 offen offset:2048 sc1\n"
    "v_mfma_f32_16x16x32_bf16 a[68:71], v[116:119], v[154:157], a[68:71]\n"
    "ds_read_b64_tr_b16 v[114:115], v252 offset:38400\n"
    "s_add_u32 s65, s78, 36864\n"
    "v_mfma_f32_16x16x32_bf16 a[72:75], v[116:119], v[158:161], a[72:75]\n"
    "buffer_load_dwordx4 v240, s[48:51], s65 offen offset:0 lds\n"
    "v_mfma_f32_16x16x32_bf16 a[76:79], v[116:119], v[162:165], a[76:79]\n"
    "v_rcp_f32 v127, v127\n"
    "v_fma_f32 v128, v128, v128, 1.0\n"
    "s_waitcnt lgkmcnt(2)\n"
    "v_mfma_f32_16x16x32_bf16 a[80:83], v[120:123], v[150:153], a[80:83]\n"
    "ds_read_b64_tr_b16 v[116:117], v253 offset:37376\n"
    "v_add_f32 v129, 1.0, v129\n"
    "v_mfma_f32_16x16x32_bf16 a[84:87], v[120:123], v[154:157], a[84:87]\n"
    "ds_read_b64_tr_b16 v[118:119], v253 offset:38400\n"
    "v_exp_f32 v130, v130\n"
    "v_mul_f32 v124, s80, v78\n"
    "s_add_u32 s61, s73, 16384\n"
    "buffer_load_dwordx4 v[146:149], v241, s[56:59], s61 offen offset:3072 sc1\n"
    "v_mfma_f32_16x16x32_bf16 a[88:91], v[120:123], v[158:161], a[88:91]\n"
    "v_mul_f32 v73, v126, v73\n"
    "v_fma_f32 v127, v127, v247, v245\n"
    "v_rcp_f32 v128, v128\n"
    "v_mfma_f32_16x16x32_bf16 a[92:95], v[120:123], v[162:165], a[92:95]\n"
    "v_fma_f32 v129, v129, v129, 1.0\n"
    "v_add_f32 v130, 1.0, v130\n"
    "s_waitcnt lgkmcnt(2)\n"
    "v_mfma_f32_16x16x32_bf16 a[96:99], v[112:115], v[150:153], a[96:99]\n"
    "ds_read_b64_tr_b16 v[120:121], v252 offset:45056\n"
    "s_add_u32 m0, s64, 57344\n"
    "v_mfma_f32_16x16x32_bf16 a[100:103], v[112:115], v[154:157], a[100:103]\n"
    "ds_read_b64_tr_b16 v[122:123], v252 offset:46080\n"
    "s_add_u32 s65, s26, 0\n"
    "v_mfma_f32_16x16x32_bf16 a[104:107], v[112:115], v[158:161], a[104:107]\n"
    "buffer_load_dwordx4 v240, s[48:51], s65 offen offset:0 lds\n"
    "v_exp_f32 v124, v124\n"
    "v_mfma_f32_16x16x32_bf16 a[108:111], v[112:115], v[162:165], a[108:111]\n"
    "v_mul_f32 v125, s80, v79\n"
    "s_waitcnt lgkmcnt(2)\n"
    "v_mfma_f32_16x16x32_bf16 a[112:115], v[116:119], v[150:153], a[112:115]\n"
    "ds_read_b64_tr_b16 v[112:113], v253 offset:45056\n"
    "s_waitcnt vmcnt(19)\n"
    "s_add_u32 s99, s70, 4\n"
    "v_mov_b32 v133, s21\n"
    "v_mov_b32 v132, s99\n"
    "buffer_store_dwordx2 v[132:133], v242, s[56:59], s89 offen offset:0 sc1\n"
    "v_cvt_pk_bf16_f32 v186, v72, v73\n"
    "v_mfma_f32_16x16x32_bf16 a[116:119], v[116:119], v[154:157], a[116:119]\n"
    "ds_read_b64_tr_b16 v[114:115], v253 offset:46080\n"
    "v_mul_f32 v74, v127, v74\n"
    "v_fma_f32 v128, v128, v247, v245\n"
    "v_mfma_f32_16x16x32_bf16 a[120:123], v[116:119], v[158:161], a[120:123]\n"
    "v_rcp_f32 v129, v129\n"
    "v_fma_f32 v130, v130, v130, 1.0\n"
    "v_add_f32 v124, 1.0, v124\n"
    "v_mfma_f32_16x16x32_bf16 a[124:127], v[116:119], v[162:165], a[124:127]\n"
    "v_exp_f32 v125, v125\n"
    "s_waitcnt lgkmcnt(2)\n"
    "v_mfma_f32_16x16x32_bf16 a[128:131], v[120:123], v[150:153], a[128:131]\n"
    "ds_read_b64_tr_b16 v[116:117], v252 offset:45568\n"
    "s_add_u32 m0, s64, 61440\n"
    "v_mul_f32 v75, v128, v75\n"
    "s_waitcnt vmcnt(13)\n"
    "v_subrev_u32 v234, s70, v234\n"
    "v_cmp_eq_u32_e64 s[16:17], s21, v235\n"
    "v_subrev_u32 v234, 3, v234\n"
    "v_cmp_gt_u32 vcc, 16, v234\n"
    "s_nop 0\n"
    "s_and_b64 vcc, vcc, s[16:17]\n"
    "s_cmp_eq_u64 vcc, -1\n"
    "s_cbranch_scc0 LslowK2_%=\n"
    "LbackK2_%=:\n"
    "v_mfma_f32_16x16x32_bf16 a[132:135], v[120:123], v[154:157], a[132:135]\n"
    "ds_read_b64_tr_b16 v[118:119], v252 offset:46592\n"
    "s_add_u32 s65, s26, 36864\n"
    "v_mfma_f32_16x16x32_bf16 a[136:139], v[120:123], v[158:161], a[136:139]\n"
    "buffer_load_dwordx4 v240, s[48:51], s65 offen offset:0 lds\n"
    "v_mfma_f32_16x16x32_bf16 a[140:143], v[120:123], v[162:165], a[140:143]\n"
    "v_fma_f32 v129, v129, v247, v245\n"
    "v_rcp_f32 v130, v130\n"
    "s_waitcnt lgkmcnt(2)\n"
    "v_mfma_f32_16x16x32_bf16 a[144:147], v[112:115], v[150:153], a[144:147]\n"
    "ds_read_b64_tr_b16 v[120:121], v253 offset:45568\n"
    "v_fma_f32 v124, v124, v124, 1.0\n"
    "v_add_f32 v125, 1.0, v125\n"
    "v_mfma_f32_16x16x32_bf16 a[148:151], v[112:115], v[154:157], a[148:151]\n"
    "ds_read_b64_tr_b16 v[122:123], v253 offset:46592\n"
    "v_cvt_pk_bf16_f32 v187, v74, v75\n"
    "v_mul_f32 v76, v129, v76\n"
    "v_mfma_f32_16x16x32_bf16 a[152:155], v[112:115], v[158:161], a[152:155]\n"
    "v_fma_f32 v130, v130, v247, v245\n"
    "v_rcp_f32 v124, v124\n"
    "v_fma_f32 v125, v125, v125, 1.0\n"
    "v_mfma_f32_16x16x32_bf16 a[156:159], v[112:115], v[162:165], a[156:159]\n"
    "v_mul_f32 v77, v130, v77\n"
    "v_fma_f32 v124, v124, v247, v245\n"
    "s_waitcnt lgkmcnt(2)\n"
    "v_mfma_f32_16x16x32_bf16 a[160:163], v[116:119], v[150:153], a[160:163]\n"
    "s_barrier\n"
    "ds_read_b64_tr_b16 v[112:113], v252 offset:53248\n"
    "v_mfma_f32_16x16x32_bf16 a[164:167], v[116:119], v[154:157], a[164:167]\n"
    "ds_read_b64_tr_b16 v[114:115], v252 offset:54272\n"
    "s_add_u32 m0, s64, 65536\n"
    "v_mfma_f32_16x16x32_bf16 a[168:171], v[116:119], v[158:161], a[168:171]\n"
    "s_add_u32 s65, s79, 0\n"
    "v_rcp_f32 v125, v125\n"
    "v_cvt_pk_bf16_f32 v188, v76, v77\n"
    "v_mfma_f32_16x16x32_bf16 a[172:175], v[116:119], v[162:165], a[172:175]\n"
    "buffer_load_dwordx4 v240, s[48:51], s65 offen offset:0 lds\n"
    "s_waitcnt lgkmcnt(2)\n"
    "v_mfma_f32_16x16x32_bf16 a[176:179], v[120:123], v[150:153], a[176:179]\n"
    "ds_read_b64_tr_b16 v[116:117], v253 offset:53248\n"
    "v_mul_f32 v78, v124, v78\n"
    "v_fma_f32 v125, v125, v247, v245\n"
    "v_mul_f32 v79, v125, v79\n"
    "v_mfma_f32_16x16x32_bf16 a[180:183], v[120:123], v[154:157], a[180:183]\n"
    "ds_read_b64_tr_b16 v[118:119], v253 offset:54272\n"
    "v_cvt_pk_bf16_f32 v189, v78, v79\n"
    "v_permlane32_swap_b32 v182, v186\n"
    "v_mfma_f32_16x16x32_bf16 a[184:187], v[120:123], v[158:161], a[184:187]\n"
    "v_permlane32_swap_b32 v183, v187\n"
    "v_permlane32_swap_b32 v184, v188\n"
    "v_permlane32_swap_b32 v185, v189\n"
    "v_permlane16_swap_b32 v182, v186\n"
    "v_mfma_f32_16x16x32_bf16 a[188:191], v[120:123], v[162:165], a[188:191]\n"
    "v_permlane16_swap_b32 v183, v187\n"
    "v_permlane16_swap_b32 v184, v188\n"
    "s_waitcnt lgkmcnt(2)\n"
    "v_mfma_f32_16x16x32_bf16 a[192:195], v[112:115], v[166:169], a[192:195]\n"
    "ds_read_b64_tr_b16 v[120:121], v252 offset:53760\n"
    "s_add_u32 m0, s64, 69632\n"
    "v_mfma_f32_16x16x32_bf16 a[196:199], v[112:115], v[170:173], a[196:199]\n"
    "ds_read_b64_tr_b16 v[122:123], v252 offset:54784\n"
    "s_add_u32 s65, s79, 36864\n"
    "v_permlane16_swap_b32 v185, v189\n"
    "v_mfma_f32_16x16x32_bf16 a[200:203], v[112:115], v[174:177], a[200:203]\n"
    "buffer_load_dwordx4 v240, s[48:51], s65 offen offset:0 lds\n"
    "v_mfma_f32_16x16x32_bf16 a[204:207], v[112:115], v[178:181], a[204:207]\n"
    "s_add_u32 s60, s72, 49152\n"
    "buffer_store_dwordx4 v[182:185], v241, s[56:59], s60 offen offset:0 sc1\n"
    "s_waitcnt lgkmcnt(2)\n"
    "v_mfma_f32_16x16x32_bf16 a[208:211], v[116:119], v[166:169], a[208:211]\n"
    "ds_read_b64_tr_b16 v[112:113], v253 offset:53760\n"
    "s_add_u32 s61, s74, 0\n"
    "buffer_load_dwordx4 v[150:153], v241, s[56:59], s61 offen offset:0 sc1\n"
    "v_mfma_f32_16x16x32_bf16 a[212:215], v[116:119], v[170:173], a[212:215]\n"
    "ds_read_b64_tr_b16 v[114:115], v253 offset:54784\n"
    "buffer_store_dwordx4 v[186:189], v241, s[56:59], s60 offen offset:1024 sc1\n"
    "v_mfma_f32_16x16x32_bf16 a[216:219], v[116:119], v[174:177], a[216:219]\n"
    "v_mul_f32 v124, s80, v80\n"
    "v_exp_f32 v124, v124\n"
    "v_mul_f32 v125, s80, v81\n"
    "v_mfma_f32_16x16x32_bf16 a[220:223], v[116:119], v[178:181], a[220:223]\n"
    "v_add_f32 v124, 1.0, v124\n"
    "s_waitcnt lgkmcnt(2)\n"
    "v_mfma_f32_16x16x32_bf16 a[224:227], v[120:123], v[166:169], a[224:227]\n"
    "ds_read_b64_tr_b16 v[116:117], v252 offset:61440\n"
    "s_add_u32 m0, s64, 73728\n"
    "v_exp_f32 v125, v125\n"
    "s_add_u32 s61, s74, 0\n"
    "buffer_load_dwordx4 v[154:157], v241, s[56:59], s61 offen offset:1024 sc1\n"
    "v_mfma_f32_16x16x32_bf16 a[228:231], v[120:123], v[170:173], a[228:231]\n"
    "ds_read_b64_tr_b16 v[118:119], v252 offset:62464\n"
    "s_add_u32 s65, s27, 0\n"
    "v_mfma_f32_16x16x32_bf16 a[232:235], v[120:123], v[174:177], a[232:235]\n"
    "buffer_load_dwordx4 v240, s[48:51], s65 offen offset:0 lds\n"
    "v_mfma_f32_16x16x32_bf16 a[236:239], v[120:123], v[178:181], a[236:239]\n"
    "v_mul_f32 v126, s80, v82\n"
    "v_fma_f32 v124, v124, v124, 1.0\n"
    "s_waitcnt lgkmcnt(2)\n"
    "v_mfma_f32_16x16x32_bf16 a[240:243], v[112:115], v[166:169], a[240:243]\n"
    "ds_read_b64_tr_b16 v[120:121], v253 offset:61440\n"
    "v_add_f32 v125, 1.0, v125\n"
    "v_exp_f32 v126, v126\n"
    "v_mfma_f32_16x16x32_bf16 a[244:247], v[112:115], v[170:173], a[244:247]\n"
    "ds_read_b64_tr_b16 v[122:123], v253 offset:62464\n"
    "v_mul_f32 v127, s80, v83\n"
    "v_mfma_f32_16x16x32_bf16 a[248:251], v[112:115], v[174:177], a[248:251]\n"
    "v_rcp_f32 v124, v124\n"
    "v_fma_f32 v125, v125, v125, 1.0\n"
    "v_add_f32 v126, 1.0, v126\n"
    "s_add_u32 s61, s74, 0\n"
    "buffer_load_dwordx4 v[158:161], v241, s[56:59], s61 offen offset:2048 sc1\n"
    "v_mfma_f32_16x16x32_bf16 a[252:255], v[112:115], v[178:181], a[252:255]\n"
    "v_exp_f32 v127, v127\n"
    "v_mul_f32 v128, s80, v84\n"
    "s_waitcnt lgkmcnt(2)\n"
    "v_mfma_f32_16x16x32_bf16 a[64:67], v[116:119], v[166:169], a[64:67]\n"
    "ds_read_b64_tr_b16 v[112:113], v252 offset:61952\n"
    "s_add_u32 m0, s64, 77824\n"
    "v_mfma_f32_16x16x32_bf16 a[68:71], v[116:119], v[170:173], a[68:71]\n"
    "ds_read_b64_tr_b16 v[114:115], v252 offset:62976\n"
    "s_add_u32 s65, s27, 36864\n"
    "v_mfma_f32_16x16x32_bf16 a[72:75], v[116:119], v[174:177], a[72:75]\n"
    "buffer_load_dwordx4 v240, s[48:51], s65 offen offset:0 lds\n"
    "v_fma_f32 v124, v124, v248, v246\n"
    "v_mfma_f32_16x16x32_bf16 a[76:79], v[116:119], v[178:181], a[76:79]\n"
    "v_rcp_f32 v125, v125\n"
    "s_waitcnt lgkmcnt(2)\n"
    "v_mfma_f32_16x16x32_bf16 a[80:83], v[120:123], v[166:169], a[80:83]\n"
    "ds_read_b64_tr_b16 v[116:117], v253 offset:61952\n"
    "v_fma_f32 v126, v126, v126, 1.0\n"
    "v_add_f32 v127, 1.0, v127\n"
    "s_add_u32 s61, s74, 0\n"
    "buffer_load_dwordx4 v[162:165], v241, s[56:59], s61 offen offset:3072 sc1\n"
    "v_mfma_f32_16x16x32_bf16 a[84:87], v[120:123], v[170:173], a[84:87]\n"
    "ds_read_b64_tr_b16 v[118:119], v253 offset:62976\n"
    "v_exp_f32 v128, v128\n"
    "v_mfma_f32_16x16x32_bf16 a[88:91], v[120:123], v[174:177], a[88:91]\n"
    "v_mul_f32 v129, s80, v85\n"
    "v_mul_f32 v80, v124, v80\n"
    "v_fma_f32 v125, v125, v248, v246\n"
    "v_rcp_f32 v126, v126\n"
    "v_mfma_f32_16x16x32_bf16 a[92:95], v[120:123], v[178:181], a[92:95]\n"
    "v_fma_f32 v127, v127, v127, 1.0\n"
    "v_add_f32 v128, 1.0, v128\n"
    "s_waitcnt lgkmcnt(2)\n"
    "v_mfma_f32_16x16x32_bf16 a[96:99], v[112:115], v[166:169], a[96:99]\n"
    "ds_read_b64_tr_b16 v[120:121], v254 offset:4096\n"
    "s_add_u32 m0, s64, 81920\n"
    "v_mfma_f32_16x16x32_bf16 a[100:103], v[112:115], v[170:173], a[100:103]\n"
    "ds_read_b64_tr_b16 v[122:123], v254 offset:5120\n"
    "s_add_u32 s65, s79, 73728\n"
    "v_mfma_f32_16x16x32_bf16 a[104:107], v[112:115], v[174:177], a[104:107]\n"
    "buffer_load_dwordx4 v240, s[48:51], s65 offen offset:0 lds\n"
    "v_mfma_f32_16x16x32_bf16 a[108:111], v[112:115], v[178:181], a[108:111]\n"
    "v_exp_f32 v129, v129\n"
    "v_mul_f32 v130, s80, v86\n"
    "s_waitcnt lgkmcnt(2)\n"
    "v_mfma_f32_16x16x32_bf16 a[112:115], v[116:119], v[166:169], a[112:115]\n"
    "ds_read_b64_tr_b16 v[112:113], v255 offset:4096\n"
    "v_mul_f32 v81, v125, v81\n"
    "v_fma_f32 v126, v126, v248, v246\n"
    "v_mfma_f32_16x16x32_bf16 a[116:119], v[116:119], v[170:173], a[116:119]\n"
    "ds_read_b64_tr_b16 v[114:115], v255 offset:5120\n"
    "v_rcp_f32 v127, v127\n"
    "v_mfma_f32_16x16x32_bf16 a[120:123], v[116:119], v[174:177], a[120:123]\n"
    "v_fma_f32 v128, v128, v128, 1.0\n"
    "v_add_f32 v129, 1.0, v129\n"
    "v_exp_f32 v130, v130\n"
    "v_mfma_f32_16x16x32_bf16 a[124:127], v[116:119], v[178:181], a[124:127]\n"
    "v_mul_f32 v124, s80, v87\n"
    "v_cvt_pk_bf16_f32 v190, v80, v81\n"
    "v_mul_f32 v82, v126, v82\n"
    "s_waitcnt lgkmcnt(2)\n"
    "v_mfma_f32_16x16x32_bf16 a[128:131], v[120:123], v[166:169], a[128:131]\n"
    "ds_read_b64_tr_b16 v[116:117], v254 offset:4608\n"
    "s_add_u32 m0, s64, 86016\n"
    "v_mfma_f32_16x16x32_bf16 a[132:135], v[120:123], v[170:173], a[132:135]\n"
    "ds_read_b64_tr_b16 v[118:119], v254 offset:5632\n"
    "s_add_u32 s65, s79, 110592\n"
    "v_mfma_f32_16x16x32_bf16 a[136:139], v[120:123], v[174:177], a[136:139]\n"
    "buffer_load_dwordx4 v240, s[48:51], s65 offen offset:0 lds\n"
    "v_fma_f32 v127, v127, v248, v246\n"
    "v_mfma_f32_16x16x32_bf16 a[140:143], v[120:123], v[178:181], a[140:143]\n"
    "v_rcp_f32 v128, v128\n"
    "s_waitcnt lgkmcnt(2)\n"
    "v_mfma_f32_16x16x32_bf16 a[144:147], v[112:115], v[166:169], a[144:147]\n"
    "ds_read_b64_tr_b16 v[120:121], v255 offset:4608\n"
    "v_fma_f32 v129, v129, v129, 1.0\n"
    "v_add_f32 v130, 1.0, v130\n"
    "v_mfma_f32_16x16x32_bf16 a[148:151], v[112:115], v[170:173], a[148:151]\n"
    "ds_read_b64_tr_b16 v[122:123], v255 offset:5632\n"
    "v_exp_f32 v124, v124\n"
    "v_mfma_f32_16x16x32_bf16 a[152:155], v[112:115], v[174:177], a[152:155]\n"
    "v_mul_f32 v125, s80, v88\n"
    "v_mul_f32 v83, v127, v83\n"
    "v_fma_f32 v128, v128, v248, v246\n"
    "v_rcp_f32 v129, v129\n"
    "s_add_u32 s61, s75, 0\n"
    "buffer_load_dwordx4 v[16:19], v241, s[56:59], s61 offen offset:0 sc1\n"
    "v_mfma_f32_16x16x32_bf16 a[156:159], v[112:115], v[178:181], a[156:159]\n"
    "v_fma_f32 v130, v130, v130, 1.0\n"
    "s_waitcnt lgkmcnt(2)\n"
    "v_mfma_f32_16x16x32_bf16 a[160:163], v[116:119], v[166:169], a[160:163]\n"
    "s_barrier\n"
    "ds_read_b64_tr_b16 v[112:113], v250 offset:0\n"
    "v_add_f32 v124, 1.0, v124\n"
    "v_mfma_f32_16x16x32_bf16 a[164:167], v[116:119], v[170:173], a[164:167]\n"
    "ds_read_b64_tr_b16 v[114:115], v250 offset:1024\n"
    "s_add_u32 m0, s64, 90112\n"
    "v_mfma_f32_16x16x32_bf16 a[168:171], v[116:119], v[174:177], a[168:171]\n"
    "s_add_u32 s65, s27, 73728\n"
    "v_exp_f32 v125, v125\n"
    "v_mfma_f32_16x16x32_bf16 a[172:175], v[116:119], v[178:181], a[172:175]\n"
    "buffer_load_dwordx4 v240, s[48:51], s65 offen offset:0 lds\n"
    "v_mul_f32 v126, s80, v89\n"
    "s_add_u32 s61, s75, 0\n"
    "buffer_load_dwordx4 v[20:23], v241, s[56:59], s61 offen offset:1024 sc1\n"
    "s_waitcnt lgkmcnt(2)\n"
    "v_mfma_f32_16x16x32_bf16 a[176:179], v[120:123], v[166:169], a[176:179]\n"
    "ds_read_b64_tr_b16 v[116:117], v251 offset:0\n"
    "v_cvt_pk_bf16_f32 v191, v82, v83\n"
    "v_mul_f32 v84, v128, v84\n"
    "v_mfma_f32_16x16x32_bf16 a[180:183], v[120:123], v[170:173], a[180:183]\n"
    "ds_read_b64_tr_b16 v[118:119], v251 offset:1024\n"
    "v_fma_f32 v129, v129, v248, v246\n"
    "v_rcp_f32 v130, v130\n"
    "v_mfma_f32_16x16x32_bf16 a[184:187], v[120:123], v[174:177], a[184:187]\n"
    "v_fma_f32 v124, v124, v124, 1.0\n"
    "v_add_f32 v125, 1.0, v125\n"
    "v_exp_f32 v126, v126\n"
    "v_mfma_f32_16x16x32_bf16 a[188:191], v[120:123], v[178:181], a[188:191]\n"
    "v_mul_f32 v127, s80, v90\n"
    "s_waitcnt lgkmcnt(2)\n"
    "v_mfma_f32_16x16x32_bf16 a[192:195], v[112:115], v[0:3], a[192:195]\n"
    "ds_read_b64_tr_b16 v[120:121], v250 offset:512\n"
    "s_add_u32 m0, s64, 94208\n"
    "v_mul_f32 v85, v129, v85\n"
    "s_add_u32 s61, s75, 0\n"
    "buffer_load_dwordx4 v[24:27], v241, s[56:59], s61 offen offset:2048 sc1\n"
    "v_mfma_f32_16x16x32_bf16 a[196:199], v[112:115], v[4:7], a[196:199]\n"
    "ds_read_b64_tr_b16 v[122:123], v250 offset:1536\n"
    "s_add_u32 s65, s27, 110592\n"
    "v_mfma_f32_16x16x32_bf16 a[200:203], v[112:115], v[8:11], a[200:203]\n"
    "buffer_load_dwordx4 v240, s[48:51], s65 offen offset:0 lds\n"
    "s_waitcnt vmcnt(30)\n"
    "v_mfma_f32_16x16x32_bf16 a[204:207], v[112:115], v[12:15], a[204:207]\n"
    "v_fma_f32 v130, v130, v248, v246\n"
    "v_rcp_f32 v124, v124\n"
    "s_waitcnt lgkmcnt(2)\n"
    "v_mfma_f32_16x16x32_bf16 a[208:211], v[116:119], v[0:3], a[208:211]\n"
    "ds_read_b64_tr_b16 v[112:113], v251 offset:512\n"
    "v_fma_f32 v125, v125, v125, 1.0\n"
    "v_add_f32 v126, 1.0, v126\n"
    "v_mfma_f32_16x16x32_bf16 a[212:215], v[116:119], v[4:7], a[212:215]\n"
    "ds_read_b64_tr_b16 v[114:115], v251 offset:1536\n"
    "v_exp_f32 v127, v127\n"
    "s_add_u32 s61, s75, 0\n"
    "buffer_load_dwordx4 v[28:31], v241, s[56:59], s61 offen offset:3072 sc1\n"
    "v_mfma_f32_16x16x32_bf16 a[216:219], v[116:119], v[8:11], a[216:219]\n"
    "v_mul_f32 v128, s80, v91\n"
    "v_cvt_pk_bf16_f32 v192, v84, v85\n"
    "v_mul_f32 v86, v130, v86\n"
    "v_fma_f32 v124, v124, v248, v246\n"
    "v_mfma_f32_16x16x32_bf16 a[220:223], v[116:119], v[12:15], a[220:223]\n"
    "v_rcp_f32 v125, v125\n"
    "s_waitcnt lgkmcnt(2)\n"
    "v_mfma_f32_16x16x32_bf16 a[224:227], v[120:123], v[0:3], a[224:227]\n"
    "ds_read_b64_tr_b16 v[116:117], v250 offset:8192\n"
    "s_add_u32 m0, s64, 98304\n"
    "v_fma_f32 v126, v126, v126, 1.0\n"
    "v_mfma_f32_16x16x32_bf16 a[228:231], v[120:123], v[4:7], a[228:231]\n"
    "ds_read_b64_tr_b16 v[118:119], v250 offset:9216\n"
    "s_add_u32 s65, s82, 147456\n"
    "v_mfma_f32_16x16x32_bf16 a[232:235], v[120:123], v[8:11], a[232:235]\n"
    "buffer_load_dwordx4 v240, s[48:51], s65 offen offset:0 lds\n"
    "v_mfma_f32_16x16x32_bf16 a[236:239], v[120:123], v[12:15], a[236:239]\n"
    "v_add_f32 v127, 1.0, v127\n"
    "v_exp_f32 v128, v128\n"
    "s_waitcnt lgkmcnt(2)\n"
    "v_mfma_f32_16x16x32_bf16 a[240:243], v[112:115], v[0:3], a[240:243]\n"
    "ds_read_b64_tr_b16 v[120:121], v251 offset:8192\n"
    "v_mul_f32 v129, s80, v92\n"
    "v_mul_f32 v87, v124, v87\n"
    "v_mfma_f32_16x16x32_bf16 a[244:247], v[112:115], v[4:7], a[244:247]\n"
    "ds_read_b64_tr_b16 v[122:123], v251 offset:9216\n"
    "v_fma_f32 v125, v125, v248, v246\n"
    "v_mfma_f32_16x16x32_bf16 a[248:251], v[112:115], v[8:11], a[248:251]\n"
    "v_rcp_f32 v126, v126\n"
    "v_fma_f32 v127, v127, v127, 1.0\n"
    "v_add_f32 v128, 1.0, v128\n"
    "v_mfma_f32_16x16x32_bf16 a[252:255], v[112:115], v[12:15], a[252:255]\n"
    "v_exp_f32 v129, v129\n"
    "v_mul_f32 v130, s80, v93\n"
    "s_waitcnt lgkmcnt(2)\n"
    "v_mfma_f32_16x16x32_bf16 a[64:67], v[116:119], v[0:3], a[64:67]\n"
    "ds_read_b64_tr_b16 v[112:113], v250 offset:8704\n"
    "s_add_u32 m0, s64, 102400\n"
    "v_mfma_f32_16x16x32_bf16 a[68:71], v[116:119], v[4:7], a[68:71]\n"
    "ds_read_b64_tr_b16 v[114:115], v250 offset:9728\n"
    "s_add_u32 s65, s82, 184320\n"
    "v_cvt_pk_bf16_f32 v193, v86, v87\n"
    "v_mfma_f32_16x16x32_bf16 a[72:75], v[116:119], v[8:11], a[72:75]\n"
    "buffer_load_dwordx4 v240, s[48:51], s65 offen offset:0 lds\n"
    "v_mfma_f32_16x16x32_bf16 a[76:79], v[116:119], v[12:15], a[76:79]\n"
    "v_mul_f32 v88, v125, v88\n"
    "v_fma_f32 v126, v126, v248, v246\n"
    "s_waitcnt lgkmcnt(2)\n"
    "v_mfma_f32_16x16x32_bf16 a[80:83], v[120:123], v[0:3], a[80:83]\n"
    "ds_read_b64_tr_b16 v[116:117], v251 offset:8704\n"
    "v_rcp_f32 v127, v127\n"
    "v_fma_f32 v128, v128, v128, 1.0\n"
    "v_mfma_f32_16x16x32_bf16 a[84:87], v[120:123], v[4:7], a[84:87]\n"
    "ds_read_b64_tr_b16 v[118:119], v251 offset:9728\n"
    "v_add_f32 v129, 1.0, v129\n"
    "v_mfma_f32_16x16x32_bf16 a[88:91], v[120:123], v[8:11], a[88:91]\n"
    "v_exp_f32 v130, v130\n"
    "v_mul_f32 v124, s80, v94\n"
    "v_mul_f32 v89, v126, v89\n"
    "v_fma_f32 v127, v127, v248, v246\n"
    "s_add_u32 s61, s75, 16384\n"
    "buffer_load_dwordx4 v[166:169], v241, s[56:59], s61 offen offset:0 sc1\n"
    "v_mfma_f32_16x16x32_bf16 a[92:95], v[120:123], v[12:15], a[92:95]\n"
    "v_rcp_f32 v128, v128\n"
    "s_waitcnt lgkmcnt(2)\n"
    "v_mfma_f32_16x16x32_bf16 a[96:99], v[112:115], v[0:3], a[96:99]\n"
    "ds_read_b64_tr_b16 v[120:121], v250 offset:16384\n"
    "s_add_u32 m0, s64, 106496\n"
    "v_mfma_f32_16x16x32_bf16 a[100:103], v[112:115], v[4:7], a[100:103]\n"
    "ds_read_b64_tr_b16 v[122:123], v250 offset:17408\n"
    "s_add_u32 s65, s77, 147456\n"
    "v_fma_f32 v129, v129, v129, 1.0\n"
    "v_mfma_f32_16x16x32_bf16 a[104:107], v[112:115], v[8:11], a[104:107]\n"
    "buffer_load_dwordx4 v240, s[48:51], s65 offen offset:0 lds\n"
    "v_mfma_f32_16x16x32_bf16 a[108:111], v[112:115], v[12:15], a[108:111]\n"
    "v_add_f32 v130, 1.0, v130\n"
    "v_exp_f32 v124, v124\n"
    "s_add_u32 s61, s75, 16384\n"
    "buffer_load_dwordx4 v[170:173], v241, s[56:59], s61 offen offset:1024 sc1\n"
    "s_waitcnt lgkmcnt(2)\n"
    "v_mfma_f32_16x16x32_bf16 a[112:115], v[116:119], v[0:3], a[112:115]\n"
    "ds_read_b64_tr_b16 v[112:113], v251 offset:16384\n"
    "v_mul_f32 v125, s80, v95\n"
    "v_mfma_f32_16x16x32_bf16 a[116:119], v[116:119], v[4:7], a[116:119]\n"
    "ds_read_b64_tr_b16 v[114:115], v251 offset:17408\n"
    "v_cvt_pk_bf16_f32 v194, v88, v89\n"
    "v_mul_f32 v90, v127, v90\n"
    "v_fma_f32 v128, v128, v248, v246\n"
    "v_mfma_f32_16x16x32_bf16 a[120:123], v[116:119], v[8:11], a[120:123]\n"
    "v_rcp_f32 v129, v129\n"
    "v_fma_f32 v130, v130, v130, 1.0\n"
    "v_add_f32 v124, 1.0, v124\n"
    "v_mfma_f32_16x16x32_bf16 a[124:127], v[116:119], v[12:15], a[124:127]\n"
    "v_exp_f32 v125, v125\n"
    "s_waitcnt lgkmcnt(2)\n"
    "v_mfma_f32_16x16x32_bf16 a[128:131], v[120:123], v[0:3], a[128:131]\n"
    "ds_read_b64_tr_b16 v[116:117], v250 offset:16896\n"
    "s_add_u32 m0, s64, 110592\n"
    "s_add_u32 s61, s75, 16384\n"
    "buffer_load_dwordx4 v[174:177], v241, s[56:59], s61 offen offset:2048 sc1\n"
    "v_mfma_f32_16x16x32_bf16 a[132:135], v[120:123], v[4:7], a[132:135]\n"
    "ds_read_b64_tr_b16 v[118:119], v250 offset:17920\n"
    "s_add_u32 s65, s77, 184320\n"
    "v_mul_f32 v91, v128, v91\n"
    "v_mfma_f32_16x16x32_bf16 a[136:139], v[120:123], v[8:11], a[136:139]\n"
    "buffer_load_dwordx4 v240, s[48:51], s65 offen offset:0 lds\n"
    "v_mfma_f32_16x16x32_bf16 a[140:143], v[120:123], v[12:15], a[140:143]\n"
    "v_fma_f32 v129, v129, v248, v246\n"
    "v_rcp_f32 v130, v130\n"
    "s_waitcnt lgkmcnt(2)\n"
    "v_mfma_f32_16x16x32_bf16 a[144:147], v[112:115], v[0:3], a[144:147]\n"
    "ds_read_b64_tr_b16 v[120:121], v251 offset:16896\n"
    "v_fma_f32 v124, v124, v124, 1.0\n"
    "v_bfe_u32 v243, v241, 4, 2\n"
    "v_max_u32 v243, 1, v243\n"
    "v_xor_b32 v243, s69, v243\n"
    "v_lshl_add_u32 v243, v243, 6, s22\n"
    "buffer_load_dwordx2 v[234:235], v243, s[56:59], 0 offen offset:0 sc1\n"
    "v_mfma_f32_16x16x32_bf16 a[148:151], v[112:115], v[4:7], a[148:151]\n"
    "ds_read_b64_tr_b16 v[122:123], v251 offset:17920\n"
    "v_add_f32 v125, 1.0, v125\n"
    "v_cvt_pk_bf16_f32 v195, v90, v91\n"
    "v_mul_f32 v92, v129, v92\n"
    "s_add_u32 s61, s75, 16384\n"
    "buffer_load_dwordx4 v[178:181], v241, s[56:59], s61 offen offset:3072 sc1\n"
    "v_mfma_f32_16x16x32_bf16 a[152:155], v[112:115], v[8:11], a[152:155]\n"
    "v_fma_f32 v130, v130, v248, v246\n"
    "v_rcp_f32 v124, v124\n"
    "v_fma_f32 v125, v125, v125, 1.0\n"
    "v_mfma_f32_16x16x32_bf16 a[156:159], v[112:115], v[12:15], a[156:159]\n"
    "v_mul_f32 v93, v130, v93\n"
    "v_fma_f32 v124, v124, v248, v246\n"
    "s_waitcnt lgkmcnt(2)\n"
    "v_mfma_f32_16x16x32_bf16 a[160:163], v[116:119], v[0:3], a[160:163]\n"
    "s_waitcnt vmcnt(36)\n"
    "s_barrier\n"
    "ds_read_b64_tr_b16 v[112:113], v250 offset:24576\n"
    "v_mfma_f32_16x16x32_bf16 a[164:167], v[116:119], v[4:7], a[164:167]\n"
    "ds_read_b64_tr_b16 v[114:115], v250 offset:25600\n"
    "s_add_u32 m0, s64, 114688\n"
    "v_mfma_f32_16x16x32_bf16 a[168:171], v[116:119], v[8:11], a[168:171]\n"
    "s_add_u32 s65, s25, 147456\n"
    "v_rcp_f32 v125, v125\n"
    "v_cvt_pk_bf16_f32 v196, v92, v93\n"
    "v_mfma_f32_16x16x32_bf16 a[172:175], v[116:119], v[12:15], a[172:175]\n"
    "buffer_load_dwordx4 v240, s[48:51], s65 offen offset:0 lds\n"
    "s_waitcnt lgkmcnt(2)\n"
    "v_mfma_f32_16x16x32_bf16 a[176:179], v[120:123], v[0:3], a[176:179]\n"
    "ds_read_b64_tr_b16 v[116:117], v251 offset:24576\n"
    "v_mul_f32 v94, v124, v94\n"
    "v_fma_f32 v125, v125, v248, v246\n"
    "v_mfma_f32_16x16x32_bf16 a[180:183], v[120:123], v[4:7], a[180:183]\n"
    "ds_read_b64_tr_b16 v[118:119], v251 offset:25600\n"
    "v_mul_f32 v95, v125, v95\n"
    "v_cvt_pk_bf16_f32 v197, v94, v95\n"
    "v_mfma_f32_16x16x32_bf16 a[184:187], v[120:123], v[8:11], a[184:187]\n"
    "s_add_u32 m0, s64, 118784\n"
    "v_permlane32_swap_b32 v190, v194\n"
    "v_permlane32_swap_b32 v191, v195\n"
    "v_permlane32_swap_b32 v192, v196\n"
    "v_mfma_f32_16x16x32_bf16 a[188:191], v[120:123], v[12:15], a[188:191]\n"
    "s_add_u32 s65, s25, 184320\n"
    "s_waitcnt vmcnt(36) lgkmcnt(2)\n"
    "v_mfma_f32_16x16x32_bf16 a[192:195], v[112:115], v[134:137], a[192:195]\n"
    "ds_read_b64_tr_b16 v[120:121], v250 offset:25088\n"
    "buffer_load_dwordx4 v240, s[48:51], s65 offen offset:0 lds\n"
    "s_waitcnt vmcnt(35)\n"
    "v_mfma_f32_16x16x32_bf16 a[196:199], v[112:115], v[138:141], a[196:199]\n"
    "ds_read_b64_tr_b16 v[122:123], v250 offset:26112\n"
    "v_permlane32_swap_b32 v193, v197\n"
    "v_permlane16_swap_b32 v190, v194\n"
    "v_permlane16_swap_b32 v191, v195\n"
    "s_waitcnt vmcnt(34)\n"
    "v_mfma_f32_16x16x32_bf16 a[200:203], v[112:115], v[142:145], a[200:203]\n"
    "v_permlane16_swap_b32 v192, v196\n"
    "v_permlane16_swap_b32 v193, v197\n"
    "s_waitcnt vmcnt(32)\n"
    "v_mfma_f32_16x16x32_bf16 a[204:207], v[112:115], v[146:149], a[204:207]\n"
    "s_add_u32 s60, s72, 49152\n"
    "buffer_store_dwordx4 v[190:193], v241, s[56:59], s60 offen offset:2048 sc1\n"
    "s_waitcnt lgkmcnt(2)\n"
    "v_mfma_f32_16x16x32_bf16 a[208:211], v[116:119], v[134:137], a[208:211]\n"
    "ds_read_b64_tr_b16 v[112:113], v251 offset:25088\n"
    "s_add_u32 m0, s64, 122880\n"
    "v_mfma_f32_16x16x32_bf16 a[212:215], v[116:119], v[138:141], a[212:215]\n"
    "ds_read_b64_tr_b16 v[114:115], v251 offset:26112\n"
    "s_add_u32 s65, s78, 73728\n"
    "v_mfma_f32_16x16x32_bf16 a[216:219], v[116:119], v[142:145], a[216:219]\n"
    "buffer_load_dwordx4 v240, s[48:51], s65 offen offset:0 lds\n"
    "v_mfma_f32_16x16x32_bf16 a[220:223], v[116:119], v[146:149], a[220:223]\n"
    "buffer_store_dwordx4 v[194:197], v241, s[56:59], s60 offen offset:3072 sc1\n"
    "s_waitcnt lgkmcnt(2)\n"
    "v_mfma_f32_16x16x32_bf16 a[224:227], v[120:123], v[134:137], a[224:227]\n"
    "ds_read_b64_tr_b16 v[116:117], v250 offset:32768\n"
    "s_add_u32 s61, s73, 32768\n"
    "buffer_load_dwordx4 v[0:3], v241, s[56:59], s61 offen offset:0 sc1\n"
    "v_mfma_f32_16x16x32_bf16 a[228:231], v[120:123], v[138:141], a[228:231]\n"
    "ds_read_b64_tr_b16 v[118:119], v250 offset:33792\n"
    "v_mfma_f32_16x16x32_bf16 a[232:235], v[120:123], v[142:145], a[232:235]\n"
    "s_add_u32 m0, s64, 126976\n"
    "v_mfma_f32_16x16x32_bf16 a[236:239], v[120:123], v[146:149], a[236:239]\n"
    "s_add_u32 s65, s78, 110592\n"
    "s_waitcnt lgkmcnt(2)\n"
    "v_mfma_f32_16x16x32_bf16 a[240:243], v[112:115], v[134:137], a[240:243]\n"
    "ds_read_b64_tr_b16 v[120:121], v251 offset:32768\n"
    "buffer_load_dwordx4 v240, s[48:51], s65 offen offset:0 lds\n"
    "v_mfma_f32_16x16x32_bf16 a[244:247], v[112:115], v[138:141], a[244:247]\n"
    "ds_read_b64_tr_b16 v[122:123], v251 offset:33792\n"
    "s_add_u32 s61, s73, 32768\n"
    "buffer_load_dwordx4 v[4:7], v241, s[56:59], s61 offen offset:1024 sc1\n"
    "v_mfma_f32_16x16x32_bf16 a[248:251], v[112:115], v[142:145], a[248:251]\n"
    "v_mfma_f32_16x16x32_bf16 a[252:255], v[112:115], v[146:149], a[252:255]\n"
    "s_add_u32 s61, s73, 32768\n"
    "buffer_load_dwordx4 v[8:11], v241, s[56:59], s61 offen offset:2048 sc1\n"
    "s_waitcnt lgkmcnt(2)\n"
    "v_mfma_f32_16x16x32_bf16 a[64:67], v[116:119], v[134:137], a[64:67]\n"
    "ds_read_b64_tr_b16 v[112:113], v250 offset:33280\n"
    "s_add_u32 m0, s64, 131072\n"
    "v_mfma_f32_16x16x32_bf16 a[68:71], v[116:119], v[138:141], a[68:71]\n"
    "ds_read_b64_tr_b16 v[114:115], v250 offset:34304\n"
    "s_add_u32 s65, s26, 73728\n"
    "v_mfma_f32_16x16x32_bf16 a[72:75], v[116:119], v[142:145], a[72:75]\n"
    "buffer_load_dwordx4 v240, s[48:51], s65 offen offset:0 lds\n"
    "v_mfma_f32_16x16x32_bf16 a[76:79], v[116:119], v[146:149], a[76:79]\n"
    "s_waitcnt lgkmcnt(2)\n"
    "v_mfma_f32_16x16x32_bf16 a[80:83], v[120:123], v[134:137], a[80:83]\n"
    "ds_read_b64_tr_b16 v[116:117], v251 offset:33280\n"
    "v_mfma_f32_16x16x32_bf16 a[84:87], v[120:123], v[138:141], a[84:87]\n"
    "ds_read_b64_tr_b16 v[118:119], v251 offset:34304\n"
    "s_add_u32 s61, s73, 32768\n"
    "buffer_load_dwordx4 v[12:15], v241, s[56:59], s61 offen offset:3072 sc1\n"
    "v_mfma_f32_16x16x32_bf16 a[88:91], v[120:123], v[142:145], a[88:91]\n"
    "s_add_u32 m0, s64, 135168\n"
    "v_mfma_f32_16x16x32_bf16 a[92:95], v[120:123], v[146:149], a[92:95]\n"
    "s_add_u32 s65, s26, 110592\n"
    "s_waitcnt lgkmcnt(2)\n"
    "v_mfma_f32_16x16x32_bf16 a[96:99], v[112:115], v[134:137], a[96:99]\n"
    "ds_read_b64_tr_b16 v[120:121], v250 offset:40960\n"
    "buffer_load_dwordx4 v240, s[48:51], s65 offen offset:0 lds\n"
    "v_mfma_f32_16x16x32_bf16 a[100:103], v[112:115], v[138:141], a[100:103]\n"
    "ds_read_b64_tr_b16 v[122:123], v250 offset:41984\n"
    "v_mfma_f32_16x16x32_bf16 a[104:107], v[112:115], v[142:145], a[104:107]\n"
    "v_mfma_f32_16x16x32_bf16 a[108:111], v[112:115], v[146:149], a[108:111]\n"
    "s_waitcnt lgkmcnt(2)\n"
    "v_mfma_f32_16x16x32_bf16 a[112:115], v[116:119], v[134:137], a[112:115]\n"
    "ds_read_b64_tr_b16 v[112:113], v251 offset:40960\n"
    "s_add_u32 m0, s64, 139264\n"
    "v_mfma_f32_16x16x32_bf16 a[116:119], v[116:119], v[138:141], a[116:119]\n"
    "ds_read_b64_tr_b16 v[114:115], v251 offset:41984\n"
    "s_add_u32 s65, s79, 147456\n"
    "v_mfma_f32_16x16x32_bf16 a[120:123], v[116:119], v[142:145], a[120:123]\n"
    "buffer_load_dwordx4 v240, s[48:51], s65 offen offset:0 lds\n"
    "v_mfma_f32_16x16x32_bf16 a[124:127], v[116:119], v[146:149], a[124:127]\n"
    "s_waitcnt lgkmcnt(2)\n"
    "v_mfma_f32_16x16x32_bf16 a[128:131], v[120:123], v[134:137], a[128:131]\n"
    "ds_read_b64_tr_b16 v[116:117], v250 offset:41472\n"
    "v_mfma_f32_16x16x32_bf16 a[132:135], v[120:123], v[138:141], a[132:135]\n"
    "ds_read_b64_tr_b16 v[118:119], v250 offset:42496\n"
    "v_mfma_f32_16x16x32_bf16 a[136:139], v[120:123], v[142:145], a[136:139]\n"
    "s_add_u32 m0, s64, 143360\n"
    "v_mfma_f32_16x16x32_bf16 a[140:143], v[120:123], v[146:149], a[140:143]\n"
    "s_add_u32 s65, s79, 184320\n"
    "s_waitcnt lgkmcnt(2)\n"
    "v_mfma_f32_16x16x32_bf16 a[144:147], v[112:115], v[134:137], a[144:147]\n"
    "ds_read_b64_tr_b16 v[120:121], v251 offset:41472\n"
    "buffer_load_dwordx4 v240, s[48:51], s65 offen offset:0 lds\n"
    "v_mfma_f32_16x16x32_bf16 a[148:151], v[112:115], v[138:141], a[148:151]\n"
    "ds_read_b64_tr_b16 v[122:123], v251 offset:42496\n"
    "v_mfma_f32_16x16x32_bf16 a[152:155], v[112:115], v[142:145], a[152:155]\n"
    "v_mfma_f32_16x16x32_bf16 a[156:159], v[112:115], v[146:149], a[156:159]\n"
    "s_waitcnt lgkmcnt(2)\n"
    "v_mfma_f32_16x16x32_bf16 a[160:163], v[116:119], v[134:137], a[160:163]\n"
    "s_waitcnt vmcnt(39)\n"
    "s_barrier\n"
    "ds_read_b64_tr_b16 v[112:113], v250 offset:49152\n"
    "v_mfma_f32_16x16x32_bf16 a[164:167], v[116:119], v[138:141], a[164:167]\n"
    "ds_read_b64_tr_b16 v[114:115], v250 offset:50176\n"
    "s_add_u32 m0, s64, 0\n"
    "v_mfma_f32_16x16x32_bf16 a[168:171], v[116:119], v[142:145], a[168:171]\n"
    "s_add_u32 s65, s27, 147456\n"
    "v_mfma_f32_16x16x32_bf16 a[172:175], v[116:119], v[146:149], a[172:175]\n"
    "buffer_load_dwordx4 v240, s[48:51], s65 offen offset:0 lds\n"
    "s_waitcnt lgkmcnt(2)\n"
    "v_mfma_f32_16x16x32_bf16 a[176:179], v[120:123], v[134:137], a[176:179]\n"
    "ds_read_b64_tr_b16 v[116:117], v251 offset:49152\n"
    "v_mfma_f32_16x16x32_bf16 a[180:183], v[120:123], v[138:141], a[180:183]\n"
    "ds_read_b64_tr_b16 v[118:119], v251 offset:50176\n"
    "v_mfma_f32_16x16x32_bf16 a[184:187], v[120:123], v[142:145], a[184:187]\n"
    "v_mfma_f32_16x16x32_bf16 a[188:191], v[120:123], v[146:149], a[188:191]\n"
    "s_waitcnt vmcnt(38) lgkmcnt(2)\n"
    "v_mfma_f32_16x16x32_bf16 a[64:67], v[112:115], v[150:153], a[64:67]\n"
    "ds_read_b64_tr_b16 v[120:121], v250 offset:49664\n"
    "s_add_u32 m0, s64, 4096\n"
    "s_waitcnt vmcnt(16)\n"
    "v_subrev_u32 v234, s70, v234\n"
    "v_cmp_eq_u32_e64 s[16:17], s21, v235\n"
    "v_subrev_u32 v234, 4, v234\n"
    "v_cmp_gt_u32 vcc, 16, v234\n"
    "s_nop 0\n"
    "s_and_b64 vcc, vcc, s[16:17]\n"
    "s_cmp_eq_u64 vcc, -1\n"
    "s_cbranch_scc0 LslowK3_%=\n"
    "LbackK3_%=:\n"
    "v_mfma_f32_16x16x32_bf16 a[68:71], v[112:115], v[154:157], a[68:71]\n"
    "ds_read_b64_tr_b16 v[122:123], v250 offset:50688\n"
    "s_add_u32 s65, s27, 184320\n"
    "v_mfma_f32_16x16x32_bf16 a[72:75], v[112:115], v[158:161], a[72:75]\n"
    "buffer_load_dwordx4 v240, s[48:51], s65 offen offset:0 lds\n"
    "v_mfma_f32_16x16x32_bf16 a[76:79], v[112:115], v[162:165], a[76:79]\n"
    "s_waitcnt lgkmcnt(2)\n"
    "v_mfma_f32_16x16x32_bf16 a[80:83], v[116:119], v[150:153], a[80:83]\n"
    "ds_read_b64_tr_b16 v[112:113], v251 offset:49664\n"
    "v_mfma_f32_16x16x32_bf16 a[84:87], v[116:119], v[154:157], a[84:87]\n"
    "ds_read_b64_tr_b16 v[114:115], v251 offset:50688\n"
    "v_mfma_f32_16x16x32_bf16 a[88:91], v[116:119], v[158:161], a[88:91]\n"
    "s_add_u32 s61, s74, 16384\n"
    "buffer_load_dwordx4 v[134:137], v241, s[56:59], s61 offen offset:0 sc1\n"
    "v_mfma_f32_16x16x32_bf16 a[92:95], v[116:119], v[162:165], a[92:95]\n"
    "s_waitcnt lgkmcnt(2)\n"
    "v_mfma_f32_16x16x32_bf16 a[96:99], v[120:123], v[150:153], a[96:99]\n"
    "ds_read_b64_tr_b16 v[116:117], v250 offset:57344\n"
    "s_add_u32 m0, s64, 8192\n"
    "v_mfma_f32_16x16x32_bf16 a[100:103], v[120:123], v[154:157], a[100:103]\n"
    "ds_read_b64_tr_b16 v[118:119], v250 offset:58368\n"
    "s_add_u32 s65, s81, 221184\n"
    "v_mfma_f32_16x16x32_bf16 a[104:107], v[120:123], v[158:161], a[104:107]\n"
    "buffer_load_dwordx4 v240, s[48:51], s65 offen offset:0 lds\n"
    "v_mfma_f32_16x16x32_bf16 a[108:111], v[120:123], v[162:165], a[108:111]\n"
    "s_waitcnt lgkmcnt(2)\n"
    "v_mfma_f32_16x16x32_bf16 a[112:115], v[112:115], v[150:153], a[112:115]\n"
    "ds_read_b64_tr_b16 v[120:121], v251 offset:57344\n"
    "s_add_u32 s61, s74, 16384\n"
    "buffer_load_dwordx4 v[138:141], v241, s[56:59], s61 offen offset:1024 sc1\n"
    "v_mfma_f32_16x16x32_bf16 a[116:119], v[112:115], v[154:157], a[116:119]\n"
    "ds_read_b64_tr_b16 v[122:123], v251 offset:58368\n"
    "v_mfma_f32_16x16x32_bf16 a[120:123], v[112:115], v[158:161], a[120:123]\n"
    "v_mfma_f32_16x16x32_bf16 a[124:127], v[112:115], v[162:165], a[124:127]\n"
    "s_add_u32 s61, s74, 16384\n"
    "buffer_load_dwordx4 v[142:145], v241, s[56:59], s61 offen offset:2048 sc1\n"
    "s_waitcnt lgkmcnt(2)\n"
    "v_mfma_f32_16x16x32_bf16 a[128:131], v[116:119], v[150:153], a[128:131]\n"
    "ds_read_b64_tr_b16 v[112:113], v250 offset:57856\n"
    "s_add_u32 m0, s64, 12288\n"
    "v_mfma_f32_16x16x32_bf16 a[132:135], v[116:119], v[154:157], a[132:135]\n"
    "ds_read_b64_tr_b16 v[114:115], v250 offset:58880\n"
    "s_add_u32 s65, s81, 258048\n"
    "v_mfma_f32_16x16x32_bf16 a[136:139], v[116:119], v[158:161], a[136:139]\n"
    "buffer_load_dwordx4 v240, s[48:51], s65 offen offset:0 lds\n"
    "v_mfma_f32_16x16x32_bf16 a[140:143], v[116:119], v[162:165], a[140:143]\n"
    "s_waitcnt lgkmcnt(2)\n"
    "v_mfma_f32_16x16x32_bf16 a[144:147], v[120:123], v[150:153], a[144:147]\n"
    "ds_read_b64_tr_b16 v[116:117], v251 offset:57856\n"
    "v_mfma_f32_16x16x32_bf16 a[148:151], v[120:123], v[154:157], a[148:151]\n"
    "ds_read_b64_tr_b16 v[118:119], v251 offset:58880\n"
    "v_mfma_f32_16x16x32_bf16 a[152:155], v[120:123], v[158:161], a[152:155]\n"
    "s_add_u32 s61, s74, 16384\n"
    "buffer_load_dwordx4 v[146:149], v241, s[56:59], s61 offen offset:3072 sc1\n"
    "v_mfma_f32_16x16x32_bf16 a[156:159], v[120:123], v[162:165], a[156:159]\n"
    "s_waitcnt lgkmcnt(2)\n"
    "v_mfma_f32_16x16x32_bf16 a[160:163], v[112:115], v[150:153], a[160:163]\n"
    "ds_read_b64_tr_b16 v[120:121], v252 offset:0\n"
    "s_add_u32 m0, s64, 16384\n"
    "v_mfma_f32_16x16x32_bf16 a[164:167], v[112:115], v[154:157], a[164:167]\n"
    "ds_read_b64_tr_b16 v[122:123], v252 offset:1024\n"
    "s_add_u32 s65, s76, 221184\n"
    "v_mfma_f32_16x16x32_bf16 a[168:171], v[112:115], v[158:161], a[168:171]\n"
    "buffer_load_dwordx4 v240, s[48:51], s65 offen offset:0 lds\n"
    "v_mfma_f32_16x16x32_bf16 a[172:175], v[112:115], v[162:165], a[172:175]\n"
    "s_waitcnt lgkmcnt(2)\n"
    "v_mfma_f32_16x16x32_bf16 a[176:179], v[116:119], v[150:153], a[176:179]\n"
    "ds_read_b64_tr_b16 v[112:113], v253 offset:0\n"
    "v_mfma_f32_16x16x32_bf16 a[180:183], v[116:119], v[154:157], a[180:183]\n"
    "ds_read_b64_tr_b16 v[114:115], v253 offset:1024\n"
    "v_mfma_f32_16x16x32_bf16 a[184:187], v[116:119], v[158:161], a[184:187]\n"
    "v_mfma_f32_16x16x32_bf16 a[188:191], v[116:119], v[162:165], a[188:191]\n"
    "s_waitcnt lgkmcnt(2)\n"
    "v_mfma_f32_16x16x32_bf16 a[64:67], v[120:123], v[16:19], a[64:67]\n"
    "ds_read_b64_tr_b16 v[116:117], v252 offset:512\n"
    "s_add_u32 m0, s64, 20480\n"
    "v_mfma_f32_16x16x32_bf16 a[68:71], v[120:123], v[20:23], a[68:71]\n"
    "ds_read_b64_tr_b16 v[118:119], v252 offset:1536\n"
    "s_add_u32 s65, s76, 258048\n"
    "v_mfma_f32_16x16x32_bf16 a[72:75], v[120:123], v[24:27], a[72:75]\n"
    "buffer_load_dwordx4 v240, s[48:51], s65 offen offset:0 lds\n"
    "v_mfma_f32_16x16x32_bf16 a[76:79], v[120:123], v[28:31], a[76:79]\n"
    "s_waitcnt lgkmcnt(2)\n"
    "v_mfma_f32_16x16x32_bf16 a[80:83], v[112:115], v[16:19], a[80:83]\n"
    "ds_read_b64_tr_b16 v[120:121], v253 offset:512\n"
    "v_mfma_f32_16x16x32_bf16 a[84:87], v[112:115], v[20:23], a[84:87]\n"
    "ds_read_b64_tr_b16 v[122:123], v253 offset:1536\n"
    "v_mfma_f32_16x16x32_bf16 a[88:91], v[112:115], v[24:27], a[88:91]\n"
    "s_add_u32 s61, s75, 32768\n"
    "buffer_load_dwordx4 v[150:153], v241, s[56:59], s61 offen offset:0 sc1\n"
    "v_mfma_f32_16x16x32_bf16 a[92:95], v[112:115], v[28:31], a[92:95]\n"
    "s_waitcnt vmcnt(20)\n"
    "s_add_u32 s99, s70, 5\n"
    "v_mov_b32 v133, s21\n"
    "v_mov_b32 v132, s99\n"
    "buffer_store_dwordx2 v[132:133], v242, s[56:59], s89 offen offset:0 sc1\n"
    "s_waitcnt lgkmcnt(2)\n"
    "v_mfma_f32_16x16x32_bf16 a[96:99], v[116:119], v[16:19], a[96:99]\n"
    "s_barrier\n"
    "ds_read_b64_tr_b16 v[112:113], v252 offset:8192\n"
    "s_and_b32 s96, s66, 1\n"
    "s_lshl_b32 s96, s96, 7\n"
    "s_add_u32 s96, s96, 72\n"
    "s_add_u32 s96, s96, s22\n"
    "v_and_b32 v243, 16, v241\n"
    "v_lshlrev_b32 v243, 4, v243\n"
    "v_xor_b32 v243, s96, v243\n"
    "buffer_load_dwordx2 v[234:235], v243, s[56:59], 0 offen offset:0 sc1\n"
    "v_mfma_f32_16x16x32_bf16 a[100:103], v[116:119], v[20:23], a[100:103]\n"
    "ds_read_b64_tr_b16 v[114:115], v252 offset:9216\n"
    "s_add_u32 m0, s64, 24576\n"
    "v_mfma_f32_16x16x32_bf16 a[104:107], v[116:119], v[24:27], a[104:107]\n"
    "s_add_u32 s65, s24, 221184\n"
    "v_mfma_f32_16x16x32_bf16 a[108:111], v[116:119], v[28:31], a[108:111]\n"
    "buffer_load_dwordx4 v240, s[48:51], s65 offen offset:0 lds\n"
    "s_waitcnt lgkmcnt(2)\n"
    "v_mfma_f32_16x16x32_bf16 a[112:115], v[120:123], v[16:19], a[112:115]\n"
    "ds_read_b64_tr_b16 v[116:117], v253 offset:8192\n"
    "s_add_u32 s61, s75, 32768\n"
    "buffer_load_dwordx4 v[154:157], v241, s[56:59], s61 offen offset:1024 sc1\n"
    "v_mfma_f32_16x16x32_bf16 a[116:119], v[120:123], v[20:23], a[116:119]\n"
    "ds_read_b64_tr_b16 v[118:119], v253 offset:9216\n"
    "v_mfma_f32_16x16x32_bf16 a[120:123], v[120:123], v[24:27], a[120:123]\n"
    "v_mfma_f32_16x16x32_bf16 a[124:127], v[120:123], v[28:31], a[124:127]\n"
    "s_waitcnt lgkmcnt(2)\n"
    "v_mfma_f32_16x16x32_bf16 a[128:131], v[112:115], v[16:19], a[128:131]\n"
    "ds_read_b64_tr_b16 v[120:121], v252 offset:8704\n"
    "s_add_u32 m0, s64, 28672\n"
    "s_add_u32 s61, s75, 32768\n"
    "buffer_load_dwordx4 v[158:161], v241, s[56:59], s61 offen offset:2048 sc1\n"
    "v_mfma_f32_16x16x32_bf16 a[132:135], v[112:115], v[20:23], a[132:135]\n"
    "ds_read_b64_tr_b16 v[122:123], v252 offset:9728\n"
    "s_add_u32 s65, s24, 258048\n"
    "v_mfma_f32_16x16x32_bf16 a[136:139], v[112:115], v[24:27], a[136:139]\n"
    "buffer_load_dwordx4 v240, s[48:51], s65 offen offset:0 lds\n"
    "v_mfma_f32_16x16x32_bf16 a[140:143], v[112:115], v[28:31], a[140:143]\n"
    "s_waitcnt lgkmcnt(2)\n"
    "v_mfma_f32_16x16x32_bf16 a[144:147], v[116:119], v[16:19], a[144:147]\n"
    "ds_read_b64_tr_b16 v[112:113], v253 offset:8704\n"
    "v_mfma_f32_16x16x32_bf16 a[148:151], v[116:119], v[20:23], a[148:151]\n"
    "ds_read_b64_tr_b16 v[114:115], v253 offset:9728\n"
    "v_mfma_f32_16x16x32_bf16 a[152:155], v[116:119], v[24:27], a[152:155]\n"
    "s_add_u32 s61, s75, 32768\n"
    "buffer_load_dwordx4 v[162:165], v241, s[56:59], s61 offen offset:3072 sc1\n"
    "v_mfma_f32_16x16x32_bf16 a[156:159], v[116:119], v[28:31], a[156:159]\n"
    "s_waitcnt lgkmcnt(2)\n"
    "v_mfma_f32_16x16x32_bf16 a[160:163], v[120:123], v[16:19], a[160:163]\n"
    "ds_read_b64_tr_b16 v[116:117], v252 offset:16384\n"
    "s_add_u32 m0, s64, 32768\n"
    "v_mfma_f32_16x16x32_bf16 a[164:167], v[120:123], v[20:23], a[164:167]\n"
    "ds_read_b64_tr_b16 v[118:119], v252 offset:17408\n"
    "s_add_u32 s65, s82, 221184\n"
    "v_mfma_f32_16x16x32_bf16 a[168:171], v[120:123], v[24:27], a[168:171]\n"
    "buffer_load_dwordx4 v240, s[48:51], s65 offen offset:0 lds\n"
    "v_mfma_f32_16x16x32_bf16 a[172:175], v[120:123], v[28:31], a[172:175]\n"
    "s_waitcnt lgkmcnt(2)\n"
    "v_mfma_f32_16x16x32_bf16 a[176:179], v[112:115], v[16:19], a[176:179]\n"
    "ds_read_b64_tr_b16 v[120:121], v253 offset:16384\n"
    "v_mfma_f32_16x16x32_bf16 a[180:183], v[112:115], v[20:23], a[180:183]\n"
    "ds_read_b64_tr_b16 v[122:123], v253 offset:17408\n"
    "v_mfma_f32_16x16x32_bf16 a[184:187], v[112:115], v[24:27], a[184:187]\n"
    "v_mfma_f32_16x16x32_bf16 a[188:191], v[112:115], v[28:31], a[188:191]\n"
    "s_waitcnt lgkmcnt(2)\n"
    "v_mfma_f32_16x16x32_bf16 a[64:67], v[116:119], v[166:169], a[64:67]\n"
    "ds_read_b64_tr_b16 v[112:113], v252 offset:16896\n"
    "s_add_u32 m0, s64, 36864\n"
    "v_mfma_f32_16x16x32_bf16 a[68:71], v[116:119], v[170:173], a[68:71]\n"
    "ds_read_b64_tr_b16 v[114:115], v252 offset:17920\n"
    "s_add_u32 s65, s82, 258048\n"
    "v_mfma_f32_16x16x32_bf16 a[72:75], v[116:119], v[174:177], a[72:75]\n"
    "buffer_load_dwordx4 v240, s[48:51], s65 offen offset:0 lds\n"
    "v_mfma_f32_16x16x32_bf16 a[76:79], v[116:119], v[178:181], a[76:79]\n"
    "s_waitcnt lgkmcnt(2)\n"
    "v_mfma_f32_16x16x32_bf16 a[80:83], v[120:123], v[166:169], a[80:83]\n"
    "ds_read_b64_tr_b16 v[116:117], v253 offset:16896\n"
    "v_mfma_f32_16x16x32_bf16 a[84:87], v[120:123], v[170:173], a[84:87]\n"
    "ds_read_b64_tr_b16 v[118:119], v253 offset:17920\n"
    "v_mfma_f32_16x16x32_bf16 a[88:91], v[120:123], v[174:177], a[88:91]\n"
    "v_mfma_f32_16x16x32_bf16 a[92:95], v[120:123], v[178:181], a[92:95]\n"
    "s_waitcnt lgkmcnt(2)\n"
    "v_mfma_f32_16x16x32_bf16 a[96:99], v[112:115], v[166:169], a[96:99]\n"
    "ds_read_b64_tr_b16 v[120:121], v252 offset:24576\n"
    "s_add_u32 m0, s64, 40960\n"
    "v_mfma_f32_16x16x32_bf16 a[100:103], v[112:115], v[170:173], a[100:103]\n"
    "ds_read_b64_tr_b16 v[122:123], v252 offset:25600\n"
    "s_add_u32 s65, s82, 294912\n"
    "v_mfma_f32_16x16x32_bf16 a[104:107], v[112:115], v[174:177], a[104:107]\n"
    "buffer_load_dwordx4 v240, s[48:51], s65 offen offset:0 lds\n"
    "v_mfma_f32_16x16x32_bf16 a[108:111], v[112:115], v[178:181], a[108:111]\n"
    "s_waitcnt lgkmcnt(2)\n"
    "v_mfma_f32_16x16x32_bf16 a[112:115], v[116:119], v[166:169], a[112:115]\n"
    "ds_read_b64_tr_b16 v[112:113], v253 offset:24576\n"
    "v_mfma_f32_16x16x32_bf16 a[116:119], v[116:119], v[170:173], a[116:119]\n"
    "ds_read_b64_tr_b16 v[114:115], v253 offset:25600\n"
    "v_mfma_f32_16x16x32_bf16 a[120:123], v[116:119], v[174:177], a[120:123]\n"
    "v_mfma_f32_16x16x32_bf16 a[124:127], v[116:119], v[178:181], a[124:127]\n"
    "s_waitcnt lgkmcnt(2)\n"
    "v_mfma_f32_16x16x32_bf16 a[128:131], v[120:123], v[166:169], a[128:131]\n"
    "ds_read_b64_tr_b16 v[116:117], v252 offset:25088\n"
    "s_add_u32 m0, s64, 45056\n"
    "v_mfma_f32_16x16x32_bf16 a[132:135], v[120:123], v[170:173], a[132:135]\n"
    "ds_read_b64_tr_b16 v[118:119], v252 offset:26112\n"
    "s_add_u32 s65, s82, 331776\n"
    "v_mfma_f32_16x16x32_bf16 a[136:139], v[120:123], v[174:177], a[136:139]\n"
    "buffer_load_dwordx4 v240, s[48:51], s65 offen offset:0 lds\n"
    "v_mfma_f32_16x16x32_bf16 a[140:143], v[120:123], v[178:181], a[140:143]\n"
    "s_waitcnt lgkmcnt(2)\n"
    "v_mfma_f32_16x16x32_bf16 a[144:147], v[112:115], v[166:169], a[144:147]\n"
    "ds_read_b64_tr_b16 v[120:121], v253 offset:25088\n"
    "v_mfma_f32_16x16x32_bf16 a[148:151], v[112:115], v[170:173], a[148:151]\n"
    "ds_read_b64_tr_b16 v[122:123], v253 offset:26112\n"
    "v_mfma_f32_16x16x32_bf16 a[152:155], v[112:115], v[174:177], a[152:155]\n"
    "v_mfma_f32_16x16x32_bf16 a[156:159], v[112:115], v[178:181], a[156:159]\n"
    "s_waitcnt lgkmcnt(2)\n"
    "v_mfma_f32_16x16x32_bf16 a[160:163], v[116:119], v[166:169], a[160:163]\n"
    "s_barrier\n"
    "ds_read_b64_tr_b16 v[112:113], v252 offset:32768\n"
    "v_mfma_f32_16x16x32_bf16 a[164:167], v[116:119], v[170:173], a[164:167]\n"
    "ds_read_b64_tr_b16 v[114:115], v252 offset:33792\n"
    "s_add_u32 m0, s64, 49152\n"
    "v_mfma_f32_16x16x32_bf16 a[168:171], v[116:119], v[174:177], a[168:171]\n"
    "s_add_u32 s65, s84, 0\n"
    "v_mfma_f32_16x16x32_bf16 a[172:175], v[116:119], v[178:181], a[172:175]\n"
    "buffer_load_dwordx4 v240, s[52:55], s65 offen offset:0 lds\n"
    "s_waitcnt lgkmcnt(2)\n"
    "v_mfma_f32_16x16x32_bf16 a[176:179], v[120:123], v[166:169], a[176:179]\n"
    "ds_read_b64_tr_b16 v[116:117], v253 offset:32768\n"
    "v_mfma_f32_16x16x32_bf16 a[180:183], v[120:123], v[170:173], a[180:183]\n"
    "ds_read_b64_tr_b16 v[118:119], v253 offset:33792\n"
    "v_mfma_f32_16x16x32_bf16 a[184:187], v[120:123], v[174:177], a[184:187]\n"
    "v_mfma_f32_16x16x32_bf16 a[188:191], v[120:123], v[178:181], a[188:191]\n"
    "s_waitcnt vmcnt(31) lgkmcnt(2)\n"
    "v_mfma_f32_16x16x32_bf16 a[192:195], v[112:115], v[0:3], a[192:195]\n"
    "ds_read_b64_tr_b16 v[120:121], v252 offset:33280\n"
    "s_add_u32 m0, s64, 53248\n"
    "s_waitcnt vmcnt(29)\n"
    "v_mfma_f32_16x16x32_bf16 a[196:199], v[112:115], v[4:7], a[196:199]\n"
    "ds_read_b64_tr_b16 v[122:123], v252 offset:34304\n"
    "s_add_u32 s65, s84, 82944\n"
    "s_waitcnt vmcnt(28)\n"
    "v_mfma_f32_16x16x32_bf16 a[200:203], v[112:115], v[8:11], a[200:203]\n"
    "buffer_load_dwordx4 v240, s[52:55], s65 offen offset:0 lds\n"
    "s_waitcnt vmcnt(27)\n"
    "v_mfma_f32_16x16x32_bf16 a[204:207], v[112:115], v[12:15], a[204:207]\n"
    "s_waitcnt lgkmcnt(2)\n"
    "v_mfma_f32_16x16x32_bf16 a[208:211], v[116:119], v[0:3], a[208:211]\n"
    "ds_read_b64_tr_b16 v[112:113], v253 offset:33280\n"
    "s_waitcnt vmcnt(11)\n"
    "v_subrev_u32 v234, s70, v234\n"
    "v_cmp_eq_u32_e64 s[16:17], s21, v235\n"
    "v_subrev_u32 v234, 1, v234\n"
    "v_cmp_gt_u32 vcc, 16, v234\n"
    "s_nop 0\n"
    "s_and_b64 vcc, vcc, s[16:17]\n"
    "s_cmp_eq_u64 vcc, -1\n"
    "s_cbranch_scc0 LslowK4_%=\n"
    "LbackK4_%=:\n"
    "v_mfma_f32_16x16x32_bf16 a[212:215], v[116:119], v[4:7], a[212:215]\n"
    "ds_read_b64_tr_b16 v[114:115], v253 offset:34304\n"
    "v_bfe_u32 v243, v241, 4, 2\n"
    "v_max_u32 v243, 1, v243\n"
    "v_xor_b32 v243, s69, v243\n"
    "v_lshl_add_u32 v243, v243, 6, s22\n"
    "buffer_load_dwordx2 v[234:235], v243, s[56:59], 0 offen offset:0 sc1\n"
    "v_mfma_f32_16x16x32_bf16 a[216:219], v[116:119], v[8:11], a[216:219]\n"
    "v_mfma_f32_16x16x32_bf16 a[220:223], v[116:119], v[12:15], a[220:223]\n"
    "s_waitcnt lgkmcnt(2)\n"
    "v_mfma_f32_16x16x32_bf16 a[224:227], v[120:123], v[0:3], a[224:227]\n"
    "ds_read_b64_tr_b16 v[116:117], v252 offset:40960\n"
    "s_add_u32 m0, s64, 57344\n"
    "v_mfma_f32_16x16x32_bf16 a[228:231], v[120:123], v[4:7], a[228:231]\n"
    "ds_read_b64_tr_b16 v[118:119], v252 offset:41984\n"
    "s_add_u32 s65, s84, 36864\n"
    "v_mfma_f32_16x16x32_bf16 a[232:235], v[120:123], v[8:11], a[232:235]\n"
    "buffer_load_dwordx4 v240, s[52:55], s65 offen offset:0 lds\n"
    "v_mfma_f32_16x16x32_bf16 a[236:239], v[120:123], v[12:15], a[236:239]\n"
    "s_waitcnt lgkmcnt(2)\n"
    "v_mfma_f32_16x16x32_bf16 a[240:243], v[112:115], v[0:3], a[240:243]\n"
    "ds_read_b64_tr_b16 v[120:121], v253 offset:40960\n"
    "v_mfma_f32_16x16x32_bf16 a[244:247], v[112:115], v[4:7], a[244:247]\n"
    "ds_read_b64_tr_b16 v[122:123], v253 offset:41984\n"
    "v_mfma_f32_16x16x32_bf16 a[248:251], v[112:115], v[8:11], a[248:251]\n"
    "v_mfma_f32_16x16x32_bf16 a[252:255], v[112:115], v[12:15], a[252:255]\n"
    "s_waitcnt lgkmcnt(2)\n"
    "v_mfma_f32_16x16x32_bf16 a[64:67], v[116:119], v[0:3], a[64:67]\n"
    "ds_read_b64_tr_b16 v[112:113], v252 offset:41472\n"
    "s_add_u32 m0, s64, 61440\n"
    "v_mfma_f32_16x16x32_bf16 a[68:71], v[116:119], v[4:7], a[68:71]\n"
    "ds_read_b64_tr_b16 v[114:115], v252 offset:42496\n"
    "s_add_u32 s65, s84, 119808\n"
    "v_mfma_f32_16x16x32_bf16 a[72:75], v[116:119], v[8:11], a[72:75]\n"
    "buffer_load_dwordx4 v240, s[52:55], s65 offen offset:0 lds\n"
    "v_mfma_f32_16x16x32_bf16 a[76:79], v[116:119], v[12:15], a[76:79]\n"
    "s_waitcnt lgkmcnt(2)\n"
    "v_mfma_f32_16x16x32_bf16 a[80:83], v[120:123], v[0:3], a[80:83]\n"
    "ds_read_b64_tr_b16 v[116:117], v253 offset:41472\n"
    "v_mfma_f32_16x16x32_bf16 a[84:87], v[120:123], v[4:7], a[84:87]\n"
    "ds_read_b64_tr_b16 v[118:119], v253 offset:42496\n"
    "v_mfma_f32_16x16x32_bf16 a[88:91], v[120:123], v[8:11], a[88:91]\n"
    "s_add_u32 s61, s73, 49152\n"
    "buffer_load_dwordx4 a[48:51], v241, s[56:59], s61 offen offset:0 sc1\n"
    "v_mfma_f32_16x16x32_bf16 a[92:95], v[120:123], v[12:15], a[92:95]\n"
    "s_waitcnt lgkmcnt(2)\n"
    "v_mfma_f32_16x16x32_bf16 a[96:99], v[112:115], v[0:3], a[96:99]\n"
    "ds_read_b64_tr_b16 v[120:121], v252 offset:49152\n"
    "s_add_u32 m0, s64, 65536\n"
    "v_mfma_f32_16x16x32_bf16 a[100:103], v[112:115], v[4:7], a[100:103]\n"
    "ds_read_b64_tr_b16 v[122:123], v252 offset:50176\n"
    "s_add_u32 s65, s84, 73728\n"
    "v_mfma_f32_16x16x32_bf16 a[104:107], v[112:115], v[8:11], a[104:107]\n"
    "buffer_load_dwordx4 v240, s[52:55], s65 offen offset:0 lds\n"
    "v_mfma_f32_16x16x32_bf16 a[108:111], v[112:115], v[12:15], a[108:111]\n"
    "s_waitcnt lgkmcnt(2)\n"
    "v_mfma_f32_16x16x32_bf16 a[112:115], v[116:119], v[0:3], a[112:115]\n"
    "ds_read_b64_tr_b16 v[112:113], v253 offset:49152\n"
    "s_add_u32 s61, s73, 49152\n"
    "buffer_load_dwordx4 a[52:55], v241, s[56:59], s61 offen offset:1024 sc1\n"
    "v_mfma_f32_16x16x32_bf16 a[116:119], v[116:119], v[4:7], a[116:119]\n"
    "ds_read_b64_tr_b16 v[114:115], v253 offset:50176\n"
    "v_mfma_f32_16x16x32_bf16 a[120:123], v[116:119], v[8:11], a[120:123]\n"
    "v_mfma_f32_16x16x32_bf16 a[124:127], v[116:119], v[12:15], a[124:127]\n"
    "s_add_u32 s61, s73, 49152\n"
    "buffer_load_dwordx4 a[56:59], v241, s[56:59], s61 offen offset:2048 sc1\n"
    "s_waitcnt lgkmcnt(2)\n"
    "v_mfma_f32_16x16x32_bf16 a[128:131], v[120:123], v[0:3], a[128:131]\n"
    "ds_read_b64_tr_b16 v[116:117], v252 offset:49664\n"
    "s_add_u32 m0, s64, 69632\n"
    "v_mfma_f32_16x16x32_bf16 a[132:135], v[120:123], v[4:7], a[132:135]\n"
    "ds_read_b64_tr_b16 v[118:119], v252 offset:50688\n"
    "s_add_u32 s65, s84, 156672\n"
    "v_mfma_f32_16x16x32_bf16 a[136:139], v[120:123], v[8:11], a[136:139]\n"
    "buffer_load_dwordx4 v240, s[52:55], s65 offen offset:0 lds\n"
    "v_mfma_f32_16x16x32_bf16 a[140:143], v[120:123], v[12:15], a[140:143]\n"
    "s_waitcnt lgkmcnt(2)\n"
    "v_mfma_f32_16x16x32_bf16 a[144:147], v[112:115], v[0:3], a[144:147]\n"
    "ds_read_b64_tr_b16 v[120:121], v253 offset:49664\n"
    "v_mfma_f32_16x16x32_bf16 a[148:151], v[112:115], v[4:7], a[148:151]\n"
    "ds_read_b64_tr_b16 v[122:123], v253 offset:50688\n"
    "v_mfma_f32_16x16x32_bf16 a[152:155], v[112:115], v[8:11], a[152:155]\n"
    "s_add_u32 s61, s73, 49152\n"
    "buffer_load_dwordx4 a[60:63], v241, s[56:59], s61 offen offset:3072 sc1\n"
    "v_mfma_f32_16x16x32_bf16 a[156:159], v[112:115], v[12:15], a[156:159]\n"
    "s_waitcnt lgkmcnt(2)\n"
    "v_mfma_f32_16x16x32_bf16 a[160:163], v[116:119], v[0:3], a[160:163]\n"
    "s_barrier\n"
    "ds_read_b64_tr_b16 v[112:113], v252 offset:57344\n"
    "v_mfma_f32_16x16x32_bf16 a[164:167], v[116:119], v[4:7], a[164:167]\n"
    "ds_read_b64_tr_b16 v[114:115], v252 offset:58368\n"
    "s_add_u32 m0, s64, 73728\n"
    "v_mfma_f32_16x16x32_bf16 a[168:171], v[116:119], v[8:11], a[168:171]\n"
    "s_add_u32 s65, s25, 221184\n"
    "v_mfma_f32_16x16x32_bf16 a[172:175], v[116:119], v[12:15], a[172:175]\n"
    "buffer_load_dwordx4 v240, s[48:51], s65 offen offset:0 lds\n"
    "s_waitcnt lgkmcnt(2)\n"
    "v_mfma_f32_16x16x32_bf16 a[176:179], v[120:123], v[0:3], a[176:179]\n"
    "ds_read_b64_tr_b16 v[116:117], v253 offset:57344\n"
    "v_mfma_f32_16x16x32_bf16 a[180:183], v[120:123], v[4:7], a[180:183]\n"
    "ds_read_b64_tr_b16 v[118:119], v253 offset:58368\n"
    "v_mfma_f32_16x16x32_bf16 a[184:187], v[120:123], v[8:11], a[184:187]\n"
    "v_mfma_f32_16x16x32_bf16 a[188:191], v[120:123], v[12:15], a[188:191]\n"
    "s_waitcnt lgkmcnt(2)\n"
    "v_mfma_f32_16x16x32_bf16 a[64:67], v[112:115], v[134:137], a[64:67]\n"
    "ds_read_b64_tr_b16 v[120:121], v252 offset:57856\n"
    "s_add_u32 m0, s64, 77824\n"
    "v_mfma_f32_16x16x32_bf16 a[68:71], v[112:115], v[138:141], a[68:71]\n"
    "ds_read_b64_tr_b16 v[122:123], v252 offset:58880\n"
    "s_add_u32 s65, s25, 258048\n"
    "v_mfma_f32_16x16x32_bf16 a[72:75], v[112:115], v[142:145], a[72:75]\n"
    "buffer_load_dwordx4 v240, s[48:51], s65 offen offset:0 lds\n"
    "v_mfma_f32_16x16x32_bf16 a[76:79], v[112:115], v[146:149], a[76:79]\n"
    "s_waitcnt lgkmcnt(2)\n"
    "v_mfma_f32_16x16x32_bf16 a[80:83], v[116:119], v[134:137], a[80:83]\n"
    "ds_read_b64_tr_b16 v[112:113], v253 offset:57856\n"
    "v_mfma_f32_16x16x32_bf16 a[84:87], v[116:119], v[138:141], a[84:87]\n"
    "ds_read_b64_tr_b16 v[114:115], v253 offset:58880\n"
    "s_add_u32 s61, s86, 0\n"
    "buffer_load_dwordx4 a[16:19], v241, s[56:59], s61 offen offset:0 sc1\n"
    "v_mfma_f32_16x16x32_bf16 a[88:91], v[116:119], v[142:145], a[88:91]\n"
    "v_mfma_f32_16x16x32_bf16 a[92:95], v[116:119], v[146:149], a[92:95]\n"
    "s_waitcnt lgkmcnt(2)\n"
    "v_mfma_f32_16x16x32_bf16 a[96:99], v[120:123], v[134:137], a[96:99]\n"
    "ds_read_b64_tr_b16 v[116:117], v254 offset:0\n"
    "s_add_u32 m0, s64, 81920\n"
    "v_mfma_f32_16x16x32_bf16 a[100:103], v[120:123], v[138:141], a[100:103]\n"
    "ds_read_b64_tr_b16 v[118:119], v254 offset:1024\n"
    "s_add_u32 s65, s26, 147456\n"
    "v_mfma_f32_16x16x32_bf16 a[104:107], v[120:123], v[142:145], a[104:107]\n"
    "buffer_load_dwordx4 v240, s[48:51], s65 offen offset:0 lds\n"
    "v_mfma_f32_16x16x32_bf16 a[108:111], v[120:123], v[146:149], a[108:111]\n"
    "s_add_u32 s61, s86, 0\n"
    "buffer_load_dwordx4 a[20:23], v241, s[56:59], s61 offen offset:1024 sc1\n"
    "s_waitcnt lgkmcnt(2)\n"
    "v_mfma_f32_16x16x32_bf16 a[112:115], v[112:115], v[134:137], a[112:115]\n"
    "ds_read_b64_tr_b16 v[120:121], v255 offset:0\n"
    "v_mfma_f32_16x16x32_bf16 a[116:119], v[112:115], v[138:141], a[116:119]\n"
    "ds_read_b64_tr_b16 v[122:123], v255 offset:1024\n"
    "s_add_u32 s61, s86, 2048\n"
    "buffer_load_dwordx4 a[0:3], v241, s[56:59], s61 offen offset:0 sc1\n"
    "v_mfma_f32_16x16x32_bf16 a[120:123], v[112:115], v[142:145], a[120:123]\n"
    "s_add_u32 s61, s87, 0\n"
    "buffer_load_dwordx4 a[24:27], v241, s[56:59], s61 offen offset:0 sc1\n"
    "v_mfma_f32_16x16x32_bf16 a[124:127], v[112:115], v[146:149], a[124:127]\n"
    "s_waitcnt lgkmcnt(2)\n"
    "v_mfma_f32_16x16x32_bf16 a[128:131], v[116:119], v[134:137], a[128:131]\n"
    "ds_read_b64_tr_b16 v[112:113], v254 offset:512\n"
    "s_add_u32 m0, s64, 86016\n"
    "v_mfma_f32_16x16x32_bf16 a[132:135], v[116:119], v[138:141], a[132:135]\n"
    "ds_read_b64_tr_b16 v[114:115], v254 offset:1536\n"
    "s_add_u32 s65, s26, 184320\n"
    "s_waitcnt vmcnt(15)\n"
    "v_subrev_u32 v234, s70, v234\n"
    "v_cmp_eq_u32_e64 s[16:17], s21, v235\n"
    "v_subrev_u32 v234, 5, v234\n"
    "v_cmp_gt_u32 vcc, 16, v234\n"
    "s_nop 0\n"
    "s_and_b64 vcc, vcc, s[16:17]\n"
    "s_cmp_eq_u64 vcc, -1\n"
    "s_cbranch_scc0 LslowK5_%=\n"
    "LbackK5_%=:\n"
    "v_mfma_f32_16x16x32_bf16 a[136:139], v[116:119], v[142:145], a[136:139]\n"
    "buffer_load_dwordx4 v240, s[48:51], s65 offen offset:0 lds\n"
    "v_mfma_f32_16x16x32_bf16 a[140:143], v[116:119], v[146:149], a[140:143]\n"
    "s_add_u32 s61, s86, 2048\n"
    "buffer_load_dwordx4 a[4:7], v241, s[56:59], s61 offen offset:1024 sc1\n"
    "s_waitcnt lgkmcnt(2)\n"
    "v_mfma_f32_16x16x32_bf16 a[144:147], v[120:123], v[134:137], a[144:147]\n"
    "ds_read_b64_tr_b16 v[116:117], v255 offset:512\n"
    "s_add_u32 s61, s73, 65536\n"
    "buffer_load_dwordx4 a[32:35], v241, s[56:59], s61 offen offset:0 sc1\n"
    "v_mfma_f32_16x16x32_bf16 a[148:151], v[120:123], v[138:141], a[148:151]\n"
    "ds_read_b64_tr_b16 v[118:119], v255 offset:1536\n"
    "v_mfma_f32_16x16x32_bf16 a[152:155], v[120:123], v[142:145], a[152:155]\n"
    "s_add_u32 s61, s87, 0\n"
    "buffer_load_dwordx4 a[28:31], v241, s[56:59], s61 offen offset:1024 sc1\n"
    "s_add_u32 s61, s86, 4096\n"
    "buffer_load_dwordx4 v[0:3], v241, s[56:59], s61 offen offset:0 sc1\n"
    "v_mfma_f32_16x16x32_bf16 a[156:159], v[120:123], v[146:149], a[156:159]\n"
    "s_add_u32 s61, s73, 65536\n"
    "buffer_load_dwordx4 a[36:39], v241, s[56:59], s61 offen offset:1024 sc1\n"
    "s_waitcnt lgkmcnt(2)\n"
    "v_mfma_f32_16x16x32_bf16 a[160:163], v[112:115], v[134:137], a[160:163]\n"
    "ds_read_b64_tr_b16 v[120:121], v254 offset:8192\n"
    "s_add_u32 m0, s64, 90112\n"
    "v_mfma_f32_16x16x32_bf16 a[164:167], v[112:115], v[138:141], a[164:167]\n"
    "ds_read_b64_tr_b16 v[122:123], v254 offset:9216\n"
    "s_add_u32 s65, s27, 221184\n"
    "v_mfma_f32_16x16x32_bf16 a[168:171], v[112:115], v[142:145], a[168:171]\n"
    "buffer_load_dwordx4 v240, s[48:51], s65 offen offset:0 lds\n"
    "v_mfma_f32_16x16x32_bf16 a[172:175], v[112:115], v[146:149], a[172:175]\n"
    "s_add_u32 s61, s87, 2048\n"
    "buffer_load_dwordx4 a[8:11], v241, s[56:59], s61 offen offset:0 sc1\n"
    "s_waitcnt lgkmcnt(2)\n"
    "v_mfma_f32_16x16x32_bf16 a[176:179], v[116:119], v[134:137], a[176:179]\n"
    "ds_read_b64_tr_b16 v[112:113], v255 offset:8192\n"
    "v_mfma_f32_16x16x32_bf16 a[180:183], v[116:119], v[138:141], a[180:183]\n"
    "ds_read_b64_tr_b16 v[114:115], v255 offset:9216\n"
    "s_add_u32 s61, s87, 2048\n"
    "buffer_load_dwordx4 a[12:15], v241, s[56:59], s61 offen offset:1024 sc1\n"
    "v_mfma_f32_16x16x32_bf16 a[184:187], v[116:119], v[142:145], a[184:187]\n"
    "s_add_u32 s61, s73, 65536\n"
    "buffer_load_dwordx4 a[40:43], v241, s[56:59], s61 offen offset:2048 sc1\n"
    "v_mfma_f32_16x16x32_bf16 a[188:191], v[116:119], v[146:149], a[188:191]\n"
    "s_add_u32 s61, s86, 4096\n"
    "buffer_load_dwordx4 v[4:7], v241, s[56:59], s61 offen offset:1024 sc1\n"
    "s_waitcnt lgkmcnt(2)\n"
    "v_mfma_f32_16x16x32_bf16 a[64:67], v[120:123], v[150:153], a[64:67]\n"
    "ds_read_b64_tr_b16 v[116:117], v254 offset:8704\n"
    "s_add_u32 m0, s64, 94208\n"
    "v_mfma_f32_16x16x32_bf16 a[68:71], v[120:123], v[154:157], a[68:71]\n"
    "ds_read_b64_tr_b16 v[118:119], v254 offset:9728\n"
    "s_add_u32 s65, s27, 258048\n"
    "v_mfma_f32_16x16x32_bf16 a[72:75], v[120:123], v[158:161], a[72:75]\n"
    "buffer_load_dwordx4 v240, s[48:51], s65 offen offset:0 lds\n"
    "v_mfma_f32_16x16x32_bf16 a[76:79], v[120:123], v[162:165], a[76:79]\n"
    "s_waitcnt lgkmcnt(2)\n"
    "v_mfma_f32_16x16x32_bf16 a[80:83], v[112:115], v[150:153], a[80:83]\n"
    "ds_read_b64_tr_b16 v[120:121], v255 offset:8704\n"
    "s_add_u32 s61, s73, 65536\n"
    "buffer_load_dwordx4 a[44:47], v241, s[56:59], s61 offen offset:3072 sc1\n"
    "v_mfma_f32_16x16x32_bf16 a[84:87], v[112:115], v[154:157], a[84:87]\n"
    "ds_read_b64_tr_b16 v[122:123], v255 offset:9728\n"
    "s_add_u32 s61, s87, 4096\n"
    "buffer_load_dwordx4 v[8:11], v241, s[56:59], s61 offen offset:0 sc1\n"
    "v_mfma_f32_16x16x32_bf16 a[88:91], v[112:115], v[158:161], a[88:91]\n"
    "s_add_u32 s61, s87, 4096\n"
    "buffer_load_dwordx4 v[12:15], v241, s[56:59], s61 offen offset:1024 sc1\n"
    "v_mfma_f32_16x16x32_bf16 a[92:95], v[112:115], v[162:165], a[92:95]\n"
    "s_waitcnt lgkmcnt(2)\n"
    "v_mfma_f32_16x16x32_bf16 a[96:99], v[116:119], v[150:153], a[96:99]\n"
    "s_barrier\n"
    "ds_read_b64_tr_b16 v[112:113], v250 offset:0\n"
    "v_mfma_f32_16x16x32_bf16 a[100:103], v[116:119], v[154:157], a[100:103]\n"
    "ds_read_b64_tr_b16 v[114:115], v250 offset:1024\n"
    "v_mfma_f32_16x16x32_bf16 a[104:107], v[116:119], v[158:161], a[104:107]\n"
    "v_mfma_f32_16x16x32_bf16 a[108:111], v[116:119], v[162:165], a[108:111]\n"
    "s_waitcnt lgkmcnt(2)\n"
    "v_mfma_f32_16x16x32_bf16 a[112:115], v[120:123], v[150:153], a[112:115]\n"
    "ds_read_b64_tr_b16 v[116:117], v251 offset:0\n"
    "v_mfma_f32_16x16x32_bf16 a[116:119], v[120:123], v[154:157], a[116:119]\n"
    "ds_read_b64_tr_b16 v[118:119], v251 offset:1024\n"
    "v_mfma_f32_16x16x32_bf16 a[120:123], v[120:123], v[158:161], a[120:123]\n"
    "s_add_u32 s61, s74, 32768\n"
    "buffer_load_dwordx4 v[134:137], v241, s[56:59], s61 offen offset:0 sc1\n"
    "v_mfma_f32_16x16x32_bf16 a[124:127], v[120:123], v[162:165], a[124:127]\n"
    "s_waitcnt lgkmcnt(2)\n"
    "v_mfma_f32_16x16x32_bf16 a[128:131], v[112:115], v[150:153], a[128:131]\n"
    "ds_read_b64_tr_b16 v[120:121], v250 offset:512\n"
    "v_mfma_f32_16x16x32_bf16 a[132:135], v[112:115], v[154:157], a[132:135]\n"
    "ds_read_b64_tr_b16 v[122:123], v250 offset:1536\n"
    "v_mfma_f32_16x16x32_bf16 a[136:139], v[112:115], v[158:161], a[136:139]\n"
    "v_mfma_f32_16x16x32_bf16 a[140:143], v[112:115], v[162:165], a[140:143]\n"
    "s_add_u32 s61, s74, 32768\n"
    "buffer_load_dwordx4 v[138:141], v241, s[56:59], s61 offen offset:1024 sc1\n"
    "s_waitcnt lgkmcnt(2)\n"
    "v_mfma_f32_16x16x32_bf16 a[144:147], v[116:119], v[150:153], a[144:147]\n"
    "ds_read_b64_tr_b16 v[112:113], v251 offset:512\n"
    "v_mfma_f32_16x16x32_bf16 a[148:151], v[116:119], v[154:157], a[148:151]\n"
    "ds_read_b64_tr_b16 v[114:115], v251 offset:1536\n"
    "v_mfma_f32_16x16x32_bf16 a[152:155], v[116:119], v[158:161], a[152:155]\n"
    "v_mfma_f32_16x16x32_bf16 a[156:159], v[116:119], v[162:165], a[156:159]\n"
    "s_waitcnt lgkmcnt(2)\n"
    "v_mfma_f32_16x16x32_bf16 a[160:163], v[120:123], v[150:153], a[160:163]\n"
    "ds_read_b64_tr_b16 v[116:117], v250 offset:8192\n"
    "s_add_u32 s61, s74, 32768\n"
    "buffer_load_dwordx4 v[142:145], v241, s[56:59], s61 offen offset:2048 sc1\n"
    "v_mfma_f32_16x16x32_bf16 a[164:167], v[120:123], v[154:157], a[164:167]\n"
    "ds_read_b64_tr_b16 v[118:119], v250 offset:9216\n"
    "v_mfma_f32_16x16x32_bf16 a[168:171], v[120:123], v[158:161], a[168:171]\n"
    "v_mfma_f32_16x16x32_bf16 a[172:175], v[120:123], v[162:165], a[172:175]\n"
    "s_waitcnt lgkmcnt(2)\n"
    "v_mfma_f32_16x16x32_bf16 a[176:179], v[112:115], v[150:153], a[176:179]\n"
    "ds_read_b64_tr_b16 v[120:121], v251 offset:8192\n"
    "v_mfma_f32_16x16x32_bf16 a[180:183], v[112:115], v[154:157], a[180:183]\n"
    "ds_read_b64_tr_b16 v[122:123], v251 offset:9216\n"
    "s_add_u32 s61, s74, 32768\n"
    "buffer_load_dwordx4 v[146:149], v241, s[56:59], s61 offen offset:3072 sc1\n"
    "v_mfma_f32_16x16x32_bf16 a[184:187], v[112:115], v[158:161], a[184:187]\n"
    "s_add_u32 s61, s75, 49152\n"
    "buffer_load_dwordx4 v[166:169], v241, s[56:59], s61 offen offset:0 sc1\n"
    "v_mfma_f32_16x16x32_bf16 a[188:191], v[112:115], v[162:165], a[188:191]\n"
    "s_waitcnt lgkmcnt(2)\n"
    "v_mfma_f32_16x16x32_bf16 a[192:195], v[116:119], v[182:185], a[192:195]\n"
    "ds_read_b64_tr_b16 v[112:113], v250 offset:8704\n"
    "v_mfma_f32_16x16x32_bf16 a[196:199], v[116:119], v[186:189], a[196:199]\n"
    "ds_read_b64_tr_b16 v[114:115], v250 offset:9728\n"
    "v_mfma_f32_16x16x32_bf16 a[200:203], v[116:119], v[190:193], a[200:203]\n"
    "v_mfma_f32_16x16x32_bf16 a[204:207], v[116:119], v[194:197], a[204:207]\n"
    "s_add_u32 s61, s75, 49152\n"
    "buffer_load_dwordx4 v[170:173], v241, s[56:59], s61 offen offset:1024 sc1\n"
    "s_waitcnt lgkmcnt(2)\n"
    "v_mfma_f32_16x16x32_bf16 a[208:211], v[120:123], v[182:185], a[208:211]\n"
    "ds_read_b64_tr_b16 v[116:117], v251 offset:8704\n"
    "v_mfma_f32_16x16x32_bf16 a[212:215], v[120:123], v[186:189], a[212:215]\n"
    "ds_read_b64_tr_b16 v[118:119], v251 offset:9728\n"
    "v_mfma_f32_16x16x32_bf16 a[216:219], v[120:123], v[190:193], a[216:219]\n"
    "v_mfma_f32_16x16x32_bf16 a[220:223], v[120:123], v[194:197], a[220:223]\n"
    "s_waitcnt lgkmcnt(2)\n"
    "v_mfma_f32_16x16x32_bf16 a[224:227], v[112:115], v[182:185], a[224:227]\n"
    "ds_read_b64_tr_b16 v[120:121], v250 offset:16384\n"
    "s_add_u32 s61, s75, 49152\n"
    "buffer_load_dwordx4 v[174:177], v241, s[56:59], s61 offen offset:2048 sc1\n"
    "v_mfma_f32_16x16x32_bf16 a[228:231], v[112:115], v[186:189], a[228:231]\n"
    "ds_read_b64_tr_b16 v[122:123], v250 offset:17408\n"
    "v_mfma_f32_16x16x32_bf16 a[232:235], v[112:115], v[190:193], a[232:235]\n"
    "v_mfma_f32_16x16x32_bf16 a[236:239], v[112:115], v[194:197], a[236:239]\n"
    "s_waitcnt lgkmcnt(2)\n"
    "v_mfma_f32_16x16x32_bf16 a[240:243], v[116:119], v[182:185], a[240:243]\n"
    "ds_read_b64_tr_b16 v[112:113], v251 offset:16384\n"
    "v_mfma_f32_16x16x32_bf16 a[244:247], v[116:119], v[186:189], a[244:247]\n"
    "ds_read_b64_tr_b16 v[114:115], v251 offset:17408\n"
    "s_add_u32 s61, s75, 49152\n"
    "buffer_load_dwordx4 v[178:181], v241, s[56:59], s61 offen offset:3072 sc1\n"
    "v_mfma_f32_16x16x32_bf16 a[248:251], v[116:119], v[190:193], a[248:251]\n"
    "v_mfma_f32_16x16x32_bf16 a[252:255], v[116:119], v[194:197], a[252:255]\n"
    "s_waitcnt lgkmcnt(2)\n"
    "v_mfma_f32_16x16x32_bf16 a[64:67], v[120:123], v[182:185], a[64:67]\n"
    "ds_read_b64_tr_b16 v[116:117], v250 offset:16896\n"
    "v_mfma_f32_16x16x32_bf16 a[68:71], v[120:123], v[186:189], a[68:71]\n"
    "ds_read_b64_tr_b16 v[118:119], v250 offset:17920\n"
    "v_mfma_f32_16x16x32_bf16 a[72:75], v[120:123], v[190:193], a[72:75]\n"
    "v_mfma_f32_16x16x32_bf16 a[76:79], v[120:123], v[194:197], a[76:79]\n"
    "s_waitcnt lgkmcnt(2)\n"
    "v_mfma_f32_16x16x32_bf16 a[80:83], v[112:115], v[182:185], a[80:83]\n"
    "ds_read_b64_tr_b16 v[120:121], v251 offset:16896\n"
    "v_mfma_f32_16x16x32_bf16 a[84:87], v[112:115], v[186:189], a[84:87]\n"
    "ds_read_b64_tr_b16 v[122:123], v251 offset:17920\n"
    "v_mfma_f32_16x16x32_bf16 a[88:91], v[112:115], v[190:193], a[88:91]\n"
    "v_mfma_f32_16x16x32_bf16 a[92:95], v[112:115], v[194:197], a[92:95]\n"
    "s_waitcnt lgkmcnt(2)\n"
    "v_mfma_f32_16x16x32_bf16 a[96:99], v[116:119], v[182:185], a[96:99]\n"
    "s_barrier\n"
    "ds_read_b64_tr_b16 v[112:113], v250 offset:24576\n"
    "v_mfma_f32_16x16x32_bf16 a[100:103], v[116:119], v[186:189], a[100:103]\n"
    "ds_read_b64_tr_b16 v[114:115], v250 offset:25600\n"
    "s_add_u32 m0, s64, 131072\n"
    "v_mfma_f32_16x16x32_bf16 a[104:107], v[116:119], v[190:193], a[104:107]\n"
    "s_add_u32 s65, s25, 294912\n"
    "v_mfma_f32_16x16x32_bf16 a[108:111], v[116:119], v[194:197], a[108:111]\n"
    "buffer_load_dwordx4 v240, s[48:51], s65 offen offset:0 lds\n"
    "s_waitcnt lgkmcnt(2)\n"
    "v_mfma_f32_16x16x32_bf16 a[112:115], v[120:123], v[182:185], a[112:115]\n"
    "ds_read_b64_tr_b16 v[116:117], v251 offset:24576\n"
    "v_mfma_f32_16x16x32_bf16 a[116:119], v[120:123], v[186:189], a[116:119]\n"
    "ds_read_b64_tr_b16 v[118:119], v251 offset:25600\n"
    "v_mfma_f32_16x16x32_bf16 a[120:123], v[120:123], v[190:193], a[120:123]\n"
    "s_add_u32 s61, s74, 49152\n"
    "buffer_load_dwordx4 v[150:153], v241, s[56:59], s61 offen offset:0 sc1\n"
    "v_mfma_f32_16x16x32_bf16 a[124:127], v[120:123], v[194:197], a[124:127]\n"
    "s_waitcnt lgkmcnt(2)\n"
    "v_mfma_f32_16x16x32_bf16 a[128:131], v[112:115], v[182:185], a[128:131]\n"
    "ds_read_b64_tr_b16 v[120:121], v250 offset:25088\n"
    "v_mfma_f32_16x16x32_bf16 a[132:135], v[112:115], v[186:189], a[132:135]\n"
    "ds_read_b64_tr_b16 v[122:123], v250 offset:26112\n"
    "v_mfma_f32_16x16x32_bf16 a[136:139], v[112:115], v[190:193], a[136:139]\n"
    "s_add_u32 s61, s74, 49152\n"
    "buffer_load_dwordx4 v[154:157], v241, s[56:59], s61 offen offset:1024 sc1\n"
    "v_mfma_f32_16x16x32_bf16 a[140:143], v[112:115], v[194:197], a[140:143]\n"
    "s_waitcnt lgkmcnt(2)\n"
    "v_mfma_f32_16x16x32_bf16 a[144:147], v[116:119], v[182:185], a[144:147]\n"
    "ds_read_b64_tr_b16 v[112:113], v251 offset:25088\n"
    "s_add_u32 m0, s64, 135168\n"
    "v_mfma_f32_16x16x32_bf16 a[148:151], v[116:119], v[186:189], a[148:151]\n"
    "ds_read_b64_tr_b16 v[114:115], v251 offset:26112\n"
    "s_add_u32 s65, s25, 331776\n"
    "v_mfma_f32_16x16x32_bf16 a[152:155], v[116:119], v[190:193], a[152:155]\n"
    "buffer_load_dwordx4 v240, s[48:51], s65 offen offset:0 lds\n"
    "v_mfma_f32_16x16x32_bf16 a[156:159], v[116:119], v[194:197], a[156:159]\n"
    "s_waitcnt lgkmcnt(2)\n"
    "v_mfma_f32_16x16x32_bf16 a[160:163], v[120:123], v[182:185], a[160:163]\n"
    "ds_read_b64_tr_b16 v[116:117], v250 offset:32768\n"
    "v_mfma_f32_16x16x32_bf16 a[164:167], v[120:123], v[186:189], a[164:167]\n"
    "ds_read_b64_tr_b16 v[118:119], v250 offset:33792\n"
    "s_add_u32 s61, s74, 49152\n"
    "buffer_load_dwordx4 v[158:161], v241, s[56:59], s61 offen offset:2048 sc1\n"
    "v_mfma_f32_16x16x32_bf16 a[168:171], v[120:123], v[190:193], a[168:171]\n"
    "v_mfma_f32_16x16x32_bf16 a[172:175], v[120:123], v[194:197], a[172:175]\n"
    "s_waitcnt lgkmcnt(2)\n"
    "v_mfma_f32_16x16x32_bf16 a[176:179], v[112:115], v[182:185], a[176:179]\n"
    "ds_read_b64_tr_b16 v[120:121], v251 offset:32768\n"
    "v_mfma_f32_16x16x32_bf16 a[180:183], v[112:115], v[186:189], a[180:183]\n"
    "ds_read_b64_tr_b16 v[122:123], v251 offset:33792\n"
    "s_add_u32 s61, s75, 65536\n"
    "buffer_load_dwordx4 v[16:19], v241, s[56:59], s61 offen offset:0 sc1\n"
    "v_mfma_f32_16x16x32_bf16 a[184:187], v[112:115], v[190:193], a[184:187]\n"
    "s_add_u32 s61, s74, 49152\n"
    "buffer_load_dwordx4 v[162:165], v241, s[56:59], s61 offen offset:3072 sc1\n"
    "v_mfma_f32_16x16x32_bf16 a[188:191], v[112:115], v[194:197], a[188:191]\n"
    "s_waitcnt vmcnt(42) lgkmcnt(2)\n"
    "v_mfma_f32_16x16x32_bf16 a[192:195], v[116:119], a[48:51], a[192:195]\n"
    "ds_read_b64_tr_b16 v[112:113], v250 offset:33280\n"
    "s_add_u32 m0, s64, 139264\n"
    "s_waitcnt vmcnt(40)\n"
    "v_mfma_f32_16x16x32_bf16 a[196:199], v[116:119], a[52:55], a[196:199]\n"
    "ds_read_b64_tr_b16 v[114:115], v250 offset:34304\n"
    "s_add_u32 s65, s26, 221184\n"
    "s_waitcnt vmcnt(39)\n"
    "v_mfma_f32_16x16x32_bf16 a[200:203], v[116:119], a[56:59], a[200:203]\n"
    "buffer_load_dwordx4 v240, s[48:51], s65 offen offset:0 lds\n"
    "s_waitcnt vmcnt(38)\n"
    "v_mfma_f32_16x16x32_bf16 a[204:207], v[116:119], a[60:63], a[204:207]\n"
    "s_waitcnt lgkmcnt(2)\n"
    "v_mfma_f32_16x16x32_bf16 a[208:211], v[120:123], a[48:51], a[208:211]\n"
    "ds_read_b64_tr_b16 v[116:117], v251 offset:33280\n"
    "v_mfma_f32_16x16x32_bf16 a[212:215], v[120:123], a[52:55], a[212:215]\n"
    "ds_read_b64_tr_b16 v[118:119], v251 offset:34304\n"
    "s_add_u32 s61, s75, 65536\n"
    "buffer_load_dwordx4 v[20:23], v241, s[56:59], s61 offen offset:1024 sc1\n"
    "v_mfma_f32_16x16x32_bf16 a[216:219], v[120:123], a[56:59], a[216:219]\n"
    "v_mfma_f32_16x16x32_bf16 a[220:223], v[120:123], a[60:63], a[220:223]\n"
    "s_waitcnt lgkmcnt(2)\n"
    "v_mfma_f32_16x16x32_bf16 a[224:227], v[112:115], a[48:51], a[224:227]\n"
    "ds_read_b64_tr_b16 v[120:121], v250 offset:40960\n"
    "s_add_u32 s61, s75, 65536\n"
    "buffer_load_dwordx4 v[24:27], v241, s[56:59], s61 offen offset:2048 sc1\n"
    "v_mfma_f32_16x16x32_bf16 a[228:231], v[112:115], a[52:55], a[228:231]\n"
    "ds_read_b64_tr_b16 v[122:123], v250 offset:41984\n"
    "v_mfma_f32_16x16x32_bf16 a[232:235], v[112:115], a[56:59], a[232:235]\n"
    "v_mfma_f32_16x16x32_bf16 a[236:239], v[112:115], a[60:63], a[236:239]\n"
    "s_add_u32 s61, s75, 65536\n"
    "buffer_load_dwordx4 v[28:31], v241, s[56:59], s61 offen offset:3072 sc1\n"
    "s_waitcnt lgkmcnt(2)\n"
    "v_mfma_f32_16x16x32_bf16 a[240:243], v[116:119], a[48:51], a[240:243]\n"
    "ds_read_b64_tr_b16 v[112:113], v251 offset:40960\n"
    "s_add_u32 m0, s64, 143360\n"
    "v_mfma_f32_16x16x32_bf16 a[244:247], v[116:119], a[52:55], a[244:247]\n"
    "ds_read_b64_tr_b16 v[114:115], v251 offset:41984\n"
    "s_add_u32 s65, s26, 258048\n"
    "v_mfma_f32_16x16x32_bf16 a[248:251], v[116:119], a[56:59], a[248:251]\n"
    "buffer_load_dwordx4 v240, s[48:51], s65 offen offset:0 lds\n"
    "v_mfma_f32_16x16x32_bf16 a[252:255], v[116:119], a[60:63], a[252:255]\n"
    "s_waitcnt vmcnt(32) lgkmcnt(2)\n"
    "v_mfma_f32_16x16x32_bf16 a[192:195], v[120:123], a[32:35], a[192:195]\n"
    "ds_read_b64_tr_b16 v[116:117], v250 offset:41472\n"
    "s_waitcnt vmcnt(29)\n"
    "v_mfma_f32_16x16x32_bf16 a[196:199], v[120:123], a[36:39], a[196:199]\n"
    "ds_read_b64_tr_b16 v[118:119], v250 offset:42496\n"
    "s_waitcnt vmcnt(25)\n"
    "v_mfma_f32_16x16x32_bf16 a[200:203], v[120:123], a[40:43], a[200:203]\n"
    "s_waitcnt vmcnt(22)\n"
    "v_mfma_f32_16x16x32_bf16 a[204:207], v[120:123], a[44:47], a[204:207]\n"
    "s_waitcnt lgkmcnt(2)\n"
    "v_mfma_f32_16x16x32_bf16 a[208:211], v[112:115], a[32:35], a[208:211]\n"
    "ds_read_b64_tr_b16 v[120:121], v251 offset:41472\n"
    "v_mfma_f32_16x16x32_bf16 a[212:215], v[112:115], a[36:39], a[212:215]\n"
    "ds_read_b64_tr_b16 v[122:123], v251 offset:42496\n"
    "v_mfma_f32_16x16x32_bf16 a[216:219], v[112:115], a[40:43], a[216:219]\n"
    "v_mfma_f32_16x16x32_bf16 a[220:223], v[112:115], a[44:47], a[220:223]\n"
    "s_waitcnt lgkmcnt(2)\n"
    "v_mfma_f32_16x16x32_bf16 a[224:227], v[116:119], a[32:35], a[224:227]\n"
    "s_barrier\n"
    "ds_read_b64_tr_b16 v[112:113], v250 offset:49152\n"
    "v_mfma_f32_16x16x32_bf16 a[228:231], v[116:119], a[36:39], a[228:231]\n"
    "ds_read_b64_tr_b16 v[114:115], v250 offset:50176\n"
    "s_add_u32 m0, s64, 0\n"
    "v_mfma_f32_16x16x32_bf16 a[232:235], v[116:119], a[40:43], a[232:235]\n"
    "s_add_u32 s65, s27, 294912\n"
    "v_mfma_f32_16x16x32_bf16 a[236:239], v[116:119], a[44:47], a[236:239]\n"
    "buffer_load_dwordx4 v240, s[48:51], s65 offen offset:0 lds\n"
    "s_waitcnt lgkmcnt(2)\n"
    "v_mfma_f32_16x16x32_bf16 a[240:243], v[120:123], a[32:35], a[240:243]\n"
    "ds_read_b64_tr_b16 v[116:117], v251 offset:49152\n"
    "s_add_u32 m0, s64, 4096\n"
    "v_mfma_f32_16x16x32_bf16 a[244:247], v[120:123], a[36:39], a[244:247]\n"
    "ds_read_b64_tr_b16 v[118:119], v251 offset:50176\n"
    "s_add_u32 s65, s27, 331776\n"
    "v_mfma_f32_16x16x32_bf16 a[248:251], v[120:123], a[40:43], a[248:251]\n"
    "buffer_load_dwordx4 v240, s[48:51], s65 offen offset:0 lds\n"
    "v_mfma_f32_16x16x32_bf16 a[252:255], v[120:123], a[44:47], a[252:255]\n"
    "s_waitcnt lgkmcnt(2)\n"
    "v_mfma_f32_16x16x32_bf16 a[192:195], v[112:115], a[16:19], a[192:195]\n"
    "ds_read_b64_tr_b16 v[120:121], v250 offset:57344\n"
    "s_add_u32 m0, s64, 8192\n"
    "v_mfma_f32_16x16x32_bf16 a[196:199], v[112:115], a[20:23], a[196:199]\n"
    "ds_read_b64_tr_b16 v[122:123], v250 offset:58368\n"
    "s_add_u32 s65, s28, 0\n"
    "v_mfma_f32_16x16x32_bf16 a[200:203], v[112:115], a[24:27], a[200:203]\n"
    "buffer_load_dwordx4 v240, s[52:55], s65 offen offset:0 lds\n"
    "v_mfma_f32_16x16x32_bf16 a[204:207], v[112:115], a[28:31], a[204:207]\n"
    "s_waitcnt lgkmcnt(2)\n"
    "v_mfma_f32_16x16x32_bf16 a[208:211], v[116:119], a[16:19], a[208:211]\n"
    "ds_read_b64_tr_b16 v[112:113], v251 offset:57344\n"
    "s_add_u32 m0, s64, 12288\n"
    "v_mfma_f32_16x16x32_bf16 a[212:215], v[116:119], a[20:23], a[212:215]\n"
    "ds_read_b64_tr_b16 v[114:115], v251 offset:58368\n"
    "s_add_u32 s65, s28, 82944\n"
    "v_mfma_f32_16x16x32_bf16 a[216:219], v[116:119], a[24:27], a[216:219]\n"
    "buffer_load_dwordx4 v240, s[52:55], s65 offen offset:0 lds\n"
    "v_mfma_f32_16x16x32_bf16 a[220:223], v[116:119], a[28:31], a[220:223]\n"
    "s_waitcnt lgkmcnt(2)\n"
    "v_mfma_f32_16x16x32_bf16 a[192:195], v[120:123], a[0:3], a[192:195]\n"
    "ds_read_b64_tr_b16 v[116:117], v252 offset:0\n"
    "s_add_u32 m0, s64, 16384\n"
    "v_mfma_f32_16x16x32_bf16 a[196:199], v[120:123], a[4:7], a[196:199]\n"
    "ds_read_b64_tr_b16 v[118:119], v252 offset:1024\n"
    "s_add_u32 s65, s28, 36864\n"
    "v_mfma_f32_16x16x32_bf16 a[200:203], v[120:123], a[8:11], a[200:203]\n"
    "buffer_load_dwordx4 v240, s[52:55], s65 offen offset:0 lds\n"
    "v_mfma_f32_16x16x32_bf16 a[204:207], v[120:123], a[12:15], a[204:207]\n"
    "s_waitcnt lgkmcnt(2)\n"
    "v_mfma_f32_16x16x32_bf16 a[208:211], v[112:115], a[0:3], a[208:211]\n"
    "ds_read_b64_tr_b16 v[120:121], v253 offset:0\n"
    "s_add_u32 m0, s64, 20480\n"
    "v_mfma_f32_16x16x32_bf16 a[212:215], v[112:115], a[4:7], a[212:215]\n"
    "ds_read_b64_tr_b16 v[122:123], v253 offset:1024\n"
    "s_add_u32 s65, s28, 119808\n"
    "v_mfma_f32_16x16x32_bf16 a[216:219], v[112:115], a[8:11], a[216:219]\n"
    "buffer_load_dwordx4 v240, s[52:55], s65 offen offset:0 lds\n"
    "v_mfma_f32_16x16x32_bf16 a[220:223], v[112:115], a[12:15], a[220:223]\n"
    "s_waitcnt lgkmcnt(2)\n"
    "v_mfma_f32_16x16x32_bf16 a[192:195], v[116:119], v[0:3], a[192:195]\n"
    "s_barrier\n"
    "ds_read_b64_tr_b16 v[112:113], v252 offset:8192\n"
    "v_mfma_f32_16x16x32_bf16 a[196:199], v[116:119], v[4:7], a[196:199]\n"
    "ds_read_b64_tr_b16 v[114:115], v252 offset:9216\n"
    "s_add_u32 m0, s64, 24576\n"
    "s_waitcnt vmcnt(27)\n"
    "v_mfma_f32_16x16x32_bf16 a[200:203], v[116:119], v[8:11], a[200:203]\n"
    "s_add_u32 s65, s28, 73728\n"
    "s_waitcnt vmcnt(26)\n"
    "v_mfma_f32_16x16x32_bf16 a[204:207], v[116:119], v[12:15], a[204:207]\n"
    "buffer_load_dwordx4 v240, s[52:55], s65 offen offset:0 lds\n"
    "s_waitcnt lgkmcnt(2)\n"
    "v_mfma_f32_16x16x32_bf16 a[208:211], v[120:123], v[0:3], a[208:211]\n"
    "ds_read_b64_tr_b16 v[116:117], v253 offset:8192\n"
    "v_mfma_f32_16x16x32_bf16 a[212:215], v[120:123], v[4:7], a[212:215]\n"
    "ds_read_b64_tr_b16 v[118:119], v253 offset:9216\n"
    "v_mfma_f32_16x16x32_bf16 a[216:219], v[120:123], v[8:11], a[216:219]\n"
    "v_mfma_f32_16x16x32_bf16 a[220:223], v[120:123], v[12:15], a[220:223]\n"
    "s_waitcnt lgkmcnt(2)\n"
    "v_mfma_f32_16x16x32_bf16 a[128:131], v[112:115], a[48:51], a[128:131]\n"
    "ds_read_b64_tr_b16 v[120:121], v252 offset:8704\n"
    "s_add_u32 m0, s64, 28672\n"
    "v_mfma_f32_16x16x32_bf16 a[132:135], v[112:115], a[52:55], a[132:135]\n"
    "ds_read_b64_tr_b16 v[122:123], v252 offset:9728\n"
    "s_add_u32 s65, s28, 156672\n"
    "v_mfma_f32_16x16x32_bf16 a[136:139], v[112:115], a[56:59], a[136:139]\n"
    "buffer_load_dwordx4 v240, s[52:55], s65 offen offset:0 lds\n"
    "v_lshl_add_u32 v128, s66, 13, v241\n"
    "v_mfma_f32_16x16x32_bf16 a[140:143], v[112:115], a[60:63], a[140:143]\n"
    "v_xor_b32 v129, 0x2000, v128\n"
    "s_waitcnt lgkmcnt(2)\n"
    "v_mfma_f32_16x16x32_bf16 a[144:147], v[116:119], a[48:51], a[144:147]\n"
    "ds_read_b64_tr_b16 v[112:113], v253 offset:8704\n"
    "v_add_u32 v128, 98304, v128\n"
    "v_add_u32 v129, 98304, v129\n"
    "v_mfma_f32_16x16x32_bf16 a[148:151], v[116:119], a[52:55], a[148:151]\n"
    "ds_read_b64_tr_b16 v[114:115], v253 offset:9728\n"
    "v_mfma_f32_16x16x32_bf16 a[152:155], v[116:119], a[56:59], a[152:155]\n"
    "ds_write_b128 v128, a[200:203] offset:0\n"
    "v_mfma_f32_16x16x32_bf16 a[156:159], v[116:119], a[60:63], a[156:159]\n"
    "ds_write_b128 v128, a[216:219] offset:1024\n"
    "s_waitcnt lgkmcnt(4)\n"
    "v_mfma_f32_16x16x32_bf16 a[160:163], v[120:123], a[48:51], a[160:163]\n"
    "ds_read_b64_tr_b16 v[116:117], v252 offset:16384\n"
    "s_add_u32 m0, s64, 32768\n"
    "v_mfma_f32_16x16x32_bf16 a[164:167], v[120:123], a[52:55], a[164:167]\n"
    "ds_read_b64_tr_b16 v[118:119], v252 offset:17408\n"
    "s_add_u32 s65, s77, 221184\n"
    "v_mfma_f32_16x16x32_bf16 a[168:171], v[120:123], a[56:59], a[168:171]\n"
    "buffer_load_dwordx4 v240, s[48:51], s65 offen offset:0 lds\n"
    "v_mfma_f32_16x16x32_bf16 a[172:175], v[120:123], a[60:63], a[172:175]\n"
    "ds_write_b128 v128, a[232:235] offset:2048\n"
    "s_waitcnt lgkmcnt(5)\n"
    "v_mfma_f32_16x16x32_bf16 a[176:179], v[112:115], a[48:51], a[176:179]\n"
    "ds_read_b64_tr_b16 v[120:121], v253 offset:16384\n"
    "v_mfma_f32_16x16x32_bf16 a[180:183], v[112:115], a[52:55], a[180:183]\n"
    "ds_read_b64_tr_b16 v[122:123], v253 offset:17408\n"
    "ds_write_b128 v128, a[248:251] offset:3072\n"
    "v_mfma_f32_16x16x32_bf16 a[184:187], v[112:115], a[56:59], a[184:187]\n"
    "v_mfma_f32_16x16x32_bf16 a[188:191], v[112:115], a[60:63], a[188:191]\n"
    "ds_write_b128 v128, a[204:207] offset:4096\n"
    "s_waitcnt vmcnt(28) lgkmcnt(5)\n"
    "v_mfma_f32_16x16x32_bf16 a[128:131], v[116:119], v[134:137], a[128:131]\n"
    "ds_read_b64_tr_b16 v[112:113], v252 offset:16896\n"
    "s_add_u32 m0, s64, 36864\n"
    "s_waitcnt vmcnt(27)\n"
    "v_mfma_f32_16x16x32_bf16 a[132:135], v[116:119], v[138:141], a[132:135]\n"
    "ds_read_b64_tr_b16 v[114:115], v252 offset:17920\n"
    "s_add_u32 s65, s77, 258048\n"
    "s_waitcnt vmcnt(26)\n"
    "v_mfma_f32_16x16x32_bf16 a[136:139], v[116:119], v[142:145], a[136:139]\n"
    "buffer_load_dwordx4 v240, s[48:51], s65 offen offset:0 lds\n"
    "s_waitcnt vmcnt(26)\n"
    "v_mfma_f32_16x16x32_bf16 a[140:143], v[116:119], v[146:149], a[140:143]\n"
    "ds_write_b128 v128, a[220:223] offset:5120\n"
    "s_waitcnt lgkmcnt(5)\n"
    "v_mfma_f32_16x16x32_bf16 a[144:147], v[120:123], v[134:137], a[144:147]\n"
    "ds_read_b64_tr_b16 v[116:117], v253 offset:16896\n"
    "v_mfma_f32_16x16x32_bf16 a[148:151], v[120:123], v[138:141], a[148:151]\n"
    "ds_read_b64_tr_b16 v[118:119], v253 offset:17920\n"
    "ds_write_b128 v128, a[236:239] offset:6144\n"
    "v_mfma_f32_16x16x32_bf16 a[152:155], v[120:123], v[142:145], a[152:155]\n"
    "v_mfma_f32_16x16x32_bf16 a[156:159], v[120:123], v[146:149], a[156:159]\n"
    "ds_write_b128 v128, a[252:255] offset:7168\n"
    "s_waitcnt lgkmcnt(5)\n"
    "v_mfma_f32_16x16x32_bf16 a[160:163], v[112:115], v[134:137], a[160:163]\n"
    "ds_read_b64_tr_b16 v[120:121], v252 offset:24576\n"
    "s_add_u32 m0, s64, 40960\n"
    "v_mfma_f32_16x16x32_bf16 a[164:167], v[112:115], v[138:141], a[164:167]\n"
    "ds_read_b64_tr_b16 v[122:123], v252 offset:25600\n"
    "s_add_u32 s65, s78, 147456\n"
    "v_mfma_f32_16x16x32_bf16 a[168:171], v[112:115], v[142:145], a[168:171]\n"
    "buffer_load_dwordx4 v240, s[48:51], s65 offen offset:0 lds\n"
    "v_mfma_f32_16x16x32_bf16 a[172:175], v[112:115], v[146:149], a[172:175]\n"
    "s_waitcnt lgkmcnt(4)\n"
    "v_mfma_f32_16x16x32_bf16 a[176:179], v[116:119], v[134:137], a[176:179]\n"
    "ds_read_b64_tr_b16 v[112:113], v253 offset:24576\n"
    "v_mfma_f32_16x16x32_bf16 a[180:183], v[116:119], v[138:141], a[180:183]\n"
    "ds_read_b64_tr_b16 v[114:115], v253 offset:25600\n"
    "v_mfma_f32_16x16x32_bf16 a[184:187], v[116:119], v[142:145], a[184:187]\n"
    "v_mfma_f32_16x16x32_bf16 a[188:191], v[116:119], v[146:149], a[188:191]\n"
    "s_waitcnt vmcnt(26) lgkmcnt(2)\n"
    "v_mfma_f32_16x16x32_bf16 a[128:131], v[120:123], v[166:169], a[128:131]\n"
    "ds_read_b64_tr_b16 v[116:117], v252 offset:25088\n"
    "s_add_u32 m0, s64, 45056\n"
    "s_waitcnt vmcnt(25)\n"
    "v_mfma_f32_16x16x32_bf16 a[132:135], v[120:123], v[170:173], a[132:135]\n"
    "ds_read_b64_tr_b16 v[118:119], v252 offset:26112\n"
    "s_add_u32 s65, s78, 184320\n"
    "s_waitcnt vmcnt(24)\n"
    "v_mfma_f32_16x16x32_bf16 a[136:139], v[120:123], v[174:177], a[136:139]\n"
    "buffer_load_dwordx4 v240, s[48:51], s65 offen offset:0 lds\n"
    "s_waitcnt vmcnt(24)\n"
    "v_mfma_f32_16x16x32_bf16 a[140:143], v[120:123], v[178:181], a[140:143]\n"
    "s_waitcnt lgkmcnt(2)\n"
    "v_mfma_f32_16x16x32_bf16 a[144:147], v[112:115], v[166:169], a[144:147]\n"
    "ds_read_b64_tr_b16 v[120:121], v253 offset:25088\n"
    "v_mfma_f32_16x16x32_bf16 a[148:151], v[112:115], v[170:173], a[148:151]\n"
    "ds_read_b64_tr_b16 v[122:123], v253 offset:26112\n"
    "v_mfma_f32_16x16x32_bf16 a[152:155], v[112:115], v[174:177], a[152:155]\n"
    "v_mfma_f32_16x16x32_bf16 a[156:159], v[112:115], v[178:181], a[156:159]\n"
    "s_waitcnt lgkmcnt(2)\n"
    "v_mfma_f32_16x16x32_bf16 a[160:163], v[116:119], v[166:169], a[160:163]\n"
    "s_waitcnt vmcnt(10)\n"
    "s_barrier\n"
    "ds_read_b64_tr_b16 v[112:113], v254 offset:0\n"
    "v_mfma_f32_16x16x32_bf16 a[164:167], v[116:119], v[170:173], a[164:167]\n"
    "ds_read_b64_tr_b16 v[114:115], v254 offset:1024\n"
    "s_add_u32 m0, s64, 49152\n"
    "v_mfma_f32_16x16x32_bf16 a[168:171], v[116:119], v[174:177], a[168:171]\n"
    "s_add_u32 s65, s79, 221184\n"
    "ds_read_b128 v[32:35], v129 offset:0\n"
    "ds_read_b128 v[36:39], v129 offset:1024\n"
    "v_mfma_f32_16x16x32_bf16 a[172:175], v[116:119], v[178:181], a[172:175]\n"
    "buffer_load_dwordx4 v240, s[48:51], s65 offen offset:0 lds\n"
    "ds_read_b128 v[40:43], v129 offset:2048\n"
    "s_waitcnt lgkmcnt(5)\n"
    "v_mfma_f32_16x16x32_bf16 a[176:179], v[120:123], v[166:169], a[176:179]\n"
    "ds_read_b64_tr_b16 v[116:117], v255 offset:0\n"
    "ds_read_b128 v[44:47], v129 offset:3072\n"
    "ds_read_b128 v[48:51], v129 offset:4096\n"
    "v_mfma_f32_16x16x32_bf16 a[180:183], v[120:123], v[170:173], a[180:183]\n"
    "ds_read_b64_tr_b16 v[118:119], v255 offset:1024\n"
    "ds_read_b128 v[52:55], v129 offset:5120\n"
    "ds_read_b128 v[56:59], v129 offset:6144\n"
    "v_mfma_f32_16x16x32_bf16 a[184:187], v[120:123], v[174:177], a[184:187]\n"
    "ds_read_b128 v[60:63], v129 offset:7168\n"
    "v_accvgpr_read_b32 v124, a192\n"
    "v_accvgpr_read_b32 v125, a193\n"
    "v_accvgpr_read_b32 v126, a194\n"
    "v_mfma_f32_16x16x32_bf16 a[188:191], v[120:123], v[178:181], a[188:191]\n"
    "v_accvgpr_read_b32 v127, a195\n"
    "s_waitcnt lgkmcnt(9)\n"
    "v_add_f32 v32, v32, v124\n"
    "v_mfma_f32_16x16x32_bf16 a[128:131], v[112:115], a[32:35], a[128:131]\n"
    "ds_read_b64_tr_b16 v[120:121], v254 offset:512\n"
    "v_add_f32 v33, v33, v125\n"
    "v_add_f32 v34, v34, v126\n"
    "v_mfma_f32_16x16x32_bf16 a[132:135], v[112:115], a[36:39], a[132:135]\n"
    "ds_read_b64_tr_b16 v[122:123], v254 offset:1536\n"
    "v_add_f32 v35, v35, v127\n"
    "v_accvgpr_read_b32 v124, a208\n"
    "v_mfma_f32_16x16x32_bf16 a[136:139], v[112:115], a[40:43], a[136:139]\n"
    "v_accvgpr_read_b32 v125, a209\n"
    "v_accvgpr_read_b32 v126, a210\n"
    "v_accvgpr_read_b32 v127, a211\n"
    "s_waitcnt lgkmcnt(10)\n"
    "v_add_f32 v36, v36, v124\n"
    "v_mfma_f32_16x16x32_bf16 a[140:143], v[112:115], a[44:47], a[140:143]\n"
    "v_add_f32 v37, v37, v125\n"
    "v_add_f32 v38, v38, v126\n"
    "s_waitcnt lgkmcnt(5)\n"
    "v_mfma_f32_16x16x32_bf16 a[144:147], v[116:119], a[32:35], a[144:147]\n"
    "ds_read_b64_tr_b16 v[112:113], v255 offset:512\n"
    "v_add_f32 v39, v39, v127\n"
    "v_accvgpr_read_b32 v124, a224\n"
    "v_mfma_f32_16x16x32_bf16 a[148:151], v[116:119], a[36:39], a[148:151]\n"
    "ds_read_b64_tr_b16 v[114:115], v255 offset:1536\n"
    "v_accvgpr_read_b32 v125, a225\n"
    "v_accvgpr_read_b32 v126, a226\n"
    "v_mfma_f32_16x16x32_bf16 a[152:155], v[116:119], a[40:43], a[152:155]\n"
    "v_accvgpr_read_b32 v127, a227\n"
    "v_add_f32 v40, v40, v124\n"
    "v_add_f32 v41, v41, v125\n"
    "v_add_f32 v42, v42, v126\n"
    "v_mfma_f32_16x16x32_bf16 a[156:159], v[116:119], a[44:47], a[156:159]\n"
    "v_add_f32 v43, v43, v127\n"
    "v_accvgpr_read_b32 v124, a240\n"
    "s_waitcnt lgkmcnt(2)\n"
    "v_mfma_f32_16x16x32_bf16 a[160:163], v[120:123], a[32:35], a[160:163]\n"
    "ds_read_b64_tr_b16 v[116:117], v254 offset:8192\n"
    "v_accvgpr_read_b32 v125, a241\n"
    "v_accvgpr_read_b32 v126, a242\n"
    "v_mfma_f32_16x16x32_bf16 a[164:167], v[120:123], a[36:39], a[164:167]\n"
    "ds_read_b64_tr_b16 v[118:119], v254 offset:9216\n"
    "v_accvgpr_read_b32 v127, a243\n"
    "v_add_f32 v44, v44, v124\n"
    "v_mfma_f32_16x16x32_bf16 a[168:171], v[120:123], a[40:43], a[168:171]\n"
    "v_add_f32 v45, v45, v125\n"
    "v_add_f32 v46, v46, v126\n"
    "v_add_f32 v47, v47, v127\n"
    "v_cvt_pk_bf16_f32 v32, v32, v33\n"
    "v_mfma_f32_16x16x32_bf16 a[172:175], v[120:123], a[44:47], a[172:175]\n"
    "v_cvt_pk_bf16_f32 v33, v34, v35\n"
    "v_cvt_pk_bf16_f32 v34, v36, v37\n"
    "s_waitcnt lgkmcnt(2)\n"
    "v_mfma_f32_16x16x32_bf16 a[176:179], v[112:115], a[32:35], a[176:179]\n"
    "ds_read_b64_tr_b16 v[120:121], v255 offset:8192\n"
    "v_cvt_pk_bf16_f32 v35, v38, v39\n"
    "v_cvt_pk_bf16_f32 v36, v40, v41\n"
    "v_mfma_f32_16x16x32_bf16 a[180:183], v[112:115], a[36:39], a[180:183]\n"
    "ds_read_b64_tr_b16 v[122:123], v255 offset:9216\n"
    "v_cvt_pk_bf16_f32 v37, v42, v43\n"
    "v_cvt_pk_bf16_f32 v38, v44, v45\n"
    "v_mfma_f32_16x16x32_bf16 a[184:187], v[112:115], a[40:43], a[184:187]\n"
    "v_cvt_pk_bf16_f32 v39, v46, v47\n"
    "s_mov_b32 s20, s92\n"
    "buffer_store_dwordx4 v[32:35], v241, s[56:59], s20 offen offset:0 sc1\n"
    "v_mfma_f32_16x16x32_bf16 a[188:191], v[112:115], a[44:47], a[188:191]\n"
    "s_waitcnt lgkmcnt(2)\n"
    "v_mfma_f32_16x16x32_bf16 a[128:131], v[116:119], v[150:153], a[128:131]\n"
    "ds_read_b64_tr_b16 v[112:113], v254 offset:8704\n"
    "s_add_u32 m0, s64, 53248\n"
    "v_mfma_f32_16x16x32_bf16 a[132:135], v[116:119], v[154:157], a[132:135]\n"
    "ds_read_b64_tr_b16 v[114:115], v254 offset:9728\n"
    "s_add_u32 s65, s79, 258048\n"
    "v_mfma_f32_16x16x32_bf16 a[136:139], v[116:119], v[158:161], a[136:139]\n"
    "buffer_load_dwordx4 v240, s[48:51], s65 offen offset:0 lds\n"
    "v_mfma_f32_16x16x32_bf16 a[140:143], v[116:119], v[162:165], a[140:143]\n"
    "s_mov_b32 s20, s92\n"
    "buffer_store_dwordx4 v[36:39], v241, s[56:59], s20 offen offset:1024 sc1\n"
    "s_waitcnt lgkmcnt(2)\n"
    "v_mfma_f32_16x16x32_bf16 a[144:147], v[120:123], v[150:153], a[144:147]\n"
    "ds_read_b64_tr_b16 v[116:117], v255 offset:8704\n"
    "v_accvgpr_read_b32 v124, a196\n"
    "v_accvgpr_read_b32 v125, a197\n"
    "v_mfma_f32_16x16x32_bf16 a[148:151], v[120:123], v[154:157], a[148:151]\n"
    "ds_read_b64_tr_b16 v[118:119], v255 offset:9728\n"
    "v_accvgpr_read_b32 v126, a198\n"
    "v_accvgpr_read_b32 v127, a199\n"
    "v_mfma_f32_16x16x32_bf16 a[152:155], v[120:123], v[158:161], a[152:155]\n"
    "v_add_f32 v48, v48, v124\n"
    "v_add_f32 v49, v49, v125\n"
    "v_add_f32 v50, v50, v126\n"
    "v_add_f32 v51, v51, v127\n"
    "v_mfma_f32_16x16x32_bf16 a[156:159], v[120:123], v[162:165], a[156:159]\n"
    "v_accvgpr_read_b32 v124, a212\n"
    "v_accvgpr_read_b32 v125, a213\n"
    "s_waitcnt lgkmcnt(2)\n"
    "v_mfma_f32_16x16x32_bf16 a[160:163], v[112:115], v[150:153], a[160:163]\n"
    "ds_read_b64_tr_b16 v[120:121], v250 offset:0\n"
    "v_accvgpr_read_b32 v126, a214\n"
    "v_accvgpr_read_b32 v127, a215\n"
    "v_mfma_f32_16x16x32_bf16 a[164:167], v[112:115], v[154:157], a[164:167]\n"
    "ds_read_b64_tr_b16 v[122:123], v250 offset:1024\n"
    "v_add_f32 v52, v52, v124\n"
    "v_add_f32 v53, v53, v125\n"
    "v_mfma_f32_16x16x32_bf16 a[168:171], v[112:115], v[158:161], a[168:171]\n"
    "v_add_f32 v54, v54, v126\n"
    "v_add_f32 v55, v55, v127\n"
    "v_accvgpr_read_b32 v124, a228\n"
    "v_accvgpr_read_b32 v125, a229\n"
    "v_mfma_f32_16x16x32_bf16 a[172:175], v[112:115], v[162:165], a[172:175]\n"
    "v_accvgpr_read_b32 v126, a230\n"
    "v_accvgpr_read_b32 v127, a231\n"
    "s_waitcnt lgkmcnt(2)\n"
    "v_mfma_f32_16x16x32_bf16 a[176:179], v[116:119], v[150:153], a[176:179]\n"
    "ds_read_b64_tr_b16 v[112:113], v251 offset:0\n"
    "v_add_f32 v56, v56, v124\n"
    "v_add_f32 v57, v57, v125\n"
    "v_mfma_f32_16x16x32_bf16 a[180:183], v[116:119], v[154:157], a[180:183]\n"
    "ds_read_b64_tr_b16 v[114:115], v251 offset:1024\n"
    "v_add_f32 v58, v58, v126\n"
    "v_add_f32 v59, v59, v127\n"
    "v_mfma_f32_16x16x32_bf16 a[184:187], v[116:119], v[158:161], a[184:187]\n"
    "v_accvgpr_read_b32 v124, a244\n"
    "v_accvgpr_read_b32 v125, a245\n"
    "v_accvgpr_read_b32 v126, a246\n"
    "v_accvgpr_read_b32 v127, a247\n"
    "v_mfma_f32_16x16x32_bf16 a[188:191], v[116:119], v[162:165], a[188:191]\n"
    "v_add_f32 v60, v60, v124\n"
    "v_add_f32 v61, v61, v125\n"
    "s_waitcnt lgkmcnt(2)\n"
    "v_mfma_f32_16x16x32_bf16 a[128:131], v[120:123], v[16:19], a[128:131]\n"
    "ds_read_b64_tr_b16 v[116:117], v250 offset:512\n"
    "v_add_f32 v62, v62, v126\n"
    "v_add_f32 v63, v63, v127\n"
    "v_mfma_f32_16x16x32_bf16 a[132:135], v[120:123], v[20:23], a[132:135]\n"
    "ds_read_b64_tr_b16 v[118:119], v250 offset:1536\n"
    "v_cvt_pk_bf16_f32 v48, v48, v49\n"
    "v_cvt_pk_bf16_f32 v49, v50, v51\n"
    "v_mfma_f32_16x16x32_bf16 a[136:139], v[120:123], v[24:27], a[136:139]\n"
    "v_cvt_pk_bf16_f32 v50, v52, v53\n"
    "v_cvt_pk_bf16_f32 v51, v54, v55\n"
    "v_cvt_pk_bf16_f32 v52, v56, v57\n"
    "v_cvt_pk_bf16_f32 v53, v58, v59\n"
    "v_mfma_f32_16x16x32_bf16 a[140:143], v[120:123], v[28:31], a[140:143]\n"
    "v_cvt_pk_bf16_f32 v54, v60, v61\n"
    "v_cvt_pk_bf16_f32 v55, v62, v63\n"
    "s_waitcnt lgkmcnt(2)\n"
    "v_mfma_f32_16x16x32_bf16 a[144:147], v[112:115], v[16:19], a[144:147]\n"
    "ds_read_b64_tr_b16 v[120:121], v251 offset:512\n"
    "s_mov_b32 s20, s92\n"
    "buffer_store_dwordx4 v[48:51], v241, s[56:59], s20 offen offset:2048 sc1\n"
    "v_mfma_f32_16x16x32_bf16 a[148:151], v[112:115], v[20:23], a[148:151]\n"
    "ds_read_b64_tr_b16 v[122:123], v251 offset:1536\n"
    "v_mfma_f32_16x16x32_bf16 a[152:155], v[112:115], v[24:27], a[152:155]\n"
    "s_mov_b32 s20, s92\n"
    "buffer_store_dwordx4 v[52:55], v241, s[56:59], s20 offen offset:3072 sc1\n"
    "v_mfma_f32_16x16x32_bf16 a[156:159], v[112:115], v[28:31], a[156:159]\n"
    "s_waitcnt lgkmcnt(2)\n"
    "v_mfma_f32_16x16x32_bf16 a[160:163], v[116:119], v[16:19], a[160:163]\n"
    "s_waitcnt vmcnt(10)\n"
    "s_barrier\n"
    "ds_read_b64_tr_b16 v[112:113], v250 offset:8192\n"
    "v_mfma_f32_16x16x32_bf16 a[164:167], v[116:119], v[20:23], a[164:167]\n"
    "ds_read_b64_tr_b16 v[114:115], v250 offset:9216\n"
    "s_add_u32 m0, s64, 90112\n"
    "v_mfma_f32_16x16x32_bf16 a[168:171], v[116:119], v[24:27], a[168:171]\n"
    "s_add_u32 s65, s77, 294912\n"
    "v_mfma_f32_16x16x32_bf16 a[172:175], v[116:119], v[28:31], a[172:175]\n"
    "buffer_load_dwordx4 v240, s[48:51], s65 offen offset:0 lds\n"
    "s_waitcnt lgkmcnt(2)\n"
    "v_mfma_f32_16x16x32_bf16 a[176:179], v[120:123], v[16:19], a[176:179]\n"
    "ds_read_b64_tr_b16 v[116:117], v251 offset:8192\n"
    "v_mfma_f32_16x16x32_bf16 a[180:183], v[120:123], v[20:23], a[180:183]\n"
    "ds_read_b64_tr_b16 v[118:119], v251 offset:9216\n"
    "v_mfma_f32_16x16x32_bf16 a[184:187], v[120:123], v[24:27], a[184:187]\n"
    "v_mfma_f32_16x16x32_bf16 a[188:191], v[120:123], v[28:31], a[188:191]\n"
    "s_waitcnt lgkmcnt(2)\n"
    "v_mfma_f32_16x16x32_bf16 a[128:131], v[112:115], a[16:19], a[128:131]\n"
    "ds_read_b64_tr_b16 v[120:121], v250 offset:8704\n"
    "v_mfma_f32_16x16x32_bf16 a[132:135], v[112:115], a[20:23], a[132:135]\n"
    "ds_read_b64_tr_b16 v[122:123], v250 offset:9728\n"
    "v_mfma_f32_16x16x32_bf16 a[136:139], v[112:115], a[24:27], a[136:139]\n"
    "v_mfma_f32_16x16x32_bf16 a[140:143], v[112:115], a[28:31], a[140:143]\n"
    "s_waitcnt lgkmcnt(2)\n"
    "v_mfma_f32_16x16x32_bf16 a[144:147], v[116:119], a[16:19], a[144:147]\n"
    "ds_read_b64_tr_b16 v[112:113], v251 offset:8704\n"
    "v_mfma_f32_16x16x32_bf16 a[148:151], v[116:119], a[20:23], a[148:151]\n"
    "ds_read_b64_tr_b16 v[114:115], v251 offset:9728\n"
    "v_mfma_f32_16x16x32_bf16 a[152:155], v[116:119], a[24:27], a[152:155]\n"
    "v_mfma_f32_16x16x32_bf16 a[156:159], v[116:119], a[28:31], a[156:159]\n"
    "s_waitcnt lgkmcnt(2)\n"
    "v_mfma_f32_16x16x32_bf16 a[160:163], v[120:123], a[16:19], a[160:163]\n"
    "ds_read_b64_tr_b16 v[116:117], v250 offset:16384\n"
    "v_mfma_f32_16x16x32_bf16 a[164:167], v[120:123], a[20:23], a[164:167]\n"
    "ds_read_b64_tr_b16 v[118:119], v250 offset:17408\n"
    "v_mfma_f32_16x16x32_bf16 a[168:171], v[120:123], a[24:27], a[168:171]\n"
    "v_mfma_f32_16x16x32_bf16 a[172:175], v[120:123], a[28:31], a[172:175]\n"
    "s_waitcnt lgkmcnt(2)\n"
    "v_mfma_f32_16x16x32_bf16 a[176:179], v[112:115], a[16:19], a[176:179]\n"
    "ds_read_b64_tr_b16 v[120:121], v251 offset:16384\n"
    "v_mfma_f32_16x16x32_bf16 a[180:183], v[112:115], a[20:23], a[180:183]\n"
    "ds_read_b64_tr_b16 v[122:123], v251 offset:17408\n"
    "v_mfma_f32_16x16x32_bf16 a[184:187], v[112:115], a[24:27], a[184:187]\n"
    "v_mfma_f32_16x16x32_bf16 a[188:191], v[112:115], a[28:31], a[188:191]\n"
    "s_waitcnt lgkmcnt(2)\n"
    "v_mfma_f32_16x16x32_bf16 a[128:131], v[116:119], a[0:3], a[128:131]\n"
    "ds_read_b64_tr_b16 v[112:113], v250 offset:16896\n"
    "s_add_u32 m0, s64, 94208\n"
    "v_mfma_f32_16x16x32_bf16 a[132:135], v[116:119], a[4:7], a[132:135]\n"
    "ds_read_b64_tr_b16 v[114:115], v250 offset:17920\n"
    "s_add_u32 s65, s77, 331776\n"
    "v_mfma_f32_16x16x32_bf16 a[136:139], v[116:119], a[8:11], a[136:139]\n"
    "buffer_load_dwordx4 v240, s[48:51], s65 offen offset:0 lds\n"
    "v_mfma_f32_16x16x32_bf16 a[140:143], v[116:119], a[12:15], a[140:143]\n"
    "s_waitcnt lgkmcnt(2)\n"
    "v_mfma_f32_16x16x32_bf16 a[144:147], v[120:123], a[0:3], a[144:147]\n"
    "ds_read_b64_tr_b16 v[116:117], v251 offset:16896\n"
    "v_mfma_f32_16x16x32_bf16 a[148:151], v[120:123], a[4:7], a[148:151]\n"
    "ds_read_b64_tr_b16 v[118:119], v251 offset:17920\n"
    "v_mfma_f32_16x16x32_bf16 a[152:155], v[120:123], a[8:11], a[152:155]\n"
    "v_mfma_f32_16x16x32_bf16 a[156:159], v[120:123], a[12:15], a[156:159]\n"
    "s_waitcnt lgkmcnt(2)\n"
    "v_mfma_f32_16x16x32_bf16 a[160:163], v[112:115], a[0:3], a[160:163]\n"
    "ds_read_b64_tr_b16 v[120:121], v250 offset:24576\n"
    "v_mfma_f32_16x16x32_bf16 a[164:167], v[112:115], a[4:7], a[164:167]\n"
    "ds_read_b64_tr_b16 v[122:123], v250 offset:25600\n"
    "v_mfma_f32_16x16x32_bf16 a[168:171], v[112:115], a[8:11], a[168:171]\n"
    "v_mfma_f32_16x16x32_bf16 a[172:175], v[112:115], a[12:15], a[172:175]\n"
    "s_waitcnt lgkmcnt(2)\n"
    "v_mfma_f32_16x16x32_bf16 a[176:179], v[116:119], a[0:3], a[176:179]\n"
    "ds_read_b64_tr_b16 v[112:113], v251 offset:24576\n"
    "v_mfma_f32_16x16x32_bf16 a[180:183], v[116:119], a[4:7], a[180:183]\n"
    "ds_read_b64_tr_b16 v[114:115], v251 offset:25600\n"
    "v_mfma_f32_16x16x32_bf16 a[184:187], v[116:119], a[8:11], a[184:187]\n"
    "v_mfma_f32_16x16x32_bf16 a[188:191], v[116:119], a[12:15], a[188:191]\n"
    "s_waitcnt lgkmcnt(2)\n"
    "v_mfma_f32_16x16x32_bf16 a[128:131], v[120:123], v[0:3], a[128:131]\n"
    "ds_read_b64_tr_b16 v[116:117], v250 offset:25088\n"
    "v_mfma_f32_16x16x32_bf16 a[132:135], v[120:123], v[4:7], a[132:135]\n"
    "ds_read_b64_tr_b16 v[118:119], v250 offset:26112\n"
    "v_mfma_f32_16x16x32_bf16 a[136:139], v[120:123], v[8:11], a[136:139]\n"
    "v_mfma_f32_16x16x32_bf16 a[140:143], v[120:123], v[12:15], a[140:143]\n"
    "s_waitcnt lgkmcnt(2)\n"
    "v_mfma_f32_16x16x32_bf16 a[144:147], v[112:115], v[0:3], a[144:147]\n"
    "ds_read_b64_tr_b16 v[120:121], v251 offset:25088\n"
    "v_mfma_f32_16x16x32_bf16 a[148:151], v[112:115], v[4:7], a[148:151]\n"
    "ds_read_b64_tr_b16 v[122:123], v251 offset:26112\n"
    "v_mfma_f32_16x16x32_bf16 a[152:155], v[112:115], v[8:11], a[152:155]\n"
    "v_mfma_f32_16x16x32_bf16 a[156:159], v[112:115], v[12:15], a[156:159]\n"
    "s_waitcnt lgkmcnt(2)\n"
    "v_mfma_f32_16x16x32_bf16 a[160:163], v[116:119], v[0:3], a[160:163]\n"
    "s_waitcnt vmcnt(5)\n"
    "s_barrier\n"
    "ds_read_b64_tr_b16 v[112:113], v250 offset:32768\n"
    "v_mfma_f32_16x16x32_bf16 a[164:167], v[116:119], v[4:7], a[164:167]\n"
    "ds_read_b64_tr_b16 v[114:115], v250 offset:33792\n"
    "s_add_u32 m0, s64, 98304\n"
    "v_mfma_f32_16x16x32_bf16 a[168:171], v[116:119], v[8:11], a[168:171]\n"
    "s_add_u32 s65, s78, 221184\n"
    "v_mfma_f32_16x16x32_bf16 a[172:175], v[116:119], v[12:15], a[172:175]\n"
    "buffer_load_dwordx4 v240, s[48:51], s65 offen offset:0 lds\n"
    "s_waitcnt lgkmcnt(2)\n"
    "v_mfma_f32_16x16x32_bf16 a[176:179], v[120:123], v[0:3], a[176:179]\n"
    "ds_read_b64_tr_b16 v[116:117], v251 offset:32768\n"
    "v_mfma_f32_16x16x32_bf16 a[180:183], v[120:123], v[4:7], a[180:183]\n"
    "ds_read_b64_tr_b16 v[118:119], v251 offset:33792\n"
    "s_add_u32 m0, s64, 102400\n"
    "v_mfma_f32_16x16x32_bf16 a[184:187], v[120:123], v[8:11], a[184:187]\n"
    "s_add_u32 s65, s78, 258048\n"
    "v_mfma_f32_16x16x32_bf16 a[188:191], v[120:123], v[12:15], a[188:191]\n"
    "buffer_load_dwordx4 v240, s[48:51], s65 offen offset:0 lds\n"
    "s_waitcnt lgkmcnt(2)\n"
    "v_mfma_f32_16x16x32_bf16 a[64:67], v[112:115], a[48:51], a[64:67]\n"
    "ds_read_b64_tr_b16 v[120:121], v250 offset:33280\n"
    "v_mfma_f32_16x16x32_bf16 a[68:71], v[112:115], a[52:55], a[68:71]\n"
    "ds_read_b64_tr_b16 v[122:123], v250 offset:34304\n"
    "v_lshl_add_u32 v128, s66, 13, v241\n"
    "v_xor_b32 v129, 0x2000, v128\n"
    "v_mfma_f32_16x16x32_bf16 a[72:75], v[112:115], a[56:59], a[72:75]\n"
    "s_add_u32 m0, s64, 106496\n"
    "v_add_u32 v128, 57344, v128\n"
    "v_add_u32 v129, 57344, v129\n"
    "v_mfma_f32_16x16x32_bf16 a[76:79], v[112:115], a[60:63], a[76:79]\n"
    "s_add_u32 s65, s79, 294912\n"
    "s_waitcnt lgkmcnt(2)\n"
    "v_mfma_f32_16x16x32_bf16 a[80:83], v[116:119], a[48:51], a[80:83]\n"
    "ds_read_b64_tr_b16 v[112:113], v251 offset:33280\n"
    "buffer_load_dwordx4 v240, s[48:51], s65 offen offset:0 lds\n"
    "v_mfma_f32_16x16x32_bf16 a[84:87], v[116:119], a[52:55], a[84:87]\n"
    "ds_read_b64_tr_b16 v[114:115], v251 offset:34304\n"
    "ds_write_b128 v128, a[136:139] offset:0\n"
    "v_mfma_f32_16x16x32_bf16 a[88:91], v[116:119], a[56:59], a[88:91]\n"
    "ds_write_b128 v128, a[152:155] offset:1024\n"
    "v_mfma_f32_16x16x32_bf16 a[92:95], v[116:119], a[60:63], a[92:95]\n"
    "s_add_u32 m0, s64, 110592\n"
    "s_waitcnt lgkmcnt(4)\n"
    "v_mfma_f32_16x16x32_bf16 a[96:99], v[120:123], a[48:51], a[96:99]\n"
    "ds_read_b64_tr_b16 v[116:117], v250 offset:40960\n"
    "s_add_u32 s65, s79, 331776\n"
    "v_mfma_f32_16x16x32_bf16 a[100:103], v[120:123], a[52:55], a[100:103]\n"
    "ds_read_b64_tr_b16 v[118:119], v250 offset:41984\n"
    "buffer_load_dwordx4 v240, s[48:51], s65 offen offset:0 lds\n"
    "v_mfma_f32_16x16x32_bf16 a[104:107], v[120:123], a[56:59], a[104:107]\n"
    "ds_write_b128 v128, a[168:171] offset:2048\n"
    "v_mfma_f32_16x16x32_bf16 a[108:111], v[120:123], a[60:63], a[108:111]\n"
    "s_add_u32 m0, s64, 114688\n"
    "ds_write_b128 v128, a[184:187] offset:3072\n"
    "s_waitcnt lgkmcnt(6)\n"
    "v_mfma_f32_16x16x32_bf16 a[112:115], v[112:115], a[48:51], a[112:115]\n"
    "ds_read_b64_tr_b16 v[120:121], v251 offset:40960\n"
    "s_add_u32 s65, s83, 0\n"
    "v_mfma_f32_16x16x32_bf16 a[116:119], v[112:115], a[52:55], a[116:119]\n"
    "ds_read_b64_tr_b16 v[122:123], v251 offset:41984\n"
    "buffer_load_dwordx4 v240, s[52:55], s65 offen offset:0 lds\n"
    "v_mfma_f32_16x16x32_bf16 a[120:123], v[112:115], a[56:59], a[120:123]\n"
    "ds_write_b128 v128, a[140:143] offset:4096\n"
    "v_mfma_f32_16x16x32_bf16 a[124:127], v[112:115], a[60:63], a[124:127]\n"
    "ds_write_b128 v128, a[156:159] offset:5120\n"
    "s_waitcnt lgkmcnt(6)\n"
    "v_mfma_f32_16x16x32_bf16 a[64:67], v[116:119], v[134:137], a[64:67]\n"
    "ds_read_b64_tr_b16 v[112:113], v250 offset:41472\n"
    "s_add_u32 m0, s64, 118784\n"
    "v_mfma_f32_16x16x32_bf16 a[68:71], v[116:119], v[138:141], a[68:71]\n"
    "ds_read_b64_tr_b16 v[114:115], v250 offset:42496\n"
    "s_add_u32 s65, s83, 82944\n"
    "v_mfma_f32_16x16x32_bf16 a[72:75], v[116:119], v[142:145], a[72:75]\n"
    "buffer_load_dwordx4 v240, s[52:55], s65 offen offset:0 lds\n"
    "v_mfma_f32_16x16x32_bf16 a[76:79], v[116:119], v[146:149], a[76:79]\n"
    "s_waitcnt lgkmcnt(4)\n"
    "v_mfma_f32_16x16x32_bf16 a[80:83], v[120:123], v[134:137], a[80:83]\n"
    "ds_read_b64_tr_b16 v[116:117], v251 offset:41472\n"
    "ds_write_b128 v128, a[172:175] offset:6144\n"
    "v_mfma_f32_16x16x32_bf16 a[84:87], v[120:123], v[138:141], a[84:87]\n"
    "ds_read_b64_tr_b16 v[118:119], v251 offset:42496\n"
    "s_add_u32 m0, s64, 122880\n"
    "v_mfma_f32_16x16x32_bf16 a[88:91], v[120:123], v[142:145], a[88:91]\n"
    "s_add_u32 s65, s83, 36864\n"
    "s_waitcnt vmcnt(8)\n"
    "s_add_u32 s99, s70, 1\n"
    "v_mov_b32 v133, s21\n"
    "v_mov_b32 v132, s99\n"
    "buffer_store_dwordx2 v[132:133], v242, s[56:59], s89 offen offset:16 sc1\n"
    "v_mfma_f32_16x16x32_bf16 a[92:95], v[120:123], v[146:149], a[92:95]\n"
    "buffer_load_dwordx4 v240, s[52:55], s65 offen offset:0 lds\n"
    "s_waitcnt lgkmcnt(3)\n"
    "v_mfma_f32_16x16x32_bf16 a[96:99], v[112:115], v[134:137], a[96:99]\n"
    "ds_read_b64_tr_b16 v[120:121], v250 offset:49152\n"
    "ds_write_b128 v128, a[188:191] offset:7168\n"
    "v_mfma_f32_16x16x32_bf16 a[100:103], v[112:115], v[138:141], a[100:103]\n"
    "ds_read_b64_tr_b16 v[122:123], v250 offset:50176\n"
    "s_add_u32 m0, s64, 126976\n"
    "v_mfma_f32_16x16x32_bf16 a[104:107], v[112:115], v[142:145], a[104:107]\n"
    "s_add_u32 s65, s83, 119808\n"
    "v_mfma_f32_16x16x32_bf16 a[108:111], v[112:115], v[146:149], a[108:111]\n"
    "buffer_load_dwordx4 v240, s[52:55], s65 offen offset:0 lds\n"
    "s_waitcnt lgkmcnt(3)\n"
    "v_mfma_f32_16x16x32_bf16 a[112:115], v[116:119], v[134:137], a[112:115]\n"
    "ds_read_b64_tr_b16 v[112:113], v251 offset:49152\n"
    "v_mfma_f32_16x16x32_bf16 a[116:119], v[116:119], v[138:141], a[116:119]\n"
    "ds_read_b64_tr_b16 v[114:115], v251 offset:50176\n"
    "v_mfma_f32_16x16x32_bf16 a[120:123], v[116:119], v[142:145], a[120:123]\n"
    "s_add_u32 m0, s64, 131072\n"
    "v_mfma_f32_16x16x32_bf16 a[124:127], v[116:119], v[146:149], a[124:127]\n"
    "s_add_u32 s65, s83, 73728\n"
    "s_waitcnt lgkmcnt(2)\n"
    "v_mfma_f32_16x16x32_bf16 a[64:67], v[120:123], v[166:169], a[64:67]\n"
    "ds_read_b64_tr_b16 v[116:117], v250 offset:49664\n"
    "buffer_load_dwordx4 v240, s[52:55], s65 offen offset:0 lds\n"
    "v_mfma_f32_16x16x32_bf16 a[68:71], v[120:123], v[170:173], a[68:71]\n"
    "ds_read_b64_tr_b16 v[118:119], v250 offset:50688\n"
    "v_mfma_f32_16x16x32_bf16 a[72:75], v[120:123], v[174:177], a[72:75]\n"
    "v_mfma_f32_16x16x32_bf16 a[76:79], v[120:123], v[178:181], a[76:79]\n"
    "s_add_u32 m0, s64, 135168\n"
    "s_waitcnt lgkmcnt(2)\n"
    "v_mfma_f32_16x16x32_bf16 a[80:83], v[112:115], v[166:169], a[80:83]\n"
    "ds_read_b64_tr_b16 v[120:121], v251 offset:49664\n"
    "s_add_u32 s65, s83, 156672\n"
    "v_mfma_f32_16x16x32_bf16 a[84:87], v[112:115], v[170:173], a[84:87]\n"
    "ds_read_b64_tr_b16 v[122:123], v251 offset:50688\n"
    "buffer_load_dwordx4 v240, s[52:55], s65 offen offset:0 lds\n"
    "v_mfma_f32_16x16x32_bf16 a[88:91], v[112:115], v[174:177], a[88:91]\n"
    "v_mfma_f32_16x16x32_bf16 a[92:95], v[112:115], v[178:181], a[92:95]\n"
    "s_waitcnt lgkmcnt(2)\n"
    "v_mfma_f32_16x16x32_bf16 a[96:99], v[116:119], v[166:169], a[96:99]\n"
    "s_waitcnt vmcnt(7)\n"
    "s_barrier\n"
    "ds_read_b64_tr_b16 v[112:113], v252 offset:24576\n"
    "v_mfma_f32_16x16x32_bf16 a[100:103], v[116:119], v[170:173], a[100:103]\n"
    "ds_read_b64_tr_b16 v[114:115], v252 offset:25600\n"
    "ds_read_b128 v[32:35], v129 offset:0\n"
    "ds_read_b128 v[36:39], v129 offset:1024\n"
    "v_mfma_f32_16x16x32_bf16 a[104:107], v[116:119], v[174:177], a[104:107]\n"
    "ds_read_b128 v[40:43], v129 offset:2048\n"
    "ds_read_b128 v[44:47], v129 offset:3072\n"
    "ds_read_b128 v[48:51], v129 offset:4096\n"
    "v_mfma_f32_16x16x32_bf16 a[108:111], v[116:119], v[178:181], a[108:111]\n"
    "ds_read_b128 v[52:55], v129 offset:5120\n"
    "ds_read_b128 v[56:59], v129 offset:6144\n"
    "s_waitcnt lgkmcnt(9)\n"
    "v_mfma_f32_16x16x32_bf16 a[112:115], v[120:123], v[166:169], a[112:115]\n"
    "ds_read_b64_tr_b16 v[116:117], v253 offset:24576\n"
    "ds_read_b128 v[60:63], v129 offset:7168\n"
    "v_accvgpr_read_b32 v124, a128\n"
    "v_mfma_f32_16x16x32_bf16 a[116:119], v[120:123], v[170:173], a[116:119]\n"
    "ds_read_b64_tr_b16 v[118:119], v253 offset:25600\n"
    "v_accvgpr_read_b32 v125, a129\n"
    "v_accvgpr_read_b32 v126, a130\n"
    "v_mfma_f32_16x16x32_bf16 a[120:123], v[120:123], v[174:177], a[120:123]\n"
    "v_accvgpr_read_b32 v127, a131\n"
    "s_waitcnt lgkmcnt(9)\n"
    "v_add_f32 v32, v32, v124\n"
    "v_add_f32 v33, v33, v125\n"
    "v_mfma_f32_16x16x32_bf16 a[124:127], v[120:123], v[178:181], a[124:127]\n"
    "v_add_f32 v34, v34, v126\n"
    "v_add_f32 v35, v35, v127\n"
    "v_mfma_f32_16x16x32_bf16 a[64:67], v[112:115], a[32:35], a[64:67]\n"
    "ds_read_b64_tr_b16 v[120:121], v252 offset:25088\n"
    "v_accvgpr_read_b32 v124, a144\n"
    "v_accvgpr_read_b32 v125, a145\n"
    "v_mfma_f32_16x16x32_bf16 a[68:71], v[112:115], a[36:39], a[68:71]\n"
    "ds_read_b64_tr_b16 v[122:123], v252 offset:26112\n"
    "v_accvgpr_read_b32 v126, a146\n"
    "v_accvgpr_read_b32 v127, a147\n"
    "v_mfma_f32_16x16x32_bf16 a[72:75], v[112:115], a[40:43], a[72:75]\n"
    "s_waitcnt lgkmcnt(10)\n"
    "v_add_f32 v36, v36, v124\n"
    "v_add_f32 v37, v37, v125\n"
    "v_add_f32 v38, v38, v126\n"
    "v_mfma_f32_16x16x32_bf16 a[76:79], v[112:115], a[44:47], a[76:79]\n"
    "v_add_f32 v39, v39, v127\n"
    "v_accvgpr_read_b32 v124, a160\n"
    "s_waitcnt lgkmcnt(2)\n"
    "v_mfma_f32_16x16x32_bf16 a[80:83], v[116:119], a[32:35], a[80:83]\n"
    "ds_read_b64_tr_b16 v[112:113], v253 offset:25088\n"
    "v_accvgpr_read_b32 v125, a161\n"
    "v_accvgpr_read_b32 v126, a162\n"
    "v_mfma_f32_16x16x32_bf16 a[84:87], v[116:119], a[36:39], a[84:87]\n"
    "ds_read_b64_tr_b16 v[114:115], v253 offset:26112\n"
    "v_accvgpr_read_b32 v127, a163\n"
    "v_add_f32 v40, v40, v124\n"
    "v_mfma_f32_16x16x32_bf16 a[88:91], v[116:119], a[40:43], a[88:91]\n"
    "v_add_f32 v41, v41, v125\n"
    "v_add_f32 v42, v42, v126\n"
    "v_add_f32 v43, v43, v127\n"
    "v_mfma_f32_16x16x32_bf16 a[92:95], v[116:119], a[44:47], a[92:95]\n"
    "v_accvgpr_read_b32 v124, a176\n"
    "v_accvgpr_read_b32 v125, a177\n"
    "s_waitcnt lgkmcnt(2)\n"
    "v_mfma_f32_16x16x32_bf16 a[96:99], v[120:123], a[32:35], a[96:99]\n"
    "ds_read_b64_tr_b16 v[116:117], v252 offset:32768\n"
    "v_accvgpr_read_b32 v126, a178\n"
    "v_accvgpr_read_b32 v127, a179\n"
    "v_mfma_f32_16x16x32_bf16 a[100:103], v[120:123], a[36:39], a[100:103]\n"
    "ds_read_b64_tr_b16 v[118:119], v252 offset:33792\n"
    "v_add_f32 v44, v44, v124\n"
    "v_add_f32 v45, v45, v125\n"
    "v_mfma_f32_16x16x32_bf16 a[104:107], v[120:123], a[40:43], a[104:107]\n"
    "v_add_f32 v46, v46, v126\n"
    "v_add_f32 v47, v47, v127\n"
    "v_cvt_pk_bf16_f32 v32, v32, v33\n"
    "v_mfma_f32_16x16x32_bf16 a[108:111], v[120:123], a[44:47], a[108:111]\n"
    "v_cvt_pk_bf16_f32 v33, v34, v35\n"
    "v_cvt_pk_bf16_f32 v34, v36, v37\n"
    "s_waitcnt lgkmcnt(2)\n"
    "v_mfma_f32_16x16x32_bf16 a[112:115], v[112:115], a[32:35], a[112:115]\n"
    "ds_read_b64_tr_b16 v[120:121], v253 offset:32768\n"
    "v_cvt_pk_bf16_f32 v35, v38, v39\n"
    "v_cvt_pk_bf16_f32 v36, v40, v41\n"
    "v_mfma_f32_16x16x32_bf16 a[116:119], v[112:115], a[36:39], a[116:119]\n"
    "ds_read_b64_tr_b16 v[122:123], v253 offset:33792\n"
    "v_cvt_pk_bf16_f32 v37, v42, v43\n"
    "v_cvt_pk_bf16_f32 v38, v44, v45\n"
    "v_mfma_f32_16x16x32_bf16 a[120:123], v[112:115], a[40:43], a[120:123]\n"
    "v_cvt_pk_bf16_f32 v39, v46, v47\n"
    "s_xor_b32 s20, s91, 69632\n"
    "buffer_store_dwordx4 v[32:35], v241, s[56:59], s20 offen offset:0 sc1\n"
    "v_mfma_f32_16x16x32_bf16 a[124:127], v[112:115], a[44:47], a[124:127]\n"
    "s_waitcnt lgkmcnt(2)\n"
    "v_mfma_f32_16x16x32_bf16 a[64:67], v[116:119], v[150:153], a[64:67]\n"
    "ds_read_b64_tr_b16 v[112:113], v252 offset:33280\n"
    "v_mfma_f32_16x16x32_bf16 a[68:71], v[116:119], v[154:157], a[68:71]\n"
    "ds_read_b64_tr_b16 v[114:115], v252 offset:34304\n"
    "s_xor_b32 s20, s91, 69632\n"
    "buffer_store_dwordx4 v[36:39], v241, s[56:59], s20 offen offset:1024 sc1\n"
    "v_mfma_f32_16x16x32_bf16 a[72:75], v[116:119], v[158:161], a[72:75]\n"
    "v_accvgpr_read_b32 v124, a132\n"
    "v_accvgpr_read_b32 v125, a133\n"
    "v_accvgpr_read_b32 v126, a134\n"
    "v_mfma_f32_16x16x32_bf16 a[76:79], v[116:119], v[162:165], a[76:79]\n"
    "v_accvgpr_read_b32 v127, a135\n"
    "v_add_f32 v48, v48, v124\n"
    "s_waitcnt lgkmcnt(2)\n"
    "v_mfma_f32_16x16x32_bf16 a[80:83], v[120:123], v[150:153], a[80:83]\n"
    "ds_read_b64_tr_b16 v[116:117], v253 offset:33280\n"
    "v_add_f32 v49, v49, v125\n"
    "v_add_f32 v50, v50, v126\n"
    "v_mfma_f32_16x16x32_bf16 a[84:87], v[120:123], v[154:157], a[84:87]\n"
    "ds_read_b64_tr_b16 v[118:119], v253 offset:34304\n"
    "v_add_f32 v51, v51, v127\n"
    "v_accvgpr_read_b32 v124, a148\n"
    "v_mfma_f32_16x16x32_bf16 a[88:91], v[120:123], v[158:161], a[88:91]\n"
    "v_accvgpr_read_b32 v125, a149\n"
    "v_accvgpr_read_b32 v126, a150\n"
    "v_accvgpr_read_b32 v127, a151\n"
    "v_mfma_f32_16x16x32_bf16 a[92:95], v[120:123], v[162:165], a[92:95]\n"
    "v_add_f32 v52, v52, v124\n"
    "v_add_f32 v53, v53, v125\n"
    "s_waitcnt lgkmcnt(2)\n"
    "v_mfma_f32_16x16x32_bf16 a[96:99], v[112:115], v[150:153], a[96:99]\n"
    "ds_read_b64_tr_b16 v[120:121], v252 offset:40960\n"
    "v_add_f32 v54, v54, v126\n"
    "v_add_f32 v55, v55, v127\n"
    "v_mfma_f32_16x16x32_bf16 a[100:103], v[112:115], v[154:157], a[100:103]\n"
    "ds_read_b64_tr_b16 v[122:123], v252 offset:41984\n"
    "v_accvgpr_read_b32 v124, a164\n"
    "v_accvgpr_read_b32 v125, a165\n"
    "v_mfma_f32_16x16x32_bf16 a[104:107], v[112:115], v[158:161], a[104:107]\n"
    "v_accvgpr_read_b32 v126, a166\n"
    "v_accvgpr_read_b32 v127, a167\n"
    "v_add_f32 v56, v56, v124\n"
    "v_mfma_f32_16x16x32_bf16 a[108:111], v[112:115], v[162:165], a[108:111]\n"
    "v_add_f32 v57, v57, v125\n"
    "v_add_f32 v58, v58, v126\n"
    "s_waitcnt lgkmcnt(2)\n"
    "v_mfma_f32_16x16x32_bf16 a[112:115], v[116:119], v[150:153], a[112:115]\n"
    "ds_read_b64_tr_b16 v[112:113], v253 offset:40960\n"
    "v_add_f32 v59, v59, v127\n"
    "v_accvgpr_read_b32 v124, a180\n"
    "v_mfma_f32_16x16x32_bf16 a[116:119], v[116:119], v[154:157], a[116:119]\n"
    "ds_read_b64_tr_b16 v[114:115], v253 offset:41984\n"
    "v_accvgpr_read_b32 v125, a181\n"
    "v_accvgpr_read_b32 v126, a182\n"
    "v_mfma_f32_16x16x32_bf16 a[120:123], v[116:119], v[158:161], a[120:123]\n"
    "v_accvgpr_read_b32 v127, a183\n"
    "v_add_f32 v60, v60, v124\n"
    "v_add_f32 v61, v61, v125\n"
    "v_mfma_f32_16x16x32_bf16 a[124:127], v[116:119], v[162:165], a[124:127]\n"
    "v_add_f32 v62, v62, v126\n"
    "v_add_f32 v63, v63, v127\n"
    "s_waitcnt lgkmcnt(2)\n"
    "v_mfma_f32_16x16x32_bf16 a[64:67], v[120:123], v[16:19], a[64:67]\n"
    "ds_read_b64_tr_b16 v[116:117], v252 offset:41472\n"
    "v_cvt_pk_bf16_f32 v48, v48, v49\n"
    "v_cvt_pk_bf16_f32 v49, v50, v51\n"
    "v_mfma_f32_16x16x32_bf16 a[68:71], v[120:123], v[20:23], a[68:71]\n"
    "ds_read_b64_tr_b16 v[118:119], v252 offset:42496\n"
    "v_cvt_pk_bf16_f32 v50, v52, v53\n"
    "v_cvt_pk_bf16_f32 v51, v54, v55\n"
    "v_mfma_f32_16x16x32_bf16 a[72:75], v[120:123], v[24:27], a[72:75]\n"
    "v_cvt_pk_bf16_f32 v52, v56, v57\n"
    "v_cvt_pk_bf16_f32 v53, v58, v59\n"
    "v_cvt_pk_bf16_f32 v54, v60, v61\n"
    "v_mfma_f32_16x16x32_bf16 a[76:79], v[120:123], v[28:31], a[76:79]\n"
    "v_cvt_pk_bf16_f32 v55, v62, v63\n"
    "s_waitcnt lgkmcnt(2)\n"
    "v_mfma_f32_16x16x32_bf16 a[80:83], v[112:115], v[16:19], a[80:83]\n"
    "ds_read_b64_tr_b16 v[120:121], v253 offset:41472\n"
    "s_xor_b32 s20, s91, 104448\n"
    "buffer_store_dwordx4 v[48:51], v241, s[56:59], s20 offen offset:0 sc1\n"
    "v_mfma_f32_16x16x32_bf16 a[84:87], v[112:115], v[20:23], a[84:87]\n"
    "ds_read_b64_tr_b16 v[122:123], v253 offset:42496\n"
    "v_mfma_f32_16x16x32_bf16 a[88:91], v[112:115], v[24:27], a[88:91]\n"
    "s_xor_b32 s20, s91, 104448\n"
    "buffer_store_dwordx4 v[52:55], v241, s[56:59], s20 offen offset:1024 sc1\n"
    "v_mfma_f32_16x16x32_bf16 a[92:95], v[112:115], v[28:31], a[92:95]\n"
    "s_waitcnt lgkmcnt(2)\n"
    "v_mfma_f32_16x16x32_bf16 a[96:99], v[116:119], v[16:19], a[96:99]\n"
    "s_waitcnt vmcnt(4)\n"
    "s_barrier\n"
    "ds_read_b64_tr_b16 v[112:113], v252 offset:49152\n"
    "v_mfma_f32_16x16x32_bf16 a[100:103], v[116:119], v[20:23], a[100:103]\n"
    "ds_read_b64_tr_b16 v[114:115], v252 offset:50176\n"
    "v_mfma_f32_16x16x32_bf16 a[104:107], v[116:119], v[24:27], a[104:107]\n"
    "v_mfma_f32_16x16x32_bf16 a[108:111], v[116:119], v[28:31], a[108:111]\n"
    "s_waitcnt lgkmcnt(2)\n"
    "v_mfma_f32_16x16x32_bf16 a[112:115], v[120:123], v[16:19], a[112:115]\n"
    "ds_read_b64_tr_b16 v[116:117], v253 offset:49152\n"
    "v_mfma_f32_16x16x32_bf16 a[116:119], v[120:123], v[20:23], a[116:119]\n"
    "ds_read_b64_tr_b16 v[118:119], v253 offset:50176\n"
    "v_mfma_f32_16x16x32_bf16 a[120:123], v[120:123], v[24:27], a[120:123]\n"
    "v_mfma_f32_16x16x32_bf16 a[124:127], v[120:123], v[28:31], a[124:127]\n"
    "s_waitcnt lgkmcnt(2)\n"
    "v_mfma_f32_16x16x32_bf16 a[64:67], v[112:115], a[16:19], a[64:67]\n"
    "ds_read_b64_tr_b16 v[120:121], v252 offset:49664\n"
    "v_mfma_f32_16x16x32_bf16 a[68:71], v[112:115], a[20:23], a[68:71]\n"
    "ds_read_b64_tr_b16 v[122:123], v252 offset:50688\n"
    "v_mfma_f32_16x16x32_bf16 a[72:75], v[112:115], a[24:27], a[72:75]\n"
    "v_mfma_f32_16x16x32_bf16 a[76:79], v[112:115], a[28:31], a[76:79]\n"
    "s_waitcnt lgkmcnt(2)\n"
    "v_mfma_f32_16x16x32_bf16 a[80:83], v[116:119], a[16:19], a[80:83]\n"
    "ds_read_b64_tr_b16 v[112:113], v253 offset:49664\n"
    "v_mfma_f32_16x16x32_bf16 a[84:87], v[116:119], a[20:23], a[84:87]\n"
    "ds_read_b64_tr_b16 v[114:115], v253 offset:50688\n"
    "v_mfma_f32_16x16x32_bf16 a[88:91], v[116:119], a[24:27], a[88:91]\n"
    "v_mfma_f32_16x16x32_bf16 a[92:95], v[116:119], a[28:31], a[92:95]\n"
    "s_waitcnt lgkmcnt(2)\n"
    "v_mfma_f32_16x16x32_bf16 a[96:99], v[120:123], a[16:19], a[96:99]\n"
    "ds_read_b64_tr_b16 v[116:117], v252 offset:57344\n"
    "v_mfma_f32_16x16x32_bf16 a[100:103], v[120:123], a[20:23], a[100:103]\n"
    "ds_read_b64_tr_b16 v[118:119], v252 offset:58368\n"
    "v_mfma_f32_16x16x32_bf16 a[104:107], v[120:123], a[24:27], a[104:107]\n"
    "v_mfma_f32_16x16x32_bf16 a[108:111], v[120:123], a[28:31], a[108:111]\n"
    "s_waitcnt lgkmcnt(2)\n"
    "v_mfma_f32_16x16x32_bf16 a[112:115], v[112:115], a[16:19], a[112:115]\n"
    "ds_read_b64_tr_b16 v[120:121], v253 offset:57344\n"
    "v_mfma_f32_16x16x32_bf16 a[116:119], v[112:115], a[20:23], a[116:119]\n"
    "ds_read_b64_tr_b16 v[122:123], v253 offset:58368\n"
    "v_mfma_f32_16x16x32_bf16 a[120:123], v[112:115], a[24:27], a[120:123]\n"
    "v_mfma_f32_16x16x32_bf16 a[124:127], v[112:115], a[28:31], a[124:127]\n"
    "s_waitcnt lgkmcnt(2)\n"
    "v_mfma_f32_16x16x32_bf16 a[64:67], v[116:119], a[0:3], a[64:67]\n"
    "ds_read_b64_tr_b16 v[112:113], v252 offset:57856\n"
    "v_mfma_f32_16x16x32_bf16 a[68:71], v[116:119], a[4:7], a[68:71]\n"
    "ds_read_b64_tr_b16 v[114:115], v252 offset:58880\n"
    "v_mfma_f32_16x16x32_bf16 a[72:75], v[116:119], a[8:11], a[72:75]\n"
    "v_mfma_f32_16x16x32_bf16 a[76:79], v[116:119], a[12:15], a[76:79]\n"
    "s_waitcnt lgkmcnt(2)\n"
    "v_mfma_f32_16x16x32_bf16 a[80:83], v[120:123], a[0:3], a[80:83]\n"
    "ds_read_b64_tr_b16 v[116:117], v253 offset:57856\n"
    "v_mfma_f32_16x16x32_bf16 a[84:87], v[120:123], a[4:7], a[84:87]\n"
    "ds_read_b64_tr_b16 v[118:119], v253 offset:58880\n"
    "v_mfma_f32_16x16x32_bf16 a[88:91], v[120:123], a[8:11], a[88:91]\n"
    "v_mfma_f32_16x16x32_bf16 a[92:95], v[120:123], a[12:15], a[92:95]\n"
    "s_waitcnt lgkmcnt(2)\n"
    "v_mfma_f32_16x16x32_bf16 a[96:99], v[112:115], a[0:3], a[96:99]\n"
    "ds_read_b64_tr_b16 v[120:121], v254 offset:0\n"
    "v_mfma_f32_16x16x32_bf16 a[100:103], v[112:115], a[4:7], a[100:103]\n"
    "ds_read_b64_tr_b16 v[122:123], v254 offset:1024\n"
    "v_mfma_f32_16x16x32_bf16 a[104:107], v[112:115], a[8:11], a[104:107]\n"
    "v_mfma_f32_16x16x32_bf16 a[108:111], v[112:115], a[12:15], a[108:111]\n"
    "s_waitcnt lgkmcnt(2)\n"
    "v_mfma_f32_16x16x32_bf16 a[112:115], v[116:119], a[0:3], a[112:115]\n"
    "ds_read_b64_tr_b16 v[112:113], v255 offset:0\n"
    "v_mfma_f32_16x16x32_bf16 a[116:119], v[116:119], a[4:7], a[116:119]\n"
    "ds_read_b64_tr_b16 v[114:115], v255 offset:1024\n"
    "v_mfma_f32_16x16x32_bf16 a[120:123], v[116:119], a[8:11], a[120:123]\n"
    "v_mfma_f32_16x16x32_bf16 a[124:127], v[116:119], a[12:15], a[124:127]\n"
    "s_waitcnt lgkmcnt(2)\n"
    "v_mfma_f32_16x16x32_bf16 a[64:67], v[120:123], v[0:3], a[64:67]\n"
    "ds_read_b64_tr_b16 v[116:117], v254 offset:512\n"
    "v_mfma_f32_16x16x32_bf16 a[68:71], v[120:123], v[4:7], a[68:71]\n"
    "ds_read_b64_tr_b16 v[118:119], v254 offset:1536\n"
    "v_mfma_f32_16x16x32_bf16 a[72:75], v[120:123], v[8:11], a[72:75]\n"
    "v_mfma_f32_16x16x32_bf16 a[76:79], v[120:123], v[12:15], a[76:79]\n"
    "s_waitcnt lgkmcnt(2)\n"
    "v_mfma_f32_16x16x32_bf16 a[80:83], v[112:115], v[0:3], a[80:83]\n"
    "ds_read_b64_tr_b16 v[120:121], v255 offset:512\n"
    "v_lshl_add_u32 v128, s66, 13, v241\n"
    "v_mfma_f32_16x16x32_bf16 a[84:87], v[112:115], v[4:7], a[84:87]\n"
    "ds_read_b64_tr_b16 v[122:123], v255 offset:1536\n"
    "v_mfma_f32_16x16x32_bf16 a[88:91], v[112:115], v[8:11], a[88:91]\n"
    "v_mfma_f32_16x16x32_bf16 a[92:95], v[112:115], v[12:15], a[92:95]\n"
    "v_xor_b32 v129, 0x2000, v128\n"
    "s_waitcnt lgkmcnt(2)\n"
    "v_mfma_f32_16x16x32_bf16 a[96:99], v[116:119], v[0:3], a[96:99]\n"
    "v_mfma_f32_16x16x32_bf16 a[100:103], v[116:119], v[4:7], a[100:103]\n"
    "v_add_u32 v128, 0, v128\n"
    "v_mfma_f32_16x16x32_bf16 a[104:107], v[116:119], v[8:11], a[104:107]\n"
    "v_mfma_f32_16x16x32_bf16 a[108:111], v[116:119], v[12:15], a[108:111]\n"
    "s_waitcnt lgkmcnt(0)\n"
    "v_mfma_f32_16x16x32_bf16 a[112:115], v[120:123], v[0:3], a[112:115]\n"
    "v_add_u32 v129, 0, v129\n"
    "v_mfma_f32_16x16x32_bf16 a[116:119], v[120:123], v[4:7], a[116:119]\n"
    "v_mfma_f32_16x16x32_bf16 a[120:123], v[120:123], v[8:11], a[120:123]\n"
    "v_mfma_f32_16x16x32_bf16 a[124:127], v[120:123], v[12:15], a[124:127]\n"
    "ds_write_b128 v128, a[76:79] offset:4096\n"
    "ds_write_b128 v128, a[92:95] offset:5120\n"
    "ds_write_b128 v128, a[108:111] offset:6144\n"
    "s_nop 4\n"
    "ds_write_b128 v128, a[124:127] offset:7168\n"
    "ds_write_b128 v128, a[72:75] offset:0\n"
    "ds_write_b128 v128, a[88:91] offset:1024\n"
    "ds_write_b128 v128, a[104:107] offset:2048\n"
    "ds_write_b128 v128, a[120:123] offset:3072\n"
    "v_accvgpr_read_b32 v96, a68\n"
    "v_accvgpr_read_b32 v97, a69\n"
    "v_accvgpr_read_b32 v98, a70\n"
    "v_accvgpr_read_b32 v99, a71\n"
    "v_accvgpr_read_b32 v100, a84\n"
    "v_accvgpr_read_b32 v101, a85\n"
    "v_accvgpr_read_b32 v102, a86\n"
    "v_accvgpr_read_b32 v103, a87\n"
    "v_accvgpr_read_b32 v104, a100\n"
    "v_accvgpr_read_b32 v105, a101\n"
    "v_accvgpr_read_b32 v106, a102\n"
    "v_accvgpr_read_b32 v107, a103\n"
    "v_accvgpr_read_b32 v108, a116\n"
    "v_accvgpr_read_b32 v109, a117\n"
    "v_accvgpr_read_b32 v110, a118\n"
    "v_accvgpr_read_b32 v111, a119\n"
    "v_accvgpr_read_b32 v198, a64\n"
    "v_accvgpr_read_b32 v199, a65\n"
    "v_accvgpr_read_b32 v200, a66\n"
    "v_accvgpr_read_b32 v201, a67\n"
    "v_accvgpr_read_b32 v202, a80\n"
    "v_accvgpr_read_b32 v203, a81\n"
    "v_accvgpr_read_b32 v204, a82\n"
    "v_accvgpr_read_b32 v205, a83\n"
    "v_accvgpr_read_b32 v206, a96\n"
    "v_accvgpr_read_b32 v207, a97\n"
    "v_accvgpr_read_b32 v208, a98\n"
    "v_accvgpr_read_b32 v209, a99\n"
    "v_accvgpr_read_b32 v210, a112\n"
    "v_accvgpr_read_b32 v211, a113\n"
    "v_accvgpr_read_b32 v212, a114\n"
    "v_accvgpr_read_b32 v213, a115\n"
    "s_waitcnt lgkmcnt(0)\n"
    "s_barrier\n"
    "ds_read_b128 v[48:51], v129 offset:4096\n"
    "ds_read_b128 v[52:55], v129 offset:5120\n"
    "ds_read_b128 v[56:59], v129 offset:6144\n"
    "ds_read_b128 v[60:63], v129 offset:7168\n"
    "ds_read_b128 v[32:35], v129 offset:0\n"
    "ds_read_b128 v[36:39], v129 offset:1024\n"
    "ds_read_b128 v[40:43], v129 offset:2048\n"
    "ds_read_b128 v[44:47], v129 offset:3072\n"
    "s_waitcnt lgkmcnt(7)\n"
    "v_add_f32 v48, v48, v96\n"
    "v_add_f32 v49, v49, v97\n"
    "v_add_f32 v50, v50, v98\n"
    "v_add_f32 v51, v51, v99\n"
    "s_waitcnt lgkmcnt(6)\n"
    "v_add_f32 v52, v52, v100\n"
    "v_add_f32 v53, v53, v101\n"
    "v_add_f32 v54, v54, v102\n"
    "v_add_f32 v55, v55, v103\n"
    "s_waitcnt lgkmcnt(5)\n"
    "v_add_f32 v56, v56, v104\n"
    "v_add_f32 v57, v57, v105\n"
    "v_add_f32 v58, v58, v106\n"
    "v_add_f32 v59, v59, v107\n"
    "s_waitcnt lgkmcnt(4)\n"
    "v_add_f32 v60, v60, v108\n"
    "v_add_f32 v61, v61, v109\n"
    "v_add_f32 v62, v62, v110\n"
    "v_add_f32 v63, v63, v111\n"
    "v_cvt_pk_bf16_f32 v48, v48, v49\n"
    "v_cvt_pk_bf16_f32 v49, v50, v51\n"
    "v_cvt_pk_bf16_f32 v50, v52, v53\n"
    "v_cvt_pk_bf16_f32 v51, v54, v55\n"
    "v_cvt_pk_bf16_f32 v52, v56, v57\n"
    "v_cvt_pk_bf16_f32 v53, v58, v59\n"
    "v_cvt_pk_bf16_f32 v54, v60, v61\n"
    "v_cvt_pk_bf16_f32 v55, v62, v63\n"
    "s_xor_b32 s20, s91, 34816\n"
    "buffer_store_dwordx4 v[48:51], v241, s[56:59], s20 offen offset:0 sc1\n"
    "s_xor_b32 s20, s91, 34816\n"
    "buffer_store_dwordx4 v[52:55], v241, s[56:59], s20 offen offset:1024 sc1\n"
    "s_waitcnt lgkmcnt(3)\n"
    "v_add_f32 v32, v32, v198\n"
    "v_add_f32 v33, v33, v199\n"
    "v_add_f32 v34, v34, v200\n"
    "v_add_f32 v35, v35, v201\n"
    "s_waitcnt lgkmcnt(2)\n"
    "v_add_f32 v36, v36, v202\n"
    "v_add_f32 v37, v37, v203\n"
    "v_add_f32 v38, v38, v204\n"
    "v_add_f32 v39, v39, v205\n"
    "s_waitcnt lgkmcnt(1)\n"
    "v_add_f32 v40, v40, v206\n"
    "v_add_f32 v41, v41, v207\n"
    "v_add_f32 v42, v42, v208\n"
    "v_add_f32 v43, v43, v209\n"
    "s_waitcnt lgkmcnt(0)\n"
    "v_add_f32 v44, v44, v210\n"
    "v_add_f32 v45, v45, v211\n"
    "v_add_f32 v46, v46, v212\n"
    "v_add_f32 v47, v47, v213\n"
    "v_bfe_u32 v130, v241, 4, 4\n"
    "v_mul_u32_u24 v130, 1152, v130\n"
    "v_bfe_u32 v131, v241, 8, 1\n"
    "v_lshl_add_u32 v130, v131, 5, v130\n"
    "v_bfe_u32 v131, v241, 9, 1\n"
    "v_lshl_add_u32 v130, v131, 4, v130\n"
    "s_lshl_b32 s96, s67, 2\n"
    "s_add_u32 s96, s96, s66\n"
    "s_lshl_b32 s96, s96, 1\n"
    "s_and_b32 s97, s68, 1\n"
    "s_add_u32 s96, s96, s97\n"
    "s_mul_i32 s96, s96, 18432\n"
    "s_lshl_b32 s97, s69, 1\n"
    "s_lshr_b32 s60, s68, 1\n"
    "s_add_u32 s97, s97, s60\n"
    "s_lshl_b32 s97, s97, 7\n"
    "s_add_u32 s96, s96, s97\n"
    "s_lshr_b32 s97, s69, 1\n"
    "s_lshl_b32 s97, s97, 6\n"
    "s_add_u32 s60, s96, s97\n"
    "s_xor_b32 s97, s97, 64\n"
    "s_add_u32 s61, s96, s97\n"
    "s_waitcnt vmcnt(0)\n"
    "s_add_u32 s99, s70, 2\n"
    "v_mov_b32 v133, s21\n"
    "v_mov_b32 v132, s99\n"
    "buffer_store_dwordx2 v[132:133], v242, s[56:59], s89 offen offset:16 sc1\n"
    "s_add_u32 s97, s89, 16\n"
    "v_bfe_u32 v243, v241, 4, 2\n"
    "v_max_u32 v243, 1, v243\n"
    "v_lshlrev_b32 v243, 10, v243\n"
    "v_xor_b32 v243, s97, v243\n"
    "buffer_load_dwordx2 v[234:235], v243, s[56:59], 0 offen offset:0 sc1\n"
    "s_waitcnt vmcnt(0)\n"
    "v_subrev_u32 v234, s70, v234\n"
    "v_cmp_eq_u32_e64 s[16:17], s21, v235\n"
    "v_subrev_u32 v234, 2, v234\n"
    "v_cmp_gt_u32 vcc, 16, v234\n"
    "s_nop 0\n"
    "s_and_b64 vcc, vcc, s[16:17]\n"
    "s_cmp_eq_u64 vcc, -1\n"
    "s_cbranch_scc0 LslowK6_%=\n"
    "LbackK6_%=:\n"
    "s_add_u32 s62, s91, 2048\n"
    "buffer_load_dwordx4 v[96:99], v241, s[56:59], s62 offen offset:0 sc1\n"
    "buffer_load_dwordx4 v[100:103], v241, s[56:59], s62 offen offset:1024 sc1\n"
    "s_add_u32 s62, s91, 4096\n"
    "buffer_load_dwordx4 v[104:107], v241, s[56:59], s62 offen offset:0 sc1\n"
    "buffer_load_dwordx4 v[108:111], v241, s[56:59], s62 offen offset:1024 sc1\n"
    "s_add_u32 s62, s91, 6144\n"
    "buffer_load_dwordx4 v[64:67], v241, s[56:59], s62 offen offset:0 sc1\n"
    "buffer_load_dwordx4 v[68:71], v241, s[56:59], s62 offen offset:1024 sc1\n"
    "s_or_b32 s96, %[bid], s66\n"
    "s_mov_b32 s97, 268435456\n"
    "s_cmp_eq_u32 s96, 0\n"
    "s_cselect_b32 s96, 98304, s97\n"
    "v_mov_b32 v132, s70\n"
    "buffer_store_dword v132, v242, s[56:59], s96 offen offset:0 sc1\n"
    "s_waitcnt vmcnt(6)\n"
    "v_lshlrev_b32 v124, 16, v96\n"
    "v_and_b32 v125, 0xffff0000, v96\n"
    "v_add_f32 v32, v32, v124\n"
    "v_add_f32 v33, v33, v125\n"
    "v_lshlrev_b32 v126, 16, v97\n"
    "v_and_b32 v127, 0xffff0000, v97\n"
    "v_add_f32 v34, v34, v126\n"
    "v_add_f32 v35, v35, v127\n"
    "v_lshlrev_b32 v124, 16, v98\n"
    "v_and_b32 v125, 0xffff0000, v98\n"
    "v_add_f32 v36, v36, v124\n"
    "v_add_f32 v37, v37, v125\n"
    "v_lshlrev_b32 v126, 16, v99\n"
    "v_and_b32 v127, 0xffff0000, v99\n"
    "v_add_f32 v38, v38, v126\n"
    "v_add_f32 v39, v39, v127\n"
    "s_waitcnt vmcnt(5)\n"
    "v_lshlrev_b32 v124, 16, v100\n"
    "v_and_b32 v125, 0xffff0000, v100\n"
    "v_add_f32 v40, v40, v124\n"
    "v_add_f32 v41, v41, v125\n"
    "v_lshlrev_b32 v126, 16, v101\n"
    "v_and_b32 v127, 0xffff0000, v101\n"
    "v_add_f32 v42, v42, v126\n"
    "v_add_f32 v43, v43, v127\n"
    "v_lshlrev_b32 v124, 16, v102\n"
    "v_and_b32 v125, 0xffff0000, v102\n"
    "v_add_f32 v44, v44, v124\n"
    "v_add_f32 v45, v45, v125\n"
    "v_lshlrev_b32 v126, 16, v103\n"
    "v_and_b32 v127, 0xffff0000, v103\n"
    "v_add_f32 v46, v46, v126\n"
    "v_add_f32 v47, v47, v127\n"
    "s_waitcnt vmcnt(4)\n"
    "v_lshlrev_b32 v124, 16, v104\n"
    "v_and_b32 v125, 0xffff0000, v104\n"
    "v_add_f32 v32, v32, v124\n"
    "v_add_f32 v33, v33, v125\n"
    "v_lshlrev_b32 v126, 16, v105\n"
    "v_and_b32 v127, 0xffff0000, v105\n"
    "v_add_f32 v34, v34, v126\n"
    "v_add_f32 v35, v35, v127\n"
    "v_lshlrev_b32 v124, 16, v106\n"
    "v_and_b32 v125, 0xffff0000, v106\n"
    "v_add_f32 v36, v36, v124\n"
    "v_add_f32 v37, v37, v125\n"
    "v_lshlrev_b32 v126, 16, v107\n"
    "v_and_b32 v127, 0xffff0000, v107\n"
    "v_add_f32 v38, v38, v126\n"
    "v_add_f32 v39, v39, v127\n"
    "s_waitcnt vmcnt(3)\n"
    "v_lshlrev_b32 v124, 16, v108\n"
    "v_and_b32 v125, 0xffff0000, v108\n"
    "v_add_f32 v40, v40, v124\n"
    "v_add_f32 v41, v41, v125\n"
    "v_lshlrev_b32 v126, 16, v109\n"
    "v_and_b32 v127, 0xffff0000, v109\n"
    "v_add_f32 v42, v42, v126\n"
    "v_add_f32 v43, v43, v127\n"
    "v_lshlrev_b32 v124, 16, v110\n"
    "v_and_b32 v125, 0xffff0000, v110\n"
    "v_add_f32 v44, v44, v124\n"
    "v_add_f32 v45, v45, v125\n"
    "v_lshlrev_b32 v126, 16, v111\n"
    "v_and_b32 v127, 0xffff0000, v111\n"
    "v_add_f32 v46, v46, v126\n"
    "v_add_f32 v47, v47, v127\n"
    "s_waitcnt vmcnt(2)\n"
    "v_lshlrev_b32 v124, 16, v64\n"
    "v_and_b32 v125, 0xffff0000, v64\n"
    "v_add_f32 v32, v32, v124\n"
    "v_add_f32 v33, v33, v125\n"
    "v_lshlrev_b32 v126, 16, v65\n"
    "v_and_b32 v127, 0xffff0000, v65\n"
    "v_add_f32 v34, v34, v126\n"
    "v_add_f32 v35, v35, v127\n"
    "v_lshlrev_b32 v124, 16, v66\n"
    "v_and_b32 v125, 0xffff0000, v66\n"
    "v_add_f32 v36, v36, v124\n"
    "v_add_f32 v37, v37, v125\n"
    "v_lshlrev_b32 v126, 16, v67\n"
    "v_and_b32 v127, 0xffff0000, v67\n"
    "v_add_f32 v38, v38, v126\n"
    "v_add_f32 v39, v39, v127\n"
    "s_waitcnt vmcnt(1)\n"
    "v_lshlrev_b32 v124, 16, v68\n"
    "v_and_b32 v125, 0xffff0000, v68\n"
    "v_add_f32 v40, v40, v124\n"
    "v_add_f32 v41, v41, v125\n"
    "v_lshlrev_b32 v126, 16, v69\n"
    "v_and_b32 v127, 0xffff0000, v69\n"
    "v_add_f32 v42, v42, v126\n"
    "v_add_f32 v43, v43, v127\n"
    "v_lshlrev_b32 v124, 16, v70\n"
    "v_and_b32 v125, 0xffff0000, v70\n"
    "v_add_f32 v44, v44, v124\n"
    "v_add_f32 v45, v45, v125\n"
    "v_lshlrev_b32 v126, 16, v71\n"
    "v_and_b32 v127, 0xffff0000, v71\n"
    "v_add_f32 v46, v46, v126\n"
    "v_add_f32 v47, v47, v127\n"
    "v_cvt_pk_bf16_f32 v32, v32, v33\n"
    "v_cvt_pk_bf16_f32 v33, v34, v35\n"
    "v_cvt_pk_bf16_f32 v34, v36, v37\n"
    "v_cvt_pk_bf16_f32 v35, v38, v39\n"
    "v_cvt_pk_bf16_f32 v36, v40, v41\n"
    "v_cvt_pk_bf16_f32 v37, v42, v43\n"
    "v_cvt_pk_bf16_f32 v38, v44, v45\n"
    "v_cvt_pk_bf16_f32 v39, v46, v47\n"
    "v_permlane16_swap_b32 v32, v34\n"
    "v_permlane16_swap_b32 v33, v35\n"
    "v_permlane16_swap_b32 v36, v38\n"
    "v_permlane16_swap_b32 v37, v39\n"
    "buffer_store_dwordx4 v[32:35], v130, s[12:15], s60 offen offset:0\n"
    "buffer_store_dwordx4 v[36:39], v130, s[12:15], s61 offen offset:0\n"
    "s_waitcnt vmcnt(0) lgkmcnt(0)\n"
    "s_branch Lend_%=\n"
    "LslowK0_%=:\n"
    "s_mov_b32 s98, 100000\n"
    "LspK0_%=:\n"
    "buffer_load_dwordx2 v[234:235], v243, s[56:59], 0 offen offset:0 sc1\n"
    "s_waitcnt vmcnt(0)\n"
    "v_subrev_u32 v234, s70, v234\n"
    "v_cmp_eq_u32_e64 s[16:17], s21, v235\n"
    "v_cmp_gt_u32 vcc, 16, v234\n"
    "s_nop 4\n"
    "s_and_b64 vcc, vcc, s[16:17]\n"
    "s_cmp_eq_u64 vcc, -1\n"
    "s_cbranch_scc1 LbackK0_%=\n"
    "s_sub_u32 s98, s98, 1\n"
    "s_cmp_eq_u32 s98, 0\n"
    "s_cbranch_scc1 LbackK0_%=\n"
    "s_sleep 1\n"
    "s_branch LspK0_%=\n"
    "LslowK1_%=:\n"
    "s_mov_b32 s98, 100000\n"
    "LspK1_%=:\n"
    "buffer_load_dwordx2 v[234:235], v243, s[56:59], 0 offen offset:0 sc1\n"
    "s_waitcnt vmcnt(0)\n"
    "v_subrev_u32 v234, s70, v234\n"
    "v_cmp_eq_u32_e64 s[16:17], s21, v235\n"
    "v_subrev_u32 v234, 2, v234\n"
    "v_cmp_gt_u32 vcc, 16, v234\n"
    "s_nop 4\n"
    "s_and_b64 vcc, vcc, s[16:17]\n"
    "s_cmp_eq_u64 vcc, -1\n"
    "s_cbranch_scc1 LbackK1_%=\n"
    "s_sub_u32 s98, s98, 1\n"
    "s_cmp_eq_u32 s98, 0\n"
    "s_cbranch_scc1 LbackK1_%=\n"
    "s_sleep 1\n"
    "s_branch LspK1_%=\n"
    "LslowK2_%=:\n"
    "s_mov_b32 s98, 100000\n"
    "LspK2_%=:\n"
    "buffer_load_dwordx2 v[234:235], v243, s[56:59], 0 offen offset:0 sc1\n"
    "s_waitcnt vmcnt(0)\n"
    "v_subrev_u32 v234, s70, v234\n"
    "v_cmp_eq_u32_e64 s[16:17], s21, v235\n"
    "v_subrev_u32 v234, 3, v234\n"
    "v_cmp_gt_u32 vcc, 16, v234\n"
    "s_nop 4\n"
    "s_and_b64 vcc, vcc, s[16:17]\n"
    "s_cmp_eq_u64 vcc, -1\n"
    "s_cbranch_scc1 LbackK2_%=\n"
    "s_sub_u32 s98, s98, 1\n"
    "s_cmp_eq_u32 s98, 0\n"
    "s_cbranch_scc1 LbackK2_%=\n"
    "s_sleep 1\n"
    "s_branch LspK2_%=\n"
    "LslowK3_%=:\n"
    "s_mov_b32 s98, 100000\n"
    "LspK3_%=:\n"
    "buffer_load_dwordx2 v[234:235], v243, s[56:59], 0 offen offset:0 sc1\n"
    "s_waitcnt vmcnt(0)\n"
    "v_subrev_u32 v234, s70, v234\n"
    "v_cmp_eq_u32_e64 s[16:17], s21, v235\n"
    "v_subrev_u32 v234, 4, v234\n"
    "v_cmp_gt_u32 vcc, 16, v234\n"
    "s_nop 4\n"
    "s_and_b64 vcc, vcc, s[16:17]\n"
    "s_cmp_eq_u64 vcc, -1\n"
    "s_cbranch_scc1 LbackK3_%=\n"
    "s_sub_u32 s98, s98, 1\n"
    "s_cmp_eq_u32 s98, 0\n"
    "s_cbranch_scc1 LbackK3_%=\n"
    "s_sleep 1\n"
    "s_branch LspK3_%=\n"
    "LslowK4_%=:\n"
    "s_mov_b32 s98, 100000\n"
    "LspK4_%=:\n"
    "buffer_load_dwordx2 v[234:235], v243, s[56:59], 0 offen offset:0 sc1\n"
    "s_waitcnt vmcnt(0)\n"
    "v_subrev_u32 v234, s70, v234\n"
    "v_cmp_eq_u32_e64 s[16:17], s21, v235\n"
    "v_subrev_u32 v234, 1, v234\n"
    "v_cmp_gt_u32 vcc, 16, v234\n"
    "s_nop 4\n"
    "s_and_b64 vcc, vcc, s[16:17]\n"
    "s_cmp_eq_u64 vcc, -1\n"
    "s_cbranch_scc1 LbackK4_%=\n"
    "s_sub_u32 s98, s98, 1\n"
    "s_cmp_eq_u32 s98, 0\n"
    "s_cbranch_scc1 LbackK4_%=\n"
    "s_sleep 1\n"
    "s_branch LspK4_%=\n"
    "LslowK5_%=:\n"
    "s_mov_b32 s98, 100000\n"
    "LspK5_%=:\n"
    "buffer_load_dwordx2 v[234:235], v243, s[56:59], 0 offen offset:0 sc1\n"
    "s_waitcnt vmcnt(0)\n"
    "v_subrev_u32 v234, s70, v234\n"
    "v_cmp_eq_u32_e64 s[16:17], s21, v235\n"
    "v_subrev_u32 v234, 5, v234\n"
    "v_cmp_gt_u32 vcc, 16, v234\n"
    "s_nop 4\n"
    "s_and_b64 vcc, vcc, s[16:17]\n"
    "s_cmp_eq_u64 vcc, -1\n"
    "s_cbranch_scc1 LbackK5_%=\n"
    "s_sub_u32 s98, s98, 1\n"
    "s_cmp_eq_u32 s98, 0\n"
    "s_cbranch_scc1 LbackK5_%=\n"
    "s_sleep 1\n"
    "s_branch LspK5_%=\n"
    "LslowK6_%=:\n"
    "s_mov_b32 s98, 100000\n"
    "LspK6_%=:\n"
    "buffer_load_dwordx2 v[234:235], v243, s[56:59], 0 offen offset:0 sc1\n"
    "s_waitcnt vmcnt(0)\n"
    "v_subrev_u32 v234, s70, v234\n"
    "v_cmp_eq_u32_e64 s[16:17], s21, v235\n"
    "v_subrev_u32 v234, 2, v234\n"
    "v_cmp_gt_u32 vcc, 16, v234\n"
    "s_nop 4\n"
    "s_and_b64 vcc, vcc, s[16:17]\n"
    "s_cmp_eq_u64 vcc, -1\n"
    "s_cbranch_scc1 LbackK6_%=\n"
    "s_sub_u32 s98, s98, 1\n"
    "s_cmp_eq_u32 s98, 0\n"
    "s_cbranch_scc1 LbackK6_%=\n"
    "s_sleep 1\n"
    "s_branch LspK6_%=\n"
    "LceK_%=:\n"
    "LroleQ_%=:\n"
    "s_nop 1\n"
    "v_bfe_u32 v125, v124, 2, 3\n"
    "v_lshrrev_b32 v127, 2, v125\n"
    "v_lshl_add_u32 v125, s66, 3, v125\n"
    "v_lshrrev_b32 v126, 5, v124\n"
    "v_lshrrev_b32 v128, 2, v125\n"
    "v_xor_b32 v128, v128, v124\n"
    "v_and_b32 v128, 3, v128\n"
    "v_lshl_add_u32 v128, v126, 2, v128\n"
    "v_lshlrev_b32 v128, 4, v128\n"
    "v_mul_u32_u24 v129, 1152, v125\n"
    "v_add_u32 v174, v129, v128\n"
    "s_mul_i32 s96, s66, 55296\n"
    "s_mul_i32 s93, s66, 3072\n"
    "v_add_u32 v130, 0, v124\n"
    "v_mul_u32_u24 v131, 5462, v130\n"
    "v_lshrrev_b32 v131, 16, v131\n"
    "v_mul_u32_u24 v179, 12, v131\n"
    "v_sub_u32 v130, v130, v179\n"
    "v_mul_u32_u24 v131, 3456, v131\n"
    "v_min_u32 v130, 8, v130\n"
    "v_lshlrev_b32 v130, 4, v130\n"
    "v_add3_u32 v179, v130, v131, s96\n"
    "v_add_u32 v130, 64, v124\n"
    "v_mul_u32_u24 v131, 5462, v130\n"
    "v_lshrrev_b32 v131, 16, v131\n"
    "v_mul_u32_u24 v180, 12, v131\n"
    "v_sub_u32 v130, v130, v180\n"
    "v_mul_u32_u24 v131, 3456, v131\n"
    "v_min_u32 v130, 8, v130\n"
    "v_lshlrev_b32 v130, 4, v130\n"
    "v_add3_u32 v180, v130, v131, s96\n"
    "v_add_u32 v130, 128, v124\n"
    "v_mul_u32_u24 v131, 5462, v130\n"
    "v_lshrrev_b32 v131, 16, v131\n"
    "v_mul_u32_u24 v181, 12, v131\n"
    "v_sub_u32 v130, v130, v181\n"
    "v_mul_u32_u24 v131, 3456, v131\n"
    "v_min_u32 v130, 8, v130\n"
    "v_lshlrev_b32 v130, 4, v130\n"
    "v_add3_u32 v181, v130, v131, s96\n"
    "v_and_b32 v130, 15, v124\n"
    "v_and_b32 v128, 3, v124\n"
    "v_bfe_u32 v131, v124, 4, 1\n"
    "v_lshlrev_b32 v128, 3, v128\n"
    "v_lshl_add_u32 v128, v131, 5, v128\n"
    "v_bfe_u32 v131, v124, 2, 2\n"
    "v_lshl_add_u32 v236, v131, 6, v128\n"
    "v_lshl_add_u32 v131, v126, 3, v131\n"
    "v_mul_u32_u24 v131, 192, v131\n"
    "v_add_u32 v184, v131, v128\n"
    "v_mul_u32_u24 v131, 192, v130\n"
    "v_lshrrev_b32 v128, 4, v124\n"
    "v_lshl_add_u32 v131, v128, 2, v131\n"
    "s_add_u32 s97, s93, 16528\n"
    "v_add_u32 v185, s97, v131\n"
    "v_bfe_u32 v131, v124, 3, 2\n"
    "v_lshlrev_b32 v131, 10, v131\n"
    "v_and_b32 v128, 7, v124\n"
    "v_lshl_add_u32 v131, v128, 6, v131\n"
    "v_bfe_u32 v128, v124, 2, 2\n"
    "v_xor_b32 v128, v128, v126\n"
    "v_lshl_add_u32 v131, v128, 4, v131\n"
    "v_lshl_add_u32 v175, s66, 12, v131\n"
    "s_and_b32 s97, s68, 1\n"
    "s_lshl_b32 s97, s97, 11\n"
    "v_xor_b32 v175, s97, v175\n"
    "v_xor_b32 v176, 32, v175\n"
    "v_xor_b32 v177, 0x1000, v175\n"
    "v_xor_b32 v178, 0x1000, v176\n"
    "v_lshl_add_u32 v131, s66, 4, v130\n"
    "v_lshlrev_b32 v131, 1, v131\n"
    "v_cmp_eq_u32 vcc, 0, v124\n"
    "v_mov_b32 v242, 268435456\n"
    "s_nop 0\n"
    "v_cndmask_b32_e64 v242, v242, 0, vcc\n"
    "s_mul_i32 s96, s67, 147456\n"
    "s_mul_i32 s97, s71, 144\n"
    "s_waitcnt lgkmcnt(0)\n"
    "s_add_u32 s36, s20, s96\n"
    "s_addc_u32 s37, s21, 0\n"
    "s_and_b32 s37, s37, 0xffff\n"
    "s_mov_b32 s38, 147456\n"
    "s_mov_b32 s39, 0x20000\n"
    "s_add_u32 m0, s64, 0\n"
    "s_mov_b32 s65, 0\n"
    "buffer_load_dwordx4 v174, s[36:39], s65 offen offset:0 lds\n"
    "s_add_u32 m0, s64, 4096\n"
    "s_mov_b32 s65, 36864\n"
    "buffer_load_dwordx4 v174, s[36:39], s65 offen offset:0 lds\n"
    "s_add_u32 m0, s64, 8192\n"
    "s_mov_b32 s65, 73728\n"
    "buffer_load_dwordx4 v174, s[36:39], s65 offen offset:0 lds\n"
    "s_add_u32 m0, s64, 12288\n"
    "s_mov_b32 s65, 110592\n"
    "buffer_load_dwordx4 v174, s[36:39], s65 offen offset:0 lds\n"
    "s_add_u32 s40, s22, s97\n"
    "s_addc_u32 s41, s23, 0\n"
    "s_and_b32 s41, s41, 0xffff\n"
    "s_mov_b32 s42, 1989648\n"
    "s_mov_b32 s43, 0x20000\n"
    "s_add_u32 m0, s93, 16384\n"
    "s_mov_b32 s65, 0\n"
    "buffer_load_dwordx4 v179, s[40:43], s65 offen offset:0 lds\n"
    "s_add_u32 m0, s93, 17408\n"
    "s_mov_b32 s65, 0\n"
    "buffer_load_dwordx4 v180, s[40:43], s65 offen offset:0 lds\n"
    "s_add_u32 m0, s93, 18432\n"
    "s_mov_b32 s65, 0\n"
    "buffer_load_dwordx4 v181, s[40:43], s65 offen offset:0 lds\n"
    "s_mov_b32 s60, s32\n"
    "s_and_b32 s61, s33, 0xffff\n"
    "s_mov_b32 s62, 1152\n"
    "s_mov_b32 s63, 0x20000\n"
    "buffer_load_ushort v186, v131, s[60:63], 0 offen offset:0\n"
    "buffer_load_ushort v187, v131, s[60:63], 0 offen offset:128\n"
    "buffer_load_ushort v188, v131, s[60:63], 0 offen offset:256\n"
    "buffer_load_ushort v189, v131, s[60:63], 0 offen offset:384\n"
    "buffer_load_ushort v190, v131, s[60:63], 0 offen offset:512\n"
    "buffer_load_ushort v191, v131, s[60:63], 0 offen offset:640\n"
    "buffer_load_ushort v192, v131, s[60:63], 0 offen offset:768\n"
    "buffer_load_ushort v193, v131, s[60:63], 0 offen offset:896\n"
    "buffer_load_ushort v194, v131, s[60:63], 0 offen offset:1024\n"
    "s_mov_b32 s56, s34\n"
    "s_and_b32 s57, s35, 0xffff\n"
    "s_mov_b32 s58, 50331648\n"
    "s_mov_b32 s59, 0x20000\n"
    "s_mov_b32 s96, 98304\n"
    "buffer_load_dword v100, off, s[56:59], s96 sc1\n"
    "s_add_u32 m0, s64, 28672\n"
    "s_mov_b32 s65, 128\n"
    "buffer_load_dwordx4 v174, s[36:39], s65 offen offset:0 lds\n"
    "s_add_u32 m0, s64, 32768\n"
    "s_mov_b32 s65, 36992\n"
    "buffer_load_dwordx4 v174, s[36:39], s65 offen offset:0 lds\n"
    "s_add_u32 m0, s64, 36864\n"
    "s_mov_b32 s65, 73856\n"
    "buffer_load_dwordx4 v174, s[36:39], s65 offen offset:0 lds\n"
    "s_add_u32 m0, s64, 40960\n"
    "s_mov_b32 s65, 110720\n"
    "buffer_load_dwordx4 v174, s[36:39], s65 offen offset:0 lds\n"
    "s_add_u32 m0, s93, 45056\n"
    "s_mov_b32 s65, 221184\n"
    "buffer_load_dwordx4 v179, s[40:43], s65 offen offset:0 lds\n"
    "s_add_u32 m0, s93, 46080\n"
    "s_mov_b32 s65, 221184\n"
    "buffer_load_dwordx4 v180, s[40:43], s65 offen offset:0 lds\n"
    "s_add_u32 m0, s93, 47104\n"
    "s_mov_b32 s65, 221184\n"
    "buffer_load_dwordx4 v181, s[40:43], s65 offen offset:0 lds\n"
    "s_waitcnt vmcnt(16)\n"
    "v_lshl_or_b32 v186, v186, 16, v186\n"
    "ds_write_b32 v185, v186 offset:0\n"
    "s_waitcnt lgkmcnt(0)\n"
    "s_barrier\n"
    "v_add_u32 v184, 16384, v184\n"
    "v_add_u32 v175, 0, v175\n"
    "ds_read_b128 a[0:3], v175 offset:0\n"
    "v_add_u32 v176, 0, v176\n"
    "ds_read_b128 a[8:11], v176 offset:0\n"
    "ds_read_b128 a[16:19], v175 offset:512\n"
    "ds_read_b128 a[24:27], v176 offset:512\n"
    "ds_read_b64_tr_b16 v[134:135], v184 offset:0\n"
    "ds_read_b64_tr_b16 v[136:137], v184 offset:768\n"
    "ds_read_b64_tr_b16 v[138:139], v184 offset:64\n"
    "ds_read_b64_tr_b16 v[140:141], v184 offset:832\n"
    "ds_read_b64_tr_b16 v[142:143], v184 offset:128\n"
    "ds_read_b64_tr_b16 v[144:145], v184 offset:896\n"
    "ds_read_b64_tr_b16 v[146:147], v184 offset:3072\n"
    "ds_read_b64_tr_b16 v[148:149], v184 offset:3840\n"
    "s_waitcnt lgkmcnt(6)\n"
    "v_mfma_f32_32x32x16_bf16 v[64:79], v[134:137], a[0:3], 0\n"
    "v_add_u32 v177, 0, v177\n"
    "ds_read_b128 a[4:7], v177 offset:0\n"
    "ds_read_b64_tr_b16 v[166:167], v184 offset:3136\n"
    "ds_read_b64_tr_b16 v[168:169], v184 offset:3904\n"
    "s_add_u32 m0, s64, 57344\n"
    "s_mov_b32 s65, 256\n"
    "buffer_load_dwordx4 v174, s[36:39], s65 offen offset:0 lds\n"
    "s_waitcnt vmcnt(16)\n"
    "v_lshl_or_b32 v187, v187, 16, v187\n"
    "s_waitcnt vmcnt(15)\n"
    "v_lshl_or_b32 v188, v188, 16, v188\n"
    "s_waitcnt vmcnt(14)\n"
    "v_lshl_or_b32 v189, v189, 16, v189\n"
    "s_waitcnt vmcnt(13)\n"
    "v_lshl_or_b32 v190, v190, 16, v190\n"
    "s_waitcnt lgkmcnt(7)\n"
    "v_mfma_f32_32x32x16_bf16 v[80:95], v[138:141], a[0:3], 0\n"
    "v_add_u32 v178, 0, v178\n"
    "ds_read_b128 a[12:15], v178 offset:0\n"
    "ds_read_b64_tr_b16 v[170:171], v184 offset:3200\n"
    "ds_read_b64_tr_b16 v[172:173], v184 offset:3968\n"
    "s_add_u32 m0, s64, 61440\n"
    "s_mov_b32 s65, 37120\n"
    "buffer_load_dwordx4 v174, s[36:39], s65 offen offset:0 lds\n"
    "s_waitcnt vmcnt(13)\n"
    "v_lshl_or_b32 v191, v191, 16, v191\n"
    "s_waitcnt vmcnt(12)\n"
    "v_lshl_or_b32 v192, v192, 16, v192\n"
    "s_waitcnt vmcnt(11)\n"
    "v_lshl_or_b32 v193, v193, 16, v193\n"
    "s_waitcnt lgkmcnt(8)\n"
    "v_mfma_f32_32x32x16_bf16 v[150:165], v[142:145], a[0:3], 0\n"
    "ds_read_b128 a[20:23], v177 offset:512\n"
    "ds_read_b64_tr_b16 v[134:135], v184 offset:6144\n"
    "ds_read_b64_tr_b16 v[136:137], v184 offset:6912\n"
    "s_add_u32 m0, s64, 65536\n"
    "s_mov_b32 s65, 73984\n"
    "buffer_load_dwordx4 v174, s[36:39], s65 offen offset:0 lds\n"
    "s_waitcnt vmcnt(11)\n"
    "v_lshl_or_b32 v194, v194, 16, v194\n"
    "s_mul_i32 s97, s68, 18\n"
    "s_lshr_b32 s96, s69, 1\n"
    "s_mul_i32 s96, s96, 9\n"
    "s_waitcnt lgkmcnt(9)\n"
    "v_mfma_f32_32x32x16_bf16 v[64:79], v[146:149], a[8:11], v[64:79]\n"
    "ds_read_b128 a[28:31], v178 offset:512\n"
    "ds_read_b64_tr_b16 v[138:139], v184 offset:6208\n"
    "ds_read_b64_tr_b16 v[140:141], v184 offset:6976\n"
    "s_add_u32 m0, s64, 69632\n"
    "s_mov_b32 s65, 110848\n"
    "buffer_load_dwordx4 v174, s[36:39], s65 offen offset:0 lds\n"
    "s_add_u32 s97, s97, s96\n"
    "s_add_u32 s97, s97, 4\n"
    "s_lshl_b32 s97, s97, 7\n"
    "s_waitcnt lgkmcnt(9)\n"
    "v_mfma_f32_32x32x16_bf16 v[80:95], v[166:169], a[8:11], v[80:95]\n"
    "ds_read_b64_tr_b16 v[142:143], v184 offset:6272\n"
    "ds_read_b64_tr_b16 v[144:145], v184 offset:7040\n"
    "s_add_u32 m0, s93, 73728\n"
    "s_mov_b32 s65, 442368\n"
    "buffer_load_dwordx4 v179, s[40:43], s65 offen offset:0 lds\n"
    "s_add_u32 s44, s26, s97\n"
    "s_addc_u32 s45, s27, 0\n"
    "s_and_b32 s45, s45, 0xffff\n"
    "s_mov_b32 s46, 5299840\n"
    "s_waitcnt lgkmcnt(8)\n"
    "v_mfma_f32_32x32x16_bf16 v[150:165], v[170:173], a[8:11], v[150:165]\n"
    "ds_read_b64_tr_b16 v[146:147], v184 offset:9216\n"
    "ds_read_b64_tr_b16 v[148:149], v184 offset:9984\n"
    "s_add_u32 m0, s93, 74752\n"
    "s_mov_b32 s65, 442368\n"
    "buffer_load_dwordx4 v180, s[40:43], s65 offen offset:0 lds\n"
    "v_add_u32 v185, 28672, v185\n"
    "s_waitcnt vmcnt(6)\n"
    "ds_write_b32 v185, v187 offset:0\n"
    "s_mov_b32 s47, 0x20000\n"
    "v_mul_u32_u24 v239, 9216, v125\n"
    "v_and_b32 v128, 3, v124\n"
    "v_lshl_add_u32 v239, v128, 4, v239\n"
    "s_waitcnt lgkmcnt(8)\n"
    "v_mfma_f32_32x32x16_bf16 v[64:79], v[134:137], a[16:19], v[64:79]\n"
    "ds_read_b64_tr_b16 v[166:167], v184 offset:9280\n"
    "ds_read_b64_tr_b16 v[168:169], v184 offset:10048\n"
    "s_add_u32 m0, s93, 75776\n"
    "s_mov_b32 s65, 442368\n"
    "buffer_load_dwordx4 v181, s[40:43], s65 offen offset:0 lds\n"
    "v_lshl_add_u32 v239, v126, 6, v239\n"
    "s_sub_u32 s96, 9216, s97\n"
    "s_add_u32 s60, s28, s97\n"
    "s_addc_u32 s61, s29, 0\n"
    "s_waitcnt lgkmcnt(7)\n"
    "v_mfma_f32_32x32x16_bf16 v[80:95], v[138:141], a[16:19], v[80:95]\n"
    "ds_read_b64_tr_b16 v[170:171], v184 offset:9344\n"
    "ds_read_b64_tr_b16 v[172:173], v184 offset:10112\n"
    "s_add_u32 m0, s64, 86016\n"
    "s_mov_b32 s65, 384\n"
    "buffer_load_dwordx4 v174, s[36:39], s65 offen offset:0 lds\n"
    "s_and_b32 s61, s61, 0xffff\n"
    "s_mov_b32 s62, s96\n"
    "v_lshl_add_u32 v99, s66, 6, v124\n"
    "s_waitcnt lgkmcnt(7)\n"
    "v_mfma_f32_32x32x16_bf16 v[150:165], v[142:145], a[16:19], v[150:165]\n"
    "s_waitcnt lgkmcnt(4)\n"
    "s_barrier\n"
    "v_add_u32 v184, 28672, v184\n"
    "v_add_u32 v175, 28672, v175\n"
    "ds_read_b128 a[32:35], v175 offset:0\n"
    "ds_read_b64_tr_b16 v[134:135], v184 offset:0\n"
    "ds_read_b64_tr_b16 v[136:137], v184 offset:768\n"
    "v_lshlrev_b32 v131, 1, v99\n"
    "buffer_load_ushort v97, v131, s[60:63], 0 offen offset:0\n"
    "v_mfma_f32_32x32x16_bf16 v[64:79], v[146:149], a[24:27], v[64:79]\n"
    "v_add_u32 v176, 28672, v176\n"
    "ds_read_b128 a[40:43], v176 offset:0\n"
    "ds_read_b64_tr_b16 v[138:139], v184 offset:64\n"
    "ds_read_b64_tr_b16 v[140:141], v184 offset:832\n"
    "s_add_u32 m0, s64, 90112\n"
    "s_mov_b32 s65, 37248\n"
    "buffer_load_dwordx4 v174, s[36:39], s65 offen offset:0 lds\n"
    "s_add_u32 m0, s64, 94208\n"
    "s_mov_b32 s65, 74112\n"
    "buffer_load_dwordx4 v174, s[36:39], s65 offen offset:0 lds\n"
    "buffer_load_ushort v98, v131, s[60:63], 0 offen offset:512\n"
    "s_mul_i32 s96, s68, 2304\n"
    "s_waitcnt lgkmcnt(8)\n"
    "v_mfma_f32_32x32x16_bf16 v[80:95], v[166:169], a[24:27], v[80:95]\n"
    "ds_read_b128 a[48:51], v175 offset:512\n"
    "ds_read_b64_tr_b16 v[142:143], v184 offset:128\n"
    "ds_read_b64_tr_b16 v[144:145], v184 offset:896\n"
    "s_add_u32 m0, s64, 98304\n"
    "s_mov_b32 s65, 110976\n"
    "buffer_load_dwordx4 v174, s[36:39], s65 offen offset:0 lds\n"
    "s_add_u32 s60, s26, s96\n"
    "s_addc_u32 s61, s27, 0\n"
    "s_and_b32 s61, s61, 0xffff\n"
    "s_mov_b32 s62, 5301504\n"
    "s_waitcnt lgkmcnt(9)\n"
    "v_mfma_f32_32x32x16_bf16 v[150:165], v[170:173], a[24:27], v[150:165]\n"
    "ds_read_b128 a[56:59], v176 offset:512\n"
    "ds_read_b64_tr_b16 v[146:147], v184 offset:3072\n"
    "ds_read_b64_tr_b16 v[148:149], v184 offset:3840\n"
    "s_add_u32 m0, s93, 102400\n"
    "s_mov_b32 s65, 663552\n"
    "buffer_load_dwordx4 v179, s[40:43], s65 offen offset:0 lds\n"
    "s_mov_b32 s63, 0x20000\n"
    "s_mov_b32 s52, s24\n"
    "s_and_b32 s53, s25, 0xffff\n"
    "s_waitcnt lgkmcnt(9)\n"
    "v_mfma_f32_32x32x16_bf16 v[64:79], v[134:137], a[32:35], v[64:79]\n"
    "v_add_u32 v177, 28672, v177\n"
    "ds_read_b128 a[36:39], v177 offset:0\n"
    "ds_read_b64_tr_b16 v[166:167], v184 offset:3136\n"
    "ds_read_b64_tr_b16 v[168:169], v184 offset:3904\n"
    "s_add_u32 m0, s93, 103424\n"
    "s_mov_b32 s65, 663552\n"
    "buffer_load_dwordx4 v180, s[40:43], s65 offen offset:0 lds\n"
    "s_mov_b32 s54, 663552\n"
    "s_mov_b32 s55, 0x20000\n"
    "s_mul_i32 s96, s68, 1327104\n"
    "s_waitcnt lgkmcnt(9)\n"
    "v_mfma_f32_32x32x16_bf16 v[80:95], v[138:141], a[32:35], v[80:95]\n"
    "v_add_u32 v178, 28672, v178\n"
    "ds_read_b128 a[44:47], v178 offset:0\n"
    "ds_read_b64_tr_b16 v[170:171], v184 offset:3200\n"
    "ds_read_b64_tr_b16 v[172:173], v184 offset:3968\n"
    "s_add_u32 m0, s93, 104448\n"
    "s_mov_b32 s65, 663552\n"
    "buffer_load_dwordx4 v181, s[40:43], s65 offen offset:0 lds\n"
    "s_add_u32 s48, s30, s96\n"
    "s_addc_u32 s49, s31, 0\n"
    "s_and_b32 s49, s49, 0xffff\n"
    "s_mov_b32 s50, 1327104\n"
    "s_waitcnt lgkmcnt(9)\n"
    "v_mfma_f32_32x32x16_bf16 v[150:165], v[142:145], a[32:35], v[150:165]\n"
    "ds_read_b128 a[52:55], v177 offset:512\n"
    "ds_read_b64_tr_b16 v[134:135], v184 offset:6144\n"
    "ds_read_b64_tr_b16 v[136:137], v184 offset:6912\n"
    "s_add_u32 m0, s64, 114688\n"
    "s_mov_b32 s65, 512\n"
    "buffer_load_dwordx4 v174, s[36:39], s65 offen offset:0 lds\n"
    "s_mov_b32 s51, 0x20000\n"
    "s_and_b32 s13, s13, 0xffff\n"
    "s_mov_b32 s14, 2359296\n"
    "s_mov_b32 s15, 0x20000\n"
    "s_waitcnt lgkmcnt(9)\n"
    "v_mfma_f32_32x32x16_bf16 v[64:79], v[146:149], a[40:43], v[64:79]\n"
    "ds_read_b128 a[60:63], v178 offset:512\n"
    "ds_read_b64_tr_b16 v[138:139], v184 offset:6208\n"
    "ds_read_b64_tr_b16 v[140:141], v184 offset:6976\n"
    "s_add_u32 m0, s64, 118784\n"
    "s_mov_b32 s65, 37376\n"
    "buffer_load_dwordx4 v174, s[36:39], s65 offen offset:0 lds\n"
    "v_readfirstlane_b32 s70, v100\n"
    "s_nop 0\n"
    "s_add_u32 s70, s70, 32\n"
    "s_xor_b32 s21, s70, 1540483477\n"
    "s_lshl_b32 s96, s67, 2\n"
    "s_waitcnt lgkmcnt(9)\n"
    "v_mfma_f32_32x32x16_bf16 v[80:95], v[166:169], a[40:43], v[80:95]\n"
    "ds_read_b64_tr_b16 v[142:143], v184 offset:6272\n"
    "ds_read_b64_tr_b16 v[144:145], v184 offset:7040\n"
    "s_add_u32 m0, s64, 122880\n"
    "s_mov_b32 s65, 74240\n"
    "buffer_load_dwordx4 v174, s[36:39], s65 offen offset:0 lds\n"
    "s_add_u32 s96, s96, s68\n"
    "s_lshl_b32 s22, s96, 2\n"
    "s_add_u32 s22, s22, s66\n"
    "s_waitcnt lgkmcnt(8)\n"
    "v_mfma_f32_32x32x16_bf16 v[150:165], v[170:173], a[40:43], v[150:165]\n"
    "ds_read_b64_tr_b16 v[146:147], v184 offset:9216\n"
    "ds_read_b64_tr_b16 v[148:149], v184 offset:9984\n"
    "s_add_u32 m0, s64, 126976\n"
    "s_mov_b32 s65, 111104\n"
    "buffer_load_dwordx4 v174, s[36:39], s65 offen offset:0 lds\n"
    "v_add_u32 v185, 28672, v185\n"
    "s_waitcnt vmcnt(13)\n"
    "ds_write_b32 v185, v188 offset:0\n"
    "s_lshl_b32 s22, s22, 8\n"
    "s_add_u32 s22, s22, 32768\n"
    "s_lshl_b32 s97, s69, 6\n"
    "s_add_u32 s89, s22, s97\n"
    "s_waitcnt lgkmcnt(8)\n"
    "v_mfma_f32_32x32x16_bf16 v[64:79], v[134:137], a[48:51], v[64:79]\n"
    "ds_read_b64_tr_b16 v[166:167], v184 offset:9280\n"
    "ds_read_b64_tr_b16 v[168:169], v184 offset:10048\n"
    "s_add_u32 m0, s93, 131072\n"
    "s_mov_b32 s65, 884736\n"
    "buffer_load_dwordx4 v179, s[40:43], s65 offen offset:0 lds\n"
    "s_waitcnt lgkmcnt(7)\n"
    "v_mfma_f32_32x32x16_bf16 v[80:95], v[138:141], a[48:51], v[80:95]\n"
    "ds_read_b64_tr_b16 v[170:171], v184 offset:9344\n"
    "ds_read_b64_tr_b16 v[172:173], v184 offset:10112\n"
    "s_add_u32 m0, s93, 132096\n"
    "s_mov_b32 s65, 884736\n"
    "buffer_load_dwordx4 v180, s[40:43], s65 offen offset:0 lds\n"
    "s_add_u32 s99, s70, 0\n"
    "v_mov_b32 v133, s21\n"
    "v_mov_b32 v132, s99\n"
    "buffer_store_dwordx2 v[132:133], v242, s[56:59], s89 offen offset:0 sc1\n"
    "s_waitcnt lgkmcnt(7)\n"
    "v_mfma_f32_32x32x16_bf16 v[150:165], v[142:145], a[48:51], v[150:165]\n"
    "s_waitcnt lgkmcnt(4)\n"
    "s_barrier\n"
    "v_add_u32 v184, 28672, v184\n"
    "v_add_u32 v175, 28672, v175\n"
    "ds_read_b128 a[64:67], v175 offset:0\n"
    "ds_read_b64_tr_b16 v[134:135], v184 offset:0\n"
    "ds_read_b64_tr_b16 v[136:137], v184 offset:768\n"
    "v_mfma_f32_32x32x16_bf16 v[64:79], v[146:149], a[56:59], v[64:79]\n"
    "v_add_u32 v176, 28672, v176\n"
    "ds_read_b128 a[72:75], v176 offset:0\n"
    "ds_read_b64_tr_b16 v[138:139], v184 offset:64\n"
    "ds_read_b64_tr_b16 v[140:141], v184 offset:832\n"
    "s_add_u32 m0, s93, 133120\n"
    "s_mov_b32 s65, 884736\n"
    "buffer_load_dwordx4 v181, s[40:43], s65 offen offset:0 lds\n"
    "s_waitcnt lgkmcnt(8)\n"
    "v_mfma_f32_32x32x16_bf16 v[80:95], v[166:169], a[56:59], v[80:95]\n"
    "ds_read_b128 a[80:83], v175 offset:512\n"
    "ds_read_b64_tr_b16 v[142:143], v184 offset:128\n"
    "ds_read_b64_tr_b16 v[144:145], v184 offset:896\n"
    "s_add_u32 m0, s64, 0\n"
    "s_mov_b32 s65, 640\n"
    "buffer_load_dwordx4 v174, s[36:39], s65 offen offset:0 lds\n"
    "s_waitcnt lgkmcnt(9)\n"
    "v_mfma_f32_32x32x16_bf16 v[150:165], v[170:173], a[56:59], v[150:165]\n"
    "ds_read_b128 a[88:91], v176 offset:512\n"
    "ds_read_b64_tr_b16 v[146:147], v184 offset:3072\n"
    "ds_read_b64_tr_b16 v[148:149], v184 offset:3840\n"
    "s_add_u32 m0, s64, 4096\n"
    "s_mov_b32 s65, 37504\n"
    "buffer_load_dwordx4 v174, s[36:39], s65 offen offset:0 lds\n"
    "s_getpc_b64 s[18:19]\n"
    "LpcQ_%=:\n"
    "v_lshlrev_b32 v128, 7, v124\n"
    "v_lshl_add_u32 v128, s66, 13, v128\n"
    "v_min_u32 v131, LceQ_%=-LpcQ_%=-4, v128\n"
    "global_load_dword v234, v131, s[18:19]\n"
    "v_add_u32 v128, 0x8000, v128\n"
    "v_min_u32 v131, LceQ_%=-LpcQ_%=-4, v128\n"
    "global_load_dword v234, v131, s[18:19]\n"
    "v_add_u32 v128, 0x8000, v128\n"
    "s_waitcnt lgkmcnt(9)\n"
    "v_mfma_f32_32x32x16_bf16 v[64:79], v[134:137], a[64:67], v[64:79]\n"
    "v_add_u32 v177, 28672, v177\n"
    "ds_read_b128 a[68:71], v177 offset:0\n"
    "ds_read_b64_tr_b16 v[166:167], v184 offset:3136\n"
    "ds_read_b64_tr_b16 v[168:169], v184 offset:3904\n"
    "s_waitcnt lgkmcnt(9)\n"
    "v_mfma_f32_32x32x16_bf16 v[80:95], v[138:141], a[64:67], v[80:95]\n"
    "v_add_u32 v178, 28672, v178\n"
    "ds_read_b128 a[76:79], v178 offset:0\n"
    "ds_read_b64_tr_b16 v[170:171], v184 offset:3200\n"
    "ds_read_b64_tr_b16 v[172:173], v184 offset:3968\n"
    "s_add_u32 m0, s64, 8192\n"
    "s_mov_b32 s65, 74368\n"
    "buffer_load_dwordx4 v174, s[36:39], s65 offen offset:0 lds\n"
    "v_and_b32 v128, 3, v124\n"
    "s_waitcnt lgkmcnt(9)\n"
    "v_mfma_f32_32x32x16_bf16 v[150:165], v[142:145], a[64:67], v[150:165]\n"
    "ds_read_b128 a[84:87], v177 offset:512\n"
    "ds_read_b64_tr_b16 v[134:135], v184 offset:6144\n"
    "ds_read_b64_tr_b16 v[136:137], v184 offset:6912\n"
    "s_add_u32 m0, s64, 12288\n"
    "s_mov_b32 s65, 111232\n"
    "buffer_load_dwordx4 v174, s[36:39], s65 offen offset:0 lds\n"
    "v_lshlrev_b32 v127, 1, v127\n"
    "v_xor_b32 v127, v127, v128\n"
    "s_waitcnt lgkmcnt(9)\n"
    "v_mfma_f32_32x32x16_bf16 v[64:79], v[146:149], a[72:75], v[64:79]\n"
    "ds_read_b128 a[92:95], v178 offset:512\n"
    "ds_read_b64_tr_b16 v[138:139], v184 offset:6208\n"
    "ds_read_b64_tr_b16 v[140:141], v184 offset:6976\n"
    "s_add_u32 m0, s93, 16384\n"
    "s_mov_b32 s65, 1105920\n"
    "buffer_load_dwordx4 v179, s[40:43], s65 offen offset:0 lds\n"
    "v_lshl_add_u32 v240, v127, 4, v129\n"
    "s_waitcnt lgkmcnt(9)\n"
    "v_mfma_f32_32x32x16_bf16 v[80:95], v[166:169], a[72:75], v[80:95]\n"
    "ds_read_b64_tr_b16 v[142:143], v184 offset:6272\n"
    "ds_read_b64_tr_b16 v[144:145], v184 offset:7040\n"
    "s_lshr_b32 s96, s69, 1\n"
    "v_xor_b32 v127, s96, v126\n"
    "v_lshl_add_u32 v240, v127, 6, v240\n"
    "v_lshl_add_u32 v236, v126, 10, v236\n"
    "s_and_b32 s96, s66, 1\n"
    "s_lshl_b32 s97, s96, 9\n"
    "v_add_u32 v236, s97, v236\n"
    "v_add_u32 v237, 0x10000, v236\n"
    "v_add_u32 v238, 0x20000, v236\n"
    "s_lshl_b32 s97, s96, 7\n"
    "s_add_u32 s97, s97, 147968\n"
    "v_lshl_add_u32 v244, v126, 4, s97\n"
    "s_mov_b32 s94, 0\n"
    "s_mov_b32 s95, -1\n"
    "s_waitcnt lgkmcnt(8)\n"
    "v_mfma_f32_32x32x16_bf16 v[150:165], v[170:173], a[72:75], v[150:165]\n"
    "ds_read_b64_tr_b16 v[146:147], v184 offset:9216\n"
    "ds_read_b64_tr_b16 v[148:149], v184 offset:9984\n"
    "s_add_u32 m0, s93, 17408\n"
    "s_mov_b32 s65, 1105920\n"
    "buffer_load_dwordx4 v180, s[40:43], s65 offen offset:0 lds\n"
    "v_add_u32 v185, 28672, v185\n"
    "s_waitcnt vmcnt(16)\n"
    "ds_write_b32 v185, v189 offset:0\n"
    "v_lshlrev_b32 v99, 2, v99\n"
    "s_waitcnt lgkmcnt(8)\n"
    "v_mfma_f32_32x32x16_bf16 v[64:79], v[134:137], a[80:83], v[64:79]\n"
    "ds_read_b64_tr_b16 v[166:167], v184 offset:9280\n"
    "ds_read_b64_tr_b16 v[168:169], v184 offset:10048\n"
    "s_add_u32 m0, s93, 18432\n"
    "s_mov_b32 s65, 1105920\n"
    "buffer_load_dwordx4 v181, s[40:43], s65 offen offset:0 lds\n"
    "v_add_u32 v99, 147968, v99\n"
    "v_lshlrev_b32 v97, 16, v97\n"
    "s_waitcnt lgkmcnt(7)\n"
    "v_mfma_f32_32x32x16_bf16 v[80:95], v[138:141], a[80:83], v[80:95]\n"
    "ds_read_b64_tr_b16 v[170:171], v184 offset:9344\n"
    "ds_read_b64_tr_b16 v[172:173], v184 offset:10112\n"
    "v_lshlrev_b32 v98, 16, v98\n"
    "ds_write_b32 v99, v97 offset:0\n"
    "ds_write_b32 v99, v98 offset:1024\n"
    "s_lshl_b32 s96, s67, 2\n"
    "s_add_u32 s96, s96, s68\n"
    "s_lshr_b32 s97, s68, 1\n"
    "s_lshl_b32 s91, s69, 1\n"
    "s_add_u32 s91, s91, s97\n"
    "s_lshl_b32 s91, s91, 7\n"
    "s_xor_b32 s92, s91, 128\n"
    "s_xor_b32 s97, s69, 0\n"
    "s_waitcnt lgkmcnt(9)\n"
    "v_mfma_f32_32x32x16_bf16 v[150:165], v[142:145], a[80:83], v[150:165]\n"
    "s_waitcnt lgkmcnt(6)\n"
    "s_barrier\n"
    "v_add_u32 v184, 28672, v184\n"
    "v_add_u32 v175, 28672, v175\n"
    "ds_read_b128 a[96:99], v175 offset:0\n"
    "ds_read_b64_tr_b16 v[134:135], v184 offset:0\n"
    "ds_read_b64_tr_b16 v[136:137], v184 offset:768\n"
    "s_lshl_b32 s85, s96, 2\n"
    "s_add_u32 s85, s85, s97\n"
    "v_mfma_f32_32x32x16_bf16 v[64:79], v[146:149], a[88:91], v[64:79]\n"
    "v_add_u32 v176, 28672, v176\n"
    "ds_read_b128 a[104:107], v176 offset:0\n"
    "ds_read_b64_tr_b16 v[138:139], v184 offset:64\n"
    "ds_read_b64_tr_b16 v[140:141], v184 offset:832\n"
    "s_add_u32 m0, s64, 28672\n"
    "s_mov_b32 s65, 768\n"
    "buffer_load_dwordx4 v174, s[36:39], s65 offen offset:0 lds\n"
    "s_mul_i32 s85, s85, 81920\n"
    "s_waitcnt lgkmcnt(10)\n"
    "v_mfma_f32_32x32x16_bf16 v[80:95], v[166:169], a[88:91], v[80:95]\n"
    "ds_read_b128 a[112:115], v175 offset:512\n"
    "ds_read_b64_tr_b16 v[142:143], v184 offset:128\n"
    "ds_read_b64_tr_b16 v[144:145], v184 offset:896\n"
    "s_add_u32 m0, s64, 32768\n"
    "s_mov_b32 s65, 37632\n"
    "buffer_load_dwordx4 v174, s[36:39], s65 offen offset:0 lds\n"
    "s_and_b32 s99, s67, 7\n"
    "s_lshl_b32 s99, s99, 2\n"
    "s_add_u32 s99, s99, s69\n"
    "s_lshl_b32 s99, s99, 2\n"
    "s_add_u32 s99, s99, s66\n"
    "s_lshl_b32 s99, s99, 1\n"
    "s_lshr_b32 s65, s67, 3\n"
    "s_add_u32 s99, s99, s65\n"
    "s_mul_i32 s99, s99, 41\n"
    "s_add_u32 s99, s99, 0\n"
    "v_min_u32 v132, 40, v124\n"
    "v_add_u32 v132, s99, v132\n"
    "v_mul_u32_u24 v133, 3641, v132\n"
    "v_lshrrev_b32 v133, 16, v133\n"
    "v_lshlrev_b32 v132, 7, v132\n"
    "v_mul_u32_u24 v133, 6912, v133\n"
    "v_add_u32 v132, v132, v133\n"
    "buffer_load_dword v234, v132, s[60:63], 0 offen\n"
    "s_lshl_b32 s72, s66, 12\n"
    "s_add_u32 s72, s72, s85\n"
    "s_waitcnt lgkmcnt(11)\n"
    "v_mfma_f32_32x32x16_bf16 v[150:165], v[170:173], a[88:91], v[150:165]\n"
    "ds_read_b128 a[120:123], v176 offset:512\n"
    "ds_read_b64_tr_b16 v[146:147], v184 offset:3072\n"
    "ds_read_b64_tr_b16 v[148:149], v184 offset:3840\n"
    "s_add_u32 s72, s72, 1048576\n"
    "s_lshr_b32 s85, s97, 1\n"
    "s_mul_i32 s85, s85, 9\n"
    "s_and_b32 s97, s97, 1\n"
    "s_lshl_b32 s97, s97, 2\n"
    "s_add_u32 s85, s85, s97\n"
    "s_mul_i32 s85, s85, 73728\n"
    "s_add_u32 s81, s85, 1024\n"
    "s_add_u32 s76, s85, s91\n"
    "s_waitcnt lgkmcnt(9)\n"
    "v_mfma_f32_32x32x16_bf16 v[64:79], v[134:137], a[96:99], v[64:79]\n"
    "v_add_u32 v177, 28672, v177\n"
    "ds_read_b128 a[100:103], v177 offset:0\n"
    "ds_read_b64_tr_b16 v[166:167], v184 offset:3136\n"
    "ds_read_b64_tr_b16 v[168:169], v184 offset:3904\n"
    "s_add_u32 m0, s64, 36864\n"
    "s_mov_b32 s65, 74496\n"
    "buffer_load_dwordx4 v174, s[36:39], s65 offen offset:0 lds\n"
    "s_add_u32 s24, s85, s92\n"
    "s_xor_b32 s97, s69, 1\n"
    "s_waitcnt lgkmcnt(9)\n"
    "v_mfma_f32_32x32x16_bf16 v[80:95], v[138:141], a[96:99], v[80:95]\n"
    "v_add_u32 v178, 28672, v178\n"
    "ds_read_b128 a[108:111], v178 offset:0\n"
    "ds_read_b64_tr_b16 v[170:171], v184 offset:3200\n"
    "ds_read_b64_tr_b16 v[172:173], v184 offset:3968\n"
    "s_add_u32 m0, s64, 40960\n"
    "s_mov_b32 s65, 111360\n"
    "buffer_load_dwordx4 v174, s[36:39], s65 offen offset:0 lds\n"
    "s_lshl_b32 s85, s96, 2\n"
    "s_waitcnt lgkmcnt(9)\n"
    "v_mfma_f32_32x32x16_bf16 v[150:165], v[142:145], a[96:99], v[150:165]\n"
    "ds_read_b128 a[116:119], v177 offset:512\n"
    "ds_read_b64_tr_b16 v[134:135], v184 offset:6144\n"
    "ds_read_b64_tr_b16 v[136:137], v184 offset:6912\n"
    "s_add_u32 s85, s85, s97\n"
    "s_mul_i32 s85, s85, 81920\n"
    "s_lshl_b32 s73, s66, 12\n"
    "s_add_u32 s73, s73, s85\n"
    "s_add_u32 s73, s73, 1048576\n"
    "s_lshr_b32 s85, s97, 1\n"
    "s_mul_i32 s85, s85, 9\n"
    "s_and_b32 s97, s97, 1\n"
    "s_lshl_b32 s97, s97, 2\n"
    "s_add_u32 s85, s85, s97\n"
    "s_waitcnt lgkmcnt(9)\n"
    "v_mfma_f32_32x32x16_bf16 v[64:79], v[146:149], a[104:107], v[64:79]\n"
    "ds_read_b128 a[124:127], v178 offset:512\n"
    "ds_read_b64_tr_b16 v[138:139], v184 offset:6208\n"
    "ds_read_b64_tr_b16 v[140:141], v184 offset:6976\n"
    "s_add_u32 m0, s93, 45056\n"
    "s_mov_b32 s65, 1327104\n"
    "buffer_load_dwordx4 v179, s[40:43], s65 offen offset:0 lds\n"
    "s_mul_i32 s85, s85, 73728\n"
    "s_waitcnt lgkmcnt(9)\n"
    "v_mfma_f32_32x32x16_bf16 v[80:95], v[166:169], a[104:107], v[80:95]\n"
    "ds_read_b64_tr_b16 v[142:143], v184 offset:6272\n"
    "ds_read_b64_tr_b16 v[144:145], v184 offset:7040\n"
    "s_add_u32 m0, s93, 46080\n"
    "s_mov_b32 s65, 1327104\n"
    "buffer_load_dwordx4 v180, s[40:43], s65 offen offset:0 lds\n"
    "s_add_u32 s82, s85, 1024\n"
    "s_add_u32 s77, s85, s91\n"
    "s_waitcnt lgkmcnt(8)\n"
    "v_mfma_f32_32x32x16_bf16 v[150:165], v[170:173], a[104:107], v[150:165]\n"
    "ds_read_b64_tr_b16 v[146:147], v184 offset:9216\n"
    "ds_read_b64_tr_b16 v[148:149], v184 offset:9984\n"
    "v_add_u32 v185, 28672, v185\n"
    "s_waitcnt vmcnt(16)\n"
    "ds_write_b32 v185, v190 offset:0\n"
    "s_add_u32 s25, s85, s92\n"
    "s_waitcnt lgkmcnt(8)\n"
    "v_mfma_f32_32x32x16_bf16 v[64:79], v[134:137], a[112:115], v[64:79]\n"
    "ds_read_b64_tr_b16 v[166:167], v184 offset:9280\n"
    "ds_read_b64_tr_b16 v[168:169], v184 offset:10048\n"
    "s_add_u32 m0, s93, 47104\n"
    "s_mov_b32 s65, 1327104\n"
    "buffer_load_dwordx4 v181, s[40:43], s65 offen offset:0 lds\n"
    "s_xor_b32 s97, s69, 2\n"
    "s_lshl_b32 s85, s96, 2\n"
    "s_waitcnt lgkmcnt(7)\n"
    "v_mfma_f32_32x32x16_bf16 v[80:95], v[138:141], a[112:115], v[80:95]\n"
    "ds_read_b64_tr_b16 v[170:171], v184 offset:9344\n"
    "ds_read_b64_tr_b16 v[172:173], v184 offset:10112\n"
    "s_add_u32 s85, s85, s97\n"
    "s_mul_i32 s85, s85, 81920\n"
    "s_lshl_b32 s74, s66, 12\n"
    "s_add_u32 s74, s74, s85\n"
    "s_add_u32 s74, s74, 1048576\n"
    "s_lshr_b32 s85, s97, 1\n"
    "s_mul_i32 s85, s85, 9\n"
    "s_and_b32 s97, s97, 1\n"
    "s_lshl_b32 s97, s97, 2\n"
    "s_add_u32 s85, s85, s97\n"
    "s_mul_i32 s85, s85, 73728\n"
    "s_add_u32 s78, s85, s91\n"
    "s_add_u32 s26, s85, s92\n"
    "s_xor_b32 s97, s69, 3\n"
    "s_waitcnt lgkmcnt(7)\n"
    "v_mfma_f32_32x32x16_bf16 v[150:165], v[142:145], a[112:115], v[150:165]\n"
    "s_waitcnt lgkmcnt(4)\n"
    "s_barrier\n"
    "v_add_u32 v184, 28672, v184\n"
    "v_add_u32 v175, 28672, v175\n"
    "ds_read_b128 a[128:131], v175 offset:0\n"
    "ds_read_b64_tr_b16 v[134:135], v184 offset:0\n"
    "ds_read_b64_tr_b16 v[136:137], v184 offset:768\n"
    "s_lshl_b32 s85, s96, 2\n"
    "v_mfma_f32_32x32x16_bf16 v[64:79], v[146:149], a[120:123], v[64:79]\n"
    "v_add_u32 v176, 28672, v176\n"
    "ds_read_b128 a[136:139], v176 offset:0\n"
    "ds_read_b64_tr_b16 v[138:139], v184 offset:64\n"
    "ds_read_b64_tr_b16 v[140:141], v184 offset:832\n"
    "s_add_u32 m0, s64, 57344\n"
    "s_mov_b32 s65, 896\n"
    "buffer_load_dwordx4 v174, s[36:39], s65 offen offset:0 lds\n"
    "s_add_u32 s85, s85, s97\n"
    "s_mul_i32 s85, s85, 81920\n"
    "s_waitcnt lgkmcnt(8)\n"
    "v_mfma_f32_32x32x16_bf16 v[80:95], v[166:169], a[120:123], v[80:95]\n"
    "ds_read_b128 a[144:147], v175 offset:512\n"
    "ds_read_b64_tr_b16 v[142:143], v184 offset:128\n"
    "ds_read_b64_tr_b16 v[144:145], v184 offset:896\n"
    "s_add_u32 m0, s64, 61440\n"
    "s_mov_b32 s65, 37760\n"
    "buffer_load_dwordx4 v174, s[36:39], s65 offen offset:0 lds\n"
    "s_lshl_b32 s75, s66, 12\n"
    "s_waitcnt lgkmcnt(9)\n"
    "v_mfma_f32_32x32x16_bf16 v[150:165], v[170:173], a[120:123], v[150:165]\n"
    "ds_read_b128 a[152:155], v176 offset:512\n"
    "ds_read_b64_tr_b16 v[146:147], v184 offset:3072\n"
    "ds_read_b64_tr_b16 v[148:149], v184 offset:3840\n"
    "s_add_u32 s75, s75, s85\n"
    "s_add_u32 s75, s75, 1048576\n"
    "s_lshr_b32 s85, s97, 1\n"
    "s_mul_i32 s85, s85, 9\n"
    "s_and_b32 s97, s97, 1\n"
    "s_lshl_b32 s97, s97, 2\n"
    "s_add_u32 s85, s85, s97\n"
    "s_mul_i32 s85, s85, 73728\n"
    "s_add_u32 s79, s85, s91\n"
    "s_add_u32 s27, s85, s92\n"
    "s_waitcnt lgkmcnt(9)\n"
    "v_mfma_f32_32x32x16_bf16 v[64:79], v[134:137], a[128:131], v[64:79]\n"
    "v_add_u32 v177, 28672, v177\n"
    "ds_read_b128 a[132:135], v177 offset:0\n"
    "ds_read_b64_tr_b16 v[166:167], v184 offset:3136\n"
    "ds_read_b64_tr_b16 v[168:169], v184 offset:3904\n"
    "s_add_u32 m0, s64, 65536\n"
    "s_mov_b32 s65, 74624\n"
    "buffer_load_dwordx4 v174, s[36:39], s65 offen offset:0 lds\n"
    "s_mul_i32 s97, s68, 165888\n"
    "s_waitcnt lgkmcnt(9)\n"
    "v_mfma_f32_32x32x16_bf16 v[80:95], v[138:141], a[128:131], v[80:95]\n"
    "v_add_u32 v178, 28672, v178\n"
    "ds_read_b128 a[140:143], v178 offset:0\n"
    "ds_read_b64_tr_b16 v[170:171], v184 offset:3200\n"
    "ds_read_b64_tr_b16 v[172:173], v184 offset:3968\n"
    "s_add_u32 m0, s64, 69632\n"
    "s_mov_b32 s65, 111488\n"
    "buffer_load_dwordx4 v174, s[36:39], s65 offen offset:0 lds\n"
    "s_add_u32 s83, s97, s91\n"
    "s_add_u32 s28, s97, s92\n"
    "s_waitcnt lgkmcnt(9)\n"
    "v_mfma_f32_32x32x16_bf16 v[150:165], v[142:145], a[128:131], v[150:165]\n"
    "ds_read_b128 a[148:151], v177 offset:512\n"
    "ds_read_b64_tr_b16 v[134:135], v184 offset:6144\n"
    "ds_read_b64_tr_b16 v[136:137], v184 offset:6912\n"
    "s_add_u32 s84, s97, 1024\n"
    "s_lshl_b32 s88, s67, 3\n"
    "s_add_u32 s97, s88, s71\n"
    "s_lshl_b32 s97, s97, 2\n"
    "s_add_u32 s97, s97, s66\n"
    "s_mul_i32 s97, s97, 6144\n"
    "s_add_u32 s85, s97, 22020096\n"
    "s_lshl_b32 s97, s68, 1\n"
    "s_add_u32 s88, s88, s97\n"
    "s_waitcnt lgkmcnt(9)\n"
    "v_mfma_f32_32x32x16_bf16 v[64:79], v[146:149], a[136:139], v[64:79]\n"
    "ds_read_b128 a[156:159], v178 offset:512\n"
    "ds_read_b64_tr_b16 v[138:139], v184 offset:6208\n"
    "ds_read_b64_tr_b16 v[140:141], v184 offset:6976\n"
    "s_add_u32 m0, s93, 73728\n"
    "s_mov_b32 s65, 1548288\n"
    "buffer_load_dwordx4 v179, s[40:43], s65 offen offset:0 lds\n"
    "s_and_b32 s97, s66, 1\n"
    "s_add_u32 s88, s88, s97\n"
    "s_waitcnt lgkmcnt(9)\n"
    "v_mfma_f32_32x32x16_bf16 v[80:95], v[166:169], a[136:139], v[80:95]\n"
    "ds_read_b64_tr_b16 v[142:143], v184 offset:6272\n"
    "ds_read_b64_tr_b16 v[144:145], v184 offset:7040\n"
    "s_add_u32 m0, s93, 74752\n"
    "s_mov_b32 s65, 1548288\n"
    "buffer_load_dwordx4 v180, s[40:43], s65 offen offset:0 lds\n"
    "s_lshl_b32 s88, s88, 2\n"
    "s_waitcnt lgkmcnt(8)\n"
    "v_mfma_f32_32x32x16_bf16 v[150:165], v[170:173], a[136:139], v[150:165]\n"
    "ds_read_b64_tr_b16 v[146:147], v184 offset:9216\n"
    "ds_read_b64_tr_b16 v[148:149], v184 offset:9984\n"
    "v_add_u32 v185, 4294852608, v185\n"
    "s_waitcnt vmcnt(14)\n"
    "ds_write_b32 v185, v191 offset:0\n"
    "s_xor_b32 s97, s66, 0\n"
    "s_add_u32 s97, s97, s88\n"
    "s_waitcnt lgkmcnt(8)\n"
    "v_mfma_f32_32x32x16_bf16 v[64:79], v[134:137], a[144:147], v[64:79]\n"
    "ds_read_b64_tr_b16 v[166:167], v184 offset:9280\n"
    "ds_read_b64_tr_b16 v[168:169], v184 offset:10048\n"
    "s_add_u32 m0, s93, 75776\n"
    "s_mov_b32 s65, 1548288\n"
    "buffer_load_dwordx4 v181, s[40:43], s65 offen offset:0 lds\n"
    "s_mul_i32 s97, s97, 6144\n"
    "s_waitcnt lgkmcnt(7)\n"
    "v_mfma_f32_32x32x16_bf16 v[80:95], v[138:141], a[144:147], v[80:95]\n"
    "ds_read_b64_tr_b16 v[170:171], v184 offset:9344\n"
    "ds_read_b64_tr_b16 v[172:173], v184 offset:10112\n"
    "s_add_u32 s86, s97, 22020096\n"
    "s_xor_b32 s97, s66, 1\n"
    "s_add_u32 s97, s97, s88\n"
    "s_mul_i32 s97, s97, 6144\n"
    "s_add_u32 s87, s97, 22020096\n"
    "s_lshl_b32 s88, s71, 19\n"
    "s_add_u32 s88, s88, 25165824\n"
    "s_lshl_b32 s96, s67, 2\n"
    "s_add_u32 s97, s96, s69\n"
    "s_lshl_b32 s97, s97, 2\n"
    "s_add_u32 s97, s97, s68\n"
    "s_lshl_b32 s97, s97, 2\n"
    "s_add_u32 s97, s97, s66\n"
    "s_lshl_b32 s97, s97, 13\n"
    "s_waitcnt lgkmcnt(7)\n"
    "v_mfma_f32_32x32x16_bf16 v[150:165], v[142:145], a[144:147], v[150:165]\n"
    "s_waitcnt lgkmcnt(4)\n"
    "s_barrier\n"
    "v_add_u32 v184, 4294852608, v184\n"
    "v_add_u32 v175, 4294852608, v175\n"
    "ds_read_b128 a[160:163], v175 offset:0\n"
    "ds_read_b64_tr_b16 v[134:135], v184 offset:0\n"
    "ds_read_b64_tr_b16 v[136:137], v184 offset:768\n"
    "s_add_u32 s91, s97, 29360128\n"
    "v_mfma_f32_32x32x16_bf16 v[64:79], v[146:149], a[152:155], v[64:79]\n"
    "v_add_u32 v176, 4294852608, v176\n"
    "ds_read_b128 a[168:171], v176 offset:0\n"
    "ds_read_b64_tr_b16 v[138:139], v184 offset:64\n"
    "ds_read_b64_tr_b16 v[140:141], v184 offset:832\n"
    "s_add_u32 m0, s64, 86016\n"
    "s_mov_b32 s65, 1024\n"
    "buffer_load_dwordx4 v174, s[36:39], s65 offen offset:0 lds\n"
    "s_add_u32 s96, s96, s66\n"
    "s_lshl_b32 s96, s96, 2\n"
    "s_waitcnt lgkmcnt(8)\n"
    "v_mfma_f32_32x32x16_bf16 v[80:95], v[166:169], a[152:155], v[80:95]\n"
    "ds_read_b128 a[176:179], v175 offset:512\n"
    "ds_read_b64_tr_b16 v[142:143], v184 offset:128\n"
    "ds_read_b64_tr_b16 v[144:145], v184 offset:896\n"
    "s_add_u32 m0, s64, 90112\n"
    "s_mov_b32 s65, 37888\n"
    "buffer_load_dwordx4 v174, s[36:39], s65 offen offset:0 lds\n"
    "s_add_u32 s96, s96, s68\n"
    "s_waitcnt lgkmcnt(9)\n"
    "v_mfma_f32_32x32x16_bf16 v[150:165], v[170:173], a[152:155], v[150:165]\n"
    "ds_read_b128 a[184:187], v176 offset:512\n"
    "ds_read_b64_tr_b16 v[146:147], v184 offset:3072\n"
    "ds_read_b64_tr_b16 v[148:149], v184 offset:3840\n"
    "s_add_u32 m0, s64, 94208\n"
    "s_mov_b32 s65, 74752\n"
    "buffer_load_dwordx4 v174, s[36:39], s65 offen offset:0 lds\n"
    "s_lshl_b32 s96, s96, 1\n"
    "s_lshr_b32 s97, s69, 1\n"
    "s_waitcnt lgkmcnt(9)\n"
    "v_mfma_f32_32x32x16_bf16 v[64:79], v[134:137], a[160:163], v[64:79]\n"
    "v_add_u32 v177, 4294852608, v177\n"
    "ds_read_b128 a[164:167], v177 offset:0\n"
    "ds_read_b64_tr_b16 v[166:167], v184 offset:3136\n"
    "ds_read_b64_tr_b16 v[168:169], v184 offset:3904\n"
    "s_add_u32 s96, s96, s97\n"
    "s_lshl_b32 s96, s96, 12\n"
    "s_add_u32 s92, s96, 37748736\n"
    "v_mov_b32 v206, 0\n"
    "v_mov_b32 v207, 0\n"
    "s_waitcnt lgkmcnt(9)\n"
    "v_mfma_f32_32x32x16_bf16 v[80:95], v[138:141], a[160:163], v[80:95]\n"
    "v_add_u32 v178, 4294852608, v178\n"
    "ds_read_b128 a[172:175], v178 offset:0\n"
    "ds_read_b64_tr_b16 v[170:171], v184 offset:3200\n"
    "ds_read_b64_tr_b16 v[172:173], v184 offset:3968\n"
    "s_add_u32 m0, s64, 98304\n"
    "s_mov_b32 s65, 111616\n"
    "buffer_load_dwordx4 v174, s[36:39], s65 offen offset:0 lds\n"
    "v_mov_b32 v208, 0\n"
    "v_mov_b32 v209, 0\n"
    "s_waitcnt lgkmcnt(9)\n"
    "v_mfma_f32_32x32x16_bf16 v[150:165], v[142:145], a[160:163], v[150:165]\n"
    "ds_read_b128 a[180:183], v177 offset:512\n"
    "ds_read_b64_tr_b16 v[134:135], v184 offset:6144\n"
    "ds_read_b64_tr_b16 v[136:137], v184 offset:6912\n"
    "s_add_u32 m0, s93, 102400\n"
    "s_mov_b32 s65, 1769472\n"
    "buffer_load_dwordx4 v179, s[40:43], s65 offen offset:0 lds\n"
    "v_add_u32 v127, s64, v241\n"
    "s_waitcnt lgkmcnt(9)\n"
    "v_mfma_f32_32x32x16_bf16 v[64:79], v[146:149], a[168:171], v[64:79]\n"
    "ds_read_b128 a[188:191], v178 offset:512\n"
    "ds_read_b64_tr_b16 v[138:139], v184 offset:6208\n"
    "ds_read_b64_tr_b16 v[140:141], v184 offset:6976\n"
    "s_add_u32 m0, s93, 103424\n"
    "s_mov_b32 s65, 1769472\n"
    "buffer_load_dwordx4 v180, s[40:43], s65 offen offset:0 lds\n"
    "v_add_u32 v127, 150016, v127\n"
    "s_waitcnt lgkmcnt(9)\n"
    "v_mfma_f32_32x32x16_bf16 v[80:95], v[166:169], a[168:171], v[80:95]\n"
    "ds_read_b64_tr_b16 v[142:143], v184 offset:6272\n"
    "ds_read_b64_tr_b16 v[144:145], v184 offset:7040\n"
    "ds_write_b128 v127, v[206:209] offset:0\n"
    "ds_write_b128 v127, v[206:209] offset:4096\n"
    "ds_write_b128 v127, v[206:209] offset:8192\n"
    "ds_write_b128 v127, v[206:209] offset:9344\n"
    "s_waitcnt lgkmcnt(12)\n"
    "v_mfma_f32_32x32x16_bf16 v[150:165], v[170:173], a[168:171], v[150:165]\n"
    "ds_read_b64_tr_b16 v[146:147], v184 offset:9216\n"
    "ds_read_b64_tr_b16 v[148:149], v184 offset:9984\n"
    "s_add_u32 m0, s93, 104448\n"
    "s_mov_b32 s65, 1769472\n"
    "buffer_load_dwordx4 v181, s[40:43], s65 offen offset:0 lds\n"
    "v_add_u32 v185, 28672, v185\n"
    "s_waitcnt vmcnt(14)\n"
    "ds_write_b32 v185, v192 offset:0\n"
    "s_lshl_b32 s96, s66, 2\n"
    "s_lshr_b32 s96, 0xd940, s96\n"
    "s_waitcnt lgkmcnt(12)\n"
    "v_mfma_f32_32x32x16_bf16 v[64:79], v[134:137], a[176:179], v[64:79]\n"
    "ds_read_b64_tr_b16 v[166:167], v184 offset:9280\n"
    "ds_read_b64_tr_b16 v[168:169], v184 offset:10048\n"
    "s_add_u32 m0, s64, 143360\n"
    "s_mov_b32 s65, 0\n"
    "buffer_load_dwordx4 v239, s[44:47], s65 offen offset:0 lds\n"
    "s_and_b32 s96, s96, 15\n"
    "s_waitcnt lgkmcnt(11)\n"
    "v_mfma_f32_32x32x16_bf16 v[80:95], v[138:141], a[176:179], v[80:95]\n"
    "ds_read_b64_tr_b16 v[170:171], v184 offset:9344\n"
    "ds_read_b64_tr_b16 v[172:173], v184 offset:10112\n"
    "s_mul_i32 s97, s66, 18\n"
    "s_add_u32 s90, s66, 72\n"
    "v_add_u32 v126, 0, v124\n"
    "v_mul_u32_u24 v125, 3450, v126\n"
    "v_lshrrev_b32 v125, 16, v125\n"
    "v_cmp_gt_u32_e64 s[16:17], 5, v125\n"
    "v_mul_u32_u24 v127, 19, v125\n"
    "v_sub_u32 v126, v126, v127\n"
    "v_add_u32 v125, s96, v125\n"
    "v_add_u32 v127, s97, v126\n"
    "v_lshrrev_b32 v128, 5, v127\n"
    "v_and_b32 v129, 31, v127\n"
    "v_cmp_eq_u32 vcc, 2, v128\n"
    "v_add_u32 v130, 8, v129\n"
    "s_waitcnt lgkmcnt(11)\n"
    "v_mfma_f32_32x32x16_bf16 v[150:165], v[142:145], a[176:179], v[150:165]\n"
    "s_waitcnt lgkmcnt(4)\n"
    "s_barrier\n"
    "v_add_u32 v184, 28672, v184\n"
    "v_add_u32 v175, 28672, v175\n"
    "ds_read_b128 a[192:195], v175 offset:0\n"
    "ds_read_b64_tr_b16 v[134:135], v184 offset:0\n"
    "ds_read_b64_tr_b16 v[136:137], v184 offset:768\n"
    "v_cndmask_b32_e64 v129, v129, v130, vcc\n"
    "v_cmp_eq_u32 vcc, 18, v126\n"
    "v_mfma_f32_32x32x16_bf16 v[64:79], v[146:149], a[184:187], v[64:79]\n"
    "v_add_u32 v176, 28672, v176\n"
    "ds_read_b128 a[200:203], v176 offset:0\n"
    "ds_read_b64_tr_b16 v[138:139], v184 offset:64\n"
    "ds_read_b64_tr_b16 v[140:141], v184 offset:832\n"
    "s_add_u32 m0, s64, 114688\n"
    "s_mov_b32 s65, 294912\n"
    "buffer_load_dwordx4 v239, s[44:47], s65 offen offset:0 lds\n"
    "v_mov_b32 v130, s90\n"
    "s_waitcnt lgkmcnt(8)\n"
    "v_mfma_f32_32x32x16_bf16 v[80:95], v[166:169], a[184:187], v[80:95]\n"
    "ds_read_b128 a[208:211], v175 offset:512\n"
    "ds_read_b64_tr_b16 v[142:143], v184 offset:128\n"
    "ds_read_b64_tr_b16 v[144:145], v184 offset:896\n"
    "s_add_u32 m0, s64, 118784\n"
    "s_mov_b32 s65, 589824\n"
    "buffer_load_dwordx4 v239, s[44:47], s65 offen offset:0 lds\n"
    "v_cndmask_b32_e64 v129, v129, 16, vcc\n"
    "v_cndmask_b32_e64 v128, v128, 2, vcc\n"
    "s_waitcnt lgkmcnt(9)\n"
    "v_mfma_f32_32x32x16_bf16 v[150:165], v[170:173], a[184:187], v[150:165]\n"
    "ds_read_b128 a[216:219], v176 offset:512\n"
    "ds_read_b64_tr_b16 v[146:147], v184 offset:3072\n"
    "ds_read_b64_tr_b16 v[148:149], v184 offset:3840\n"
    "v_cndmask_b32_e64 v127, v127, v130, vcc\n"
    "v_mul_u32_u24 v127, 168, v127\n"
    "v_lshl_add_u32 v127, v125, 3, v127\n"
    "v_add_u32 v127, 150016, v127\n"
    "v_mov_b32 v130, 163456\n"
    "v_cndmask_b32_e64 v200, v130, v127, s[16:17]\n"
    "s_and_b64 vcc, vcc, s[16:17]\n"
    "v_add_u32 v127, 672, v127\n"
    "v_cndmask_b32_e64 v202, v130, v127, vcc\n"
    "s_waitcnt lgkmcnt(9)\n"
    "v_mfma_f32_32x32x16_bf16 v[64:79], v[134:137], a[192:195], v[64:79]\n"
    "v_add_u32 v177, 28672, v177\n"
    "ds_read_b128 a[196:199], v177 offset:0\n"
    "ds_read_b64_tr_b16 v[166:167], v184 offset:3136\n"
    "ds_read_b64_tr_b16 v[168:169], v184 offset:3904\n"
    "s_add_u32 m0, s64, 122880\n"
    "s_mov_b32 s65, 884736\n"
    "buffer_load_dwordx4 v239, s[44:47], s65 offen offset:0 lds\n"
    "v_lshlrev_b32 v127, 2, v125\n"
    "v_subrev_u32 v204, s97, v127\n"
    "s_waitcnt lgkmcnt(9)\n"
    "v_mfma_f32_32x32x16_bf16 v[80:95], v[138:141], a[192:195], v[80:95]\n"
    "v_add_u32 v178, 28672, v178\n"
    "ds_read_b128 a[204:207], v178 offset:0\n"
    "ds_read_b64_tr_b16 v[170:171], v184 offset:3200\n"
    "ds_read_b64_tr_b16 v[172:173], v184 offset:3968\n"
    "s_add_u32 m0, s64, 126976\n"
    "s_mov_b32 s65, 1179648\n"
    "buffer_load_dwordx4 v239, s[44:47], s65 offen offset:0 lds\n"
    "v_lshrrev_b32 v127, 3, v125\n"
    "s_waitcnt lgkmcnt(9)\n"
    "v_mfma_f32_32x32x16_bf16 v[150:165], v[142:145], a[192:195], v[150:165]\n"
    "ds_read_b128 a[212:215], v177 offset:512\n"
    "ds_read_b64_tr_b16 v[134:135], v184 offset:6144\n"
    "ds_read_b64_tr_b16 v[136:137], v184 offset:6912\n"
    "v_lshl_add_u32 v130, v127, 1, v128\n"
    "v_add_u32 v126, 4, v127\n"
    "v_cmp_eq_u32 vcc, 2, v128\n"
    "s_nop 1\n"
    "v_cndmask_b32_e64 v130, v130, v126, vcc\n"
    "v_cmp_eq_u32 vcc, 2, v127\n"
    "v_add_u32 v126, 5, v128\n"
    "s_nop 0\n"
    "v_cndmask_b32_e64 v130, v130, v126, vcc\n"
    "v_lshlrev_b32 v130, 12, v130\n"
    "v_bfe_u32 v126, v125, 1, 2\n"
    "v_lshl_add_u32 v130, v126, 10, v130\n"
    "s_waitcnt lgkmcnt(9)\n"
    "v_mfma_f32_32x32x16_bf16 v[64:79], v[146:149], a[200:203], v[64:79]\n"
    "ds_read_b128 a[220:223], v178 offset:512\n"
    "ds_read_b64_tr_b16 v[138:139], v184 offset:6208\n"
    "ds_read_b64_tr_b16 v[140:141], v184 offset:6976\n"
    "s_add_u32 m0, s64, 131072\n"
    "s_mov_b32 s65, 1474560\n"
    "buffer_load_dwordx4 v239, s[44:47], s65 offen offset:0 lds\n"
    "v_and_b32 v126, 1, v125\n"
    "s_waitcnt lgkmcnt(9)\n"
    "v_mfma_f32_32x32x16_bf16 v[80:95], v[166:169], a[200:203], v[80:95]\n"
    "ds_read_b64_tr_b16 v[142:143], v184 offset:6272\n"
    "ds_read_b64_tr_b16 v[144:145], v184 offset:7040\n"
    "s_add_u32 m0, s64, 135168\n"
    "s_mov_b32 s65, 1769472\n"
    "buffer_load_dwordx4 v239, s[44:47], s65 offen offset:0 lds\n"
    "v_lshl_add_u32 v129, v126, 5, v129\n"
    "v_lshl_add_u32 v130, v129, 4, v130\n"
    "s_waitcnt lgkmcnt(8)\n"
    "v_mfma_f32_32x32x16_bf16 v[150:165], v[170:173], a[200:203], v[150:165]\n"
    "ds_read_b64_tr_b16 v[146:147], v184 offset:9216\n"
    "ds_read_b64_tr_b16 v[148:149], v184 offset:9984\n"
    "v_add_u32 v185, 28672, v185\n"
    "s_waitcnt vmcnt(14)\n"
    "ds_write_b32 v185, v193 offset:0\n"
    "v_mov_b32 v126, 268435456\n"
    "s_waitcnt lgkmcnt(8)\n"
    "v_mfma_f32_32x32x16_bf16 v[64:79], v[134:137], a[208:211], v[64:79]\n"
    "ds_read_b64_tr_b16 v[166:167], v184 offset:9280\n"
    "ds_read_b64_tr_b16 v[168:169], v184 offset:10048\n"
    "s_add_u32 m0, s64, 139264\n"
    "s_mov_b32 s65, 2064384\n"
    "buffer_load_dwordx4 v239, s[44:47], s65 offen offset:0 lds\n"
    "v_cndmask_b32_e64 v198, v126, v130, s[16:17]\n"
    "v_add_u32 v126, 64, v124\n"
    "s_waitcnt lgkmcnt(7)\n"
    "v_mfma_f32_32x32x16_bf16 v[80:95], v[138:141], a[208:211], v[80:95]\n"
    "ds_read_b64_tr_b16 v[170:171], v184 offset:9344\n"
    "ds_read_b64_tr_b16 v[172:173], v184 offset:10112\n"
    "v_mul_u32_u24 v125, 3450, v126\n"
    "v_lshrrev_b32 v125, 16, v125\n"
    "v_cmp_gt_u32_e64 s[16:17], 5, v125\n"
    "v_mul_u32_u24 v127, 19, v125\n"
    "v_sub_u32 v126, v126, v127\n"
    "v_add_u32 v125, s96, v125\n"
    "v_add_u32 v127, s97, v126\n"
    "v_lshrrev_b32 v128, 5, v127\n"
    "v_and_b32 v129, 31, v127\n"
    "v_cmp_eq_u32 vcc, 2, v128\n"
    "v_add_u32 v130, 8, v129\n"
    "s_nop 0\n"
    "v_cndmask_b32_e64 v129, v129, v130, vcc\n"
    "v_cmp_eq_u32 vcc, 18, v126\n"
    "s_waitcnt lgkmcnt(7)\n"
    "v_mfma_f32_32x32x16_bf16 v[150:165], v[142:145], a[208:211], v[150:165]\n"
    "s_waitcnt lgkmcnt(4)\n"
    "s_barrier\n"
    "v_add_u32 v184, 28672, v184\n"
    "v_add_u32 v175, 28672, v175\n"
    "ds_read_b128 a[224:227], v175 offset:0\n"
    "ds_read_b64_tr_b16 v[134:135], v184 offset:0\n"
    "ds_read_b64_tr_b16 v[136:137], v184 offset:768\n"
    "v_mov_b32 v130, s90\n"
    "v_cndmask_b32_e64 v129, v129, 16, vcc\n"
    "v_mfma_f32_32x32x16_bf16 v[64:79], v[146:149], a[216:219], v[64:79]\n"
    "v_add_u32 v176, 28672, v176\n"
    "ds_read_b128 a[232:235], v176 offset:0\n"
    "ds_read_b64_tr_b16 v[138:139], v184 offset:64\n"
    "ds_read_b64_tr_b16 v[140:141], v184 offset:832\n"
    "s_add_u32 m0, s64, 0\n"
    "s_mov_b32 s65, 2359296\n"
    "buffer_load_dwordx4 v239, s[44:47], s65 offen offset:0 lds\n"
    "v_cndmask_b32_e64 v128, v128, 2, vcc\n"
    "s_waitcnt lgkmcnt(8)\n"
    "v_mfma_f32_32x32x16_bf16 v[80:95], v[166:169], a[216:219], v[80:95]\n"
    "ds_read_b128 a[240:243], v175 offset:512\n"
    "ds_read_b64_tr_b16 v[142:143], v184 offset:128\n"
    "ds_read_b64_tr_b16 v[144:145], v184 offset:896\n"
    "s_add_u32 m0, s64, 4096\n"
    "s_mov_b32 s65, 2654208\n"
    "buffer_load_dwordx4 v239, s[44:47], s65 offen offset:0 lds\n"
    "s_and_b32 s99, s67, 7\n"
    "s_lshl_b32 s99, s99, 2\n"
    "s_add_u32 s99, s99, s69\n"
    "s_lshl_b32 s99, s99, 2\n"
    "s_add_u32 s99, s99, s66\n"
    "s_lshl_b32 s99, s99, 1\n"
    "s_lshr_b32 s65, s67, 3\n"
    "s_add_u32 s99, s99, s65\n"
    "s_mul_i32 s99, s99, 41\n"
    "s_add_u32 s99, s99, 0\n"
    "v_min_u32 v132, 40, v124\n"
    "v_add_u32 v132, s99, v132\n"
    "v_lshlrev_b32 v132, 7, v132\n"
    "buffer_load_dword v234, v132, s[48:51], 0 offen\n"
    "v_cndmask_b32_e64 v127, v127, v130, vcc\n"
    "v_mul_u32_u24 v127, 168, v127\n"
    "s_waitcnt lgkmcnt(9)\n"
    "v_mfma_f32_32x32x16_bf16 v[150:165], v[170:173], a[216:219], v[150:165]\n"
    "ds_read_b128 a[248:251], v176 offset:512\n"
    "ds_read_b64_tr_b16 v[146:147], v184 offset:3072\n"
    "ds_read_b64_tr_b16 v[148:149], v184 offset:3840\n"
    "v_lshl_add_u32 v127, v125, 3, v127\n"
    "v_add_u32 v127, 150016, v127\n"
    "v_mov_b32 v130, 163456\n"
    "v_cndmask_b32_e64 v201, v130, v127, s[16:17]\n"
    "s_and_b64 vcc, vcc, s[16:17]\n"
    "v_add_u32 v127, 672, v127\n"
    "v_cndmask_b32_e64 v203, v130, v127, vcc\n"
    "v_lshlrev_b32 v127, 2, v125\n"
    "v_subrev_u32 v205, s97, v127\n"
    "s_waitcnt lgkmcnt(9)\n"
    "v_mfma_f32_32x32x16_bf16 v[64:79], v[134:137], a[224:227], v[64:79]\n"
    "v_add_u32 v177, 28672, v177\n"
    "ds_read_b128 a[228:231], v177 offset:0\n"
    "ds_read_b64_tr_b16 v[166:167], v184 offset:3136\n"
    "ds_read_b64_tr_b16 v[168:169], v184 offset:3904\n"
    "s_add_u32 m0, s64, 8192\n"
    "s_mov_b32 s65, 2949120\n"
    "buffer_load_dwordx4 v239, s[44:47], s65 offen offset:0 lds\n"
    "v_lshrrev_b32 v127, 3, v125\n"
    "v_lshl_add_u32 v130, v127, 1, v128\n"
    "s_waitcnt lgkmcnt(9)\n"
    "v_mfma_f32_32x32x16_bf16 v[80:95], v[138:141], a[224:227], v[80:95]\n"
    "v_add_u32 v178, 28672, v178\n"
    "ds_read_b128 a[236:239], v178 offset:0\n"
    "ds_read_b64_tr_b16 v[170:171], v184 offset:3200\n"
    "ds_read_b64_tr_b16 v[172:173], v184 offset:3968\n"
    "s_add_u32 m0, s64, 12288\n"
    "s_mov_b32 s65, 3244032\n"
    "buffer_load_dwordx4 v239, s[44:47], s65 offen offset:0 lds\n"
    "v_add_u32 v126, 4, v127\n"
    "s_waitcnt lgkmcnt(9)\n"
    "v_mfma_f32_32x32x16_bf16 v[150:165], v[142:145], a[224:227], v[150:165]\n"
    "ds_read_b128 a[244:247], v177 offset:512\n"
    "ds_read_b64_tr_b16 v[134:135], v184 offset:6144\n"
    "ds_read_b64_tr_b16 v[136:137], v184 offset:6912\n"
    "v_cmp_eq_u32 vcc, 2, v128\n"
    "s_nop 1\n"
    "v_cndmask_b32_e64 v130, v130, v126, vcc\n"
    "v_cmp_eq_u32 vcc, 2, v127\n"
    "v_add_u32 v126, 5, v128\n"
    "s_nop 0\n"
    "v_cndmask_b32_e64 v130, v130, v126, vcc\n"
    "v_lshlrev_b32 v130, 12, v130\n"
    "v_bfe_u32 v126, v125, 1, 2\n"
    "v_lshl_add_u32 v130, v126, 10, v130\n"
    "v_and_b32 v126, 1, v125\n"
    "v_lshl_add_u32 v129, v126, 5, v129\n"
    "s_waitcnt lgkmcnt(9)\n"
    "v_mfma_f32_32x32x16_bf16 v[64:79], v[146:149], a[232:235], v[64:79]\n"
    "ds_read_b128 a[252:255], v178 offset:512\n"
    "ds_read_b64_tr_b16 v[138:139], v184 offset:6208\n"
    "ds_read_b64_tr_b16 v[140:141], v184 offset:6976\n"
    "s_add_u32 m0, s64, 16384\n"
    "s_mov_b32 s65, 3538944\n"
    "buffer_load_dwordx4 v239, s[44:47], s65 offen offset:0 lds\n"
    "v_lshl_add_u32 v130, v129, 4, v130\n"
    "s_waitcnt lgkmcnt(9)\n"
    "v_mfma_f32_32x32x16_bf16 v[80:95], v[166:169], a[232:235], v[80:95]\n"
    "ds_read_b64_tr_b16 v[142:143], v184 offset:6272\n"
    "ds_read_b64_tr_b16 v[144:145], v184 offset:7040\n"
    "s_add_u32 m0, s64, 20480\n"
    "s_mov_b32 s65, 3833856\n"
    "buffer_load_dwordx4 v239, s[44:47], s65 offen offset:0 lds\n"
    "s_and_b32 s99, s67, 7\n"
    "s_lshl_b32 s99, s99, 2\n"
    "s_add_u32 s99, s99, s69\n"
    "s_lshl_b32 s99, s99, 2\n"
    "s_add_u32 s99, s99, s66\n"
    "s_lshl_b32 s99, s99, 1\n"
    "s_lshr_b32 s65, s67, 3\n"
    "s_add_u32 s99, s99, s65\n"
    "s_mul_i32 s99, s99, 6\n"
    "v_min_u32 v132, 5, v124\n"
    "v_add_u32 v132, s99, v132\n"
    "v_lshlrev_b32 v132, 7, v132\n"
    "s_mul_i32 s99, s68, 165888\n"
    "buffer_load_dword v234, v132, s[52:55], s99 offen\n"
    "v_mov_b32 v126, 268435456\n"
    "v_cndmask_b32_e64 v199, v126, v130, s[16:17]\n"
    "s_waitcnt lgkmcnt(8)\n"
    "v_mfma_f32_32x32x16_bf16 v[150:165], v[170:173], a[232:235], v[150:165]\n"
    "ds_read_b64_tr_b16 v[146:147], v184 offset:9216\n"
    "ds_read_b64_tr_b16 v[148:149], v184 offset:9984\n"
    "v_add_u32 v185, 28672, v185\n"
    "s_waitcnt vmcnt(16)\n"
    "ds_write_b32 v185, v194 offset:0\n"
    "s_waitcnt lgkmcnt(8)\n"
    "v_mfma_f32_32x32x16_bf16 v[64:79], v[134:137], a[240:243], v[64:79]\n"
    "ds_read_b64_tr_b16 v[166:167], v184 offset:9280\n"
    "ds_read_b64_tr_b16 v[168:169], v184 offset:10048\n"
    "s_add_u32 m0, s64, 24576\n"
    "s_mov_b32 s65, 4128768\n"
    "buffer_load_dwordx4 v239, s[44:47], s65 offen offset:0 lds\n"
    "s_waitcnt lgkmcnt(7)\n"
    "v_mfma_f32_32x32x16_bf16 v[80:95], v[138:141], a[240:243], v[80:95]\n"
    "ds_read_b64_tr_b16 v[170:171], v184 offset:9344\n"
    "ds_read_b64_tr_b16 v[172:173], v184 offset:10112\n"
    "s_waitcnt lgkmcnt(7)\n"
    "v_mfma_f32_32x32x16_bf16 v[150:165], v[142:145], a[240:243], v[150:165]\n"
    "s_waitcnt lgkmcnt(4)\n"
    "s_barrier\n"
    "v_add_u32 v184, 28672, v184\n"
    "v_add_u32 v175, 28672, v175\n"
    "ds_read_b128 v[0:3], v175 offset:0\n"
    "ds_read_b64_tr_b16 v[134:135], v184 offset:0\n"
    "ds_read_b64_tr_b16 v[136:137], v184 offset:768\n"
    "v_mfma_f32_32x32x16_bf16 v[64:79], v[146:149], a[248:251], v[64:79]\n"
    "v_add_u32 v176, 28672, v176\n"
    "ds_read_b128 v[8:11], v176 offset:0\n"
    "ds_read_b64_tr_b16 v[138:139], v184 offset:64\n"
    "ds_read_b64_tr_b16 v[140:141], v184 offset:832\n"
    "s_add_u32 m0, s64, 28672\n"
    "s_mov_b32 s65, 4423680\n"
    "buffer_load_dwordx4 v239, s[44:47], s65 offen offset:0 lds\n"
    "s_waitcnt lgkmcnt(8)\n"
    "v_mfma_f32_32x32x16_bf16 v[80:95], v[166:169], a[248:251], v[80:95]\n"
    "ds_read_b128 v[16:19], v175 offset:512\n"
    "ds_read_b64_tr_b16 v[142:143], v184 offset:128\n"
    "ds_read_b64_tr_b16 v[144:145], v184 offset:896\n"
    "s_add_u32 m0, s64, 32768\n"
    "s_mov_b32 s65, 4718592\n"
    "buffer_load_dwordx4 v239, s[44:47], s65 offen offset:0 lds\n"
    "s_waitcnt lgkmcnt(9)\n"
    "v_mfma_f32_32x32x16_bf16 v[150:165], v[170:173], a[248:251], v[150:165]\n"
    "ds_read_b128 v[24:27], v176 offset:512\n"
    "ds_read_b64_tr_b16 v[146:147], v184 offset:3072\n"
    "ds_read_b64_tr_b16 v[148:149], v184 offset:3840\n"
    "s_waitcnt lgkmcnt(9)\n"
    "v_mfma_f32_32x32x16_bf16 v[64:79], v[134:137], v[0:3], v[64:79]\n"
    "v_add_u32 v177, 28672, v177\n"
    "ds_read_b128 v[4:7], v177 offset:0\n"
    "ds_read_b64_tr_b16 v[166:167], v184 offset:3136\n"
    "ds_read_b64_tr_b16 v[168:169], v184 offset:3904\n"
    "s_add_u32 m0, s64, 36864\n"
    "s_mov_b32 s65, 5013504\n"
    "buffer_load_dwordx4 v239, s[44:47], s65 offen offset:0 lds\n"
    "s_waitcnt lgkmcnt(9)\n"
    "v_mfma_f32_32x32x16_bf16 v[80:95], v[138:141], v[0:3], v[80:95]\n"
    "v_add_u32 v178, 28672, v178\n"
    "ds_read_b128 v[12:15], v178 offset:0\n"
    "ds_read_b64_tr_b16 v[170:171], v184 offset:3200\n"
    "ds_read_b64_tr_b16 v[172:173], v184 offset:3968\n"
    "ds_read_b128 v[96:99], v244 offset:0\n"
    "s_waitcnt lgkmcnt(10)\n"
    "v_mfma_f32_32x32x16_bf16 v[150:165], v[142:145], v[0:3], v[150:165]\n"
    "ds_read_b128 v[20:23], v177 offset:512\n"
    "ds_read_b64_tr_b16 v[134:135], v184 offset:6144\n"
    "ds_read_b64_tr_b16 v[136:137], v184 offset:6912\n"
    "s_add_u32 m0, s64, 40960\n"
    "s_mov_b32 s65, 128\n"
    "buffer_load_dwordx4 v239, s[44:47], s65 offen offset:0 lds\n"
    "s_waitcnt lgkmcnt(10)\n"
    "v_mfma_f32_32x32x16_bf16 v[64:79], v[146:149], v[8:11], v[64:79]\n"
    "ds_read_b128 v[28:31], v178 offset:512\n"
    "ds_read_b64_tr_b16 v[138:139], v184 offset:6208\n"
    "ds_read_b64_tr_b16 v[140:141], v184 offset:6976\n"
    "ds_read_b128 v[100:103], v244 offset:32\n"
    "s_waitcnt lgkmcnt(11)\n"
    "v_mfma_f32_32x32x16_bf16 v[80:95], v[166:169], v[8:11], v[80:95]\n"
    "ds_read_b64_tr_b16 v[142:143], v184 offset:6272\n"
    "ds_read_b64_tr_b16 v[144:145], v184 offset:7040\n"
    "s_add_u32 m0, s64, 45056\n"
    "s_mov_b32 s65, 295040\n"
    "buffer_load_dwordx4 v239, s[44:47], s65 offen offset:0 lds\n"
    "s_waitcnt lgkmcnt(10)\n"
    "v_mfma_f32_32x32x16_bf16 v[150:165], v[170:173], v[8:11], v[150:165]\n"
    "ds_read_b64_tr_b16 v[146:147], v184 offset:9216\n"
    "ds_read_b64_tr_b16 v[148:149], v184 offset:9984\n"
    "ds_read_b128 v[104:107], v244 offset:64\n"
    "s_waitcnt lgkmcnt(9)\n"
    "v_mfma_f32_32x32x16_bf16 v[64:79], v[134:137], v[16:19], v[64:79]\n"
    "ds_read_b64_tr_b16 v[166:167], v184 offset:9280\n"
    "ds_read_b64_tr_b16 v[168:169], v184 offset:10048\n"
    "s_add_u32 m0, s64, 49152\n"
    "s_mov_b32 s65, 589952\n"
    "buffer_load_dwordx4 v239, s[44:47], s65 offen offset:0 lds\n"
    "s_waitcnt lgkmcnt(8)\n"
    "v_mfma_f32_32x32x16_bf16 v[80:95], v[138:141], v[16:19], v[80:95]\n"
    "ds_read_b64_tr_b16 v[170:171], v184 offset:9344\n"
    "ds_read_b64_tr_b16 v[172:173], v184 offset:10112\n"
    "ds_read_b128 v[108:111], v244 offset:96\n"
    "s_waitcnt lgkmcnt(8)\n"
    "v_mfma_f32_32x32x16_bf16 v[150:165], v[142:145], v[16:19], v[150:165]\n"
    "s_add_u32 m0, s64, 53248\n"
    "s_mov_b32 s65, 884864\n"
    "buffer_load_dwordx4 v239, s[44:47], s65 offen offset:0 lds\n"
    "s_waitcnt lgkmcnt(6)\n"
    "v_mfma_f32_32x32x16_bf16 v[64:79], v[146:149], v[24:27], v[64:79]\n"
    "s_waitcnt lgkmcnt(3)\n"
    "v_mfma_f32_32x32x16_bf16 v[80:95], v[166:169], v[24:27], v[80:95]\n"
    "s_waitcnt vmcnt(18)\n"
    "s_barrier\n"
    "ds_read_b64_tr_b16 v[112:113], v238 offset:12288\n"
    "ds_read_b64_tr_b16 v[114:115], v238 offset:12544\n"
    "s_waitcnt lgkmcnt(3)\n"
    "v_mfma_f32_32x32x16_bf16 v[150:165], v[170:173], v[24:27], v[150:165]\n"
    "ds_read_b64_tr_b16 v[116:117], v238 offset:14336\n"
    "ds_read_b64_tr_b16 v[118:119], v238 offset:14592\n"
    "s_add_u32 m0, s64, 57344\n"
    "s_mov_b32 s65, 1179776\n"
    "buffer_load_dwordx4 v239, s[44:47], s65 offen offset:0 lds\n"
    "s_waitcnt lgkmcnt(2)\n"
    "v_mfma_f32_32x32x16_bf16 v[32:47], v[112:115], a[0:3], v[96:111]\n"
    "ds_read_b64_tr_b16 v[120:121], v237 offset:49152\n"
    "v_mfma_f32_32x32x16_bf16 v[48:63], v[112:115], a[4:7], v[96:111]\n"
    "ds_read_b64_tr_b16 v[122:123], v237 offset:49408\n"
    "s_waitcnt lgkmcnt(2)\n"
    "v_mfma_f32_32x32x16_bf16 v[32:47], v[116:119], a[8:11], v[32:47]\n"
    "ds_read_b64_tr_b16 v[112:113], v237 offset:51200\n"
    "s_add_u32 m0, s64, 61440\n"
    "s_mov_b32 s65, 1474688\n"
    "buffer_load_dwordx4 v239, s[44:47], s65 offen offset:0 lds\n"
    "v_mfma_f32_32x32x16_bf16 v[48:63], v[116:119], a[12:15], v[48:63]\n"
    "ds_read_b64_tr_b16 v[114:115], v237 offset:51456\n"
    "v_and_b32 v129, 0x1f0, v241\n"
    "v_lshrrev_b32 v129, 2, v129\n"
    "s_waitcnt lgkmcnt(2)\n"
    "v_mfma_f32_32x32x16_bf16 v[32:47], v[120:123], a[16:19], v[32:47]\n"
    "ds_read_b64_tr_b16 v[116:117], v237 offset:53248\n"
    "s_lshl_b32 s96, s66, 7\n"
    "s_add_u32 s96, s96, 147456\n"
    "v_add_u32 v129, s96, v129\n"
    "v_mfma_f32_32x32x16_bf16 v[48:63], v[120:123], a[20:23], v[48:63]\n"
    "ds_read_b64_tr_b16 v[118:119], v237 offset:53504\n"
    "s_add_u32 m0, s64, 65536\n"
    "s_mov_b32 s65, 1769600\n"
    "buffer_load_dwordx4 v239, s[44:47], s65 offen offset:0 lds\n"
    "s_waitcnt lgkmcnt(2)\n"
    "v_mfma_f32_32x32x16_bf16 v[32:47], v[112:115], a[24:27], v[32:47]\n"
    "ds_read_b64_tr_b16 v[120:121], v237 offset:55296\n"
    "v_xor_b32 v130, 0x80, v129\n"
    "ds_write_b32 v129, v154 offset:0\n"
    "v_mfma_f32_32x32x16_bf16 v[48:63], v[112:115], a[28:31], v[48:63]\n"
    "ds_read_b64_tr_b16 v[122:123], v237 offset:55552\n"
    "v_mul_f32 v124, s80, v64\n"
    "v_exp_f32_e64 v124, v124 clamp\n"
    "v_mul_f32 v125, s80, v65\n"
    "v_max_f32 v64, 0, v64\n"
    "s_waitcnt lgkmcnt(3)\n"
    "v_mfma_f32_32x32x16_bf16 v[32:47], v[116:119], a[32:35], v[32:47]\n"
    "ds_read_b64_tr_b16 v[112:113], v237 offset:57344\n"
    "s_add_u32 m0, s64, 69632\n"
    "s_mov_b32 s65, 2064512\n"
    "buffer_load_dwordx4 v239, s[44:47], s65 offen offset:0 lds\n"
    "v_mfma_f32_32x32x16_bf16 v[48:63], v[116:119], a[36:39], v[48:63]\n"
    "ds_read_b64_tr_b16 v[114:115], v237 offset:57600\n"
    "v_exp_f32_e64 v125, v125 clamp\n"
    "v_mul_f32 v126, s80, v66\n"
    "v_add_f32 v64, v64, v124\n"
    "v_max_f32 v65, 0, v65\n"
    "s_waitcnt lgkmcnt(2)\n"
    "v_mfma_f32_32x32x16_bf16 v[32:47], v[120:123], a[40:43], v[32:47]\n"
    "ds_read_b64_tr_b16 v[116:117], v237 offset:59392\n"
    "v_exp_f32_e64 v126, v126 clamp\n"
    "v_mul_f32 v127, s80, v67\n"
    "v_add_f32 v64, 0x358637bd, v64\n"
    "v_add_f32 v65, v65, v125\n"
    "v_max_f32 v66, 0, v66\n"
    "v_mfma_f32_32x32x16_bf16 v[48:63], v[120:123], a[44:47], v[48:63]\n"
    "ds_read_b64_tr_b16 v[118:119], v237 offset:59648\n"
    "s_add_u32 m0, s64, 73728\n"
    "s_mov_b32 s65, 2359424\n"
    "buffer_load_dwordx4 v239, s[44:47], s65 offen offset:0 lds\n"
    "s_waitcnt lgkmcnt(2)\n"
    "v_mfma_f32_32x32x16_bf16 v[32:47], v[112:115], a[48:51], v[32:47]\n"
    "ds_read_b64_tr_b16 v[120:121], v237 offset:61440\n"
    "v_exp_f32_e64 v127, v127 clamp\n"
    "v_mul_f32 v128, s80, v68\n"
    "v_add_f32 v65, 0x358637bd, v65\n"
    "v_add_f32 v66, v66, v126\n"
    "v_max_f32 v67, 0, v67\n"
    "v_mfma_f32_32x32x16_bf16 v[48:63], v[112:115], a[52:55], v[48:63]\n"
    "ds_read_b64_tr_b16 v[122:123], v237 offset:61696\n"
    "v_exp_f32_e64 v128, v128 clamp\n"
    "v_mul_f32 v124, s80, v69\n"
    "v_cvt_pk_bf16_f32 v214, v64, v65\n"
    "v_add_f32 v66, 0x358637bd, v66\n"
    "s_waitcnt lgkmcnt(2)\n"
    "v_mfma_f32_32x32x16_bf16 v[32:47], v[116:119], a[56:59], v[32:47]\n"
    "ds_read_b64_tr_b16 v[112:113], v237 offset:63488\n"
    "s_add_u32 m0, s64, 77824\n"
    "s_mov_b32 s65, 2654336\n"
    "buffer_load_dwordx4 v239, s[44:47], s65 offen offset:0 lds\n"
    "v_mfma_f32_32x32x16_bf16 v[48:63], v[116:119], a[60:63], v[48:63]\n"
    "ds_read_b64_tr_b16 v[114:115], v237 offset:63744\n"
    "ds_read_b128 v[96:99], v244 offset:256\n"
    "v_add_f32 v67, v67, v127\n"
    "v_max_f32 v68, 0, v68\n"
    "v_exp_f32_e64 v124, v124 clamp\n"
    "s_waitcnt lgkmcnt(3)\n"
    "v_mfma_f32_32x32x16_bf16 v[32:47], v[120:123], a[64:67], v[32:47]\n"
    "ds_read_b64_tr_b16 v[116:117], v238 offset:0\n"
    "v_mul_f32 v125, s80, v70\n"
    "v_add_f32 v67, 0x358637bd, v67\n"
    "v_add_f32 v68, v68, v128\n"
    "v_max_f32 v69, 0, v69\n"
    "v_exp_f32_e64 v125, v125 clamp\n"
    "v_mfma_f32_32x32x16_bf16 v[48:63], v[120:123], a[68:71], v[48:63]\n"
    "ds_read_b64_tr_b16 v[118:119], v238 offset:256\n"
    "s_add_u32 m0, s64, 81920\n"
    "s_mov_b32 s65, 2949248\n"
    "buffer_load_dwordx4 v239, s[44:47], s65 offen offset:0 lds\n"
    "s_waitcnt lgkmcnt(3)\n"
    "v_mfma_f32_32x32x16_bf16 v[32:47], v[112:115], a[72:75], v[32:47]\n"
    "ds_read_b64_tr_b16 v[120:121], v238 offset:2048\n"
    "v_mul_f32 v126, s80, v71\n"
    "v_cvt_pk_bf16_f32 v215, v66, v67\n"
    "v_add_f32 v68, 0x358637bd, v68\n"
    "v_add_f32 v69, v69, v124\n"
    "v_max_f32 v70, 0, v70\n"
    "v_exp_f32_e64 v126, v126 clamp\n"
    "v_mfma_f32_32x32x16_bf16 v[48:63], v[112:115], a[76:79], v[48:63]\n"
    "ds_read_b64_tr_b16 v[122:123], v238 offset:2304\n"
    "v_mul_f32 v127, s80, v72\n"
    "v_add_f32 v69, 0x358637bd, v69\n"
    "v_add_f32 v70, v70, v125\n"
    "v_max_f32 v71, 0, v71\n"
    "s_waitcnt lgkmcnt(2)\n"
    "v_mfma_f32_32x32x16_bf16 v[32:47], v[116:119], a[80:83], v[32:47]\n"
    "s_waitcnt vmcnt(18)\n"
    "s_barrier\n"
    "ds_read_b64_tr_b16 v[112:113], v238 offset:4096\n"
    "v_exp_f32_e64 v127, v127 clamp\n"
    "v_mul_f32 v128, s80, v73\n"
    "v_mfma_f32_32x32x16_bf16 v[48:63], v[116:119], a[84:87], v[48:63]\n"
    "ds_read_b64_tr_b16 v[114:115], v238 offset:4352\n"
    "s_add_u32 m0, s64, 86016\n"
    "s_mov_b32 s65, 3244160\n"
    "buffer_load_dwordx4 v239, s[44:47], s65 offen offset:0 lds\n"
    "ds_read_b32 v245, v129 offset:0\n"
    "ds_read_b32 v246, v130 offset:0\n"
    "s_waitcnt lgkmcnt(4)\n"
    "v_mfma_f32_32x32x16_bf16 v[32:47], v[120:123], a[88:91], v[32:47]\n"
    "ds_read_b64_tr_b16 v[116:117], v238 offset:6144\n"
    "v_cvt_pk_bf16_f32 v216, v68, v69\n"
    "v_add_f32 v70, 0x358637bd, v70\n"
    "v_add_f32 v71, v71, v126\n"
    "v_max_f32 v72, 0, v72\n"
    "v_exp_f32_e64 v128, v128 clamp\n"
    "s_waitcnt lgkmcnt(2)\n"
    "v_mul_f32 v249, 0xbfb8aa3b, v245\n"
    "v_mul_f32 v245, s80, v245\n"
    "v_mfma_f32_32x32x16_bf16 v[48:63], v[120:123], a[92:95], v[48:63]\n"
    "ds_read_b64_tr_b16 v[118:119], v238 offset:6400\n"
    "v_mul_f32 v124, s80, v74\n"
    "v_add_f32 v71, 0x358637bd, v71\n"
    "v_add_f32 v72, v72, v127\n"
    "v_max_f32 v73, 0, v73\n"
    "s_waitcnt lgkmcnt(2)\n"
    "v_mul_f32 v246, s80, v246\n"
    "v_exp_f32 v249, v249\n"
    "v_mfma_f32_32x32x16_bf16 v[32:47], v[112:115], a[96:99], v[32:47]\n"
    "ds_read_b64_tr_b16 v[120:121], v238 offset:8192\n"
    "s_add_u32 m0, s64, 90112\n"
    "s_mov_b32 s65, 3539072\n"
    "buffer_load_dwordx4 v239, s[44:47], s65 offen offset:0 lds\n"
    "v_exp_f32_e64 v124, v124 clamp\n"
    "v_exp_f32 v245, v245\n"
    "v_mfma_f32_32x32x16_bf16 v[48:63], v[112:115], a[100:103], v[48:63]\n"
    "ds_read_b64_tr_b16 v[122:123], v238 offset:8448\n"
    "v_mul_f32 v125, s80, v75\n"
    "v_cvt_pk_bf16_f32 v217, v70, v71\n"
    "v_add_f32 v72, 0x358637bd, v72\n"
    "v_add_f32 v73, v73, v128\n"
    "v_exp_f32 v246, v246\n"
    "s_waitcnt lgkmcnt(2)\n"
    "v_mfma_f32_32x32x16_bf16 v[32:47], v[116:119], a[104:107], v[32:47]\n"
    "ds_read_b64_tr_b16 v[112:113], v238 offset:10240\n"
    "v_max_f32 v74, 0, v74\n"
    "v_exp_f32_e64 v125, v125 clamp\n"
    "v_mul_f32 v126, s80, v76\n"
    "v_add_f32 v73, 0x358637bd, v73\n"
    "v_add_f32 v74, v74, v124\n"
    "v_add_f32 v249, 1.0, v249\n"
    "v_mfma_f32_32x32x16_bf16 v[48:63], v[116:119], a[108:111], v[48:63]\n"
    "ds_read_b64_tr_b16 v[114:115], v238 offset:10496\n"
    "s_add_u32 m0, s64, 94208\n"
    "s_mov_b32 s65, 3833984\n"
    "buffer_load_dwordx4 v239, s[44:47], s65 offen offset:0 lds\n"
    "v_add_f32 v245, 1.0, v245\n"
    "v_add_f32 v246, 1.0, v246\n"
    "s_waitcnt lgkmcnt(2)\n"
    "v_mfma_f32_32x32x16_bf16 v[32:47], v[120:123], a[112:115], v[32:47]\n"
    "ds_read_b64_tr_b16 v[116:117], v236 offset:0\n"
    "v_max_f32 v75, 0, v75\n"
    "v_exp_f32_e64 v126, v126 clamp\n"
    "v_mul_f32 v127, s80, v77\n"
    "v_cvt_pk_bf16_f32 v218, v72, v73\n"
    "v_add_f32 v74, 0x358637bd, v74\n"
    "v_rcp_f32 v249, v249\n"
    "v_mfma_f32_32x32x16_bf16 v[48:63], v[120:123], a[116:119], v[48:63]\n"
    "ds_read_b64_tr_b16 v[118:119], v236 offset:256\n"
    "ds_read_b128 v[100:103], v244 offset:288\n"
    "v_add_f32 v75, v75, v125\n"
    "v_max_f32 v76, 0, v76\n"
    "v_exp_f32_e64 v127, v127 clamp\n"
    "v_rcp_f32 v245, v245\n"
    "s_waitcnt lgkmcnt(3)\n"
    "v_mfma_f32_32x32x16_bf16 v[32:47], v[112:115], a[120:123], v[32:47]\n"
    "ds_read_b64_tr_b16 v[120:121], v236 offset:2048\n"
    "s_add_u32 m0, s64, 98304\n"
    "s_mov_b32 s65, 4128896\n"
    "buffer_load_dwordx4 v239, s[44:47], s65 offen offset:0 lds\n"
    "v_rcp_f32 v246, v246\n"
    "v_mfma_f32_32x32x16_bf16 v[48:63], v[112:115], a[124:127], v[48:63]\n"
    "ds_read_b64_tr_b16 v[122:123], v236 offset:2304\n"
    "v_mul_f32 v128, s80, v78\n"
    "v_add_f32 v75, 0x358637bd, v75\n"
    "v_add_f32 v76, v76, v126\n"
    "v_max_f32 v77, 0, v77\n"
    "v_mul_f32 v247, -2.0, v245\n"
    "v_mul_f32 v248, -2.0, v246\n"
    "s_waitcnt lgkmcnt(3)\n"
    "v_mfma_f32_32x32x16_bf16 v[32:47], v[116:119], a[128:131], v[32:47]\n"
    "ds_read_b64_tr_b16 v[112:113], v236 offset:4096\n"
    "v_exp_f32_e64 v128, v128 clamp\n"
    "v_mul_f32 v124, s80, v79\n"
    "v_cvt_pk_bf16_f32 v219, v74, v75\n"
    "v_add_f32 v76, 0x358637bd, v76\n"
    "v_add_f32 v77, v77, v127\n"
    "v_max_f32 v78, 0, v78\n"
    "v_mfma_f32_32x32x16_bf16 v[48:63], v[116:119], a[132:135], v[48:63]\n"
    "ds_read_b64_tr_b16 v[114:115], v236 offset:4352\n"
    "v_exp_f32_e64 v124, v124 clamp\n"
    "v_add_f32 v77, 0x358637bd, v77\n"
    "v_add_f32 v78, v78, v128\n"
    "s_waitcnt lgkmcnt(2)\n"
    "v_mfma_f32_32x32x16_bf16 v[32:47], v[120:123], a[136:139], v[32:47]\n"
    "ds_read_b64_tr_b16 v[116:117], v236 offset:6144\n"
    "s_add_u32 m0, s64, 102400\n"
    "s_mov_b32 s65, 4423808\n"
    "buffer_load_dwordx4 v239, s[44:47], s65 offen offset:0 lds\n"
    "v_max_f32 v79, 0, v79\n"
    "v_mfma_f32_32x32x16_bf16 v[48:63], v[120:123], a[140:143], v[48:63]\n"
    "ds_read_b64_tr_b16 v[118:119], v236 offset:6400\n"
    "v_cvt_pk_bf16_f32 v220, v76, v77\n"
    "v_add_f32 v78, 0x358637bd, v78\n"
    "v_add_f32 v79, v79, v124\n"
    "v_add_f32 v79, 0x358637bd, v79\n"
    "v_cvt_pk_bf16_f32 v221, v78, v79\n"
    "s_waitcnt lgkmcnt(2)\n"
    "v_mfma_f32_32x32x16_bf16 v[32:47], v[112:115], a[144:147], v[32:47]\n"
    "ds_read_b64_tr_b16 v[120:121], v236 offset:8192\n"
    "v_mul_f32 v124, s80, v80\n"
    "v_exp_f32_e64 v124, v124 clamp\n"
    "v_mul_f32 v125, s80, v81\n"
    "v_max_f32 v80, 0, v80\n"
    "v_mfma_f32_32x32x16_bf16 v[48:63], v[112:115], a[148:151], v[48:63]\n"
    "ds_read_b64_tr_b16 v[122:123], v236 offset:8448\n"
    "s_add_u32 m0, s64, 106496\n"
    "s_mov_b32 s65, 4718720\n"
    "buffer_load_dwordx4 v239, s[44:47], s65 offen offset:0 lds\n"
    "s_waitcnt lgkmcnt(2)\n"
    "v_mfma_f32_32x32x16_bf16 v[32:47], v[116:119], a[152:155], v[32:47]\n"
    "ds_read_b64_tr_b16 v[112:113], v236 offset:10240\n"
    "v_exp_f32_e64 v125, v125 clamp\n"
    "v_mul_f32 v126, s80, v82\n"
    "v_add_f32 v80, v80, v124\n"
    "v_max_f32 v81, 0, v81\n"
    "v_exp_f32_e64 v126, v126 clamp\n"
    "v_mfma_f32_32x32x16_bf16 v[48:63], v[116:119], a[156:159], v[48:63]\n"
    "ds_read_b64_tr_b16 v[114:115], v236 offset:10496\n"
    "v_mul_f32 v127, s80, v83\n"
    "v_add_f32 v80, 0x358637bd, v80\n"
    "v_add_f32 v81, v81, v125\n"
    "v_max_f32 v82, 0, v82\n"
    "s_waitcnt lgkmcnt(2)\n"
    "v_mfma_f32_32x32x16_bf16 v[32:47], v[120:123], a[160:163], v[32:47]\n"
    "ds_read_b64_tr_b16 v[116:117], v236 offset:12288\n"
    "s_add_u32 m0, s64, 110592\n"
    "s_mov_b32 s65, 5013632\n"
    "buffer_load_dwordx4 v239, s[44:47], s65 offen offset:0 lds\n"
    "v_exp_f32_e64 v127, v127 clamp\n"
    "v_mfma_f32_32x32x16_bf16 v[48:63], v[120:123], a[164:167], v[48:63]\n"
    "ds_read_b64_tr_b16 v[118:119], v236 offset:12544\n"
    "v_mul_f32 v128, s80, v84\n"
    "v_add_f32 v81, 0x358637bd, v81\n"
    "v_add_f32 v82, v82, v126\n"
    "v_max_f32 v83, 0, v83\n"
    "s_waitcnt lgkmcnt(2)\n"
    "v_mfma_f32_32x32x16_bf16 v[32:47], v[112:115], a[168:171], v[32:47]\n"
    "ds_read_b64_tr_b16 v[120:121], v236 offset:14336\n"
    "v_exp_f32_e64 v128, v128 clamp\n"
    "v_mul_f32 v124, s80, v85\n"
    "v_cvt_pk_bf16_f32 v222, v80, v81\n"
    "v_add_f32 v82, 0x358637bd, v82\n"
    "v_add_f32 v83, v83, v127\n"
    "v_mfma_f32_32x32x16_bf16 v[48:63], v[112:115], a[172:175], v[48:63]\n"
    "ds_read_b64_tr_b16 v[122:123], v236 offset:14592\n"
    "ds_read_b128 v[104:107], v244 offset:320\n"
    "v_max_f32 v84, 0, v84\n"
    "v_exp_f32_e64 v124, v124 clamp\n"
    "s_waitcnt lgkmcnt(3)\n"
    "v_mfma_f32_32x32x16_bf16 v[32:47], v[116:119], a[176:179], v[32:47]\n"
    "s_waitcnt vmcnt(18)\n"
    "s_barrier\n"
    "ds_read_b64_tr_b16 v[112:113], v236 offset:16384\n"
    "v_mul_f32 v125, s80, v86\n"
    "v_add_f32 v83, 0x358637bd, v83\n"
    "v_add_f32 v84, v84, v128\n"
    "v_mfma_f32_32x32x16_bf16 v[48:63], v[116:119], a[180:183], v[48:63]\n"
    "ds_read_b64_tr_b16 v[114:115], v236 offset:16640\n"
    "s_add_u32 m0, s64, 114688\n"
    "s_mov_b32 s65, 256\n"
    "buffer_load_dwordx4 v239, s[44:47], s65 offen offset:0 lds\n"
    "s_waitcnt lgkmcnt(3)\n"
    "v_mfma_f32_32x32x16_bf16 v[32:47], v[120:123], a[184:187], v[32:47]\n"
    "ds_read_b64_tr_b16 v[116:117], v236 offset:18432\n"
    "v_max_f32 v85, 0, v85\n"
    "v_exp_f32_e64 v125, v125 clamp\n"
    "v_mul_f32 v126, s80, v87\n"
    "v_cvt_pk_bf16_f32 v223, v82, v83\n"
    "v_add_f32 v84, 0x358637bd, v84\n"
    "v_mfma_f32_32x32x16_bf16 v[48:63], v[120:123], a[188:191], v[48:63]\n"
    "ds_read_b64_tr_b16 v[118:119], v236 offset:18688\n"
    "v_add_f32 v85, v85, v124\n"
    "v_max_f32 v86, 0, v86\n"
    "v_exp_f32_e64 v126, v126 clamp\n"
    "v_mul_f32 v127, s80, v88\n"
    "s_waitcnt lgkmcnt(2)\n"
    "v_mfma_f32_32x32x16_bf16 v[32:47], v[112:115], a[192:195], v[32:47]\n"
    "ds_read_b64_tr_b16 v[120:121], v236 offset:20480\n"
    "s_add_u32 m0, s64, 118784\n"
    "s_mov_b32 s65, 295168\n"
    "buffer_load_dwordx4 v239, s[44:47], s65 offen offset:0 lds\n"
    "v_mfma_f32_32x32x16_bf16 v[48:63], v[112:115], a[196:199], v[48:63]\n"
    "ds_read_b64_tr_b16 v[122:123], v236 offset:20736\n"
    "v_add_f32 v85, 0x358637bd, v85\n"
    "v_add_f32 v86, v86, v125\n"
    "v_max_f32 v87, 0, v87\n"
    "v_exp_f32_e64 v127, v127 clamp\n"
    "s_waitcnt lgkmcnt(2)\n"
    "v_mfma_f32_32x32x16_bf16 v[32:47], v[116:119], a[200:203], v[32:47]\n"
    "ds_read_b64_tr_b16 v[112:113], v236 offset:22528\n"
    "v_mul_f32 v128, s80, v89\n"
    "v_cvt_pk_bf16_f32 v224, v84, v85\n"
    "v_add_f32 v86, 0x358637bd, v86\n"
    "v_add_f32 v87, v87, v126\n"
    "v_max_f32 v88, 0, v88\n"
    "v_exp_f32_e64 v128, v128 clamp\n"
    "v_mfma_f32_32x32x16_bf16 v[48:63], v[116:119], a[204:207], v[48:63]\n"
    "ds_read_b64_tr_b16 v[114:115], v236 offset:22784\n"
    "v_mul_f32 v124, s80, v90\n"
    "v_add_f32 v87, 0x358637bd, v87\n"
    "v_add_f32 v88, v88, v127\n"
    "v_max_f32 v89, 0, v89\n"
    "s_waitcnt lgkmcnt(2)\n"
    "v_mfma_f32_32x32x16_bf16 v[32:47], v[120:123], a[208:211], v[32:47]\n"
    "ds_read_b64_tr_b16 v[116:117], v236 offset:24576\n"
    "s_add_u32 m0, s64, 122880\n"
    "s_mov_b32 s65, 590080\n"
    "buffer_load_dwordx4 v239, s[44:47], s65 offen offset:0 lds\n"
    "v_mfma_f32_32x32x16_bf16 v[48:63], v[120:123], a[212:215], v[48:63]\n"
    "ds_read_b64_tr_b16 v[118:119], v236 offset:24832\n"
    "v_exp_f32_e64 v124, v124 clamp\n"
    "v_mul_f32 v125, s80, v91\n"
    "v_cvt_pk_bf16_f32 v225, v86, v87\n"
    "v_add_f32 v88, 0x358637bd, v88\n"
    "s_waitcnt lgkmcnt(2)\n"
    "v_mfma_f32_32x32x16_bf16 v[32:47], v[112:115], a[216:219], v[32:47]\n"
    "ds_read_b64_tr_b16 v[120:121], v236 offset:26624\n"
    "v_add_f32 v89, v89, v128\n"
    "v_max_f32 v90, 0, v90\n"
    "v_exp_f32_e64 v125, v125 clamp\n"
    "v_mul_f32 v126, s80, v92\n"
    "v_add_f32 v89, 0x358637bd, v89\n"
    "v_mfma_f32_32x32x16_bf16 v[48:63], v[112:115], a[220:223], v[48:63]\n"
    "ds_read_b64_tr_b16 v[122:123], v236 offset:26880\n"
    "v_add_f32 v90, v90, v124\n"
    "v_max_f32 v91, 0, v91\n"
    "v_exp_f32_e64 v126, v126 clamp\n"
    "v_mul_f32 v127, s80, v93\n"
    "s_waitcnt lgkmcnt(2)\n"
    "v_mfma_f32_32x32x16_bf16 v[32:47], v[116:119], a[224:227], v[32:47]\n"
    "ds_read_b64_tr_b16 v[112:113], v236 offset:28672\n"
    "s_add_u32 m0, s64, 126976\n"
    "s_mov_b32 s65, 884992\n"
    "buffer_load_dwordx4 v239, s[44:47], s65 offen offset:0 lds\n"
    "v_mfma_f32_32x32x16_bf16 v[48:63], v[116:119], a[228:231], v[48:63]\n"
    "ds_read_b64_tr_b16 v[114:115], v236 offset:28928\n"
    "v_cvt_pk_bf16_f32 v226, v88, v89\n"
    "v_add_f32 v90, 0x358637bd, v90\n"
    "v_add_f32 v91, v91, v125\n"
    "v_max_f32 v92, 0, v92\n"
    "s_waitcnt lgkmcnt(2)\n"
    "v_mfma_f32_32x32x16_bf16 v[32:47], v[120:123], a[232:235], v[32:47]\n"
    "ds_read_b64_tr_b16 v[116:117], v236 offset:30720\n"
    "ds_read_b128 v[108:111], v244 offset:352\n"
    "v_exp_f32_e64 v127, v127 clamp\n"
    "v_mul_f32 v128, s80, v94\n"
    "v_add_f32 v91, 0x358637bd, v91\n"
    "v_add_f32 v92, v92, v126\n"
    "v_max_f32 v93, 0, v93\n"
    "v_mfma_f32_32x32x16_bf16 v[48:63], v[120:123], a[236:239], v[48:63]\n"
    "ds_read_b64_tr_b16 v[118:119], v236 offset:30976\n"
    "v_exp_f32_e64 v128, v128 clamp\n"
    "v_mul_f32 v124, s80, v95\n"
    "v_cvt_pk_bf16_f32 v227, v90, v91\n"
    "s_waitcnt lgkmcnt(3)\n"
    "v_mfma_f32_32x32x16_bf16 v[32:47], v[112:115], a[240:243], v[32:47]\n"
    "ds_read_b64_tr_b16 v[120:121], v236 offset:32768\n"
    "s_add_u32 m0, s64, 131072\n"
    "s_mov_b32 s65, 1179904\n"
    "buffer_load_dwordx4 v239, s[44:47], s65 offen offset:0 lds\n"
    "v_add_f32 v92, 0x358637bd, v92\n"
    "v_mfma_f32_32x32x16_bf16 v[48:63], v[112:115], a[244:247], v[48:63]\n"
    "ds_read_b64_tr_b16 v[122:123], v236 offset:33024\n"
    "v_add_f32 v93, v93, v127\n"
    "v_max_f32 v94, 0, v94\n"
    "v_exp_f32_e64 v124, v124 clamp\n"
    "v_add_f32 v93, 0x358637bd, v93\n"
    "s_waitcnt lgkmcnt(2)\n"
    "v_mfma_f32_32x32x16_bf16 v[32:47], v[116:119], a[248:251], v[32:47]\n"
    "ds_read_b64_tr_b16 v[112:113], v236 offset:34816\n"
    "v_add_f32 v94, v94, v128\n"
    "v_max_f32 v95, 0, v95\n"
    "v_cvt_pk_bf16_f32 v228, v92, v93\n"
    "v_add_f32 v94, 0x358637bd, v94\n"
    "v_add_f32 v95, v95, v124\n"
    "v_add_f32 v95, 0x358637bd, v95\n"
    "v_mfma_f32_32x32x16_bf16 v[48:63], v[116:119], a[252:255], v[48:63]\n"
    "ds_read_b64_tr_b16 v[114:115], v236 offset:35072\n"
    "v_cvt_pk_bf16_f32 v229, v94, v95\n"
    "v_mul_f32 v124, s80, v150\n"
    "v_exp_f32_e64 v124, v124 clamp\n"
    "s_waitcnt lgkmcnt(2)\n"
    "v_mfma_f32_32x32x16_bf16 v[32:47], v[120:123], v[0:3], v[32:47]\n"
    "ds_read_b64_tr_b16 v[116:117], v236 offset:36864\n"
    "s_add_u32 m0, s64, 143360\n"
    "s_mov_b32 s65, 1474816\n"
    "buffer_load_dwordx4 v239, s[44:47], s65 offen offset:0 lds\n"
    "v_mul_f32 v125, s80, v151\n"
    "v_mfma_f32_32x32x16_bf16 v[48:63], v[120:123], v[4:7], v[48:63]\n"
    "ds_read_b64_tr_b16 v[118:119], v236 offset:37120\n"
    "v_max_f32 v150, 0, v150\n"
    "v_exp_f32_e64 v125, v125 clamp\n"
    "v_mul_f32 v126, s80, v152\n"
    "s_waitcnt lgkmcnt(2)\n"
    "v_mfma_f32_32x32x16_bf16 v[32:47], v[112:115], v[8:11], v[32:47]\n"
    "ds_read_b64_tr_b16 v[120:121], v236 offset:38912\n"
    "v_add_f32 v150, v150, v124\n"
    "v_max_f32 v151, 0, v151\n"
    "v_exp_f32_e64 v126, v126 clamp\n"
    "v_mul_f32 v127, s80, v153\n"
    "v_add_f32 v150, 0x358637bd, v150\n"
    "v_mfma_f32_32x32x16_bf16 v[48:63], v[112:115], v[12:15], v[48:63]\n"
    "ds_read_b64_tr_b16 v[122:123], v236 offset:39168\n"
    "v_add_f32 v151, v151, v125\n"
    "v_max_f32 v152, 0, v152\n"
    "v_exp_f32_e64 v127, v127 clamp\n"
    "v_add_f32 v151, 0x358637bd, v151\n"
    "s_waitcnt lgkmcnt(2)\n"
    "v_mfma_f32_32x32x16_bf16 v[32:47], v[116:119], v[16:19], v[32:47]\n"
    "s_waitcnt vmcnt(18)\n"
    "s_barrier\n"
    "ds_read_b64_tr_b16 v[112:113], v236 offset:40960\n"
    "v_add_f32 v152, v152, v126\n"
    "v_max_f32 v153, 0, v153\n"
    "v_cvt_pk_bf16_f32 v230, v150, v151\n"
    "v_mfma_f32_32x32x16_bf16 v[48:63], v[116:119], v[20:23], v[48:63]\n"
    "ds_read_b64_tr_b16 v[114:115], v236 offset:41216\n"
    "s_add_u32 m0, s64, 0\n"
    "s_mov_b32 s65, 1769728\n"
    "buffer_load_dwordx4 v239, s[44:47], s65 offen offset:0 lds\n"
    "s_waitcnt lgkmcnt(2)\n"
    "v_mfma_f32_32x32x16_bf16 v[32:47], v[120:123], v[24:27], v[32:47]\n"
    "ds_read_b64_tr_b16 v[116:117], v236 offset:43008\n"
    "v_add_f32 v152, 0x358637bd, v152\n"
    "v_add_f32 v153, v153, v127\n"
    "v_add_f32 v153, 0x358637bd, v153\n"
    "v_cvt_pk_bf16_f32 v231, v152, v153\n"
    "v_mov_b32 v232, 0\n"
    "v_mov_b32 v233, 0\n"
    "v_mfma_f32_32x32x16_bf16 v[48:63], v[120:123], v[28:31], v[48:63]\n"
    "ds_read_b64_tr_b16 v[118:119], v236 offset:43264\n"
    "s_waitcnt lgkmcnt(2)\n"
    "v_mfma_f32_32x32x16_bf16 v[64:79], v[112:115], a[0:3], v[96:111]\n"
    "ds_read_b64_tr_b16 v[120:121], v236 offset:45056\n"
    "s_add_u32 m0, s64, 4096\n"
    "s_mov_b32 s65, 2064640\n"
    "buffer_load_dwordx4 v239, s[44:47], s65 offen offset:0 lds\n"
    "v_mfma_f32_32x32x16_bf16 v[80:95], v[112:115], a[4:7], v[96:111]\n"
    "ds_read_b64_tr_b16 v[122:123], v236 offset:45312\n"
    "s_waitcnt lgkmcnt(2)\n"
    "v_mfma_f32_32x32x16_bf16 v[64:79], v[116:119], a[8:11], v[64:79]\n"
    "ds_read_b64_tr_b16 v[112:113], v236 offset:47104\n"
    "v_mfma_f32_32x32x16_bf16 v[80:95], v[116:119], a[12:15], v[80:95]\n"
    "ds_read_b64_tr_b16 v[114:115], v236 offset:47360\n"
    "v_mul_f32 v124, s80, v32\n"
    "v_exp_f32 v124, v124\n"
    "v_mul_f32 v125, s80, v33\n"
    "v_add_f32 v124, 1.0, v124\n"
    "v_exp_f32 v125, v125\n"
    "s_waitcnt lgkmcnt(2)\n"
    "v_mfma_f32_32x32x16_bf16 v[64:79], v[120:123], a[16:19], v[64:79]\n"
    "ds_read_b64_tr_b16 v[116:117], v236 offset:49152\n"
    "s_add_u32 m0, s64, 8192\n"
    "s_mov_b32 s65, 2359552\n"
    "buffer_load_dwordx4 v239, s[44:47], s65 offen offset:0 lds\n"
    "v_mfma_f32_32x32x16_bf16 v[80:95], v[120:123], a[20:23], v[80:95]\n"
    "ds_read_b64_tr_b16 v[118:119], v236 offset:49408\n"
    "v_mul_f32 v126, s80, v34\n"
    "v_fma_f32 v124, v124, v124, 1.0\n"
    "v_add_f32 v125, 1.0, v125\n"
    "v_exp_f32 v126, v126\n"
    "v_mul_f32 v127, s80, v35\n"
    "s_waitcnt lgkmcnt(2)\n"
    "v_mfma_f32_32x32x16_bf16 v[64:79], v[112:115], a[24:27], v[64:79]\n"
    "ds_read_b64_tr_b16 v[120:121], v236 offset:51200\n"
    "v_rcp_f32 v124, v124\n"
    "v_fma_f32 v125, v125, v125, 1.0\n"
    "v_add_f32 v126, 1.0, v126\n"
    "v_exp_f32 v127, v127\n"
    "v_mul_f32 v128, s80, v36\n"
    "v_mfma_f32_32x32x16_bf16 v[80:95], v[112:115], a[28:31], v[80:95]\n"
    "ds_read_b64_tr_b16 v[122:123], v236 offset:51456\n"
    "v_fma_f32 v124, v124, v247, v245\n"
    "v_rcp_f32 v125, v125\n"
    "v_fma_f32 v126, v126, v126, 1.0\n"
    "v_add_f32 v127, 1.0, v127\n"
    "v_exp_f32 v128, v128\n"
    "s_waitcnt lgkmcnt(2)\n"
    "v_mfma_f32_32x32x16_bf16 v[64:79], v[116:119], a[32:35], v[64:79]\n"
    "ds_read_b64_tr_b16 v[112:113], v236 offset:53248\n"
    "s_add_u32 m0, s64, 12288\n"
    "s_mov_b32 s65, 2654464\n"
    "buffer_load_dwordx4 v239, s[44:47], s65 offen offset:0 lds\n"
    "v_mfma_f32_32x32x16_bf16 v[80:95], v[116:119], a[36:39], v[80:95]\n"
    "ds_read_b64_tr_b16 v[114:115], v236 offset:53504\n"
    "v_mul_f32 v129, s80, v37\n"
    "v_mul_f32 v32, v124, v32\n"
    "v_fma_f32 v125, v125, v247, v245\n"
    "v_rcp_f32 v126, v126\n"
    "v_fma_f32 v127, v127, v127, 1.0\n"
    "s_waitcnt lgkmcnt(2)\n"
    "v_mfma_f32_32x32x16_bf16 v[64:79], v[120:123], a[40:43], v[64:79]\n"
    "ds_read_b64_tr_b16 v[116:117], v236 offset:55296\n"
    "v_add_f32 v128, 1.0, v128\n"
    "v_exp_f32 v129, v129\n"
    "v_mul_f32 v130, s80, v38\n"
    "v_mul_f32 v33, v125, v33\n"
    "v_fma_f32 v126, v126, v247, v245\n"
    "v_rcp_f32 v127, v127\n"
    "v_mfma_f32_32x32x16_bf16 v[80:95], v[120:123], a[44:47], v[80:95]\n"
    "ds_read_b64_tr_b16 v[118:119], v236 offset:55552\n"
    "v_fma_f32 v128, v128, v128, 1.0\n"
    "v_add_f32 v129, 1.0, v129\n"
    "v_exp_f32 v130, v130\n"
    "v_mul_f32 v124, s80, v39\n"
    "v_cvt_pk_bf16_f32 v134, v32, v33\n"
    "s_waitcnt lgkmcnt(2)\n"
    "v_mfma_f32_32x32x16_bf16 v[64:79], v[112:115], a[48:51], v[64:79]\n"
    "ds_read_b64_tr_b16 v[120:121], v236 offset:57344\n"
    "s_add_u32 m0, s64, 135168\n"
    "s_mov_b32 s65, 2949376\n"
    "buffer_load_dwordx4 v239, s[44:47], s65 offen offset:0 lds\n"
    "v_mul_f32 v34, v126, v34\n"
    "v_mfma_f32_32x32x16_bf16 v[80:95], v[112:115], a[52:55], v[80:95]\n"
    "ds_read_b64_tr_b16 v[122:123], v236 offset:57600\n"
    "v_fma_f32 v127, v127, v247, v245\n"
    "v_rcp_f32 v128, v128\n"
    "v_fma_f32 v129, v129, v129, 1.0\n"
    "v_add_f32 v130, 1.0, v130\n"
    "s_waitcnt lgkmcnt(2)\n"
    "v_mfma_f32_32x32x16_bf16 v[64:79], v[116:119], a[56:59], v[64:79]\n"
    "ds_read_b64_tr_b16 v[112:113], v236 offset:59392\n"
    "v_exp_f32 v124, v124\n"
    "v_mul_f32 v125, s80, v40\n"
    "v_mul_f32 v35, v127, v35\n"
    "v_fma_f32 v128, v128, v247, v245\n"
    "v_rcp_f32 v129, v129\n"
    "v_fma_f32 v130, v130, v130, 1.0\n"
    "v_mfma_f32_32x32x16_bf16 v[80:95], v[116:119], a[60:63], v[80:95]\n"
    "ds_read_b64_tr_b16 v[114:115], v236 offset:59648\n"
    "ds_read_b128 v[96:99], v244 offset:512\n"
    "v_add_f32 v124, 1.0, v124\n"
    "v_exp_f32 v125, v125\n"
    "v_mul_f32 v126, s80, v41\n"
    "v_cvt_pk_bf16_f32 v135, v34, v35\n"
    "s_waitcnt lgkmcnt(3)\n"
    "v_mfma_f32_32x32x16_bf16 v[64:79], v[120:123], a[64:67], v[64:79]\n"
    "ds_read_b64_tr_b16 v[116:117], v236 offset:61440\n"
    "s_add_u32 m0, s64, 139264\n"
    "s_mov_b32 s65, 3244288\n"
    "buffer_load_dwordx4 v239, s[44:47], s65 offen offset:0 lds\n"
    "v_mul_f32 v36, v128, v36\n"
    "v_mfma_f32_32x32x16_bf16 v[80:95], v[120:123], a[68:71], v[80:95]\n"
    "ds_read_b64_tr_b16 v[118:119], v236 offset:61696\n"
    "v_fma_f32 v129, v129, v247, v245\n"
    "v_rcp_f32 v130, v130\n"
    "v_fma_f32 v124, v124, v124, 1.0\n"
    "v_add_f32 v125, 1.0, v125\n"
    "s_waitcnt lgkmcnt(3)\n"
    "v_mfma_f32_32x32x16_bf16 v[64:79], v[112:115], a[72:75], v[64:79]\n"
    "ds_read_b64_tr_b16 v[120:121], v236 offset:63488\n"
    "v_exp_f32 v126, v126\n"
    "v_mul_f32 v127, s80, v42\n"
    "v_mul_f32 v37, v129, v37\n"
    "v_fma_f32 v130, v130, v247, v245\n"
    "v_rcp_f32 v124, v124\n"
    "v_fma_f32 v125, v125, v125, 1.0\n"
    "v_mfma_f32_32x32x16_bf16 v[80:95], v[112:115], a[76:79], v[80:95]\n"
    "ds_read_b64_tr_b16 v[122:123], v236 offset:63744\n"
    "v_add_f32 v126, 1.0, v126\n"
    "v_exp_f32 v127, v127\n"
    "v_mul_f32 v128, s80, v43\n"
    "v_cvt_pk_bf16_f32 v136, v36, v37\n"
    "v_mul_f32 v38, v130, v38\n"
    "v_fma_f32 v124, v124, v247, v245\n"
    "s_waitcnt lgkmcnt(2)\n"
    "v_mfma_f32_32x32x16_bf16 v[64:79], v[116:119], a[80:83], v[64:79]\n"
    "s_waitcnt vmcnt(18)\n"
    "s_barrier\n"
    "ds_read_b64_tr_b16 v[112:113], v237 offset:0\n"
    "v_rcp_f32 v125, v125\n"
    "v_fma_f32 v126, v126, v126, 1.0\n"
    "v_mfma_f32_32x32x16_bf16 v[80:95], v[116:119], a[84:87], v[80:95]\n"
    "ds_read_b64_tr_b16 v[114:115], v237 offset:256\n"
    "s_add_u32 m0, s64, 16384\n"
    "s_mov_b32 s65, 3539200\n"
    "buffer_load_dwordx4 v239, s[44:47], s65 offen offset:0 lds\n"
    "v_add_f32 v127, 1.0, v127\n"
    "s_waitcnt lgkmcnt(2)\n"
    "v_mfma_f32_32x32x16_bf16 v[64:79], v[120:123], a[88:91], v[64:79]\n"
    "ds_read_b64_tr_b16 v[116:117], v237 offset:2048\n"
    "v_exp_f32 v128, v128\n"
    "v_mul_f32 v129, s80, v44\n"
    "v_mul_f32 v39, v124, v39\n"
    "v_fma_f32 v125, v125, v247, v245\n"
    "v_rcp_f32 v126, v126\n"
    "v_mfma_f32_32x32x16_bf16 v[80:95], v[120:123], a[92:95], v[80:95]\n"
    "ds_read_b64_tr_b16 v[118:119], v237 offset:2304\n"
    "v_fma_f32 v127, v127, v127, 1.0\n"
    "v_add_f32 v128, 1.0, v128\n"
    "v_exp_f32 v129, v129\n"
    "v_mul_f32 v130, s80, v45\n"
    "v_cvt_pk_bf16_f32 v137, v38, v39\n"
    "s_waitcnt lgkmcnt(2)\n"
    "v_mfma_f32_32x32x16_bf16 v[64:79], v[112:115], a[96:99], v[64:79]\n"
    "ds_read_b64_tr_b16 v[120:121], v237 offset:4096\n"
    "s_add_u32 m0, s64, 20480\n"
    "s_mov_b32 s65, 3834112\n"
    "buffer_load_dwordx4 v239, s[44:47], s65 offen offset:0 lds\n"
    "v_mul_f32 v40, v125, v40\n"
    "v_mfma_f32_32x32x16_bf16 v[80:95], v[112:115], a[100:103], v[80:95]\n"
    "ds_read_b64_tr_b16 v[122:123], v237 offset:4352\n"
    "v_fma_f32 v126, v126, v247, v245\n"
    "v_rcp_f32 v127, v127\n"
    "v_fma_f32 v128, v128, v128, 1.0\n"
    "v_add_f32 v129, 1.0, v129\n"
    "v_exp_f32 v130, v130\n"
    "s_waitcnt lgkmcnt(2)\n"
    "v_mfma_f32_32x32x16_bf16 v[64:79], v[116:119], a[104:107], v[64:79]\n"
    "ds_read_b64_tr_b16 v[112:113], v237 offset:6144\n"
    "v_mul_f32 v124, s80, v46\n"
    "v_mul_f32 v41, v126, v41\n"
    "v_fma_f32 v127, v127, v247, v245\n"
    "v_rcp_f32 v128, v128\n"
    "v_fma_f32 v129, v129, v129, 1.0\n"
    "v_add_f32 v130, 1.0, v130\n"
    "v_mfma_f32_32x32x16_bf16 v[80:95], v[116:119], a[108:111], v[80:95]\n"
    "ds_read_b64_tr_b16 v[114:115], v237 offset:6400\n"
    "v_exp_f32 v124, v124\n"
    "v_mul_f32 v125, s80, v47\n"
    "v_cvt_pk_bf16_f32 v138, v40, v41\n"
    "v_mul_f32 v42, v127, v42\n"
    "v_fma_f32 v128, v128, v247, v245\n"
    "s_waitcnt lgkmcnt(2)\n"
    "v_mfma_f32_32x32x16_bf16 v[64:79], v[120:123], a[112:115], v[64:79]\n"
    "ds_read_b64_tr_b16 v[116:117], v237 offset:8192\n"
    "s_add_u32 m0, s64, 24576\n"
    "s_mov_b32 s65, 4129024\n"
    "buffer_load_dwordx4 v239, s[44:47], s65 offen offset:0 lds\n"
    "v_mfma_f32_32x32x16_bf16 v[80:95], v[120:123], a[116:119], v[80:95]\n"
    "ds_read_b64_tr_b16 v[118:119], v237 offset:8448\n"
    "ds_read_b128 v[100:103], v244 offset:544\n"
    "v_rcp_f32 v129, v129\n"
    "v_fma_f32 v130, v130, v130, 1.0\n"
    "v_add_f32 v124, 1.0, v124\n"
    "v_exp_f32 v125, v125\n"
    "s_waitcnt lgkmcnt(3)\n"
    "v_mfma_f32_32x32x16_bf16 v[64:79], v[112:115], a[120:123], v[64:79]\n"
    "ds_read_b64_tr_b16 v[120:121], v237 offset:10240\n"
    "v_mul_f32 v43, v128, v43\n"
    "v_fma_f32 v129, v129, v247, v245\n"
    "v_rcp_f32 v130, v130\n"
    "v_fma_f32 v124, v124, v124, 1.0\n"
    "v_add_f32 v125, 1.0, v125\n"
    "v_cvt_pk_bf16_f32 v139, v42, v43\n"
    "v_mfma_f32_32x32x16_bf16 v[80:95], v[112:115], a[124:127], v[80:95]\n"
    "ds_read_b64_tr_b16 v[122:123], v237 offset:10496\n"
    "v_mul_f32 v44, v129, v44\n"
    "v_fma_f32 v130, v130, v247, v245\n"
    "v_rcp_f32 v124, v124\n"
    "v_fma_f32 v125, v125, v125, 1.0\n"
    "v_mul_f32 v45, v130, v45\n"
    "s_waitcnt lgkmcnt(3)\n"
    "v_mfma_f32_32x32x16_bf16 v[64:79], v[116:119], a[128:131], v[64:79]\n"
    "ds_read_b64_tr_b16 v[112:113], v237 offset:12288\n"
    "s_add_u32 m0, s64, 28672\n"
    "s_mov_b32 s65, 4423936\n"
    "buffer_load_dwordx4 v239, s[44:47], s65 offen offset:0 lds\n"
    "v_mfma_f32_32x32x16_bf16 v[80:95], v[116:119], a[132:135], v[80:95]\n"
    "ds_read_b64_tr_b16 v[114:115], v237 offset:12544\n"
    "v_fma_f32 v124, v124, v247, v245\n"
    "v_rcp_f32 v125, v125\n"
    "v_cvt_pk_bf16_f32 v140, v44, v45\n"
    "v_mul_f32 v46, v124, v46\n"
    "v_fma_f32 v125, v125, v247, v245\n"
    "v_mul_f32 v47, v125, v47\n"
    "s_waitcnt lgkmcnt(2)\n"
    "v_mfma_f32_32x32x16_bf16 v[64:79], v[120:123], a[136:139], v[64:79]\n"
    "ds_read_b64_tr_b16 v[116:117], v237 offset:14336\n"
    "v_cvt_pk_bf16_f32 v141, v46, v47\n"
    "v_permlane32_swap_b32 v134, v138\n"
    "v_permlane32_swap_b32 v135, v139\n"
    "v_permlane32_swap_b32 v136, v140\n"
    "v_permlane32_swap_b32 v137, v141\n"
    "v_permlane16_swap_b32 v134, v138\n"
    "v_permlane16_swap_b32 v135, v139\n"
    "v_mfma_f32_32x32x16_bf16 v[80:95], v[120:123], a[140:143], v[80:95]\n"
    "ds_read_b64_tr_b16 v[118:119], v237 offset:14592\n"
    "v_permlane16_swap_b32 v136, v140\n"
    "v_permlane16_swap_b32 v137, v141\n"
    "s_add_u32 s60, s72, 0\n"
    "buffer_store_dwordx4 v[134:137], v241, s[56:59], s60 offen offset:0 sc1\n"
    "s_waitcnt lgkmcnt(2)\n"
    "v_mfma_f32_32x32x16_bf16 v[64:79], v[112:115], a[144:147], v[64:79]\n"
    "ds_read_b64_tr_b16 v[120:121], v237 offset:16384\n"
    "s_add_u32 m0, s64, 32768\n"
    "s_mov_b32 s65, 4718848\n"
    "buffer_load_dwordx4 v239, s[44:47], s65 offen offset:0 lds\n"
    "v_mfma_f32_32x32x16_bf16 v[80:95], v[112:115], a[148:151], v[80:95]\n"
    "ds_read_b64_tr_b16 v[122:123], v237 offset:16640\n"
    "buffer_store_dwordx4 v[138:141], v241, s[56:59], s60 offen offset:1024 sc1\n"
    "v_mul_f32 v124, s80, v48\n"
    "v_exp_f32 v124, v124\n"
    "v_mul_f32 v125, s80, v49\n"
    "s_waitcnt lgkmcnt(2)\n"
    "v_mfma_f32_32x32x16_bf16 v[64:79], v[116:119], a[152:155], v[64:79]\n"
    "ds_read_b64_tr_b16 v[112:113], v237 offset:18432\n"
    "v_add_f32 v124, 1.0, v124\n"
    "v_exp_f32 v125, v125\n"
    "v_mul_f32 v126, s80, v50\n"
    "v_fma_f32 v124, v124, v124, 1.0\n"
    "v_add_f32 v125, 1.0, v125\n"
    "v_exp_f32 v126, v126\n"
    "v_mfma_f32_32x32x16_bf16 v[80:95], v[116:119], a[156:159], v[80:95]\n"
    "ds_read_b64_tr_b16 v[114:115], v237 offset:18688\n"
    "v_mul_f32 v127, s80, v51\n"
    "v_rcp_f32 v124, v124\n"
    "v_fma_f32 v125, v125, v125, 1.0\n"
    "v_add_f32 v126, 1.0, v126\n"
    "s_waitcnt lgkmcnt(2)\n"
    "v_mfma_f32_32x32x16_bf16 v[64:79], v[120:123], a[160:163], v[64:79]\n"
    "ds_read_b64_tr_b16 v[116:117], v237 offset:20480\n"
    "s_add_u32 m0, s64, 36864\n"
    "s_mov_b32 s65, 5013760\n"
    "buffer_load_dwordx4 v239, s[44:47], s65 offen offset:0 lds\n"
    "v_exp_f32 v127, v127\n"
    "v_mfma_f32_32x32x16_bf16 v[80:95], v[120:123], a[164:167], v[80:95]\n"
    "ds_read_b64_tr_b16 v[118:119], v237 offset:20736\n"
    "v_mul_f32 v128, s80, v52\n"
    "v_fma_f32 v124, v124, v248, v246\n"
    "v_rcp_f32 v125, v125\n"
    "v_fma_f32 v126, v126, v126, 1.0\n"
    "v_add_f32 v127, 1.0, v127\n"
    "s_waitcnt lgkmcnt(2)\n"
    "v_mfma_f32_32x32x16_bf16 v[64:79], v[112:115], a[168:171], v[64:79]\n"
    "ds_read_b64_tr_b16 v[120:121], v237 offset:22528\n"
    "v_exp_f32 v128, v128\n"
    "v_mul_f32 v129, s80, v53\n"
    "v_mul_f32 v48, v124, v48\n"
    "v_fma_f32 v125, v125, v248, v246\n"
    "v_rcp_f32 v126, v126\n"
    "v_mfma_f32_32x32x16_bf16 v[80:95], v[112:115], a[172:175], v[80:95]\n"
    "ds_read_b64_tr_b16 v[122:123], v237 offset:22784\n"
    "ds_read_b128 v[104:107], v244 offset:576\n"
    "v_fma_f32 v127, v127, v127, 1.0\n"
    "v_add_f32 v128, 1.0, v128\n"
    "v_exp_f32 v129, v129\n"
    "v_mul_f32 v130, s80, v54\n"
    "s_waitcnt lgkmcnt(3)\n"
    "v_mfma_f32_32x32x16_bf16 v[64:79], v[116:119], a[176:179], v[64:79]\n"
    "s_waitcnt vmcnt(20)\n"
    "s_barrier\n"
    "ds_read_b64_tr_b16 v[112:113], v237 offset:24576\n"
    "v_mul_f32 v49, v125, v49\n"
    "v_fma_f32 v126, v126, v248, v246\n"
    "v_rcp_f32 v127, v127\n"
    "v_mfma_f32_32x32x16_bf16 v[80:95], v[116:119], a[180:183], v[80:95]\n"
    "ds_read_b64_tr_b16 v[114:115], v237 offset:24832\n"
    "s_add_u32 m0, s64, 40960\n"
    "s_mov_b32 s65, 384\n"
    "buffer_load_dwordx4 v239, s[44:47], s65 offen offset:0 lds\n"
    "s_waitcnt lgkmcnt(3)\n"
    "v_mfma_f32_32x32x16_bf16 v[64:79], v[120:123], a[184:187], v[64:79]\n"
    "ds_read_b64_tr_b16 v[116:117], v237 offset:26624\n"
    "v_fma_f32 v128, v128, v128, 1.0\n"
    "v_add_f32 v129, 1.0, v129\n"
    "v_exp_f32 v130, v130\n"
    "v_mul_f32 v124, s80, v55\n"
    "v_cvt_pk_bf16_f32 v142, v48, v49\n"
    "v_mul_f32 v50, v126, v50\n"
    "v_mfma_f32_32x32x16_bf16 v[80:95], v[120:123], a[188:191], v[80:95]\n"
    "ds_read_b64_tr_b16 v[118:119], v237 offset:26880\n"
    "v_fma_f32 v127, v127, v248, v246\n"
    "v_rcp_f32 v128, v128\n"
    "v_fma_f32 v129, v129, v129, 1.0\n"
    "v_add_f32 v130, 1.0, v130\n"
    "v_exp_f32 v124, v124\n"
    "s_waitcnt lgkmcnt(2)\n"
    "v_mfma_f32_32x32x16_bf16 v[64:79], v[112:115], a[192:195], v[64:79]\n"
    "ds_read_b64_tr_b16 v[120:121], v237 offset:28672\n"
    "s_add_u32 m0, s64, 45056\n"
    "s_mov_b32 s65, 295296\n"
    "buffer_load_dwordx4 v239, s[44:47], s65 offen offset:0 lds\n"
    "v_mfma_f32_32x32x16_bf16 v[80:95], v[112:115], a[196:199], v[80:95]\n"
    "ds_read_b64_tr_b16 v[122:123], v237 offset:28928\n"
    "v_mul_f32 v125, s80, v56\n"
    "v_mul_f32 v51, v127, v51\n"
    "v_fma_f32 v128, v128, v248, v246\n"
    "v_rcp_f32 v129, v129\n"
    "v_fma_f32 v130, v130, v130, 1.0\n"
    "s_waitcnt lgkmcnt(2)\n"
    "v_mfma_f32_32x32x16_bf16 v[64:79], v[116:119], a[200:203], v[64:79]\n"
    "ds_read_b64_tr_b16 v[112:113], v237 offset:30720\n"
    "v_add_f32 v124, 1.0, v124\n"
    "v_exp_f32 v125, v125\n"
    "v_mul_f32 v126, s80, v57\n"
    "v_cvt_pk_bf16_f32 v143, v50, v51\n"
    "v_mul_f32 v52, v128, v52\n"
    "v_fma_f32 v129, v129, v248, v246\n"
    "v_mfma_f32_32x32x16_bf16 v[80:95], v[116:119], a[204:207], v[80:95]\n"
    "ds_read_b64_tr_b16 v[114:115], v237 offset:30976\n"
    "v_rcp_f32 v130, v130\n"
    "v_fma_f32 v124, v124, v124, 1.0\n"
    "v_add_f32 v125, 1.0, v125\n"
    "v_exp_f32 v126, v126\n"
    "v_mul_f32 v127, s80, v58\n"
    "s_waitcnt lgkmcnt(2)\n"
    "v_mfma_f32_32x32x16_bf16 v[64:79], v[120:123], a[208:211], v[64:79]\n"
    "ds_read_b64_tr_b16 v[116:117], v237 offset:32768\n"
    "s_add_u32 m0, s64, 49152\n"
    "s_mov_b32 s65, 590208\n"
    "buffer_load_dwordx4 v239, s[44:47], s65 offen offset:0 lds\n"
    "v_mul_f32 v53, v129, v53\n"
    "v_mfma_f32_32x32x16_bf16 v[80:95], v[120:123], a[212:215], v[80:95]\n"
    "ds_read_b64_tr_b16 v[118:119], v237 offset:33024\n"
    "v_fma_f32 v130, v130, v248, v246\n"
    "v_rcp_f32 v124, v124\n"
    "v_fma_f32 v125, v125, v125, 1.0\n"
    "v_add_f32 v126, 1.0, v126\n"
    "s_waitcnt lgkmcnt(2)\n"
    "v_mfma_f32_32x32x16_bf16 v[64:79], v[112:115], a[216:219], v[64:79]\n"
    "ds_read_b64_tr_b16 v[120:121], v237 offset:34816\n"
    "v_exp_f32 v127, v127\n"
    "v_mul_f32 v128, s80, v59\n"
    "v_cvt_pk_bf16_f32 v144, v52, v53\n"
    "v_mul_f32 v54, v130, v54\n"
    "v_fma_f32 v124, v124, v248, v246\n"
    "v_rcp_f32 v125, v125\n"
    "v_mfma_f32_32x32x16_bf16 v[80:95], v[112:115], a[220:223], v[80:95]\n"
    "ds_read_b64_tr_b16 v[122:123], v237 offset:35072\n"
    "v_fma_f32 v126, v126, v126, 1.0\n"
    "v_add_f32 v127, 1.0, v127\n"
    "v_exp_f32 v128, v128\n"
    "v_mul_f32 v129, s80, v60\n"
    "v_mul_f32 v55, v124, v55\n"
    "v_fma_f32 v125, v125, v248, v246\n"
    "s_waitcnt lgkmcnt(2)\n"
    "v_mfma_f32_32x32x16_bf16 v[64:79], v[116:119], a[224:227], v[64:79]\n"
    "ds_read_b64_tr_b16 v[112:113], v237 offset:36864\n"
    "s_add_u32 m0, s64, 53248\n"
    "s_mov_b32 s65, 885120\n"
    "buffer_load_dwordx4 v239, s[44:47], s65 offen offset:0 lds\n"
    "v_mfma_f32_32x32x16_bf16 v[80:95], v[116:119], a[228:231], v[80:95]\n"
    "ds_read_b64_tr_b16 v[114:115], v237 offset:37120\n"
    "ds_read_b128 v[108:111], v244 offset:608\n"
    "v_rcp_f32 v126, v126\n"
    "v_fma_f32 v127, v127, v127, 1.0\n"
    "v_add_f32 v128, 1.0, v128\n"
    "s_waitcnt lgkmcnt(3)\n"
    "v_mfma_f32_32x32x16_bf16 v[64:79], v[120:123], a[232:235], v[64:79]\n"
    "ds_read_b64_tr_b16 v[116:117], v237 offset:38912\n"
    "v_exp_f32 v129, v129\n"
    "v_mul_f32 v130, s80, v61\n"
    "v_cvt_pk_bf16_f32 v145, v54, v55\n"
    "v_mul_f32 v56, v125, v56\n"
    "v_fma_f32 v126, v126, v248, v246\n"
    "v_rcp_f32 v127, v127\n"
    "v_mfma_f32_32x32x16_bf16 v[80:95], v[120:123], a[236:239], v[80:95]\n"
    "ds_read_b64_tr_b16 v[118:119], v237 offset:39168\n"
    "v_fma_f32 v128, v128, v128, 1.0\n"
    "v_add_f32 v129, 1.0, v129\n"
    "v_exp_f32 v130, v130\n"
    "v_mul_f32 v124, s80, v62\n"
    "v_mul_f32 v57, v126, v57\n"
    "v_fma_f32 v127, v127, v248, v246\n"
    "s_waitcnt lgkmcnt(3)\n"
    "v_mfma_f32_32x32x16_bf16 v[64:79], v[112:115], a[240:243], v[64:79]\n"
    "ds_read_b64_tr_b16 v[120:121], v237 offset:40960\n"
    "s_add_u32 m0, s64, 57344\n"
    "s_mov_b32 s65, 1180032\n"
    "buffer_load_dwordx4 v239, s[44:47], s65 offen offset:0 lds\n"
    "v_mfma_f32_32x32x16_bf16 v[80:95], v[112:115], a[244:247], v[80:95]\n"
    "ds_read_b64_tr_b16 v[122:123], v237 offset:41216\n"
    "v_rcp_f32 v128, v128\n"
    "v_fma_f32 v129, v129, v129, 1.0\n"
    "v_add_f32 v130, 1.0, v130\n"
    "v_exp_f32 v124, v124\n"
    "s_waitcnt lgkmcnt(2)\n"
    "v_mfma_f32_32x32x16_bf16 v[64:79], v[116:119], a[248:251], v[64:79]\n"
    "ds_read_b64_tr_b16 v[112:113], v237 offset:43008\n"
    "v_mul_f32 v125, s80, v63\n"
    "v_cvt_pk_bf16_f32 v146, v56, v57\n"
    "v_mul_f32 v58, v127, v58\n"
    "v_fma_f32 v128, v128, v248, v246\n"
    "v_rcp_f32 v129, v129\n"
    "v_fma_f32 v130, v130, v130, 1.0\n"
    "v_add_f32 v124, 1.0, v124\n"
    "v_mfma_f32_32x32x16_bf16 v[80:95], v[116:119], a[252:255], v[80:95]\n"
    "ds_read_b64_tr_b16 v[114:115], v237 offset:43264\n"
    "v_exp_f32 v125, v125\n"
    "v_mul_f32 v59, v128, v59\n"
    "v_fma_f32 v129, v129, v248, v246\n"
    "v_rcp_f32 v130, v130\n"
    "s_waitcnt lgkmcnt(2)\n"
    "v_mfma_f32_32x32x16_bf16 v[64:79], v[120:123], v[0:3], v[64:79]\n"
    "ds_read_b64_tr_b16 v[116:117], v237 offset:45056\n"
    "s_add_u32 m0, s64, 61440\n"
    "s_mov_b32 s65, 1474944\n"
    "buffer_load_dwordx4 v239, s[44:47], s65 offen offset:0 lds\n"
    "v_fma_f32 v124, v124, v124, 1.0\n"
    "v_mfma_f32_32x32x16_bf16 v[80:95], v[120:123], v[4:7], v[80:95]\n"
    "ds_read_b64_tr_b16 v[118:119], v237 offset:45312\n"
    "v_add_f32 v125, 1.0, v125\n"
    "v_cvt_pk_bf16_f32 v147, v58, v59\n"
    "v_mul_f32 v60, v129, v60\n"
    "v_fma_f32 v130, v130, v248, v246\n"
    "v_rcp_f32 v124, v124\n"
    "s_waitcnt lgkmcnt(2)\n"
    "v_mfma_f32_32x32x16_bf16 v[64:79], v[112:115], v[8:11], v[64:79]\n"
    "ds_read_b64_tr_b16 v[120:121], v237 offset:47104\n"
    "v_fma_f32 v125, v125, v125, 1.0\n"
    "v_mul_f32 v61, v130, v61\n"
    "v_fma_f32 v124, v124, v248, v246\n"
    "v_rcp_f32 v125, v125\n"
    "v_cvt_pk_bf16_f32 v148, v60, v61\n"
    "v_mul_f32 v62, v124, v62\n"
    "v_mfma_f32_32x32x16_bf16 v[80:95], v[112:115], v[12:15], v[80:95]\n"
    "ds_read_b64_tr_b16 v[122:123], v237 offset:47360\n"
    "v_fma_f32 v125, v125, v248, v246\n"
    "v_mul_f32 v63, v125, v63\n"
    "v_cvt_pk_bf16_f32 v149, v62, v63\n"
    "v_permlane32_swap_b32 v142, v146\n"
    "v_permlane32_swap_b32 v143, v147\n"
    "v_permlane32_swap_b32 v144, v148\n"
    "v_permlane32_swap_b32 v145, v149\n"
    "s_waitcnt lgkmcnt(2)\n"
    "v_mfma_f32_32x32x16_bf16 v[64:79], v[116:119], v[16:19], v[64:79]\n"
    "s_waitcnt vmcnt(20)\n"
    "s_barrier\n"
    "ds_read_b64_tr_b16 v[112:113], v237 offset:49152\n"
    "v_permlane16_swap_b32 v142, v146\n"
    "v_permlane16_swap_b32 v143, v147\n"
    "v_permlane16_swap_b32 v144, v148\n"
    "v_mfma_f32_32x32x16_bf16 v[80:95], v[116:119], v[20:23], v[80:95]\n"
    "ds_read_b64_tr_b16 v[114:115], v237 offset:49408\n"
    "s_add_u32 m0, s64, 65536\n"
    "s_mov_b32 s65, 1769856\n"
    "buffer_load_dwordx4 v239, s[44:47], s65 offen offset:0 lds\n"
    "s_waitcnt lgkmcnt(2)\n"
    "v_mfma_f32_32x32x16_bf16 v[64:79], v[120:123], v[24:27], v[64:79]\n"
    "ds_read_b64_tr_b16 v[116:117], v237 offset:51200\n"
    "v_permlane16_swap_b32 v145, v149\n"
    "s_add_u32 s60, s72, 0\n"
    "buffer_store_dwordx4 v[142:145], v241, s[56:59], s60 offen offset:2048 sc1\n"
    "buffer_store_dwordx4 v[146:149], v241, s[56:59], s60 offen offset:3072 sc1\n"
    "v_mfma_f32_32x32x16_bf16 v[80:95], v[120:123], v[28:31], v[80:95]\n"
    "ds_read_b64_tr_b16 v[118:119], v237 offset:51456\n"
    "s_waitcnt lgkmcnt(2)\n"
    "v_mfma_f32_32x32x16_bf16 v[32:47], v[112:115], a[0:3], v[96:111]\n"
    "ds_read_b64_tr_b16 v[120:121], v237 offset:53248\n"
    "s_add_u32 m0, s64, 69632\n"
    "s_mov_b32 s65, 2064768\n"
    "buffer_load_dwordx4 v239, s[44:47], s65 offen offset:0 lds\n"
    "v_mfma_f32_32x32x16_bf16 v[48:63], v[112:115], a[4:7], v[96:111]\n"
    "ds_read_b64_tr_b16 v[122:123], v237 offset:53504\n"
    "s_waitcnt lgkmcnt(2)\n"
    "v_mfma_f32_32x32x16_bf16 v[32:47], v[116:119], a[8:11], v[32:47]\n"
    "ds_read_b64_tr_b16 v[112:113], v237 offset:55296\n"
    "v_mfma_f32_32x32x16_bf16 v[48:63], v[116:119], a[12:15], v[48:63]\n"
    "ds_read_b64_tr_b16 v[114:115], v237 offset:55552\n"
    "v_mul_f32 v124, s80, v64\n"
    "v_exp_f32 v124, v124\n"
    "v_mul_f32 v125, s80, v65\n"
    "v_add_f32 v124, 1.0, v124\n"
    "v_exp_f32 v125, v125\n"
    "s_waitcnt lgkmcnt(2)\n"
    "v_mfma_f32_32x32x16_bf16 v[32:47], v[120:123], a[16:19], v[32:47]\n"
    "ds_read_b64_tr_b16 v[116:117], v237 offset:57344\n"
    "s_add_u32 m0, s64, 73728\n"
    "s_mov_b32 s65, 2359680\n"
    "buffer_load_dwordx4 v239, s[44:47], s65 offen offset:0 lds\n"
    "v_mfma_f32_32x32x16_bf16 v[48:63], v[120:123], a[20:23], v[48:63]\n"
    "ds_read_b64_tr_b16 v[118:119], v237 offset:57600\n"
    "v_mul_f32 v126, s80, v66\n"
    "v_fma_f32 v124, v124, v124, 1.0\n"
    "v_add_f32 v125, 1.0, v125\n"
    "v_exp_f32 v126, v126\n"
    "v_mul_f32 v127, s80, v67\n"
    "s_waitcnt lgkmcnt(2)\n"
    "v_mfma_f32_32x32x16_bf16 v[32:47], v[112:115], a[24:27], v[32:47]\n"
    "ds_read_b64_tr_b16 v[120:121], v237 offset:59392\n"
    "v_rcp_f32 v124, v124\n"
    "v_fma_f32 v125, v125, v125, 1.0\n"
    "v_add_f32 v126, 1.0, v126\n"
    "v_exp_f32 v127, v127\n"
    "v_mul_f32 v128, s80, v68\n"
    "v_fma_f32 v124, v124, v247, v245\n"
    "v_mfma_f32_32x32x16_bf16 v[48:63], v[112:115], a[28:31], v[48:63]\n"
    "ds_read_b64_tr_b16 v[122:123], v237 offset:59648\n"
    "v_rcp_f32 v125, v125\n"
    "v_fma_f32 v126, v126, v126, 1.0\n"
    "v_add_f32 v127, 1.0, v127\n"
    "v_exp_f32 v128, v128\n"
    "s_waitcnt lgkmcnt(2)\n"
    "v_mfma_f32_32x32x16_bf16 v[32:47], v[116:119], a[32:35], v[32:47]\n"
    "ds_read_b64_tr_b16 v[112:113], v237 offset:61440\n"
    "s_add_u32 m0, s64, 77824\n"
    "s_mov_b32 s65, 2654592\n"
    "buffer_load_dwordx4 v239, s[44:47], s65 offen offset:0 lds\n"
    "v_mul_f32 v129, s80, v69\n"
    "v_mfma_f32_32x32x16_bf16 v[48:63], v[116:119], a[36:39], v[48:63]\n"
    "ds_read_b64_tr_b16 v[114:115], v237 offset:61696\n"
    "v_mul_f32 v64, v124, v64\n"
    "v_fma_f32 v125, v125, v247, v245\n"
    "v_rcp_f32 v126, v126\n"
    "v_fma_f32 v127, v127, v127, 1.0\n"
    "v_add_f32 v128, 1.0, v128\n"
    "s_waitcnt lgkmcnt(2)\n"
    "v_mfma_f32_32x32x16_bf16 v[32:47], v[120:123], a[40:43], v[32:47]\n"
    "ds_read_b64_tr_b16 v[116:117], v237 offset:63488\n"
    "v_exp_f32 v129, v129\n"
    "v_mul_f32 v130, s80, v70\n"
    "v_mul_f32 v65, v125, v65\n"
    "v_fma_f32 v126, v126, v247, v245\n"
    "v_rcp_f32 v127, v127\n"
    "v_fma_f32 v128, v128, v128, 1.0\n"
    "v_mfma_f32_32x32x16_bf16 v[48:63], v[120:123], a[44:47], v[48:63]\n"
    "ds_read_b64_tr_b16 v[118:119], v237 offset:63744\n"
    "v_add_f32 v129, 1.0, v129\n"
    "v_exp_f32 v130, v130\n"
    "v_mul_f32 v124, s80, v71\n"
    "v_cvt_pk_bf16_f32 v134, v64, v65\n"
    "v_mul_f32 v66, v126, v66\n"
    "s_waitcnt lgkmcnt(2)\n"
    "v_mfma_f32_32x32x16_bf16 v[32:47], v[112:115], a[48:51], v[32:47]\n"
    "ds_read_b64_tr_b16 v[120:121], v238 offset:0\n"
    "s_add_u32 m0, s64, 81920\n"
    "s_mov_b32 s65, 2949504\n"
    "buffer_load_dwordx4 v239, s[44:47], s65 offen offset:0 lds\n"
    "v_fma_f32 v127, v127, v247, v245\n"
    "v_mfma_f32_32x32x16_bf16 v[48:63], v[112:115], a[52:55], v[48:63]\n"
    "ds_read_b64_tr_b16 v[122:123], v238 offset:256\n"
    "v_rcp_f32 v128, v128\n"
    "v_fma_f32 v129, v129, v129, 1.0\n"
    "v_add_f32 v130, 1.0, v130\n"
    "v_exp_f32 v124, v124\n"
    "s_waitcnt lgkmcnt(2)\n"
    "v_mfma_f32_32x32x16_bf16 v[32:47], v[116:119], a[56:59], v[32:47]\n"
    "ds_read_b64_tr_b16 v[112:113], v238 offset:2048\n"
    "v_mul_f32 v125, s80, v72\n"
    "v_mul_f32 v67, v127, v67\n"
    "v_fma_f32 v128, v128, v247, v245\n"
    "v_rcp_f32 v129, v129\n"
    "v_fma_f32 v130, v130, v130, 1.0\n"
    "v_add_f32 v124, 1.0, v124\n"
    "v_mfma_f32_32x32x16_bf16 v[48:63], v[116:119], a[60:63], v[48:63]\n"
    "ds_read_b64_tr_b16 v[114:115], v238 offset:2304\n"
    "ds_read_b128 v[96:99], v244 offset:768\n"
    "v_exp_f32 v125, v125\n"
    "v_mul_f32 v126, s80, v73\n"
    "v_cvt_pk_bf16_f32 v135, v66, v67\n"
    "v_mul_f32 v68, v128, v68\n"
    "v_fma_f32 v129, v129, v247, v245\n"
    "s_waitcnt lgkmcnt(3)\n"
    "v_mfma_f32_32x32x16_bf16 v[32:47], v[120:123], a[64:67], v[32:47]\n"
    "ds_read_b64_tr_b16 v[116:117], v238 offset:12288\n"
    "s_add_u32 m0, s64, 86016\n"
    "s_mov_b32 s65, 3244416\n"
    "buffer_load_dwordx4 v239, s[44:47], s65 offen offset:0 lds\n"
    "v_mfma_f32_32x32x16_bf16 v[48:63], v[120:123], a[68:71], v[48:63]\n"
    "ds_read_b64_tr_b16 v[118:119], v238 offset:12544\n"
    "v_rcp_f32 v130, v130\n"
    "v_fma_f32 v124, v124, v124, 1.0\n"
    "v_add_f32 v125, 1.0, v125\n"
    "v_exp_f32 v126, v126\n"
    "s_waitcnt lgkmcnt(3)\n"
    "v_mfma_f32_32x32x16_bf16 v[32:47], v[112:115], a[72:75], v[32:47]\n"
    "ds_read_b64_tr_b16 v[120:121], v238 offset:14336\n"
    "v_mul_f32 v127, s80, v74\n"
    "v_mul_f32 v69, v129, v69\n"
    "v_fma_f32 v130, v130, v247, v245\n"
    "v_rcp_f32 v124, v124\n"
    "v_fma_f32 v125, v125, v125, 1.0\n"
    "v_add_f32 v126, 1.0, v126\n"
    "v_exp_f32 v127, v127\n"
    "v_mfma_f32_32x32x16_bf16 v[48:63], v[112:115], a[76:79], v[48:63]\n"
    "ds_read_b64_tr_b16 v[122:123], v238 offset:14592\n"
    "v_mul_f32 v128, s80, v75\n"
    "v_cvt_pk_bf16_f32 v136, v68, v69\n"
    "v_mul_f32 v70, v130, v70\n"
    "v_fma_f32 v124, v124, v247, v245\n"
    "v_rcp_f32 v125, v125\n"
    "s_waitcnt lgkmcnt(2)\n"
    "v_mfma_f32_32x32x16_bf16 v[32:47], v[116:119], a[80:83], v[32:47]\n"
    "s_waitcnt vmcnt(22)\n"
    "s_barrier\n"
    "ds_read_b64_tr_b16 v[112:113], v236 offset:0\n"
    "v_fma_f32 v126, v126, v126, 1.0\n"
    "v_add_f32 v127, 1.0, v127\n"
    "v_mfma_f32_32x32x16_bf16 v[48:63], v[116:119], a[84:87], v[48:63]\n"
    "ds_read_b64_tr_b16 v[114:115], v236 offset:256\n"
    "s_add_u32 m0, s64, 90112\n"
    "s_mov_b32 s65, 3539328\n"
    "buffer_load_dwordx4 v239, s[44:47], s65 offen offset:0 lds\n"
    "v_exp_f32 v128, v128\n"
    "s_waitcnt lgkmcnt(2)\n"
    "v_mfma_f32_32x32x16_bf16 v[32:47], v[120:123], a[88:91], v[32:47]\n"
    "ds_read_b64_tr_b16 v[116:117], v236 offset:2048\n"
    "v_mul_f32 v129, s80, v76\n"
    "v_mul_f32 v71, v124, v71\n"
    "v_fma_f32 v125, v125, v247, v245\n"
    "v_rcp_f32 v126, v126\n"
    "v_fma_f32 v127, v127, v127, 1.0\n"
    "v_add_f32 v128, 1.0, v128\n"
    "v_mfma_f32_32x32x16_bf16 v[48:63], v[120:123], a[92:95], v[48:63]\n"
    "ds_read_b64_tr_b16 v[118:119], v236 offset:2304\n"
    "v_exp_f32 v129, v129\n"
    "v_mul_f32 v130, s80, v77\n"
    "v_cvt_pk_bf16_f32 v137, v70, v71\n"
    "v_mul_f32 v72, v125, v72\n"
    "v_fma_f32 v126, v126, v247, v245\n"
    "s_waitcnt lgkmcnt(2)\n"
    "v_mfma_f32_32x32x16_bf16 v[32:47], v[112:115], a[96:99], v[32:47]\n"
    "ds_read_b64_tr_b16 v[120:121], v236 offset:4096\n"
    "s_add_u32 m0, s64, 94208\n"
    "s_mov_b32 s65, 3834240\n"
    "buffer_load_dwordx4 v239, s[44:47], s65 offen offset:0 lds\n"
    "v_rcp_f32 v127, v127\n"
    "v_mfma_f32_32x32x16_bf16 v[48:63], v[112:115], a[100:103], v[48:63]\n"
    "ds_read_b64_tr_b16 v[122:123], v236 offset:4352\n"
    "v_fma_f32 v128, v128, v128, 1.0\n"
    "v_add_f32 v129, 1.0, v129\n"
    "v_exp_f32 v130, v130\n"
    "v_mul_f32 v124, s80, v78\n"
    "s_waitcnt lgkmcnt(2)\n"
    "v_mfma_f32_32x32x16_bf16 v[32:47], v[116:119], a[104:107], v[32:47]\n"
    "ds_read_b64_tr_b16 v[112:113], v236 offset:6144\n"
    "v_mul_f32 v73, v126, v73\n"
    "v_fma_f32 v127, v127, v247, v245\n"
    "v_rcp_f32 v128, v128\n"
    "v_fma_f32 v129, v129, v129, 1.0\n"
    "v_add_f32 v130, 1.0, v130\n"
    "v_exp_f32 v124, v124\n"
    "v_mfma_f32_32x32x16_bf16 v[48:63], v[116:119], a[108:111], v[48:63]\n"
    "ds_read_b64_tr_b16 v[114:115], v236 offset:6400\n"
    "v_mul_f32 v125, s80, v79\n"
    "v_cvt_pk_bf16_f32 v138, v72, v73\n"
    "v_mul_f32 v74, v127, v74\n"
    "v_fma_f32 v128, v128, v247, v245\n"
    "v_rcp_f32 v129, v129\n"
    "v_fma_f32 v130, v130, v130, 1.0\n"
    "s_waitcnt lgkmcnt(2)\n"
    "v_mfma_f32_32x32x16_bf16 v[32:47], v[120:123], a[112:115], v[32:47]\n"
    "ds_read_b64_tr_b16 v[116:117], v236 offset:8192\n"
    "s_add_u32 m0, s64, 98304\n"
    "s_mov_b32 s65, 4129152\n"
    "buffer_load_dwordx4 v239, s[44:47], s65 offen offset:0 lds\n"
    "v_mfma_f32_32x32x16_bf16 v[48:63], v[120:123], a[116:119], v[48:63]\n"
    "ds_read_b64_tr_b16 v[118:119], v236 offset:8448\n"
    "ds_read_b128 v[100:103], v244 offset:800\n"
    "v_add_f32 v124, 1.0, v124\n"
    "v_exp_f32 v125, v125\n"
    "v_mul_f32 v75, v128, v75\n"
    "v_fma_f32 v129, v129, v247, v245\n"
    "s_waitcnt lgkmcnt(3)\n"
    "v_mfma_f32_32x32x16_bf16 v[32:47], v[112:115], a[120:123], v[32:47]\n"
    "ds_read_b64_tr_b16 v[120:121], v236 offset:10240\n"
    "v_rcp_f32 v130, v130\n"
    "v_fma_f32 v124, v124, v124, 1.0\n"
    "v_add_f32 v125, 1.0, v125\n"
    "v_cvt_pk_bf16_f32 v139, v74, v75\n"
    "v_mul_f32 v76, v129, v76\n"
    "v_fma_f32 v130, v130, v247, v245\n"
    "v_mfma_f32_32x32x16_bf16 v[48:63], v[112:115], a[124:127], v[48:63]\n"
    "ds_read_b64_tr_b16 v[122:123], v236 offset:10496\n"
    "v_rcp_f32 v124, v124\n"
    "v_fma_f32 v125, v125, v125, 1.0\n"
    "v_mul_f32 v77, v130, v77\n"
    "v_fma_f32 v124, v124, v247, v245\n"
    "v_rcp_f32 v125, v125\n"
    "s_waitcnt lgkmcnt(3)\n"
    "v_mfma_f32_32x32x16_bf16 v[32:47], v[116:119], a[128:131], v[32:47]\n"
    "ds_read_b64_tr_b16 v[112:113], v236 offset:12288\n"
    "s_add_u32 m0, s64, 102400\n"
    "s_mov_b32 s65, 4424064\n"
    "buffer_load_dwordx4 v239, s[44:47], s65 offen offset:0 lds\n"
    "v_cvt_pk_bf16_f32 v140, v76, v77\n"
    "v_mfma_f32_32x32x16_bf16 v[48:63], v[116:119], a[132:135], v[48:63]\n"
    "ds_read_b64_tr_b16 v[114:115], v236 offset:12544\n"
    "v_mul_f32 v78, v124, v78\n"
    "v_fma_f32 v125, v125, v247, v245\n"
    "v_mul_f32 v79, v125, v79\n"
    "v_cvt_pk_bf16_f32 v141, v78, v79\n"
    "v_permlane32_swap_b32 v134, v138\n"
    "v_permlane32_swap_b32 v135, v139\n"
    "s_waitcnt lgkmcnt(2)\n"
    "v_mfma_f32_32x32x16_bf16 v[32:47], v[120:123], a[136:139], v[32:47]\n"
    "ds_read_b64_tr_b16 v[116:117], v236 offset:14336\n"
    "v_permlane32_swap_b32 v136, v140\n"
    "v_permlane32_swap_b32 v137, v141\n"
    "v_permlane16_swap_b32 v134, v138\n"
    "v_permlane16_swap_b32 v135, v139\n"
    "v_permlane16_swap_b32 v136, v140\n"
    "v_permlane16_swap_b32 v137, v141\n"
    "v_mfma_f32_32x32x16_bf16 v[48:63], v[120:123], a[140:143], v[48:63]\n"
    "ds_read_b64_tr_b16 v[118:119], v236 offset:14592\n"
    "s_add_u32 s60, s72, 16384\n"
    "buffer_store_dwordx4 v[134:137], v241, s[56:59], s60 offen offset:0 sc1\n"
    "buffer_store_dwordx4 v[138:141], v241, s[56:59], s60 offen offset:1024 sc1\n"
    "v_mul_f32 v124, s80, v80\n"
    "s_waitcnt lgkmcnt(2)\n"
    "v_mfma_f32_32x32x16_bf16 v[32:47], v[112:115], a[144:147], v[32:47]\n"
    "ds_read_b64_tr_b16 v[120:121], v238 offset:4096\n"
    "s_add_u32 m0, s64, 106496\n"
    "s_mov_b32 s65, 4718976\n"
    "buffer_load_dwordx4 v239, s[44:47], s65 offen offset:0 lds\n"
    "v_mfma_f32_32x32x16_bf16 v[48:63], v[112:115], a[148:151], v[48:63]\n"
    "ds_read_b64_tr_b16 v[122:123], v238 offset:4352\n"
    "v_exp_f32 v124, v124\n"
    "v_mul_f32 v125, s80, v81\n"
    "v_add_f32 v124, 1.0, v124\n"
    "v_exp_f32 v125, v125\n"
    "v_mul_f32 v126, s80, v82\n"
    "s_waitcnt lgkmcnt(2)\n"
    "v_mfma_f32_32x32x16_bf16 v[32:47], v[116:119], a[152:155], v[32:47]\n"
    "ds_read_b64_tr_b16 v[112:113], v238 offset:6144\n"
    "s_waitcnt vmcnt(12)\n"
    "s_add_u32 s99, s70, 1\n"
    "v_mov_b32 v133, s21\n"
    "v_mov_b32 v132, s99\n"
    "buffer_store_dwordx2 v[132:133], v242, s[56:59], s89 offen offset:0 sc1\n"
    "v_mfma_f32_32x32x16_bf16 v[48:63], v[116:119], a[156:159], v[48:63]\n"
    "ds_read_b64_tr_b16 v[114:115], v238 offset:6400\n"
    "v_fma_f32 v124, v124, v124, 1.0\n"
    "v_add_f32 v125, 1.0, v125\n"
    "v_exp_f32 v126, v126\n"
    "v_mul_f32 v127, s80, v83\n"
    "v_rcp_f32 v124, v124\n"
    "s_waitcnt lgkmcnt(2)\n"
    "v_mfma_f32_32x32x16_bf16 v[32:47], v[120:123], a[160:163], v[32:47]\n"
    "ds_read_b64_tr_b16 v[116:117], v238 offset:8192\n"
    "s_add_u32 m0, s64, 110592\n"
    "s_mov_b32 s65, 5013888\n"
    "buffer_load_dwordx4 v239, s[44:47], s65 offen offset:0 lds\n"
    "v_mfma_f32_32x32x16_bf16 v[48:63], v[120:123], a[164:167], v[48:63]\n"
    "ds_read_b64_tr_b16 v[118:119], v238 offset:8448\n"
    "v_fma_f32 v125, v125, v125, 1.0\n"
    "v_add_f32 v126, 1.0, v126\n"
    "v_exp_f32 v127, v127\n"
    "v_mul_f32 v128, s80, v84\n"
    "v_fma_f32 v124, v124, v248, v246\n"
    "s_waitcnt lgkmcnt(2)\n"
    "v_mfma_f32_32x32x16_bf16 v[32:47], v[112:115], a[168:171], v[32:47]\n"
    "ds_read_b64_tr_b16 v[120:121], v238 offset:10240\n"
    "v_rcp_f32 v125, v125\n"
    "v_fma_f32 v126, v126, v126, 1.0\n"
    "v_add_f32 v127, 1.0, v127\n"
    "v_exp_f32 v128, v128\n"
    "v_mul_f32 v129, s80, v85\n"
    "v_mul_f32 v80, v124, v80\n"
    "v_mfma_f32_32x32x16_bf16 v[48:63], v[112:115], a[172:175], v[48:63]\n"
    "ds_read_b64_tr_b16 v[122:123], v238 offset:10496\n"
    "ds_read_b128 v[104:107], v244 offset:832\n"
    "v_fma_f32 v125, v125, v248, v246\n"
    "v_rcp_f32 v126, v126\n"
    "v_fma_f32 v127, v127, v127, 1.0\n"
    "v_add_f32 v128, 1.0, v128\n"
    "s_waitcnt lgkmcnt(3)\n"
    "v_mfma_f32_32x32x16_bf16 v[32:47], v[116:119], a[176:179], v[32:47]\n"
    "s_barrier\n"
    "ds_read_b64_tr_b16 v[112:113], v236 offset:16384\n"
    "v_exp_f32 v129, v129\n"
    "v_mul_f32 v130, s80, v86\n"
    "v_mul_f32 v81, v125, v81\n"
    "v_mfma_f32_32x32x16_bf16 v[48:63], v[116:119], a[180:183], v[48:63]\n"
    "ds_read_b64_tr_b16 v[114:115], v236 offset:16640\n"
    "s_add_u32 m0, s64, 114688\n"
    "s_mov_b32 s65, 512\n"
    "buffer_load_dwordx4 v239, s[44:47], s65 offen offset:0 lds\n"
    "s_waitcnt lgkmcnt(3)\n"
    "v_mfma_f32_32x32x16_bf16 v[32:47], v[120:123], a[184:187], v[32:47]\n"
    "ds_read_b64_tr_b16 v[116:117], v236 offset:18432\n"
    "v_fma_f32 v126, v126, v248, v246\n"
    "v_rcp_f32 v127, v127\n"
    "v_fma_f32 v128, v128, v128, 1.0\n"
    "v_add_f32 v129, 1.0, v129\n"
    "v_exp_f32 v130, v130\n"
    "v_mul_f32 v124, s80, v87\n"
    "v_mfma_f32_32x32x16_bf16 v[48:63], v[120:123], a[188:191], v[48:63]\n"
    "ds_read_b64_tr_b16 v[118:119], v236 offset:18688\n"
    "v_cvt_pk_bf16_f32 v142, v80, v81\n"
    "v_mul_f32 v82, v126, v82\n"
    "v_fma_f32 v127, v127, v248, v246\n"
    "v_rcp_f32 v128, v128\n"
    "v_fma_f32 v129, v129, v129, 1.0\n"
    "s_waitcnt lgkmcnt(2)\n"
    "v_mfma_f32_32x32x16_bf16 v[32:47], v[112:115], a[192:195], v[32:47]\n"
    "ds_read_b64_tr_b16 v[120:121], v236 offset:20480\n"
    "s_add_u32 m0, s64, 118784\n"
    "s_mov_b32 s65, 295424\n"
    "buffer_load_dwordx4 v239, s[44:47], s65 offen offset:0 lds\n"
    "v_add_f32 v130, 1.0, v130\n"
    "v_mfma_f32_32x32x16_bf16 v[48:63], v[112:115], a[196:199], v[48:63]\n"
    "ds_read_b64_tr_b16 v[122:123], v236 offset:20736\n"
    "v_exp_f32 v124, v124\n"
    "v_mul_f32 v125, s80, v88\n"
    "v_mul_f32 v83, v127, v83\n"
    "v_fma_f32 v128, v128, v248, v246\n"
    "s_waitcnt lgkmcnt(2)\n"
    "v_mfma_f32_32x32x16_bf16 v[32:47], v[116:119], a[200:203], v[32:47]\n"
    "ds_read_b64_tr_b16 v[112:113], v236 offset:22528\n"
    "v_rcp_f32 v129, v129\n"
    "v_fma_f32 v130, v130, v130, 1.0\n"
    "v_add_f32 v124, 1.0, v124\n"
    "v_exp_f32 v125, v125\n"
    "v_mul_f32 v126, s80, v89\n"
    "v_cvt_pk_bf16_f32 v143, v82, v83\n"
    "v_mul_f32 v84, v128, v84\n"
    "v_mfma_f32_32x32x16_bf16 v[48:63], v[116:119], a[204:207], v[48:63]\n"
    "ds_read_b64_tr_b16 v[114:115], v236 offset:22784\n"
    "v_fma_f32 v129, v129, v248, v246\n"
    "v_rcp_f32 v130, v130\n"
    "v_fma_f32 v124, v124, v124, 1.0\n"
    "v_add_f32 v125, 1.0, v125\n"
    "v_exp_f32 v126, v126\n"
    "s_waitcnt lgkmcnt(2)\n"
    "v_mfma_f32_32x32x16_bf16 v[32:47], v[120:123], a[208:211], v[32:47]\n"
    "ds_read_b64_tr_b16 v[116:117], v236 offset:24576\n"
    "s_add_u32 m0, s64, 122880\n"
    "s_mov_b32 s65, 590336\n"
    "buffer_load_dwordx4 v239, s[44:47], s65 offen offset:0 lds\n"
    "v_mfma_f32_32x32x16_bf16 v[48:63], v[120:123], a[212:215], v[48:63]\n"
    "ds_read_b64_tr_b16 v[118:119], v236 offset:24832\n"
    "v_mul_f32 v127, s80, v90\n"
    "v_mul_f32 v85, v129, v85\n"
    "v_fma_f32 v130, v130, v248, v246\n"
    "v_rcp_f32 v124, v124\n"
    "v_fma_f32 v125, v125, v125, 1.0\n"
    "s_waitcnt lgkmcnt(2)\n"
    "v_mfma_f32_32x32x16_bf16 v[32:47], v[112:115], a[216:219], v[32:47]\n"
    "ds_read_b64_tr_b16 v[120:121], v236 offset:26624\n"
    "v_add_f32 v126, 1.0, v126\n"
    "v_exp_f32 v127, v127\n"
    "v_mul_f32 v128, s80, v91\n"
    "v_cvt_pk_bf16_f32 v144, v84, v85\n"
    "v_mul_f32 v86, v130, v86\n"
    "v_fma_f32 v124, v124, v248, v246\n"
    "v_mfma_f32_32x32x16_bf16 v[48:63], v[112:115], a[220:223], v[48:63]\n"
    "ds_read_b64_tr_b16 v[122:123], v236 offset:26880\n"
    "v_rcp_f32 v125, v125\n"
    "v_fma_f32 v126, v126, v126, 1.0\n"
    "v_add_f32 v127, 1.0, v127\n"
    "v_exp_f32 v128, v128\n"
    "v_mul_f32 v129, s80, v92\n"
    "s_waitcnt lgkmcnt(2)\n"
    "v_mfma_f32_32x32x16_bf16 v[32:47], v[116:119], a[224:227], v[32:47]\n"
    "ds_read_b64_tr_b16 v[112:113], v236 offset:28672\n"
    "s_add_u32 m0, s64, 126976\n"
    "s_mov_b32 s65, 885248\n"
    "buffer_load_dwordx4 v239, s[44:47], s65 offen offset:0 lds\n"
    "v_mul_f32 v87, v124, v87\n"
    "v_mfma_f32_32x32x16_bf16 v[48:63], v[116:119], a[228:231], v[48:63]\n"
    "ds_read_b64_tr_b16 v[114:115], v236 offset:28928\n"
    "ds_read_b128 v[108:111], v244 offset:864\n"
    "v_fma_f32 v125, v125, v248, v246\n"
    "v_rcp_f32 v126, v126\n"
    "v_fma_f32 v127, v127, v127, 1.0\n"
    "s_waitcnt lgkmcnt(3)\n"
    "v_mfma_f32_32x32x16_bf16 v[32:47], v[120:123], a[232:235], v[32:47]\n"
    "ds_read_b64_tr_b16 v[116:117], v236 offset:30720\n"
    "v_add_f32 v128, 1.0, v128\n"
    "v_exp_f32 v129, v129\n"
    "v_mul_f32 v130, s80, v93\n"
    "v_cvt_pk_bf16_f32 v145, v86, v87\n"
    "v_mul_f32 v88, v125, v88\n"
    "v_fma_f32 v126, v126, v248, v246\n"
    "v_rcp_f32 v127, v127\n"
    "v_mfma_f32_32x32x16_bf16 v[48:63], v[120:123], a[236:239], v[48:63]\n"
    "ds_read_b64_tr_b16 v[118:119], v236 offset:30976\n"
    "v_fma_f32 v128, v128, v128, 1.0\n"
    "v_add_f32 v129, 1.0, v129\n"
    "v_exp_f32 v130, v130\n"
    "v_mul_f32 v124, s80, v94\n"
    "s_waitcnt lgkmcnt(3)\n"
    "v_mfma_f32_32x32x16_bf16 v[32:47], v[112:115], a[240:243], v[32:47]\n"
    "ds_read_b64_tr_b16 v[120:121], v236 offset:32768\n"
    "s_add_u32 m0, s64, 131072\n"
    "s_mov_b32 s65, 1180160\n"
    "buffer_load_dwordx4 v239, s[44:47], s65 offen offset:0 lds\n"
    "v_mul_f32 v89, v126, v89\n"
    "v_mfma_f32_32x32x16_bf16 v[48:63], v[112:115], a[244:247], v[48:63]\n"
    "ds_read_b64_tr_b16 v[122:123], v236 offset:33024\n"
    "v_fma_f32 v127, v127, v248, v246\n"
    "v_rcp_f32 v128, v128\n"
    "v_fma_f32 v129, v129, v129, 1.0\n"
    "v_add_f32 v130, 1.0, v130\n"
    "v_exp_f32 v124, v124\n"
    "s_waitcnt lgkmcnt(2)\n"
    "v_mfma_f32_32x32x16_bf16 v[32:47], v[116:119], a[248:251], v[32:47]\n"
    "ds_read_b64_tr_b16 v[112:113], v236 offset:34816\n"
    "v_mul_f32 v125, s80, v95\n"
    "v_cvt_pk_bf16_f32 v146, v88, v89\n"
    "v_mul_f32 v90, v127, v90\n"
    "v_fma_f32 v128, v128, v248, v246\n"
    "v_rcp_f32 v129, v129\n"
    "v_fma_f32 v130, v130, v130, 1.0\n"
    "v_mfma_f32_32x32x16_bf16 v[48:63], v[116:119], a[252:255], v[48:63]\n"
    "ds_read_b64_tr_b16 v[114:115], v236 offset:35072\n"
    "v_add_f32 v124, 1.0, v124\n"
    "v_exp_f32 v125, v125\n"
    "v_mul_f32 v91, v128, v91\n"
    "v_fma_f32 v129, v129, v248, v246\n"
    "v_rcp_f32 v130, v130\n"
    "s_waitcnt lgkmcnt(2)\n"
    "v_mfma_f32_32x32x16_bf16 v[32:47], v[120:123], v[0:3], v[32:47]\n"
    "ds_read_b64_tr_b16 v[116:117], v236 offset:36864\n"
    "s_add_u32 m0, s64, 143360\n"
    "s_mov_b32 s65, 1475072\n"
    "buffer_load_dwordx4 v239, s[44:47], s65 offen offset:0 lds\n"
    "v_mfma_f32_32x32x16_bf16 v[48:63], v[120:123], v[4:7], v[48:63]\n"
    "ds_read_b64_tr_b16 v[118:119], v236 offset:37120\n"
    "v_fma_f32 v124, v124, v124, 1.0\n"
    "v_add_f32 v125, 1.0, v125\n"
    "v_cvt_pk_bf16_f32 v147, v90, v91\n"
    "v_mul_f32 v92, v129, v92\n"
    "v_fma_f32 v130, v130, v248, v246\n"
    "v_rcp_f32 v124, v124\n"
    "s_waitcnt lgkmcnt(2)\n"
    "v_mfma_f32_32x32x16_bf16 v[32:47], v[112:115], v[8:11], v[32:47]\n"
    "ds_read_b64_tr_b16 v[120:121], v236 offset:38912\n"
    "v_fma_f32 v125, v125, v125, 1.0\n"
    "v_mul_f32 v93, v130, v93\n"
    "v_fma_f32 v124, v124, v248, v246\n"
    "v_rcp_f32 v125, v125\n"
    "v_cvt_pk_bf16_f32 v148, v92, v93\n"
    "v_mul_f32 v94, v124, v94\n"
    "v_mfma_f32_32x32x16_bf16 v[48:63], v[112:115], v[12:15], v[48:63]\n"
    "ds_read_b64_tr_b16 v[122:123], v236 offset:39168\n"
    "v_fma_f32 v125, v125, v248, v246\n"
    "v_mul_f32 v95, v125, v95\n"
    "v_cvt_pk_bf16_f32 v149, v94, v95\n"
    "v_permlane32_swap_b32 v142, v146\n"
    "v_permlane32_swap_b32 v143, v147\n"
    "v_permlane32_swap_b32 v144, v148\n"
    "s_waitcnt lgkmcnt(2)\n"
    "v_mfma_f32_32x32x16_bf16 v[32:47], v[116:119], v[16:19], v[32:47]\n"
    "s_barrier\n"
    "ds_read_b64_tr_b16 v[112:113], v236 offset:40960\n"
    "v_permlane32_swap_b32 v145, v149\n"
    "v_permlane16_swap_b32 v142, v146\n"
    "v_permlane16_swap_b32 v143, v147\n"
    "v_permlane16_swap_b32 v144, v148\n"
    "v_mfma_f32_32x32x16_bf16 v[48:63], v[116:119], v[20:23], v[48:63]\n"
    "ds_read_b64_tr_b16 v[114:115], v236 offset:41216\n"
    "s_add_u32 m0, s64, 0\n"
    "s_mov_b32 s65, 1769984\n"
    "buffer_load_dwordx4 v239, s[44:47], s65 offen offset:0 lds\n"
    "s_waitcnt lgkmcnt(2)\n"
    "v_mfma_f32_32x32x16_bf16 v[32:47], v[120:123], v[24:27], v[32:47]\n"
    "ds_read_b64_tr_b16 v[116:117], v236 offset:43008\n"
    "v_permlane16_swap_b32 v145, v149\n"
    "s_add_u32 s60, s72, 16384\n"
    "buffer_store_dwordx4 v[142:145], v241, s[56:59], s60 offen offset:2048 sc1\n"
    "buffer_store_dwordx4 v[146:149], v241, s[56:59], s60 offen offset:3072 sc1\n"
    "v_mfma_f32_32x32x16_bf16 v[48:63], v[120:123], v[28:31], v[48:63]\n"
    "ds_read_b64_tr_b16 v[118:119], v236 offset:43264\n"
    "s_waitcnt lgkmcnt(2)\n"
    "v_mfma_f32_32x32x16_bf16 v[64:79], v[112:115], a[0:3], v[96:111]\n"
    "ds_read_b64_tr_b16 v[120:121], v236 offset:45056\n"
    "s_add_u32 m0, s64, 4096\n"
    "s_mov_b32 s65, 2064896\n"
    "buffer_load_dwordx4 v239, s[44:47], s65 offen offset:0 lds\n"
    "v_mfma_f32_32x32x16_bf16 v[80:95], v[112:115], a[4:7], v[96:111]\n"
    "ds_read_b64_tr_b16 v[122:123], v236 offset:45312\n"
    "s_waitcnt lgkmcnt(2)\n"
    "v_mfma_f32_32x32x16_bf16 v[64:79], v[116:119], a[8:11], v[64:79]\n"
    "ds_read_b64_tr_b16 v[112:113], v236 offset:47104\n"
    "v_mfma_f32_32x32x16_bf16 v[80:95], v[116:119], a[12:15], v[80:95]\n"
    "ds_read_b64_tr_b16 v[114:115], v236 offset:47360\n"
    "v_mul_f32 v124, s80, v32\n"
    "v_exp_f32 v124, v124\n"
    "v_mul_f32 v125, s80, v33\n"
    "v_add_f32 v124, 1.0, v124\n"
    "v_exp_f32 v125, v125\n"
    "s_waitcnt lgkmcnt(2)\n"
    "v_mfma_f32_32x32x16_bf16 v[64:79], v[120:123], a[16:19], v[64:79]\n"
    "ds_read_b64_tr_b16 v[116:117], v236 offset:49152\n"
    "s_add_u32 m0, s64, 8192\n"
    "s_mov_b32 s65, 2359808\n"
    "buffer_load_dwordx4 v239, s[44:47], s65 offen offset:0 lds\n"
    "v_mfma_f32_32x32x16_bf16 v[80:95], v[120:123], a[20:23], v[80:95]\n"
    "ds_read_b64_tr_b16 v[118:119], v236 offset:49408\n"
    "v_mul_f32 v126, s80, v34\n"
    "v_fma_f32 v124, v124, v124, 1.0\n"
    "v_add_f32 v125, 1.0, v125\n"
    "v_exp_f32 v126, v126\n"
    "v_mul_f32 v127, s80, v35\n"
    "s_waitcnt lgkmcnt(2)\n"
    "v_mfma_f32_32x32x16_bf16 v[64:79], v[112:115], a[24:27], v[64:79]\n"
    "ds_read_b64_tr_b16 v[120:121], v236 offset:51200\n"
    "v_rcp_f32 v124, v124\n"
    "v_fma_f32 v125, v125, v125, 1.0\n"
    "v_add_f32 v126, 1.0, v126\n"
    "v_exp_f32 v127, v127\n"
    "v_mul_f32 v128, s80, v36\n"
    "v_fma_f32 v124, v124, v247, v245\n"
    "v_mfma_f32_32x32x16_bf16 v[80:95], v[112:115], a[28:31], v[80:95]\n"
    "ds_read_b64_tr_b16 v[122:123], v236 offset:51456\n"
    "v_rcp_f32 v125, v125\n"
    "v_fma_f32 v126, v126, v126, 1.0\n"
    "v_add_f32 v127, 1.0, v127\n"
    "v_exp_f32 v128, v128\n"
    "s_waitcnt lgkmcnt(2)\n"
    "v_mfma_f32_32x32x16_bf16 v[64:79], v[116:119], a[32:35], v[64:79]\n"
    "ds_read_b64_tr_b16 v[112:113], v236 offset:53248\n"
    "s_add_u32 m0, s64, 12288\n"
    "s_mov_b32 s65, 2654720\n"
    "buffer_load_dwordx4 v239, s[44:47], s65 offen offset:0 lds\n"
    "v_mul_f32 v129, s80, v37\n"
    "v_mfma_f32_32x32x16_bf16 v[80:95], v[116:119], a[36:39], v[80:95]\n"
    "ds_read_b64_tr_b16 v[114:115], v236 offset:53504\n"
    "v_mul_f32 v32, v124, v32\n"
    "v_fma_f32 v125, v125, v247, v245\n"
    "v_rcp_f32 v126, v126\n"
    "v_fma_f32 v127, v127, v127, 1.0\n"
    "v_add_f32 v128, 1.0, v128\n"
    "s_waitcnt lgkmcnt(2)\n"
    "v_mfma_f32_32x32x16_bf16 v[64:79], v[120:123], a[40:43], v[64:79]\n"
    "ds_read_b64_tr_b16 v[116:117], v236 offset:55296\n"
    "v_exp_f32 v129, v129\n"
    "v_mul_f32 v130, s80, v38\n"
    "v_mul_f32 v33, v125, v33\n"
    "v_fma_f32 v126, v126, v247, v245\n"
    "v_rcp_f32 v127, v127\n"
    "v_fma_f32 v128, v128, v128, 1.0\n"
    "v_mfma_f32_32x32x16_bf16 v[80:95], v[120:123], a[44:47], v[80:95]\n"
    "ds_read_b64_tr_b16 v[118:119], v236 offset:55552\n"
    "v_add_f32 v129, 1.0, v129\n"
    "v_exp_f32 v130, v130\n"
    "v_mul_f32 v124, s80, v39\n"
    "v_cvt_pk_bf16_f32 v150, v32, v33\n"
    "v_mul_f32 v34, v126, v34\n"
    "v_fma_f32 v127, v127, v247, v245\n"
    "s_waitcnt lgkmcnt(2)\n"
    "v_mfma_f32_32x32x16_bf16 v[64:79], v[112:115], a[48:51], v[64:79]\n"
    "ds_read_b64_tr_b16 v[120:121], v236 offset:57344\n"
    "s_add_u32 m0, s64, 135168\n"
    "s_mov_b32 s65, 2949632\n"
    "buffer_load_dwordx4 v239, s[44:47], s65 offen offset:0 lds\n"
    "v_mfma_f32_32x32x16_bf16 v[80:95], v[112:115], a[52:55], v[80:95]\n"
    "ds_read_b64_tr_b16 v[122:123], v236 offset:57600\n"
    "v_rcp_f32 v128, v128\n"
    "v_fma_f32 v129, v129, v129, 1.0\n"
    "v_add_f32 v130, 1.0, v130\n"
    "v_exp_f32 v124, v124\n"
    "s_waitcnt lgkmcnt(2)\n"
    "v_mfma_f32_32x32x16_bf16 v[64:79], v[116:119], a[56:59], v[64:79]\n"
    "ds_read_b64_tr_b16 v[112:113], v236 offset:59392\n"
    "v_mul_f32 v125, s80, v40\n"
    "v_mul_f32 v35, v127, v35\n"
    "v_fma_f32 v128, v128, v247, v245\n"
    "v_rcp_f32 v129, v129\n"
    "v_fma_f32 v130, v130, v130, 1.0\n"
    "v_add_f32 v124, 1.0, v124\n"
    "v_exp_f32 v125, v125\n"
    "v_mfma_f32_32x32x16_bf16 v[80:95], v[116:119], a[60:63], v[80:95]\n"
    "ds_read_b64_tr_b16 v[114:115], v236 offset:59648\n"
    "ds_read_b128 v[96:99], v244 offset:1024\n"
    "v_mul_f32 v126, s80, v41\n"
    "v_cvt_pk_bf16_f32 v151, v34, v35\n"
    "v_mul_f32 v36, v128, v36\n"
    "v_fma_f32 v129, v129, v247, v245\n"
    "s_waitcnt lgkmcnt(3)\n"
    "v_mfma_f32_32x32x16_bf16 v[64:79], v[120:123], a[64:67], v[64:79]\n"
    "ds_read_b64_tr_b16 v[116:117], v236 offset:61440\n"
    "s_add_u32 m0, s64, 139264\n"
    "s_mov_b32 s65, 3244544\n"
    "buffer_load_dwordx4 v239, s[44:47], s65 offen offset:0 lds\n"
    "v_mfma_f32_32x32x16_bf16 v[80:95], v[120:123], a[68:71], v[80:95]\n"
    "ds_read_b64_tr_b16 v[118:119], v236 offset:61696\n"
    "v_rcp_f32 v130, v130\n"
    "v_fma_f32 v124, v124, v124, 1.0\n"
    "v_add_f32 v125, 1.0, v125\n"
    "v_exp_f32 v126, v126\n"
    "v_mul_f32 v127, s80, v42\n"
    "s_waitcnt lgkmcnt(3)\n"
    "v_mfma_f32_32x32x16_bf16 v[64:79], v[112:115], a[72:75], v[64:79]\n"
    "ds_read_b64_tr_b16 v[120:121], v236 offset:63488\n"
    "v_mul_f32 v37, v129, v37\n"
    "v_fma_f32 v130, v130, v247, v245\n"
    "v_rcp_f32 v124, v124\n"
    "v_fma_f32 v125, v125, v125, 1.0\n"
    "v_add_f32 v126, 1.0, v126\n"
    "v_exp_f32 v127, v127\n"
    "v_mfma_f32_32x32x16_bf16 v[80:95], v[112:115], a[76:79], v[80:95]\n"
    "ds_read_b64_tr_b16 v[122:123], v236 offset:63744\n"
    "v_mul_f32 v128, s80, v43\n"
    "v_cvt_pk_bf16_f32 v152, v36, v37\n"
    "v_mul_f32 v38, v130, v38\n"
    "v_fma_f32 v124, v124, v247, v245\n"
    "v_rcp_f32 v125, v125\n"
    "s_waitcnt lgkmcnt(2)\n"
    "v_mfma_f32_32x32x16_bf16 v[64:79], v[116:119], a[80:83], v[64:79]\n"
    "s_waitcnt vmcnt(23)\n"
    "s_barrier\n"
    "ds_read_b64_tr_b16 v[112:113], v237 offset:0\n"
    "v_fma_f32 v126, v126, v126, 1.0\n"
    "v_add_f32 v127, 1.0, v127\n"
    "v_exp_f32 v128, v128\n"
    "v_mfma_f32_32x32x16_bf16 v[80:95], v[116:119], a[84:87], v[80:95]\n"
    "ds_read_b64_tr_b16 v[114:115], v237 offset:256\n"
    "s_add_u32 m0, s64, 16384\n"
    "s_mov_b32 s65, 3539456\n"
    "buffer_load_dwordx4 v239, s[44:47], s65 offen offset:0 lds\n"
    "s_waitcnt lgkmcnt(2)\n"
    "v_mfma_f32_32x32x16_bf16 v[64:79], v[120:123], a[88:91], v[64:79]\n"
    "ds_read_b64_tr_b16 v[116:117], v237 offset:2048\n"
    "v_mul_f32 v129, s80, v44\n"
    "v_mul_f32 v39, v124, v39\n"
    "v_fma_f32 v125, v125, v247, v245\n"
    "v_rcp_f32 v126, v126\n"
    "v_fma_f32 v127, v127, v127, 1.0\n"
    "v_add_f32 v128, 1.0, v128\n"
    "v_exp_f32 v129, v129\n"
    "v_mfma_f32_32x32x16_bf16 v[80:95], v[120:123], a[92:95], v[80:95]\n"
    "ds_read_b64_tr_b16 v[118:119], v237 offset:2304\n"
    "v_mul_f32 v130, s80, v45\n"
    "v_cvt_pk_bf16_f32 v153, v38, v39\n"
    "v_mul_f32 v40, v125, v40\n"
    "v_fma_f32 v126, v126, v247, v245\n"
    "v_rcp_f32 v127, v127\n"
    "s_waitcnt lgkmcnt(2)\n"
    "v_mfma_f32_32x32x16_bf16 v[64:79], v[112:115], a[96:99], v[64:79]\n"
    "ds_read_b64_tr_b16 v[120:121], v237 offset:4096\n"
    "s_add_u32 m0, s64, 20480\n"
    "s_mov_b32 s65, 3834368\n"
    "buffer_load_dwordx4 v239, s[44:47], s65 offen offset:0 lds\n"
    "v_mfma_f32_32x32x16_bf16 v[80:95], v[112:115], a[100:103], v[80:95]\n"
    "ds_read_b64_tr_b16 v[122:123], v237 offset:4352\n"
    "v_fma_f32 v128, v128, v128, 1.0\n"
    "v_add_f32 v129, 1.0, v129\n"
    "v_exp_f32 v130, v130\n"
    "v_mul_f32 v124, s80, v46\n"
    "v_mul_f32 v41, v126, v41\n"
    "s_waitcnt lgkmcnt(2)\n"
    "v_mfma_f32_32x32x16_bf16 v[64:79], v[116:119], a[104:107], v[64:79]\n"
    "ds_read_b64_tr_b16 v[112:113], v237 offset:6144\n"
    "v_fma_f32 v127, v127, v247, v245\n"
    "v_rcp_f32 v128, v128\n"
    "v_fma_f32 v129, v129, v129, 1.0\n"
    "v_add_f32 v130, 1.0, v130\n"
    "v_exp_f32 v124, v124\n"
    "v_mul_f32 v125, s80, v47\n"
    "v_mfma_f32_32x32x16_bf16 v[80:95], v[116:119], a[108:111], v[80:95]\n"
    "ds_read_b64_tr_b16 v[114:115], v237 offset:6400\n"
    "v_cvt_pk_bf16_f32 v154, v40, v41\n"
    "v_mul_f32 v42, v127, v42\n"
    "v_fma_f32 v128, v128, v247, v245\n"
    "v_rcp_f32 v129, v129\n"
    "v_fma_f32 v130, v130, v130, 1.0\n"
    "v_add_f32 v124, 1.0, v124\n"
    "s_waitcnt lgkmcnt(2)\n"
    "v_mfma_f32_32x32x16_bf16 v[64:79], v[120:123], a[112:115], v[64:79]\n"
    "ds_read_b64_tr_b16 v[116:117], v237 offset:8192\n"
    "s_add_u32 m0, s64, 24576\n"
    "s_mov_b32 s65, 4129280\n"
    "buffer_load_dwordx4 v239, s[44:47], s65 offen offset:0 lds\n"
    "v_mfma_f32_32x32x16_bf16 v[80:95], v[120:123], a[116:119], v[80:95]\n"
    "ds_read_b64_tr_b16 v[118:119], v237 offset:8448\n"
    "ds_read_b128 v[100:103], v244 offset:1056\n"
    "v_exp_f32 v125, v125\n"
    "v_mul_f32 v43, v128, v43\n"
    "v_fma_f32 v129, v129, v247, v245\n"
    "v_rcp_f32 v130, v130\n"
    "s_waitcnt lgkmcnt(3)\n"
    "v_mfma_f32_32x32x16_bf16 v[64:79], v[112:115], a[120:123], v[64:79]\n"
    "ds_read_b64_tr_b16 v[120:121], v237 offset:10240\n"
    "v_fma_f32 v124, v124, v124, 1.0\n"
    "v_add_f32 v125, 1.0, v125\n"
    "v_cvt_pk_bf16_f32 v155, v42, v43\n"
    "v_mul_f32 v44, v129, v44\n"
    "v_fma_f32 v130, v130, v247, v245\n"
    "v_rcp_f32 v124, v124\n"
    "v_mfma_f32_32x32x16_bf16 v[80:95], v[112:115], a[124:127], v[80:95]\n"
    "ds_read_b64_tr_b16 v[122:123], v237 offset:10496\n"
    "v_fma_f32 v125, v125, v125, 1.0\n"
    "v_mul_f32 v45, v130, v45\n"
    "v_fma_f32 v124, v124, v247, v245\n"
    "v_rcp_f32 v125, v125\n"
    "v_cvt_pk_bf16_f32 v156, v44, v45\n"
    "s_waitcnt lgkmcnt(3)\n"
    "v_mfma_f32_32x32x16_bf16 v[64:79], v[116:119], a[128:131], v[64:79]\n"
    "ds_read_b64_tr_b16 v[112:113], v237 offset:12288\n"
    "s_add_u32 m0, s64, 28672\n"
    "s_mov_b32 s65, 4424192\n"
    "buffer_load_dwordx4 v239, s[44:47], s65 offen offset:0 lds\n"
    "v_mul_f32 v46, v124, v46\n"
    "v_mfma_f32_32x32x16_bf16 v[80:95], v[116:119], a[132:135], v[80:95]\n"
    "ds_read_b64_tr_b16 v[114:115], v237 offset:12544\n"
    "v_fma_f32 v125, v125, v247, v245\n"
    "v_mul_f32 v47, v125, v47\n"
    "v_cvt_pk_bf16_f32 v157, v46, v47\n"
    "v_permlane32_swap_b32 v150, v154\n"
    "v_permlane32_swap_b32 v151, v155\n"
    "v_permlane32_swap_b32 v152, v156\n"
    "s_waitcnt lgkmcnt(2)\n"
    "v_mfma_f32_32x32x16_bf16 v[64:79], v[120:123], a[136:139], v[64:79]\n"
    "ds_read_b64_tr_b16 v[116:117], v237 offset:14336\n"
    "v_permlane32_swap_b32 v153, v157\n"
    "v_permlane16_swap_b32 v150, v154\n"
    "v_permlane16_swap_b32 v151, v155\n"
    "v_permlane16_swap_b32 v152, v156\n"
    "v_permlane16_swap_b32 v153, v157\n"
    "s_add_u32 s60, s72, 32768\n"
    "buffer_store_dwordx4 v[150:153], v241, s[56:59], s60 offen offset:0 sc1\n"
    "v_mfma_f32_32x32x16_bf16 v[80:95], v[120:123], a[140:143], v[80:95]\n"
    "ds_read_b64_tr_b16 v[118:119], v237 offset:14592\n"
    "buffer_store_dwordx4 v[154:157], v241, s[56:59], s60 offen offset:1024 sc1\n"
    "v_mul_f32 v124, s80, v48\n"
    "v_exp_f32 v124, v124\n"
    "s_waitcnt lgkmcnt(2)\n"
    "v_mfma_f32_32x32x16_bf16 v[64:79], v[112:115], a[144:147], v[64:79]\n"
    "ds_read_b64_tr_b16 v[120:121], v237 offset:16384\n"
    "s_add_u32 m0, s64, 32768\n"
    "s_mov_b32 s65, 4719104\n"
    "buffer_load_dwordx4 v239, s[44:47], s65 offen offset:0 lds\n"
    "v_mfma_f32_32x32x16_bf16 v[80:95], v[112:115], a[148:151], v[80:95]\n"
    "ds_read_b64_tr_b16 v[122:123], v237 offset:16640\n"
    "v_mul_f32 v125, s80, v49\n"
    "v_add_f32 v124, 1.0, v124\n"
    "v_exp_f32 v125, v125\n"
    "v_mul_f32 v126, s80, v50\n"
    "v_fma_f32 v124, v124, v124, 1.0\n"
    "s_waitcnt lgkmcnt(2)\n"
    "v_mfma_f32_32x32x16_bf16 v[64:79], v[116:119], a[152:155], v[64:79]\n"
    "ds_read_b64_tr_b16 v[112:113], v237 offset:18432\n"
    "s_waitcnt vmcnt(12)\n"
    "s_add_u32 s99, s70, 2\n"
    "v_mov_b32 v133, s21\n"
    "v_mov_b32 v132, s99\n"
    "buffer_store_dwordx2 v[132:133], v242, s[56:59], s89 offen offset:0 sc1\n"
    "v_add_f32 v125, 1.0, v125\n"
    "v_mfma_f32_32x32x16_bf16 v[80:95], v[116:119], a[156:159], v[80:95]\n"
    "ds_read_b64_tr_b16 v[114:115], v237 offset:18688\n"
    "v_exp_f32 v126, v126\n"
    "v_mul_f32 v127, s80, v51\n"
    "v_rcp_f32 v124, v124\n"
    "v_fma_f32 v125, v125, v125, 1.0\n"
    "s_waitcnt lgkmcnt(2)\n"
    "v_mfma_f32_32x32x16_bf16 v[64:79], v[120:123], a[160:163], v[64:79]\n"
    "ds_read_b64_tr_b16 v[116:117], v237 offset:20480\n"
    "s_add_u32 m0, s64, 36864\n"
    "s_mov_b32 s65, 5014016\n"
    "buffer_load_dwordx4 v239, s[44:47], s65 offen offset:0 lds\n"
    "v_add_f32 v126, 1.0, v126\n"
    "v_mfma_f32_32x32x16_bf16 v[80:95], v[120:123], a[164:167], v[80:95]\n"
    "ds_read_b64_tr_b16 v[118:119], v237 offset:20736\n"
    "v_exp_f32 v127, v127\n"
    "v_mul_f32 v128, s80, v52\n"
    "v_fma_f32 v124, v124, v248, v246\n"
    "v_rcp_f32 v125, v125\n"
    "s_waitcnt lgkmcnt(2)\n"
    "v_mfma_f32_32x32x16_bf16 v[64:79], v[112:115], a[168:171], v[64:79]\n"
    "ds_read_b64_tr_b16 v[120:121], v237 offset:22528\n"
    "v_fma_f32 v126, v126, v126, 1.0\n"
    "v_add_f32 v127, 1.0, v127\n"
    "v_exp_f32 v128, v128\n"
    "v_mul_f32 v129, s80, v53\n"
    "v_mul_f32 v48, v124, v48\n"
    "v_fma_f32 v125, v125, v248, v246\n"
    "v_mfma_f32_32x32x16_bf16 v[80:95], v[112:115], a[172:175], v[80:95]\n"
    "ds_read_b64_tr_b16 v[122:123], v237 offset:22784\n"
    "ds_read_b128 v[104:107], v244 offset:1088\n"
    "v_rcp_f32 v126, v126\n"
    "v_fma_f32 v127, v127, v127, 1.0\n"
    "v_add_f32 v128, 1.0, v128\n"
    "v_exp_f32 v129, v129\n"
    "s_waitcnt lgkmcnt(3)\n"
    "v_mfma_f32_32x32x16_bf16 v[64:79], v[116:119], a[176:179], v[64:79]\n"
    "s_barrier\n"
    "ds_read_b64_tr_b16 v[112:113], v237 offset:24576\n"
    "s_lshl_b32 s96, s71, 12\n"
    "v_lshl_add_u32 v243, v241, 2, s96\n"
    "buffer_load_dwordx2 v[234:235], v243, s[56:59], 0 offen offset:0 sc1\n"
    "v_mfma_f32_32x32x16_bf16 v[80:95], v[116:119], a[180:183], v[80:95]\n"
    "ds_read_b64_tr_b16 v[114:115], v237 offset:24832\n"
    "s_add_u32 m0, s64, 40960\n"
    "s_add_u32 s65, s76, 147456\n"
    "buffer_load_dwordx4 v240, s[48:51], s65 offen offset:0 lds\n"
    "v_mul_f32 v130, s80, v54\n"
    "s_waitcnt lgkmcnt(3)\n"
    "v_mfma_f32_32x32x16_bf16 v[64:79], v[120:123], a[184:187], v[64:79]\n"
    "ds_read_b64_tr_b16 v[116:117], v237 offset:26624\n"
    "v_mul_f32 v49, v125, v49\n"
    "v_fma_f32 v126, v126, v248, v246\n"
    "v_rcp_f32 v127, v127\n"
    "v_fma_f32 v128, v128, v128, 1.0\n"
    "v_add_f32 v129, 1.0, v129\n"
    "v_exp_f32 v130, v130\n"
    "v_mfma_f32_32x32x16_bf16 v[80:95], v[120:123], a[188:191], v[80:95]\n"
    "ds_read_b64_tr_b16 v[118:119], v237 offset:26880\n"
    "v_mul_f32 v124, s80, v55\n"
    "v_cvt_pk_bf16_f32 v158, v48, v49\n"
    "v_mul_f32 v50, v126, v50\n"
    "v_fma_f32 v127, v127, v248, v246\n"
    "v_rcp_f32 v128, v128\n"
    "s_waitcnt lgkmcnt(2)\n"
    "v_mfma_f32_32x32x16_bf16 v[64:79], v[112:115], a[192:195], v[64:79]\n"
    "ds_read_b64_tr_b16 v[120:121], v237 offset:28672\n"
    "s_add_u32 m0, s64, 45056\n"
    "s_add_u32 s65, s76, 184320\n"
    "buffer_load_dwordx4 v240, s[48:51], s65 offen offset:0 lds\n"
    "v_fma_f32 v129, v129, v129, 1.0\n"
    "v_mfma_f32_32x32x16_bf16 v[80:95], v[112:115], a[196:199], v[80:95]\n"
    "ds_read_b64_tr_b16 v[122:123], v237 offset:28928\n"
    "v_add_f32 v130, 1.0, v130\n"
    "v_exp_f32 v124, v124\n"
    "v_mul_f32 v125, s80, v56\n"
    "v_mul_f32 v51, v127, v51\n"
    "v_fma_f32 v128, v128, v248, v246\n"
    "s_waitcnt lgkmcnt(2)\n"
    "v_mfma_f32_32x32x16_bf16 v[64:79], v[116:119], a[200:203], v[64:79]\n"
    "ds_read_b64_tr_b16 v[112:113], v237 offset:30720\n"
    "v_rcp_f32 v129, v129\n"
    "v_fma_f32 v130, v130, v130, 1.0\n"
    "v_add_f32 v124, 1.0, v124\n"
    "v_exp_f32 v125, v125\n"
    "v_mul_f32 v126, s80, v57\n"
    "v_cvt_pk_bf16_f32 v159, v50, v51\n"
    "v_mfma_f32_32x32x16_bf16 v[80:95], v[116:119], a[204:207], v[80:95]\n"
    "ds_read_b64_tr_b16 v[114:115], v237 offset:30976\n"
    "v_mul_f32 v52, v128, v52\n"
    "v_fma_f32 v129, v129, v248, v246\n"
    "v_rcp_f32 v130, v130\n"
    "v_fma_f32 v124, v124, v124, 1.0\n"
    "v_add_f32 v125, 1.0, v125\n"
    "s_waitcnt lgkmcnt(2)\n"
    "v_mfma_f32_32x32x16_bf16 v[64:79], v[120:123], a[208:211], v[64:79]\n"
    "ds_read_b64_tr_b16 v[116:117], v237 offset:32768\n"
    "s_add_u32 m0, s64, 49152\n"
    "s_add_u32 s65, s24, 147456\n"
    "buffer_load_dwordx4 v240, s[48:51], s65 offen offset:0 lds\n"
    "v_mfma_f32_32x32x16_bf16 v[80:95], v[120:123], a[212:215], v[80:95]\n"
    "ds_read_b64_tr_b16 v[118:119], v237 offset:33024\n"
    "v_exp_f32 v126, v126\n"
    "v_mul_f32 v127, s80, v58\n"
    "v_mul_f32 v53, v129, v53\n"
    "v_fma_f32 v130, v130, v248, v246\n"
    "v_rcp_f32 v124, v124\n"
    "s_waitcnt lgkmcnt(2)\n"
    "v_mfma_f32_32x32x16_bf16 v[64:79], v[112:115], a[216:219], v[64:79]\n"
    "ds_read_b64_tr_b16 v[120:121], v237 offset:34816\n"
    "v_fma_f32 v125, v125, v125, 1.0\n"
    "v_add_f32 v126, 1.0, v126\n"
    "v_exp_f32 v127, v127\n"
    "v_mul_f32 v128, s80, v59\n"
    "v_cvt_pk_bf16_f32 v160, v52, v53\n"
    "v_mul_f32 v54, v130, v54\n"
    "v_fma_f32 v124, v124, v248, v246\n"
    "v_mfma_f32_32x32x16_bf16 v[80:95], v[112:115], a[220:223], v[80:95]\n"
    "ds_read_b64_tr_b16 v[122:123], v237 offset:35072\n"
    "v_rcp_f32 v125, v125\n"
    "v_fma_f32 v126, v126, v126, 1.0\n"
    "v_add_f32 v127, 1.0, v127\n"
    "v_exp_f32 v128, v128\n"
    "s_waitcnt lgkmcnt(2)\n"
    "v_mfma_f32_32x32x16_bf16 v[64:79], v[116:119], a[224:227], v[64:79]\n"
    "ds_read_b64_tr_b16 v[112:113], v237 offset:36864\n"
    "s_add_u32 m0, s64, 53248\n"
    "s_add_u32 s65, s24, 184320\n"
    "buffer_load_dwordx4 v240, s[48:51], s65 offen offset:0 lds\n"
    "v_mul_f32 v129, s80, v60\n"
    "v_mfma_f32_32x32x16_bf16 v[80:95], v[116:119], a[228:231], v[80:95]\n"
    "ds_read_b64_tr_b16 v[114:115], v237 offset:37120\n"
    "ds_read_b128 v[108:111], v244 offset:1120\n"
    "v_mul_f32 v55, v124, v55\n"
    "v_fma_f32 v125, v125, v248, v246\n"
    "v_rcp_f32 v126, v126\n"
    "v_fma_f32 v127, v127, v127, 1.0\n"
    "s_waitcnt lgkmcnt(3)\n"
    "v_mfma_f32_32x32x16_bf16 v[64:79], v[120:123], a[232:235], v[64:79]\n"
    "ds_read_b64_tr_b16 v[116:117], v237 offset:38912\n"
    "v_add_f32 v128, 1.0, v128\n"
    "v_exp_f32 v129, v129\n"
    "v_mul_f32 v130, s80, v61\n"
    "v_cvt_pk_bf16_f32 v161, v54, v55\n"
    "v_mul_f32 v56, v125, v56\n"
    "v_fma_f32 v126, v126, v248, v246\n"
    "v_mfma_f32_32x32x16_bf16 v[80:95], v[120:123], a[236:239], v[80:95]\n"
    "ds_read_b64_tr_b16 v[118:119], v237 offset:39168\n"
    "v_rcp_f32 v127, v127\n"
    "v_fma_f32 v128, v128, v128, 1.0\n"
    "v_add_f32 v129, 1.0, v129\n"
    "v_exp_f32 v130, v130\n"
    "v_mul_f32 v124, s80, v62\n"
    "s_waitcnt lgkmcnt(3)\n"
    "v_mfma_f32_32x32x16_bf16 v[64:79], v[112:115], a[240:243], v[64:79]\n"
    "ds_read_b64_tr_b16 v[120:121], v237 offset:40960\n"
    "s_add_u32 m0, s64, 57344\n"
    "s_add_u32 s65, s76, 221184\n"
    "buffer_load_dwordx4 v240, s[48:51], s65 offen offset:0 lds\n"
    "v_mul_f32 v57, v126, v57\n"
    "v_mfma_f32_32x32x16_bf16 v[80:95], v[112:115], a[244:247], v[80:95]\n"
    "ds_read_b64_tr_b16 v[122:123], v237 offset:41216\n"
    "v_fma_f32 v127, v127, v248, v246\n"
    "v_rcp_f32 v128, v128\n"
    "v_fma_f32 v129, v129, v129, 1.0\n"
    "v_add_f32 v130, 1.0, v130\n"
    "s_waitcnt lgkmcnt(2)\n"
    "v_mfma_f32_32x32x16_bf16 v[64:79], v[116:119], a[248:251], v[64:79]\n"
    "ds_read_b64_tr_b16 v[112:113], v237 offset:43008\n"
    "v_exp_f32 v124, v124\n"
    "v_mul_f32 v125, s80, v63\n"
    "v_cvt_pk_bf16_f32 v162, v56, v57\n"
    "v_mul_f32 v58, v127, v58\n"
    "v_fma_f32 v128, v128, v248, v246\n"
    "v_rcp_f32 v129, v129\n"
    "v_fma_f32 v130, v130, v130, 1.0\n"
    "v_mfma_f32_32x32x16_bf16 v[80:95], v[116:119], a[252:255], v[80:95]\n"
    "ds_read_b64_tr_b16 v[114:115], v237 offset:43264\n"
    "v_add_f32 v124, 1.0, v124\n"
    "v_exp_f32 v125, v125\n"
    "v_mul_f32 v59, v128, v59\n"
    "v_fma_f32 v129, v129, v248, v246\n"
    "v_rcp_f32 v130, v130\n"
    "s_waitcnt lgkmcnt(2)\n"
    "v_mfma_f32_32x32x16_bf16 v[64:79], v[120:123], v[0:3], v[64:79]\n"
    "ds_read_b64_tr_b16 v[116:117], v237 offset:45056\n"
    "s_add_u32 m0, s64, 61440\n"
    "s_add_u32 s65, s76, 258048\n"
    "buffer_load_dwordx4 v240, s[48:51], s65 offen offset:0 lds\n"
    "v_mfma_f32_32x32x16_bf16 v[80:95], v[120:123], v[4:7], v[80:95]\n"
    "ds_read_b64_tr_b16 v[118:119], v237 offset:45312\n"
    "v_fma_f32 v124, v124, v124, 1.0\n"
    "v_add_f32 v125, 1.0, v125\n"
    "v_cvt_pk_bf16_f32 v163, v58, v59\n"
    "v_mul_f32 v60, v129, v60\n"
    "v_fma_f32 v130, v130, v248, v246\n"
    "s_waitcnt lgkmcnt(2)\n"
    "v_mfma_f32_32x32x16_bf16 v[64:79], v[112:115], v[8:11], v[64:79]\n"
    "ds_read_b64_tr_b16 v[120:121], v237 offset:47104\n"
    "v_rcp_f32 v124, v124\n"
    "v_fma_f32 v125, v125, v125, 1.0\n"
    "v_mul_f32 v61, v130, v61\n"
    "v_fma_f32 v124, v124, v248, v246\n"
    "v_rcp_f32 v125, v125\n"
    "v_cvt_pk_bf16_f32 v164, v60, v61\n"
    "v_mul_f32 v62, v124, v62\n"
    "v_mfma_f32_32x32x16_bf16 v[80:95], v[112:115], v[12:15], v[80:95]\n"
    "ds_read_b64_tr_b16 v[122:123], v237 offset:47360\n"
    "v_fma_f32 v125, v125, v248, v246\n"
    "v_mul_f32 v63, v125, v63\n"
    "v_cvt_pk_bf16_f32 v165, v62, v63\n"
    "v_permlane32_swap_b32 v158, v162\n"
    "v_permlane32_swap_b32 v159, v163\n"
    "v_permlane32_swap_b32 v160, v164\n"
    "s_waitcnt lgkmcnt(2)\n"
    "v_mfma_f32_32x32x16_bf16 v[64:79], v[116:119], v[16:19], v[64:79]\n"
    "s_barrier\n"
    "ds_read_b64_tr_b16 v[112:113], v237 offset:49152\n"
    "v_permlane32_swap_b32 v161, v165\n"
    "v_permlane16_swap_b32 v158, v162\n"
    "v_permlane16_swap_b32 v159, v163\n"
    "v_permlane16_swap_b32 v160, v164\n"
    "v_mfma_f32_32x32x16_bf16 v[80:95], v[116:119], v[20:23], v[80:95]\n"
    "ds_read_b64_tr_b16 v[114:115], v237 offset:49408\n"
    "s_add_u32 m0, s64, 65536\n"
    "s_add_u32 s65, s24, 221184\n"
    "buffer_load_dwordx4 v240, s[48:51], s65 offen offset:0 lds\n"
    "s_waitcnt lgkmcnt(2)\n"
    "v_mfma_f32_32x32x16_bf16 v[64:79], v[120:123], v[24:27], v[64:79]\n"
    "ds_read_b64_tr_b16 v[116:117], v237 offset:51200\n"
    "v_permlane16_swap_b32 v161, v165\n"
    "s_add_u32 s60, s72, 32768\n"
    "buffer_store_dwordx4 v[158:161], v241, s[56:59], s60 offen offset:2048 sc1\n"
    "buffer_store_dwordx4 v[162:165], v241, s[56:59], s60 offen offset:3072 sc1\n"
    "v_mfma_f32_32x32x16_bf16 v[80:95], v[120:123], v[28:31], v[80:95]\n"
    "ds_read_b64_tr_b16 v[118:119], v237 offset:51456\n"
    "s_waitcnt lgkmcnt(2)\n"
    "v_mfma_f32_32x32x16_bf16 v[32:47], v[112:115], a[0:3], v[96:111]\n"
    "ds_read_b64_tr_b16 v[120:121], v237 offset:53248\n"
    "s_add_u32 m0, s64, 69632\n"
    "s_add_u32 s65, s24, 258048\n"
    "buffer_load_dwordx4 v240, s[48:51], s65 offen offset:0 lds\n"
    "v_mfma_f32_32x32x16_bf16 v[48:63], v[112:115], a[4:7], v[96:111]\n"
    "ds_read_b64_tr_b16 v[122:123], v237 offset:53504\n"
    "s_waitcnt lgkmcnt(2)\n"
    "v_mfma_f32_32x32x16_bf16 v[32:47], v[116:119], a[8:11], v[32:47]\n"
    "ds_read_b64_tr_b16 v[112:113], v237 offset:55296\n"
    "v_lshrrev_b32 v250, 9, v241\n"
    "s_waitcnt vmcnt(10)\n"
    "v_subrev_u32 v234, s70, v234\n"
    "v_cmp_eq_u32_e64 s[16:17], s21, v235\n"
    "v_subrev_u32 v234, 1, v234\n"
    "v_cmp_gt_u32 vcc, 16, v234\n"
    "s_nop 0\n"
    "s_and_b64 vcc, vcc, s[16:17]\n"
    "s_cmp_eq_u64 vcc, -1\n"
    "s_cbranch_scc0 LslowQ0_%=\n"
    "LbackQ0_%=:\n"
    "v_mfma_f32_32x32x16_bf16 v[48:63], v[116:119], a[12:15], v[48:63]\n"
    "ds_read_b64_tr_b16 v[114:115], v237 offset:55552\n"
    "v_lshlrev_b32 v250, 11, v250\n"
    "s_add_u32 s62, s88, 0\n"
    "buffer_load_dwordx4 v[206:209], v198, s[56:59], s62 offen offset:0 sc1\n"
    "v_mul_f32 v124, s80, v64\n"
    "s_waitcnt lgkmcnt(2)\n"
    "v_mfma_f32_32x32x16_bf16 v[32:47], v[120:123], a[16:19], v[32:47]\n"
    "ds_read_b64_tr_b16 v[116:117], v237 offset:57344\n"
    "s_add_u32 m0, s64, 73728\n"
    "s_add_u32 s65, s76, 0\n"
    "buffer_load_dwordx4 v240, s[48:51], s65 offen offset:0 lds\n"
    "v_exp_f32 v124, v124\n"
    "v_mfma_f32_32x32x16_bf16 v[48:63], v[120:123], a[20:23], v[48:63]\n"
    "ds_read_b64_tr_b16 v[118:119], v237 offset:57600\n"
    "buffer_load_dwordx4 v[210:213], v199, s[56:59], s62 offen offset:0 sc1\n"
    "v_mul_f32 v125, s80, v65\n"
    "v_add_f32 v124, 1.0, v124\n"
    "v_exp_f32 v125, v125\n"
    "v_mul_f32 v126, s80, v66\n"
    "v_fma_f32 v124, v124, v124, 1.0\n"
    "v_add_f32 v125, 1.0, v125\n"
    "v_exp_f32 v126, v126\n"
    "s_waitcnt lgkmcnt(2)\n"
    "v_mfma_f32_32x32x16_bf16 v[32:47], v[112:115], a[24:27], v[32:47]\n"
    "ds_read_b64_tr_b16 v[120:121], v237 offset:59392\n"
    "v_bfe_u32 v251, v241, 6, 3\n"
    "s_add_u32 s62, s88, 32768\n"
    "buffer_load_dwordx4 v[134:137], v198, s[56:59], s62 offen offset:0 sc1\n"
    "v_mul_f32 v127, s80, v67\n"
    "v_rcp_f32 v124, v124\n"
    "v_fma_f32 v125, v125, v125, 1.0\n"
    "v_mfma_f32_32x32x16_bf16 v[48:63], v[112:115], a[28:31], v[48:63]\n"
    "ds_read_b64_tr_b16 v[122:123], v237 offset:59648\n"
    "v_lshl_add_u32 v250, v251, 6, v250\n"
    "buffer_load_dwordx4 v[138:141], v199, s[56:59], s62 offen offset:0 sc1\n"
    "v_add_f32 v126, 1.0, v126\n"
    "v_exp_f32 v127, v127\n"
    "v_mul_f32 v128, s80, v68\n"
    "s_waitcnt lgkmcnt(2)\n"
    "v_mfma_f32_32x32x16_bf16 v[32:47], v[116:119], a[32:35], v[32:47]\n"
    "ds_read_b64_tr_b16 v[112:113], v237 offset:61440\n"
    "s_add_u32 m0, s64, 77824\n"
    "s_add_u32 s65, s76, 36864\n"
    "buffer_load_dwordx4 v240, s[48:51], s65 offen offset:0 lds\n"
    "v_fma_f32 v124, v124, v247, v245\n"
    "v_rcp_f32 v125, v125\n"
    "v_mfma_f32_32x32x16_bf16 v[48:63], v[116:119], a[36:39], v[48:63]\n"
    "ds_read_b64_tr_b16 v[114:115], v237 offset:61696\n"
    "v_bfe_u32 v251, v241, 8, 1\n"
    "s_add_u32 s62, s88, 65536\n"
    "buffer_load_dwordx4 v[142:145], v198, s[56:59], s62 offen offset:0 sc1\n"
    "v_fma_f32 v126, v126, v126, 1.0\n"
    "s_waitcnt lgkmcnt(2)\n"
    "v_mfma_f32_32x32x16_bf16 v[32:47], v[120:123], a[40:43], v[32:47]\n"
    "ds_read_b64_tr_b16 v[116:117], v237 offset:63488\n"
    "v_lshl_add_u32 v250, v251, 5, v250\n"
    "buffer_load_dwordx4 v[146:149], v199, s[56:59], s62 offen offset:0 sc1\n"
    "v_add_f32 v127, 1.0, v127\n"
    "v_exp_f32 v128, v128\n"
    "v_mul_f32 v129, s80, v69\n"
    "v_mul_f32 v64, v124, v64\n"
    "v_fma_f32 v125, v125, v247, v245\n"
    "v_rcp_f32 v126, v126\n"
    "v_mfma_f32_32x32x16_bf16 v[48:63], v[120:123], a[44:47], v[48:63]\n"
    "ds_read_b64_tr_b16 v[118:119], v237 offset:63744\n"
    "s_add_u32 s62, s88, 98304\n"
    "buffer_load_dwordx4 v[182:185], v198, s[56:59], s62 offen offset:0 sc1\n"
    "v_fma_f32 v127, v127, v127, 1.0\n"
    "v_add_f32 v128, 1.0, v128\n"
    "v_exp_f32 v129, v129\n"
    "v_mul_f32 v130, s80, v70\n"
    "s_waitcnt lgkmcnt(2)\n"
    "v_mfma_f32_32x32x16_bf16 v[32:47], v[112:115], a[48:51], v[32:47]\n"
    "ds_read_b64_tr_b16 v[120:121], v238 offset:0\n"
    "s_add_u32 m0, s64, 81920\n"
    "s_add_u32 s65, s24, 0\n"
    "buffer_load_dwordx4 v240, s[48:51], s65 offen offset:0 lds\n"
    "v_mul_f32 v65, v125, v65\n"
    "v_bfe_u32 v243, v241, 4, 2\n"
    "v_max_u32 v243, 1, v243\n"
    "v_xor_b32 v243, s69, v243\n"
    "v_lshl_add_u32 v243, v243, 6, s22\n"
    "buffer_load_dwordx2 v[234:235], v243, s[56:59], 0 offen offset:0 sc1\n"
    "v_mfma_f32_32x32x16_bf16 v[48:63], v[112:115], a[52:55], v[48:63]\n"
    "ds_read_b64_tr_b16 v[122:123], v238 offset:256\n"
    "v_bfe_u32 v251, v241, 4, 2\n"
    "buffer_load_dwordx4 v[186:189], v199, s[56:59], s62 offen offset:0 sc1\n"
    "v_fma_f32 v126, v126, v247, v245\n"
    "v_rcp_f32 v127, v127\n"
    "v_fma_f32 v128, v128, v128, 1.0\n"
    "s_waitcnt lgkmcnt(2)\n"
    "v_mfma_f32_32x32x16_bf16 v[32:47], v[116:119], a[56:59], v[32:47]\n"
    "ds_read_b64_tr_b16 v[112:113], v238 offset:2048\n"
    "v_lshl_add_u32 v250, v251, 3, v250\n"
    "s_add_u32 s62, s88, 131072\n"
    "buffer_load_dwordx4 v[190:193], v198, s[56:59], s62 offen offset:0 sc1\n"
    "v_add_f32 v129, 1.0, v129\n"
    "v_exp_f32 v130, v130\n"
    "v_mul_f32 v124, s80, v71\n"
    "v_mfma_f32_32x32x16_bf16 v[48:63], v[116:119], a[60:63], v[48:63]\n"
    "ds_read_b64_tr_b16 v[114:115], v238 offset:2304\n"
    "buffer_load_dwordx4 v[194:197], v199, s[56:59], s62 offen offset:0 sc1\n"
    "s_waitcnt vmcnt(10)\n"
    "v_add_f32 v206, v206, v134\n"
    "v_cvt_pk_bf16_f32 v166, v64, v65\n"
    "v_mul_f32 v66, v126, v66\n"
    "v_fma_f32 v127, v127, v247, v245\n"
    "v_rcp_f32 v128, v128\n"
    "s_waitcnt lgkmcnt(2)\n"
    "v_mfma_f32_32x32x16_bf16 v[32:47], v[120:123], a[64:67], v[32:47]\n"
    "ds_read_b64_tr_b16 v[116:117], v238 offset:12288\n"
    "s_add_u32 m0, s64, 86016\n"
    "s_add_u32 s65, s24, 36864\n"
    "buffer_load_dwordx4 v240, s[48:51], s65 offen offset:0 lds\n"
    "v_fma_f32 v129, v129, v129, 1.0\n"
    "v_mfma_f32_32x32x16_bf16 v[48:63], v[120:123], a[68:71], v[48:63]\n"
    "ds_read_b64_tr_b16 v[118:119], v238 offset:12544\n"
    "v_add_f32 v207, v207, v135\n"
    "v_add_f32 v208, v208, v136\n"
    "v_add_f32 v209, v209, v137\n"
    "v_add_f32 v130, 1.0, v130\n"
    "v_exp_f32 v124, v124\n"
    "v_mul_f32 v125, s80, v72\n"
    "v_mul_f32 v67, v127, v67\n"
    "v_fma_f32 v128, v128, v247, v245\n"
    "s_waitcnt lgkmcnt(2)\n"
    "v_mfma_f32_32x32x16_bf16 v[32:47], v[112:115], a[72:75], v[32:47]\n"
    "ds_read_b64_tr_b16 v[120:121], v238 offset:14336\n"
    "s_and_b32 s96, s66, 1\n"
    "s_lshl_b32 s96, s96, 12\n"
    "v_add_u32 v250, s96, v250\n"
    "s_add_u32 s62, s88, 163840\n"
    "buffer_load_dwordx4 v[134:137], v198, s[56:59], s62 offen offset:0 sc1\n"
    "v_rcp_f32 v129, v129\n"
    "v_mfma_f32_32x32x16_bf16 v[48:63], v[112:115], a[76:79], v[48:63]\n"
    "ds_read_b64_tr_b16 v[122:123], v238 offset:14592\n"
    "s_waitcnt vmcnt(11)\n"
    "v_add_f32 v210, v210, v138\n"
    "v_add_f32 v211, v211, v139\n"
    "v_fma_f32 v130, v130, v130, 1.0\n"
    "v_add_f32 v124, 1.0, v124\n"
    "v_exp_f32 v125, v125\n"
    "v_mul_f32 v126, s80, v73\n"
    "v_cvt_pk_bf16_f32 v167, v66, v67\n"
    "v_mul_f32 v68, v128, v68\n"
    "v_fma_f32 v129, v129, v247, v245\n"
    "v_rcp_f32 v130, v130\n"
    "v_fma_f32 v124, v124, v124, 1.0\n"
    "s_waitcnt lgkmcnt(2)\n"
    "v_mfma_f32_32x32x16_bf16 v[32:47], v[116:119], a[80:83], v[32:47]\n"
    "s_barrier\n"
    "ds_read_b64_tr_b16 v[112:113], v236 offset:0\n"
    "v_add_f32 v212, v212, v140\n"
    "v_add_f32 v213, v213, v141\n"
    "v_add_f32 v125, 1.0, v125\n"
    "v_exp_f32 v126, v126\n"
    "v_mfma_f32_32x32x16_bf16 v[48:63], v[116:119], a[84:87], v[48:63]\n"
    "ds_read_b64_tr_b16 v[114:115], v236 offset:256\n"
    "s_add_u32 m0, s64, 90112\n"
    "s_add_u32 s65, s76, 73728\n"
    "buffer_load_dwordx4 v240, s[48:51], s65 offen offset:0 lds\n"
    "v_mul_f32 v127, s80, v74\n"
    "s_waitcnt lgkmcnt(2)\n"
    "v_mfma_f32_32x32x16_bf16 v[32:47], v[120:123], a[88:91], v[32:47]\n"
    "ds_read_b64_tr_b16 v[116:117], v236 offset:2048\n"
    "v_xor_b32 v251, 32, v250\n"
    "buffer_load_dwordx4 v[138:141], v199, s[56:59], s62 offen offset:0 sc1\n"
    "s_waitcnt vmcnt(11)\n"
    "v_add_f32 v206, v206, v142\n"
    "v_mul_f32 v69, v129, v69\n"
    "v_fma_f32 v130, v130, v247, v245\n"
    "v_rcp_f32 v124, v124\n"
    "v_mfma_f32_32x32x16_bf16 v[48:63], v[120:123], a[92:95], v[48:63]\n"
    "ds_read_b64_tr_b16 v[118:119], v236 offset:2304\n"
    "v_add_u32 v252, 65536, v250\n"
    "v_add_f32 v207, v207, v143\n"
    "v_add_f32 v208, v208, v144\n"
    "v_fma_f32 v125, v125, v125, 1.0\n"
    "v_add_f32 v126, 1.0, v126\n"
    "v_exp_f32 v127, v127\n"
    "v_mul_f32 v128, s80, v75\n"
    "v_cvt_pk_bf16_f32 v168, v68, v69\n"
    "s_waitcnt lgkmcnt(2)\n"
    "v_mfma_f32_32x32x16_bf16 v[32:47], v[112:115], a[96:99], v[32:47]\n"
    "ds_read_b64_tr_b16 v[120:121], v236 offset:4096\n"
    "s_add_u32 m0, s64, 94208\n"
    "s_add_u32 s65, s76, 110592\n"
    "buffer_load_dwordx4 v240, s[48:51], s65 offen offset:0 lds\n"
    "v_add_f32 v209, v209, v145\n"
    "v_mul_f32 v70, v130, v70\n"
    "v_fma_f32 v124, v124, v247, v245\n"
    "v_mfma_f32_32x32x16_bf16 v[48:63], v[112:115], a[100:103], v[48:63]\n"
    "ds_read_b64_tr_b16 v[122:123], v236 offset:4352\n"
    "v_add_u32 v253, 65536, v251\n"
    "s_add_u32 s62, s88, 196608\n"
    "buffer_load_dwordx4 v[142:145], v198, s[56:59], s62 offen offset:0 sc1\n"
    "v_rcp_f32 v125, v125\n"
    "s_waitcnt lgkmcnt(2)\n"
    "v_mfma_f32_32x32x16_bf16 v[32:47], v[116:119], a[104:107], v[32:47]\n"
    "ds_read_b64_tr_b16 v[112:113], v236 offset:6144\n"
    "v_add_u32 v254, 131072, v250\n"
    "s_waitcnt vmcnt(12)\n"
    "v_add_f32 v210, v210, v146\n"
    "v_add_f32 v211, v211, v147\n"
    "v_fma_f32 v126, v126, v126, 1.0\n"
    "v_add_f32 v127, 1.0, v127\n"
    "v_exp_f32 v128, v128\n"
    "v_mul_f32 v129, s80, v76\n"
    "v_mul_f32 v71, v124, v71\n"
    "v_fma_f32 v125, v125, v247, v245\n"
    "v_rcp_f32 v126, v126\n"
    "v_fma_f32 v127, v127, v127, 1.0\n"
    "v_add_f32 v128, 1.0, v128\n"
    "v_mfma_f32_32x32x16_bf16 v[48:63], v[116:119], a[108:111], v[48:63]\n"
    "ds_read_b64_tr_b16 v[114:115], v236 offset:6400\n"
    "v_add_f32 v212, v212, v148\n"
    "v_add_f32 v213, v213, v149\n"
    "v_exp_f32 v129, v129\n"
    "v_mul_f32 v130, s80, v77\n"
    "v_cvt_pk_bf16_f32 v169, v70, v71\n"
    "v_mul_f32 v72, v125, v72\n"
    "v_fma_f32 v126, v126, v247, v245\n"
    "v_rcp_f32 v127, v127\n"
    "v_fma_f32 v128, v128, v128, 1.0\n"
    "v_add_f32 v129, 1.0, v129\n"
    "s_waitcnt lgkmcnt(2)\n"
    "v_mfma_f32_32x32x16_bf16 v[32:47], v[120:123], a[112:115], v[32:47]\n"
    "ds_read_b64_tr_b16 v[116:117], v236 offset:8192\n"
    "s_add_u32 m0, s64, 98304\n"
    "s_add_u32 s65, s24, 73728\n"
    "buffer_load_dwordx4 v240, s[48:51], s65 offen offset:0 lds\n"
    "buffer_load_dwordx4 v[146:149], v199, s[56:59], s62 offen offset:0 sc1\n"
    "v_exp_f32 v130, v130\n"
    "v_mfma_f32_32x32x16_bf16 v[48:63], v[120:123], a[116:119], v[48:63]\n"
    "ds_read_b64_tr_b16 v[118:119], v236 offset:8448\n"
    "v_add_u32 v255, 131072, v251\n"
    "s_waitcnt vmcnt(13)\n"
    "v_add_f32 v206, v206, v182\n"
    "v_mul_f32 v124, s80, v78\n"
    "v_mul_f32 v73, v126, v73\n"
    "v_fma_f32 v127, v127, v247, v245\n"
    "v_rcp_f32 v128, v128\n"
    "v_fma_f32 v129, v129, v129, 1.0\n"
    "v_add_f32 v130, 1.0, v130\n"
    "v_exp_f32 v124, v124\n"
    "v_mul_f32 v125, s80, v79\n"
    "v_cvt_pk_bf16_f32 v170, v72, v73\n"
    "v_mul_f32 v74, v127, v74\n"
    "s_waitcnt lgkmcnt(2)\n"
    "v_mfma_f32_32x32x16_bf16 v[32:47], v[112:115], a[120:123], v[32:47]\n"
    "ds_read_b64_tr_b16 v[120:121], v236 offset:10240\n"
    "v_add_f32 v207, v207, v183\n"
    "v_add_f32 v208, v208, v184\n"
    "v_add_f32 v209, v209, v185\n"
    "v_fma_f32 v128, v128, v247, v245\n"
    "v_rcp_f32 v129, v129\n"
    "v_fma_f32 v130, v130, v130, 1.0\n"
    "v_add_f32 v124, 1.0, v124\n"
    "v_exp_f32 v125, v125\n"
    "v_mul_f32 v75, v128, v75\n"
    "v_fma_f32 v129, v129, v247, v245\n"
    "v_rcp_f32 v130, v130\n"
    "v_mfma_f32_32x32x16_bf16 v[48:63], v[112:115], a[124:127], v[48:63]\n"
    "ds_read_b64_tr_b16 v[122:123], v236 offset:10496\n"
    "s_add_u32 s62, s88, 229376\n"
    "buffer_load_dwordx4 v[182:185], v198, s[56:59], s62 offen offset:0 sc1\n"
    "s_waitcnt vmcnt(11)\n"
    "v_add_f32 v210, v210, v186\n"
    "v_fma_f32 v124, v124, v124, 1.0\n"
    "s_waitcnt lgkmcnt(2)\n"
    "v_mfma_f32_32x32x16_bf16 v[32:47], v[116:119], a[128:131], v[32:47]\n"
    "ds_read_b64_tr_b16 v[112:113], v236 offset:12288\n"
    "s_add_u32 m0, s64, 102400\n"
    "s_add_u32 s65, s24, 110592\n"
    "buffer_load_dwordx4 v240, s[48:51], s65 offen offset:0 lds\n"
    "v_add_f32 v211, v211, v187\n"
    "v_add_f32 v125, 1.0, v125\n"
    "v_cvt_pk_bf16_f32 v171, v74, v75\n"
    "v_mfma_f32_32x32x16_bf16 v[48:63], v[116:119], a[132:135], v[48:63]\n"
    "ds_read_b64_tr_b16 v[114:115], v236 offset:12544\n"
    "v_add_f32 v212, v212, v188\n"
    "v_add_f32 v213, v213, v189\n"
    "v_mul_f32 v76, v129, v76\n"
    "v_fma_f32 v130, v130, v247, v245\n"
    "v_rcp_f32 v124, v124\n"
    "v_fma_f32 v125, v125, v125, 1.0\n"
    "v_mul_f32 v77, v130, v77\n"
    "v_fma_f32 v124, v124, v247, v245\n"
    "v_rcp_f32 v125, v125\n"
    "v_cvt_pk_bf16_f32 v172, v76, v77\n"
    "v_mul_f32 v78, v124, v78\n"
    "s_waitcnt lgkmcnt(2)\n"
    "v_mfma_f32_32x32x16_bf16 v[32:47], v[120:123], a[136:139], v[32:47]\n"
    "ds_read_b64_tr_b16 v[116:117], v236 offset:14336\n"
    "buffer_load_dwordx4 v[186:189], v199, s[56:59], s62 offen offset:0 sc1\n"
    "s_waitcnt vmcnt(12)\n"
    "v_add_f32 v206, v206, v190\n"
    "v_add_f32 v207, v207, v191\n"
    "v_fma_f32 v125, v125, v247, v245\n"
    "v_mul_f32 v79, v125, v79\n"
    "v_cvt_pk_bf16_f32 v173, v78, v79\n"
    "v_permlane32_swap_b32 v166, v170\n"
    "v_mfma_f32_32x32x16_bf16 v[48:63], v[120:123], a[140:143], v[48:63]\n"
    "ds_read_b64_tr_b16 v[118:119], v236 offset:14592\n"
    "v_add_f32 v208, v208, v192\n"
    "v_add_f32 v209, v209, v193\n"
    "v_permlane32_swap_b32 v167, v171\n"
    "v_permlane32_swap_b32 v168, v172\n"
    "v_permlane32_swap_b32 v169, v173\n"
    "v_permlane16_swap_b32 v166, v170\n"
    "v_permlane16_swap_b32 v167, v171\n"
    "v_permlane16_swap_b32 v168, v172\n"
    "v_permlane16_swap_b32 v169, v173\n"
    "s_add_u32 s60, s72, 49152\n"
    "buffer_store_dwordx4 v[166:169], v241, s[56:59], s60 offen offset:0 sc1\n"
    "s_waitcnt lgkmcnt(2)\n"
    "v_mfma_f32_32x32x16_bf16 v[32:47], v[112:115], a[144:147], v[32:47]\n"
    "ds_read_b64_tr_b16 v[120:121], v238 offset:4096\n"
    "s_add_u32 m0, s64, 106496\n"
    "s_add_u32 s65, s77, 0\n"
    "buffer_load_dwordx4 v240, s[48:51], s65 offen offset:0 lds\n"
    "s_add_u32 s62, s88, 262144\n"
    "buffer_load_dwordx4 v[190:193], v198, s[56:59], s62 offen offset:0 sc1\n"
    "buffer_store_dwordx4 v[170:173], v241, s[56:59], s60 offen offset:1024 sc1\n"
    "v_mfma_f32_32x32x16_bf16 v[48:63], v[112:115], a[148:151], v[48:63]\n"
    "ds_read_b64_tr_b16 v[122:123], v238 offset:4352\n"
    "s_waitcnt vmcnt(15)\n"
    "v_add_f32 v210, v210, v194\n"
    "v_add_f32 v211, v211, v195\n"
    "v_mul_f32 v124, s80, v80\n"
    "v_exp_f32 v124, v124\n"
    "v_mul_f32 v125, s80, v81\n"
    "v_add_f32 v124, 1.0, v124\n"
    "v_exp_f32 v125, v125\n"
    "v_mul_f32 v126, s80, v82\n"
    "v_fma_f32 v124, v124, v124, 1.0\n"
    "v_add_f32 v125, 1.0, v125\n"
    "s_waitcnt lgkmcnt(2)\n"
    "v_mfma_f32_32x32x16_bf16 v[32:47], v[116:119], a[152:155], v[32:47]\n"
    "ds_read_b64_tr_b16 v[112:113], v238 offset:6144\n"
    "v_add_f32 v212, v212, v196\n"
    "v_add_f32 v213, v213, v197\n"
    "buffer_load_dwordx4 v[194:197], v199, s[56:59], s62 offen offset:0 sc1\n"
    "s_add_u32 s99, s70, 3\n"
    "v_mov_b32 v133, s21\n"
    "v_mov_b32 v132, s99\n"
    "buffer_store_dwordx2 v[132:133], v242, s[56:59], s89 offen offset:0 sc1\n"
    "v_exp_f32 v126, v126\n"
    "v_mfma_f32_32x32x16_bf16 v[48:63], v[116:119], a[156:159], v[48:63]\n"
    "ds_read_b64_tr_b16 v[114:115], v238 offset:6400\n"
    "s_waitcnt vmcnt(15)\n"
    "v_add_f32 v206, v206, v134\n"
    "v_add_f32 v207, v207, v135\n"
    "v_add_f32 v208, v208, v136\n"
    "v_mul_f32 v127, s80, v83\n"
    "v_rcp_f32 v124, v124\n"
    "v_fma_f32 v125, v125, v125, 1.0\n"
    "v_add_f32 v126, 1.0, v126\n"
    "s_waitcnt lgkmcnt(2)\n"
    "v_mfma_f32_32x32x16_bf16 v[32:47], v[120:123], a[160:163], v[32:47]\n"
    "ds_read_b64_tr_b16 v[116:117], v238 offset:8192\n"
    "s_add_u32 m0, s64, 110592\n"
    "s_add_u32 s65, s77, 36864\n"
    "buffer_load_dwordx4 v240, s[48:51], s65 offen offset:0 lds\n"
    "v_exp_f32 v127, v127\n"
    "v_mul_f32 v128, s80, v84\n"
    "v_mfma_f32_32x32x16_bf16 v[48:63], v[120:123], a[164:167], v[48:63]\n"
    "ds_read_b64_tr_b16 v[118:119], v238 offset:8448\n"
    "v_add_f32 v209, v209, v137\n"
    "s_add_u32 s62, s88, 294912\n"
    "buffer_load_dwordx4 v[134:137], v198, s[56:59], s62 offen offset:0 sc1\n"
    "v_fma_f32 v124, v124, v248, v246\n"
    "s_waitcnt lgkmcnt(2)\n"
    "v_mfma_f32_32x32x16_bf16 v[32:47], v[112:115], a[168:171], v[32:47]\n"
    "ds_read_b64_tr_b16 v[120:121], v238 offset:10240\n"
    "s_waitcnt vmcnt(15)\n"
    "v_add_f32 v210, v210, v138\n"
    "v_add_f32 v211, v211, v139\n"
    "v_add_f32 v212, v212, v140\n"
    "v_rcp_f32 v125, v125\n"
    "v_fma_f32 v126, v126, v126, 1.0\n"
    "v_add_f32 v127, 1.0, v127\n"
    "v_exp_f32 v128, v128\n"
    "v_mul_f32 v129, s80, v85\n"
    "v_mul_f32 v80, v124, v80\n"
    "v_fma_f32 v125, v125, v248, v246\n"
    "v_rcp_f32 v126, v126\n"
    "v_subrev_u32 v234, s70, v234\n"
    "v_cmp_eq_u32_e64 s[16:17], s21, v235\n"
    "v_subrev_u32 v234, 2, v234\n"
    "v_cmp_gt_u32 vcc, 16, v234\n"
    "s_nop 0\n"
    "s_and_b64 vcc, vcc, s[16:17]\n"
    "s_cmp_eq_u64 vcc, -1\n"
    "s_cbranch_scc0 LslowQ1_%=\n"
    "LbackQ1_%=:\n"
    "v_mfma_f32_32x32x16_bf16 v[48:63], v[112:115], a[172:175], v[48:63]\n"
    "ds_read_b64_tr_b16 v[122:123], v238 offset:10496\n"
    "v_add_f32 v213, v213, v141\n"
    "buffer_load_dwordx4 v[138:141], v199, s[56:59], s62 offen offset:0 sc1\n"
    "v_fma_f32 v127, v127, v127, 1.0\n"
    "v_add_f32 v128, 1.0, v128\n"
    "v_exp_f32 v129, v129\n"
    "v_mul_f32 v130, s80, v86\n"
    "s_waitcnt lgkmcnt(2)\n"
    "v_mfma_f32_32x32x16_bf16 v[32:47], v[116:119], a[176:179], v[32:47]\n"
    "s_barrier\n"
    "ds_read_b64_tr_b16 v[112:113], v236 offset:16384\n"
    "s_waitcnt vmcnt(14)\n"
    "v_add_f32 v206, v206, v142\n"
    "v_mul_f32 v81, v125, v81\n"
    "v_fma_f32 v126, v126, v248, v246\n"
    "v_rcp_f32 v127, v127\n"
    "v_fma_f32 v128, v128, v128, 1.0\n"
    "v_add_f32 v129, 1.0, v129\n"
    "v_mfma_f32_32x32x16_bf16 v[48:63], v[116:119], a[180:183], v[48:63]\n"
    "ds_read_b64_tr_b16 v[114:115], v236 offset:16640\n"
    "s_add_u32 m0, s64, 114688\n"
    "s_add_u32 s65, s25, 0\n"
    "buffer_load_dwordx4 v240, s[48:51], s65 offen offset:0 lds\n"
    "v_exp_f32 v130, v130\n"
    "s_waitcnt lgkmcnt(2)\n"
    "v_mfma_f32_32x32x16_bf16 v[32:47], v[120:123], a[184:187], v[32:47]\n"
    "ds_read_b64_tr_b16 v[116:117], v236 offset:18432\n"
    "v_add_f32 v207, v207, v143\n"
    "v_add_f32 v208, v208, v144\n"
    "v_add_f32 v209, v209, v145\n"
    "v_mul_f32 v124, s80, v87\n"
    "v_cvt_pk_bf16_f32 v174, v80, v81\n"
    "v_mul_f32 v82, v126, v82\n"
    "v_fma_f32 v127, v127, v248, v246\n"
    "v_rcp_f32 v128, v128\n"
    "v_fma_f32 v129, v129, v129, 1.0\n"
    "v_add_f32 v130, 1.0, v130\n"
    "v_exp_f32 v124, v124\n"
    "v_mul_f32 v125, s80, v88\n"
    "v_mfma_f32_32x32x16_bf16 v[48:63], v[120:123], a[188:191], v[48:63]\n"
    "ds_read_b64_tr_b16 v[118:119], v236 offset:18688\n"
    "s_add_u32 s62, s88, 327680\n"
    "buffer_load_dwordx4 v[142:145], v198, s[56:59], s62 offen offset:0 sc1\n"
    "s_waitcnt vmcnt(14)\n"
    "v_add_f32 v210, v210, v146\n"
    "v_mul_f32 v83, v127, v83\n"
    "s_add_u32 s61, s72, 0\n"
    "buffer_load_dwordx4 a[48:51], v241, s[56:59], s61 offen offset:0 sc1\n"
    "s_waitcnt lgkmcnt(2)\n"
    "v_mfma_f32_32x32x16_bf16 v[32:47], v[112:115], a[192:195], v[32:47]\n"
    "ds_read_b64_tr_b16 v[120:121], v236 offset:20480\n"
    "v_add_f32 v211, v211, v147\n"
    "v_add_f32 v212, v212, v148\n"
    "v_add_f32 v213, v213, v149\n"
    "buffer_load_dwordx4 v[146:149], v199, s[56:59], s62 offen offset:0 sc1\n"
    "v_fma_f32 v128, v128, v248, v246\n"
    "v_rcp_f32 v129, v129\n"
    "v_mfma_f32_32x32x16_bf16 v[48:63], v[112:115], a[196:199], v[48:63]\n"
    "ds_read_b64_tr_b16 v[122:123], v236 offset:20736\n"
    "s_waitcnt vmcnt(15)\n"
    "v_add_f32 v206, v206, v182\n"
    "v_add_f32 v207, v207, v183\n"
    "v_fma_f32 v130, v130, v130, 1.0\n"
    "v_add_f32 v124, 1.0, v124\n"
    "v_exp_f32 v125, v125\n"
    "v_mul_f32 v126, s80, v89\n"
    "v_cvt_pk_bf16_f32 v175, v82, v83\n"
    "v_mul_f32 v84, v128, v84\n"
    "v_fma_f32 v129, v129, v248, v246\n"
    "v_rcp_f32 v130, v130\n"
    "s_add_u32 s61, s72, 0\n"
    "buffer_load_dwordx4 a[52:55], v241, s[56:59], s61 offen offset:1024 sc1\n"
    "s_waitcnt lgkmcnt(2)\n"
    "v_mfma_f32_32x32x16_bf16 v[32:47], v[116:119], a[200:203], v[32:47]\n"
    "ds_read_b64_tr_b16 v[112:113], v236 offset:22528\n"
    "s_add_u32 m0, s64, 118784\n"
    "s_add_u32 s65, s25, 36864\n"
    "buffer_load_dwordx4 v240, s[48:51], s65 offen offset:0 lds\n"
    "v_fma_f32 v124, v124, v124, 1.0\n"
    "v_add_f32 v125, 1.0, v125\n"
    "v_mfma_f32_32x32x16_bf16 v[48:63], v[116:119], a[204:207], v[48:63]\n"
    "ds_read_b64_tr_b16 v[114:115], v236 offset:22784\n"
    "v_add_f32 v208, v208, v184\n"
    "v_add_f32 v209, v209, v185\n"
    "v_exp_f32 v126, v126\n"
    "v_mul_f32 v127, s80, v90\n"
    "v_mul_f32 v85, v129, v85\n"
    "v_fma_f32 v130, v130, v248, v246\n"
    "v_rcp_f32 v124, v124\n"
    "v_fma_f32 v125, v125, v125, 1.0\n"
    "v_add_f32 v126, 1.0, v126\n"
    "v_exp_f32 v127, v127\n"
    "s_waitcnt lgkmcnt(2)\n"
    "v_mfma_f32_32x32x16_bf16 v[32:47], v[120:123], a[208:211], v[32:47]\n"
    "ds_read_b64_tr_b16 v[116:117], v236 offset:24576\n"
    "s_add_u32 s62, s88, 360448\n"
    "buffer_load_dwordx4 v[182:185], v198, s[56:59], s62 offen offset:0 sc1\n"
    "s_waitcnt vmcnt(16)\n"
    "v_add_f32 v210, v210, v186\n"
    "v_add_f32 v211, v211, v187\n"
    "v_mul_f32 v128, s80, v91\n"
    "v_cvt_pk_bf16_f32 v176, v84, v85\n"
    "s_add_u32 s61, s72, 0\n"
    "buffer_load_dwordx4 a[56:59], v241, s[56:59], s61 offen offset:2048 sc1\n"
    "v_mfma_f32_32x32x16_bf16 v[48:63], v[120:123], a[212:215], v[48:63]\n"
    "ds_read_b64_tr_b16 v[118:119], v236 offset:24832\n"
    "v_add_f32 v212, v212, v188\n"
    "v_add_f32 v213, v213, v189\n"
    "buffer_load_dwordx4 v[186:189], v199, s[56:59], s62 offen offset:0 sc1\n"
    "v_mul_f32 v86, v130, v86\n"
    "s_waitcnt lgkmcnt(2)\n"
    "v_mfma_f32_32x32x16_bf16 v[32:47], v[112:115], a[216:219], v[32:47]\n"
    "ds_read_b64_tr_b16 v[120:121], v236 offset:26624\n"
    "s_waitcnt vmcnt(15)\n"
    "v_add_f32 v206, v206, v190\n"
    "v_add_f32 v207, v207, v191\n"
    "v_add_f32 v208, v208, v192\n"
    "v_fma_f32 v124, v124, v248, v246\n"
    "v_rcp_f32 v125, v125\n"
    "v_fma_f32 v126, v126, v126, 1.0\n"
    "v_add_f32 v127, 1.0, v127\n"
    "v_exp_f32 v128, v128\n"
    "v_mul_f32 v129, s80, v92\n"
    "v_mul_f32 v87, v124, v87\n"
    "v_fma_f32 v125, v125, v248, v246\n"
    "v_rcp_f32 v126, v126\n"
    "s_add_u32 s61, s72, 0\n"
    "buffer_load_dwordx4 a[60:63], v241, s[56:59], s61 offen offset:3072 sc1\n"
    "v_mfma_f32_32x32x16_bf16 v[48:63], v[112:115], a[220:223], v[48:63]\n"
    "ds_read_b64_tr_b16 v[122:123], v236 offset:26880\n"
    "v_add_f32 v209, v209, v193\n"
    "s_add_u32 s62, s88, 393216\n"
    "buffer_load_dwordx4 v[190:193], v198, s[56:59], s62 offen offset:0 sc1\n"
    "v_fma_f32 v127, v127, v127, 1.0\n"
    "s_waitcnt lgkmcnt(2)\n"
    "v_mfma_f32_32x32x16_bf16 v[32:47], v[116:119], a[224:227], v[32:47]\n"
    "ds_read_b64_tr_b16 v[112:113], v236 offset:28672\n"
    "s_add_u32 m0, s64, 122880\n"
    "s_add_u32 s65, s78, 0\n"
    "buffer_load_dwordx4 v240, s[48:51], s65 offen offset:0 lds\n"
    "v_add_f32 v128, 1.0, v128\n"
    "v_mfma_f32_32x32x16_bf16 v[48:63], v[116:119], a[228:231], v[48:63]\n"
    "ds_read_b64_tr_b16 v[114:115], v236 offset:28928\n"
    "s_waitcnt vmcnt(16)\n"
    "v_add_f32 v210, v210, v194\n"
    "v_add_f32 v211, v211, v195\n"
    "v_exp_f32 v129, v129\n"
    "v_mul_f32 v130, s80, v93\n"
    "v_cvt_pk_bf16_f32 v177, v86, v87\n"
    "v_mul_f32 v88, v125, v88\n"
    "v_fma_f32 v126, v126, v248, v246\n"
    "v_rcp_f32 v127, v127\n"
    "v_fma_f32 v128, v128, v128, 1.0\n"
    "v_add_f32 v129, 1.0, v129\n"
    "v_exp_f32 v130, v130\n"
    "s_waitcnt lgkmcnt(2)\n"
    "v_mfma_f32_32x32x16_bf16 v[32:47], v[120:123], a[232:235], v[32:47]\n"
    "ds_read_b64_tr_b16 v[116:117], v236 offset:30720\n"
    "v_add_f32 v212, v212, v196\n"
    "v_add_f32 v213, v213, v197\n"
    "buffer_load_dwordx4 v[194:197], v199, s[56:59], s62 offen offset:0 sc1\n"
    "v_mul_f32 v124, s80, v94\n"
    "v_mul_f32 v89, v126, v89\n"
    "v_fma_f32 v127, v127, v248, v246\n"
    "v_mfma_f32_32x32x16_bf16 v[48:63], v[120:123], a[236:239], v[48:63]\n"
    "ds_read_b64_tr_b16 v[118:119], v236 offset:30976\n"
    "s_waitcnt vmcnt(14)\n"
    "v_add_f32 v206, v206, v134\n"
    "v_add_f32 v207, v207, v135\n"
    "v_add_f32 v208, v208, v136\n"
    "v_rcp_f32 v128, v128\n"
    "v_fma_f32 v129, v129, v129, 1.0\n"
    "v_add_f32 v130, 1.0, v130\n"
    "v_exp_f32 v124, v124\n"
    "v_mul_f32 v125, s80, v95\n"
    "s_waitcnt lgkmcnt(2)\n"
    "v_mfma_f32_32x32x16_bf16 v[32:47], v[112:115], a[240:243], v[32:47]\n"
    "ds_read_b64_tr_b16 v[120:121], v236 offset:32768\n"
    "v_add_f32 v209, v209, v137\n"
    "s_add_u32 s62, s88, 425984\n"
    "buffer_load_dwordx4 v[134:137], v198, s[56:59], s62 offen offset:0 sc1\n"
    "v_cvt_pk_bf16_f32 v178, v88, v89\n"
    "v_mul_f32 v90, v127, v90\n"
    "v_fma_f32 v128, v128, v248, v246\n"
    "v_rcp_f32 v129, v129\n"
    "v_mfma_f32_32x32x16_bf16 v[48:63], v[112:115], a[244:247], v[48:63]\n"
    "ds_read_b64_tr_b16 v[122:123], v236 offset:33024\n"
    "s_waitcnt vmcnt(14)\n"
    "v_add_f32 v210, v210, v138\n"
    "v_add_f32 v211, v211, v139\n"
    "v_add_f32 v212, v212, v140\n"
    "v_fma_f32 v130, v130, v130, 1.0\n"
    "v_add_f32 v124, 1.0, v124\n"
    "v_exp_f32 v125, v125\n"
    "v_mul_f32 v91, v128, v91\n"
    "v_fma_f32 v129, v129, v248, v246\n"
    "s_waitcnt lgkmcnt(2)\n"
    "v_mfma_f32_32x32x16_bf16 v[32:47], v[116:119], a[248:251], v[32:47]\n"
    "ds_read_b64_tr_b16 v[112:113], v236 offset:34816\n"
    "s_add_u32 m0, s64, 126976\n"
    "s_add_u32 s65, s78, 36864\n"
    "buffer_load_dwordx4 v240, s[48:51], s65 offen offset:0 lds\n"
    "v_rcp_f32 v130, v130\n"
    "v_mfma_f32_32x32x16_bf16 v[48:63], v[116:119], a[252:255], v[48:63]\n"
    "ds_read_b64_tr_b16 v[114:115], v236 offset:35072\n"
    "v_add_f32 v213, v213, v141\n"
    "buffer_load_dwordx4 v[138:141], v199, s[56:59], s62 offen offset:0 sc1\n"
    "v_fma_f32 v124, v124, v124, 1.0\n"
    "v_add_f32 v125, 1.0, v125\n"
    "v_cvt_pk_bf16_f32 v179, v90, v91\n"
    "v_mul_f32 v92, v129, v92\n"
    "s_add_u32 s61, s72, 16384\n"
    "buffer_load_dwordx4 a[32:35], v241, s[56:59], s61 offen offset:0 sc1\n"
    "s_waitcnt lgkmcnt(2)\n"
    "v_mfma_f32_32x32x16_bf16 v[32:47], v[120:123], v[0:3], v[32:47]\n"
    "ds_read_b64_tr_b16 v[116:117], v236 offset:36864\n"
    "s_waitcnt vmcnt(15)\n"
    "v_add_f32 v206, v206, v142\n"
    "v_add_f32 v207, v207, v143\n"
    "v_add_f32 v208, v208, v144\n"
    "v_add_f32 v209, v209, v145\n"
    "v_fma_f32 v130, v130, v248, v246\n"
    "v_rcp_f32 v124, v124\n"
    "v_fma_f32 v125, v125, v125, 1.0\n"
    "v_mul_f32 v93, v130, v93\n"
    "v_fma_f32 v124, v124, v248, v246\n"
    "v_mfma_f32_32x32x16_bf16 v[48:63], v[120:123], v[4:7], v[48:63]\n"
    "ds_read_b64_tr_b16 v[118:119], v236 offset:37120\n"
    "s_add_u32 s62, s88, 458752\n"
    "buffer_load_dwordx4 v[142:145], v198, s[56:59], s62 offen offset:0 sc1\n"
    "v_rcp_f32 v125, v125\n"
    "v_cvt_pk_bf16_f32 v180, v92, v93\n"
    "v_mul_f32 v94, v124, v94\n"
    "s_waitcnt lgkmcnt(2)\n"
    "v_mfma_f32_32x32x16_bf16 v[32:47], v[112:115], v[8:11], v[32:47]\n"
    "ds_read_b64_tr_b16 v[120:121], v236 offset:38912\n"
    "s_waitcnt vmcnt(14)\n"
    "v_add_f32 v210, v210, v146\n"
    "v_add_f32 v211, v211, v147\n"
    "v_add_f32 v212, v212, v148\n"
    "v_add_f32 v213, v213, v149\n"
    "v_fma_f32 v125, v125, v248, v246\n"
    "v_mul_f32 v95, v125, v95\n"
    "v_cvt_pk_bf16_f32 v181, v94, v95\n"
    "v_permlane32_swap_b32 v174, v178\n"
    "v_permlane32_swap_b32 v175, v179\n"
    "v_permlane32_swap_b32 v176, v180\n"
    "s_add_u32 s61, s72, 16384\n"
    "buffer_load_dwordx4 a[36:39], v241, s[56:59], s61 offen offset:1024 sc1\n"
    "v_mfma_f32_32x32x16_bf16 v[48:63], v[112:115], v[12:15], v[48:63]\n"
    "ds_read_b64_tr_b16 v[122:123], v236 offset:39168\n"
    "buffer_load_dwordx4 v[146:149], v199, s[56:59], s62 offen offset:0 sc1\n"
    "s_waitcnt vmcnt(13)\n"
    "v_add_f32 v206, v206, v182\n"
    "v_permlane32_swap_b32 v177, v181\n"
    "v_permlane16_swap_b32 v174, v178\n"
    "v_permlane16_swap_b32 v175, v179\n"
    "v_permlane16_swap_b32 v176, v180\n"
    "s_waitcnt lgkmcnt(2)\n"
    "v_mfma_f32_32x32x16_bf16 v[32:47], v[116:119], v[16:19], v[32:47]\n"
    "s_barrier\n"
    "ds_read_b64_tr_b16 v[112:113], v250 offset:40960\n"
    "v_add_f32 v207, v207, v183\n"
    "v_add_f32 v208, v208, v184\n"
    "v_permlane16_swap_b32 v177, v181\n"
    "v_mfma_f32_32x32x16_bf16 v[48:63], v[116:119], v[20:23], v[48:63]\n"
    "ds_read_b64_tr_b16 v[114:115], v250 offset:41984\n"
    "s_add_u32 m0, s64, 131072\n"
    "s_add_u32 s65, s26, 0\n"
    "buffer_load_dwordx4 v240, s[48:51], s65 offen offset:0 lds\n"
    "s_add_u32 s60, s72, 49152\n"
    "buffer_store_dwordx4 v[174:177], v241, s[56:59], s60 offen offset:2048 sc1\n"
    "s_add_u32 s61, s72, 16384\n"
    "buffer_load_dwordx4 a[40:43], v241, s[56:59], s61 offen offset:2048 sc1\n"
    "s_waitcnt lgkmcnt(2)\n"
    "v_mfma_f32_32x32x16_bf16 v[32:47], v[120:123], v[24:27], v[32:47]\n"
    "ds_read_b64_tr_b16 v[116:117], v251 offset:40960\n"
    "v_add_f32 v209, v209, v185\n"
    "s_add_u32 s62, s88, 491520\n"
    "buffer_load_dwordx4 v[182:185], v198, s[56:59], s62 offen offset:0 sc1\n"
    "buffer_store_dwordx4 v[178:181], v241, s[56:59], s60 offen offset:3072 sc1\n"
    "v_mfma_f32_32x32x16_bf16 v[48:63], v[120:123], v[28:31], v[48:63]\n"
    "ds_read_b64_tr_b16 v[118:119], v251 offset:41984\n"
    "s_waitcnt vmcnt(16)\n"
    "v_add_f32 v210, v210, v186\n"
    "v_add_f32 v211, v211, v187\n"
    "v_add_f32 v212, v212, v188\n"
    "s_add_u32 s61, s72, 16384\n"
    "buffer_load_dwordx4 a[44:47], v241, s[56:59], s61 offen offset:3072 sc1\n"
    "s_waitcnt lgkmcnt(2)\n"
    "v_mfma_f32_16x16x32_bf16 a[64:67], v[112:115], v[150:153], 0\n"
    "ds_read_b64_tr_b16 v[120:121], v250 offset:41472\n"
    "v_add_f32 v213, v213, v189\n"
    "v_mfma_f32_16x16x32_bf16 a[68:71], v[112:115], v[154:157], 0\n"
    "ds_read_b64_tr_b16 v[122:123], v250 offset:42496\n"
    "s_add_u32 m0, s64, 135168\n"
    "v_mfma_f32_16x16x32_bf16 a[72:75], v[112:115], v[158:161], 0\n"
    "s_add_u32 s65, s26, 36864\n"
    "v_bfe_u32 v16, v241, 4, 5\n"
    "v_mul_u32_u24 v16, 168, v16\n"
    "v_mfma_f32_16x16x32_bf16 a[76:79], v[112:115], v[162:165], 0\n"
    "buffer_load_dwordx4 v240, s[48:51], s65 offen offset:0 lds\n"
    "s_waitcnt lgkmcnt(2)\n"
    "v_mfma_f32_16x16x32_bf16 a[80:83], v[116:119], v[150:153], 0\n"
    "ds_read_b64_tr_b16 v[112:113], v251 offset:41472\n"
    "buffer_load_dwordx4 v[186:189], v199, s[56:59], s62 offen offset:0 sc1\n"
    "v_lshrrev_b32 v17, 9, v241\n"
    "v_mfma_f32_16x16x32_bf16 a[84:87], v[116:119], v[154:157], 0\n"
    "ds_read_b64_tr_b16 v[114:115], v251 offset:42496\n"
    "v_lshl_add_u32 v16, v17, 3, v16\n"
    "v_mfma_f32_16x16x32_bf16 a[88:91], v[116:119], v[158:161], 0\n"
    "s_add_u32 m0, s64, 139264\n"
    "s_waitcnt vmcnt(17)\n"
    "v_add_f32 v206, v206, v190\n"
    "v_add_f32 v207, v207, v191\n"
    "v_add_f32 v208, v208, v192\n"
    "v_add_f32 v209, v209, v193\n"
    "v_add_u32 v16, 150016, v16\n"
    "v_mul_f32 v124, s80, v32\n"
    "v_mfma_f32_16x16x32_bf16 a[92:95], v[116:119], v[162:165], 0\n"
    "s_add_u32 s65, s78, 73728\n"
    "s_waitcnt lgkmcnt(2)\n"
    "v_mfma_f32_16x16x32_bf16 a[96:99], v[120:123], v[150:153], 0\n"
    "ds_read_b64_tr_b16 v[116:117], v250 offset:49152\n"
    "buffer_load_dwordx4 v240, s[48:51], s65 offen offset:0 lds\n"
    "s_waitcnt vmcnt(16)\n"
    "v_add_f32 v210, v210, v194\n"
    "v_mfma_f32_16x16x32_bf16 a[100:103], v[120:123], v[154:157], 0\n"
    "ds_read_b64_tr_b16 v[118:119], v250 offset:50176\n"
    "v_add_f32 v211, v211, v195\n"
    "v_add_f32 v212, v212, v196\n"
    "v_add_f32 v213, v213, v197\n"
    "s_waitcnt vmcnt(15)\n"
    "v_add_f32 v206, v206, v134\n"
    "v_exp_f32 v124, v124\n"
    "v_mfma_f32_16x16x32_bf16 a[104:107], v[120:123], v[158:161], 0\n"
    "v_add_f32 v207, v207, v135\n"
    "v_add_f32 v208, v208, v136\n"
    "v_add_f32 v209, v209, v137\n"
    "s_waitcnt vmcnt(13)\n"
    "v_add_f32 v210, v210, v138\n"
    "v_add_f32 v211, v211, v139\n"
    "v_add_f32 v212, v212, v140\n"
    "v_add_f32 v213, v213, v141\n"
    "v_mfma_f32_16x16x32_bf16 a[108:111], v[120:123], v[162:165], 0\n"
    "s_add_u32 m0, s64, 143360\n"
    "s_waitcnt lgkmcnt(2)\n"
    "v_mfma_f32_16x16x32_bf16 a[112:115], v[112:115], v[150:153], 0\n"
    "ds_read_b64_tr_b16 v[120:121], v251 offset:49152\n"
    "s_add_u32 s65, s78, 110592\n"
    "s_waitcnt vmcnt(11)\n"
    "v_add_f32 v206, v206, v142\n"
    "v_mul_f32 v125, s80, v33\n"
    "v_mfma_f32_16x16x32_bf16 a[116:119], v[112:115], v[154:157], 0\n"
    "ds_read_b64_tr_b16 v[122:123], v251 offset:50176\n"
    "buffer_load_dwordx4 v240, s[48:51], s65 offen offset:0 lds\n"
    "v_add_f32 v207, v207, v143\n"
    "v_mfma_f32_16x16x32_bf16 a[120:123], v[112:115], v[158:161], 0\n"
    "v_add_f32 v208, v208, v144\n"
    "v_add_f32 v209, v209, v145\n"
    "s_waitcnt vmcnt(10)\n"
    "v_add_f32 v210, v210, v146\n"
    "v_add_f32 v211, v211, v147\n"
    "v_add_f32 v212, v212, v148\n"
    "v_add_f32 v213, v213, v149\n"
    "s_waitcnt vmcnt(6)\n"
    "v_add_f32 v206, v206, v182\n"
    "v_add_f32 v124, 1.0, v124\n"
    "s_add_u32 s61, s73, 0\n"
    "buffer_load_dwordx4 a[192:195], v241, s[56:59], s61 offen offset:0 sc1\n"
    "v_mfma_f32_16x16x32_bf16 a[124:127], v[112:115], v[162:165], 0\n"
    "v_add_f32 v207, v207, v183\n"
    "v_add_f32 v208, v208, v184\n"
    "v_add_f32 v209, v209, v185\n"
    "s_waitcnt lgkmcnt(2)\n"
    "v_mfma_f32_16x16x32_bf16 a[128:131], v[116:119], v[150:153], 0\n"
    "ds_read_b64_tr_b16 v[112:113], v250 offset:49664\n"
    "s_add_u32 m0, s64, 0\n"
    "s_waitcnt vmcnt(3)\n"
    "v_add_f32 v210, v210, v186\n"
    "v_mfma_f32_16x16x32_bf16 a[132:135], v[116:119], v[154:157], 0\n"
    "ds_read_b64_tr_b16 v[114:115], v250 offset:50688\n"
    "s_add_u32 s65, s26, 73728\n"
    "v_add_f32 v211, v211, v187\n"
    "v_exp_f32 v125, v125\n"
    "v_mfma_f32_16x16x32_bf16 a[136:139], v[116:119], v[158:161], 0\n"
    "buffer_load_dwordx4 v240, s[48:51], s65 offen offset:0 lds\n"
    "v_mfma_f32_16x16x32_bf16 a[140:143], v[116:119], v[162:165], 0\n"
    "v_add_f32 v212, v212, v188\n"
    "v_add_f32 v213, v213, v189\n"
    "v_add_u32 v134, 0, v204\n"
    "v_cmp_gt_u32 vcc, 18, v134\n"
    "s_nop 1\n"
    "v_cndmask_b32_e64 v206, 0, v206, vcc\n"
    "s_add_u32 s61, s73, 0\n"
    "buffer_load_dwordx4 a[196:199], v241, s[56:59], s61 offen offset:1024 sc1\n"
    "s_waitcnt lgkmcnt(2)\n"
    "v_mfma_f32_16x16x32_bf16 a[144:147], v[120:123], v[150:153], 0\n"
    "ds_read_b64_tr_b16 v[116:117], v251 offset:49664\n"
    "v_add_u32 v134, 1, v204\n"
    "v_cmp_gt_u32 vcc, 18, v134\n"
    "s_nop 1\n"
    "v_cndmask_b32_e64 v207, 0, v207, vcc\n"
    "v_mul_f32 v126, s80, v34\n"
    "v_bfe_u32 v243, v241, 4, 2\n"
    "v_max_u32 v243, 1, v243\n"
    "v_xor_b32 v243, s69, v243\n"
    "v_lshl_add_u32 v243, v243, 6, s22\n"
    "buffer_load_dwordx2 v[234:235], v243, s[56:59], 0 offen offset:0 sc1\n"
    "v_mfma_f32_16x16x32_bf16 a[148:151], v[120:123], v[154:157], 0\n"
    "ds_read_b64_tr_b16 v[118:119], v251 offset:50688\n"
    "s_add_u32 m0, s64, 4096\n"
    "v_mfma_f32_16x16x32_bf16 a[152:155], v[120:123], v[158:161], 0\n"
    "s_add_u32 s65, s26, 110592\n"
    "v_add_u32 v134, 2, v204\n"
    "v_cmp_gt_u32 vcc, 18, v134\n"
    "s_nop 1\n"
    "v_cndmask_b32_e64 v208, 0, v208, vcc\n"
    "v_fma_f32 v124, v124, v124, 1.0\n"
    "v_mfma_f32_16x16x32_bf16 a[156:159], v[120:123], v[162:165], 0\n"
    "buffer_load_dwordx4 v240, s[48:51], s65 offen offset:0 lds\n"
    "v_add_u32 v134, 3, v204\n"
    "v_cmp_gt_u32 vcc, 18, v134\n"
    "s_nop 1\n"
    "v_cndmask_b32_e64 v209, 0, v209, vcc\n"
    "s_waitcnt lgkmcnt(2)\n"
    "v_mfma_f32_16x16x32_bf16 a[160:163], v[112:115], v[150:153], 0\n"
    "ds_read_b64_tr_b16 v[120:121], v250 offset:57344\n"
    "v_cvt_pk_bf16_f32 v206, v206, v207\n"
    "v_cvt_pk_bf16_f32 v207, v208, v209\n"
    "v_add_f32 v125, 1.0, v125\n"
    "s_add_u32 s61, s73, 0\n"
    "buffer_load_dwordx4 a[200:203], v241, s[56:59], s61 offen offset:2048 sc1\n"
    "v_mfma_f32_16x16x32_bf16 a[164:167], v[112:115], v[154:157], 0\n"
    "ds_read_b64_tr_b16 v[122:123], v250 offset:58368\n"
    "ds_write_b64 v200, v[206:207] offset:0\n"
    "ds_write_b64 v202, v[206:207] offset:0\n"
    "v_mfma_f32_16x16x32_bf16 a[168:171], v[112:115], v[158:161], 0\n"
    "s_add_u32 m0, s64, 8192\n"
    "v_add_u32 v134, 0, v205\n"
    "v_cmp_gt_u32 vcc, 18, v134\n"
    "s_nop 1\n"
    "v_cndmask_b32_e64 v210, 0, v210, vcc\n"
    "v_mfma_f32_16x16x32_bf16 a[172:175], v[112:115], v[162:165], 0\n"
    "s_add_u32 s65, s79, 0\n"
    "v_add_u32 v134, 1, v205\n"
    "v_cmp_gt_u32 vcc, 18, v134\n"
    "s_nop 1\n"
    "v_cndmask_b32_e64 v211, 0, v211, vcc\n"
    "v_exp_f32 v126, v126\n"
    "s_waitcnt lgkmcnt(4)\n"
    "v_mfma_f32_16x16x32_bf16 a[176:179], v[116:119], v[150:153], 0\n"
    "ds_read_b64_tr_b16 v[112:113], v251 offset:57344\n"
    "buffer_load_dwordx4 v240, s[48:51], s65 offen offset:0 lds\n"
    "v_mfma_f32_16x16x32_bf16 a[180:183], v[116:119], v[154:157], 0\n"
    "ds_read_b64_tr_b16 v[114:115], v251 offset:58368\n"
    "v_add_u32 v134, 2, v205\n"
    "v_cmp_gt_u32 vcc, 18, v134\n"
    "s_nop 1\n"
    "v_cndmask_b32_e64 v212, 0, v212, vcc\n"
    "s_add_u32 s61, s73, 0\n"
    "buffer_load_dwordx4 a[204:207], v241, s[56:59], s61 offen offset:3072 sc1\n"
    "v_mfma_f32_16x16x32_bf16 a[184:187], v[116:119], v[158:161], 0\n"
    "v_add_u32 v134, 3, v205\n"
    "v_cmp_gt_u32 vcc, 18, v134\n"
    "s_nop 1\n"
    "v_cndmask_b32_e64 v213, 0, v213, vcc\n"
    "v_cvt_pk_bf16_f32 v210, v210, v211\n"
    "v_cvt_pk_bf16_f32 v211, v212, v213\n"
    "ds_write_b64 v201, v[210:211] offset:0\n"
    "v_mul_f32 v127, s80, v35\n"
    "v_mfma_f32_16x16x32_bf16 a[188:191], v[116:119], v[162:165], 0\n"
    "s_add_u32 m0, s64, 12288\n"
    "s_waitcnt lgkmcnt(5)\n"
    "v_mfma_f32_16x16x32_bf16 a[64:67], v[120:123], v[166:169], a[64:67]\n"
    "ds_read_b64_tr_b16 v[116:117], v250 offset:57856\n"
    "s_add_u32 s65, s79, 36864\n"
    "ds_write_b64 v203, v[210:211] offset:0\n"
    "v_mfma_f32_16x16x32_bf16 a[68:71], v[120:123], v[170:173], a[68:71]\n"
    "ds_read_b64_tr_b16 v[118:119], v250 offset:58880\n"
    "buffer_load_dwordx4 v240, s[48:51], s65 offen offset:0 lds\n"
    "v_rcp_f32 v124, v124\n"
    "v_mfma_f32_16x16x32_bf16 a[72:75], v[120:123], v[174:177], a[72:75]\n"
    "v_fma_f32 v125, v125, v125, 1.0\n"
    "v_add_f32 v126, 1.0, v126\n"
    "v_exp_f32 v127, v127\n"
    "v_mul_f32 v128, s80, v36\n"
    "v_mfma_f32_16x16x32_bf16 a[76:79], v[120:123], v[178:181], a[76:79]\n"
    "v_fma_f32 v124, v124, v247, v245\n"
    "v_rcp_f32 v125, v125\n"
    "v_fma_f32 v126, v126, v126, 1.0\n"
    "s_waitcnt lgkmcnt(4)\n"
    "v_mfma_f32_16x16x32_bf16 a[80:83], v[112:115], v[166:169], a[80:83]\n"
    "ds_read_b64_tr_b16 v[120:121], v251 offset:57856\n"
    "s_waitcnt lgkmcnt(2)\n"
    "s_barrier\n"
    "ds_read_b64 v[134:135], v16 offset:10752\n"
    "ds_read_b64 v[136:137], v16 offset:10768\n"
    "v_mfma_f32_16x16x32_bf16 a[84:87], v[112:115], v[170:173], a[84:87]\n"
    "ds_read_b64_tr_b16 v[122:123], v251 offset:58880\n"
    "s_add_u32 m0, s64, 16384\n"
    "v_add_f32 v127, 1.0, v127\n"
    "v_mfma_f32_16x16x32_bf16 a[88:91], v[112:115], v[174:177], a[88:91]\n"
    "s_add_u32 s65, s27, 0\n"
    "v_exp_f32 v128, v128\n"
    "v_mul_f32 v129, s80, v37\n"
    "v_mfma_f32_16x16x32_bf16 a[92:95], v[112:115], v[178:181], a[92:95]\n"
    "buffer_load_dwordx4 v240, s[48:51], s65 offen offset:0 lds\n"
    "s_waitcnt lgkmcnt(4)\n"
    "v_mfma_f32_16x16x32_bf16 a[96:99], v[116:119], v[166:169], a[96:99]\n"
    "ds_read_b64 v[138:139], v16 offset:10784\n"
    "ds_read_b64 v[140:141], v16 offset:10800\n"
    "v_mul_f32 v32, v124, v32\n"
    "v_mfma_f32_16x16x32_bf16 a[100:103], v[116:119], v[170:173], a[100:103]\n"
    "v_fma_f32 v125, v125, v247, v245\n"
    "v_rcp_f32 v126, v126\n"
    "v_fma_f32 v127, v127, v127, 1.0\n"
    "v_add_f32 v128, 1.0, v128\n"
    "v_mfma_f32_16x16x32_bf16 a[104:107], v[116:119], v[174:177], a[104:107]\n"
    "v_exp_f32 v129, v129\n"
    "v_mul_f32 v130, s80, v38\n"
    "v_mul_f32 v33, v125, v33\n"
    "v_fma_f32 v126, v126, v247, v245\n"
    "v_rcp_f32 v127, v127\n"
    "v_mfma_f32_16x16x32_bf16 a[108:111], v[116:119], v[178:181], a[108:111]\n"
    "v_fma_f32 v128, v128, v128, 1.0\n"
    "v_add_f32 v129, 1.0, v129\n"
    "v_exp_f32 v130, v130\n"
    "s_waitcnt lgkmcnt(2)\n"
    "v_mfma_f32_16x16x32_bf16 a[112:115], v[120:123], v[166:169], a[112:115]\n"
    "ds_read_b64 v[142:143], v16 offset:10816\n"
    "ds_read_b64 v[144:145], v16 offset:10832\n"
    "v_mfma_f32_16x16x32_bf16 a[116:119], v[120:123], v[170:173], a[116:119]\n"
    "v_mul_f32 v124, s80, v39\n"
    "v_cvt_pk_bf16_f32 v0, v32, v33\n"
    "v_mul_f32 v34, v126, v34\n"
    "v_fma_f32 v127, v127, v247, v245\n"
    "v_rcp_f32 v128, v128\n"
    "v_mfma_f32_16x16x32_bf16 a[120:123], v[120:123], v[174:177], a[120:123]\n"
    "v_fma_f32 v129, v129, v129, 1.0\n"
    "v_add_f32 v130, 1.0, v130\n"
    "v_exp_f32 v124, v124\n"
    "v_mul_f32 v125, s80, v40\n"
    "s_add_u32 s61, s74, 0\n"
    "buffer_load_dwordx4 v[182:185], v241, s[56:59], s61 offen offset:0 sc1\n"
    "v_mfma_f32_16x16x32_bf16 a[124:127], v[120:123], v[178:181], a[124:127]\n"
    "s_add_u32 m0, s64, 20480\n"
    "v_mul_f32 v35, v127, v35\n"
    "v_mfma_f32_32x32x16_bf16 v[96:111], v[134:137], v[214:217], 0\n"
    "ds_read_b64 v[146:147], v16 offset:10848\n"
    "ds_read_b64 v[148:149], v16 offset:10864\n"
    "s_add_u32 s65, s27, 36864\n"
    "v_fma_f32 v128, v128, v247, v245\n"
    "v_rcp_f32 v129, v129\n"
    "v_fma_f32 v130, v130, v130, 1.0\n"
    "v_add_f32 v124, 1.0, v124\n"
    "v_exp_f32 v125, v125\n"
    "s_waitcnt lgkmcnt(4)\n"
    "v_mfma_f32_32x32x16_bf16 v[96:111], v[138:141], v[218:221], v[96:111]\n"
    "ds_read_b64 v[134:135], v16 offset:10880\n"
    "ds_read_b64 v[136:137], v16 offset:10896\n"
    "buffer_load_dwordx4 v240, s[48:51], s65 offen offset:0 lds\n"
    "s_add_u32 s61, s74, 0\n"
    "buffer_load_dwordx4 v[186:189], v241, s[56:59], s61 offen offset:1024 sc1\n"
    "s_waitcnt lgkmcnt(4)\n"
    "v_mfma_f32_32x32x16_bf16 v[96:111], v[142:145], v[222:225], v[96:111]\n"
    "ds_read_b64 v[138:139], v16 offset:0\n"
    "ds_read_b64 v[140:141], v16 offset:16\n"
    "v_mul_f32 v126, s80, v41\n"
    "v_cvt_pk_bf16_f32 v1, v34, v35\n"
    "v_mul_f32 v36, v128, v36\n"
    "v_fma_f32 v129, v129, v247, v245\n"
    "v_rcp_f32 v130, v130\n"
    "v_fma_f32 v124, v124, v124, 1.0\n"
    "v_add_f32 v125, 1.0, v125\n"
    "v_exp_f32 v126, v126\n"
    "s_waitcnt lgkmcnt(4)\n"
    "v_mfma_f32_32x32x16_bf16 v[96:111], v[146:149], v[226:229], v[96:111]\n"
    "ds_read_b64 v[142:143], v16 offset:32\n"
    "ds_read_b64 v[144:145], v16 offset:48\n"
    "v_mul_f32 v127, s80, v42\n"
    "v_mul_f32 v37, v129, v37\n"
    "v_fma_f32 v130, v130, v247, v245\n"
    "v_rcp_f32 v124, v124\n"
    "v_fma_f32 v125, v125, v125, 1.0\n"
    "v_add_f32 v126, 1.0, v126\n"
    "v_exp_f32 v127, v127\n"
    "s_waitcnt lgkmcnt(4)\n"
    "v_mfma_f32_32x32x16_bf16 v[96:111], v[134:137], v[230:233], v[96:111]\n"
    "ds_read_b64 v[146:147], v16 offset:64\n"
    "ds_read_b64 v[148:149], v16 offset:80\n"
    "v_mul_f32 v128, s80, v43\n"
    "v_cvt_pk_bf16_f32 v2, v36, v37\n"
    "v_mul_f32 v38, v130, v38\n"
    "v_fma_f32 v124, v124, v247, v245\n"
    "v_rcp_f32 v125, v125\n"
    "v_fma_f32 v126, v126, v126, 1.0\n"
    "v_add_f32 v127, 1.0, v127\n"
    "v_exp_f32 v128, v128\n"
    "s_add_u32 s61, s74, 0\n"
    "buffer_load_dwordx4 v[190:193], v241, s[56:59], s61 offen offset:2048 sc1\n"
    "s_waitcnt lgkmcnt(4)\n"
    "v_mfma_f32_32x32x16_bf16 v[64:79], v[138:141], v[214:217], 0\n"
    "ds_read_b64 v[134:135], v16 offset:5408\n"
    "ds_read_b64 v[136:137], v16 offset:5424\n"
    "v_mul_f32 v129, s80, v44\n"
    "v_mul_f32 v39, v124, v39\n"
    "v_fma_f32 v125, v125, v247, v245\n"
    "v_rcp_f32 v126, v126\n"
    "v_fma_f32 v127, v127, v127, 1.0\n"
    "v_add_f32 v128, 1.0, v128\n"
    "v_exp_f32 v129, v129\n"
    "s_waitcnt lgkmcnt(4)\n"
    "v_mfma_f32_32x32x16_bf16 v[64:79], v[142:145], v[218:221], v[64:79]\n"
    "ds_read_b64 v[138:139], v16 offset:5440\n"
    "ds_read_b64 v[140:141], v16 offset:5456\n"
    "v_mul_f32 v130, s80, v45\n"
    "v_cvt_pk_bf16_f32 v3, v38, v39\n"
    "v_mul_f32 v40, v125, v40\n"
    "v_fma_f32 v126, v126, v247, v245\n"
    "v_rcp_f32 v127, v127\n"
    "v_fma_f32 v128, v128, v128, 1.0\n"
    "v_add_f32 v129, 1.0, v129\n"
    "s_add_u32 s61, s74, 0\n"
    "buffer_load_dwordx4 v[194:197], v241, s[56:59], s61 offen offset:3072 sc1\n"
    "s_waitcnt lgkmcnt(4)\n"
    "v_mfma_f32_32x32x16_bf16 v[64:79], v[146:149], v[222:225], v[64:79]\n"
    "ds_read_b64 v[142:143], v16 offset:5472\n"
    "ds_read_b64 v[144:145], v16 offset:5488\n"
    "v_exp_f32 v130, v130\n"
    "v_mul_f32 v124, s80, v46\n"
    "v_mul_f32 v41, v126, v41\n"
    "v_fma_f32 v127, v127, v247, v245\n"
    "v_rcp_f32 v128, v128\n"
    "v_fma_f32 v129, v129, v129, 1.0\n"
    "v_add_f32 v130, 1.0, v130\n"
    "s_waitcnt lgkmcnt(4)\n"
    "v_mfma_f32_32x32x16_bf16 v[80:95], v[134:137], v[218:221], 0\n"
    "ds_read_b64 v[146:147], v16 offset:5504\n"
    "ds_read_b64 v[148:149], v16 offset:5520\n"
    "v_exp_f32 v124, v124\n"
    "v_mul_f32 v125, s80, v47\n"
    "v_cvt_pk_bf16_f32 v4, v40, v41\n"
    "v_mul_f32 v42, v127, v42\n"
    "v_fma_f32 v128, v128, v247, v245\n"
    "v_rcp_f32 v129, v129\n"
    "v_fma_f32 v130, v130, v130, 1.0\n"
    "v_add_f32 v124, 1.0, v124\n"
    "s_waitcnt lgkmcnt(4)\n"
    "v_mfma_f32_32x32x16_bf16 v[80:95], v[138:141], v[222:225], v[80:95]\n"
    "v_exp_f32 v125, v125\n"
    "v_mul_f32 v43, v128, v43\n"
    "v_fma_f32 v129, v129, v247, v245\n"
    "v_rcp_f32 v130, v130\n"
    "v_fma_f32 v124, v124, v124, 1.0\n"
    "v_add_f32 v125, 1.0, v125\n"
    "v_cvt_pk_bf16_f32 v5, v42, v43\n"
    "v_mul_f32 v44, v129, v44\n"
    "v_fma_f32 v130, v130, v247, v245\n"
    "v_rcp_f32 v124, v124\n"
    "v_fma_f32 v125, v125, v125, 1.0\n"
    "v_mul_f32 v45, v130, v45\n"
    "s_waitcnt lgkmcnt(2)\n"
    "v_mfma_f32_32x32x16_bf16 v[80:95], v[142:145], v[226:229], v[80:95]\n"
    "ds_read_b64_tr_b16 v[112:113], v252 offset:0\n"
    "ds_read_b64_tr_b16 v[114:115], v252 offset:1024\n"
    "s_add_u32 m0, s64, 24576\n"
    "s_add_u32 s65, s77, 73728\n"
    "buffer_load_dwordx4 v240, s[48:51], s65 offen offset:0 lds\n"
    "v_fma_f32 v124, v124, v247, v245\n"
    "s_waitcnt lgkmcnt(2)\n"
    "v_mfma_f32_32x32x16_bf16 v[80:95], v[146:149], v[230:233], v[80:95]\n"
    "ds_read_b64_tr_b16 v[116:117], v253 offset:0\n"
    "ds_read_b64_tr_b16 v[118:119], v253 offset:1024\n"
    "v_rcp_f32 v125, v125\n"
    "v_cvt_pk_bf16_f32 v6, v44, v45\n"
    "v_mul_f32 v46, v124, v46\n"
    "v_fma_f32 v125, v125, v247, v245\n"
    "v_mul_f32 v47, v125, v47\n"
    "v_cvt_pk_bf16_f32 v7, v46, v47\n"
    "v_permlane32_swap_b32 v0, v4\n"
    "v_permlane32_swap_b32 v1, v5\n"
    "s_waitcnt lgkmcnt(2)\n"
    "v_mfma_f32_16x16x32_bf16 a[128:131], v[112:115], v[166:169], a[128:131]\n"
    "ds_read_b64_tr_b16 v[120:121], v252 offset:512\n"
    "v_permlane32_swap_b32 v2, v6\n"
    "v_permlane32_swap_b32 v3, v7\n"
    "v_permlane16_swap_b32 v0, v4\n"
    "v_mfma_f32_16x16x32_bf16 a[132:135], v[112:115], v[170:173], a[132:135]\n"
    "ds_read_b64_tr_b16 v[122:123], v252 offset:1536\n"
    "v_permlane16_swap_b32 v1, v5\n"
    "v_permlane16_swap_b32 v2, v6\n"
    "v_permlane16_swap_b32 v3, v7\n"
    "v_mfma_f32_16x16x32_bf16 a[136:139], v[112:115], v[174:177], a[136:139]\n"
    "s_add_u32 s60, s72, 65536\n"
    "buffer_store_dwordx4 v[0:3], v241, s[56:59], s60 offen offset:0 sc1\n"
    "buffer_store_dwordx4 v[4:7], v241, s[56:59], s60 offen offset:1024 sc1\n"
    "v_mfma_f32_16x16x32_bf16 a[140:143], v[112:115], v[178:181], a[140:143]\n"
    "v_mul_f32 v124, s80, v48\n"
    "v_exp_f32 v124, v124\n"
    "s_waitcnt lgkmcnt(2)\n"
    "v_mfma_f32_16x16x32_bf16 a[144:147], v[116:119], v[166:169], a[144:147]\n"
    "ds_read_b64_tr_b16 v[112:113], v253 offset:512\n"
    "s_add_u32 s99, s70, 4\n"
    "v_mov_b32 v133, s21\n"
    "v_mov_b32 v132, s99\n"
    "buffer_store_dwordx2 v[132:133], v242, s[56:59], s89 offen offset:0 sc1\n"
    "v_mfma_f32_16x16x32_bf16 a[148:151], v[116:119], v[170:173], a[148:151]\n"
    "ds_read_b64_tr_b16 v[114:115], v253 offset:1536\n"
    "v_mul_f32 v125, s80, v49\n"
    "v_add_f32 v124, 1.0, v124\n"
    "v_exp_f32 v125, v125\n"
    "v_mfma_f32_16x16x32_bf16 a[152:155], v[116:119], v[174:177], a[152:155]\n"
    "v_add_f32 v20, 0x358637bd, v100\n"
    "v_add_f32 v21, 0x358637bd, v101\n"
    "v_add_f32 v22, 0x358637bd, v102\n"
    "s_add_u32 s61, s74, 16384\n"
    "buffer_load_dwordx4 v[198:201], v241, s[56:59], s61 offen offset:0 sc1\n"
    "v_mfma_f32_16x16x32_bf16 a[156:159], v[116:119], v[178:181], a[156:159]\n"
    "v_add_f32 v23, 0x358637bd, v103\n"
    "v_rcp_f32 v20, v20\n"
    "s_waitcnt lgkmcnt(2)\n"
    "v_mfma_f32_16x16x32_bf16 a[160:163], v[120:123], v[166:169], a[160:163]\n"
    "ds_read_b64_tr_b16 v[116:117], v252 offset:8192\n"
    "s_add_u32 m0, s64, 28672\n"
    "v_mul_f32 v126, s80, v50\n"
    "s_waitcnt vmcnt(16)\n"
    "v_subrev_u32 v234, s70, v234\n"
    "v_cmp_eq_u32_e64 s[16:17], s21, v235\n"
    "v_subrev_u32 v234, 3, v234\n"
    "v_cmp_gt_u32 vcc, 16, v234\n"
    "s_nop 0\n"
    "s_and_b64 vcc, vcc, s[16:17]\n"
    "s_cmp_eq_u64 vcc, -1\n"
    "s_cbranch_scc0 LslowQ2_%=\n"
    "LbackQ2_%=:\n"
    "v_mfma_f32_16x16x32_bf16 a[164:167], v[120:123], v[170:173], a[164:167]\n"
    "ds_read_b64_tr_b16 v[118:119], v252 offset:9216\n"
    "s_add_u32 s65, s77, 110592\n"
    "v_mfma_f32_16x16x32_bf16 a[168:171], v[120:123], v[174:177], a[168:171]\n"
    "buffer_load_dwordx4 v240, s[48:51], s65 offen offset:0 lds\n"
    "v_fma_f32 v124, v124, v124, 1.0\n"
    "v_mfma_f32_16x16x32_bf16 a[172:175], v[120:123], v[178:181], a[172:175]\n"
    "v_rcp_f32 v21, v21\n"
    "s_add_u32 s61, s74, 16384\n"
    "buffer_load_dwordx4 v[202:205], v241, s[56:59], s61 offen offset:1024 sc1\n"
    "s_waitcnt lgkmcnt(2)\n"
    "v_mfma_f32_16x16x32_bf16 a[176:179], v[112:115], v[166:169], a[176:179]\n"
    "ds_read_b64_tr_b16 v[120:121], v253 offset:8192\n"
    "v_rcp_f32 v22, v22\n"
    "v_add_f32 v125, 1.0, v125\n"
    "v_mfma_f32_16x16x32_bf16 a[180:183], v[112:115], v[170:173], a[180:183]\n"
    "ds_read_b64_tr_b16 v[122:123], v253 offset:9216\n"
    "v_rcp_f32 v23, v23\n"
    "v_mfma_f32_16x16x32_bf16 a[184:187], v[112:115], v[174:177], a[184:187]\n"
    "v_mul_f32 v20, v249, v20\n"
    "v_mul_f32 v21, v249, v21\n"
    "v_mul_f32 v22, v249, v22\n"
    "v_mfma_f32_16x16x32_bf16 a[188:191], v[112:115], v[178:181], a[188:191]\n"
    "v_mul_f32 v23, v249, v23\n"
    "v_cndmask_b32_e64 v24, v20, v21, s[94:95]\n"
    "v_exp_f32 v126, v126\n"
    "s_waitcnt lgkmcnt(2)\n"
    "v_mfma_f32_16x16x32_bf16 a[64:67], v[116:119], a[48:51], a[64:67]\n"
    "ds_read_b64_tr_b16 v[112:113], v252 offset:8704\n"
    "v_cndmask_b32_e64 v25, v21, v22, s[94:95]\n"
    "s_add_u32 s61, s74, 16384\n"
    "buffer_load_dwordx4 v[206:209], v241, s[56:59], s61 offen offset:2048 sc1\n"
    "v_mfma_f32_16x16x32_bf16 a[68:71], v[116:119], a[52:55], a[68:71]\n"
    "ds_read_b64_tr_b16 v[114:115], v252 offset:9728\n"
    "v_cndmask_b32_e64 v26, v22, v23, s[94:95]\n"
    "v_mul_f32 v96, v23, v96\n"
    "v_mfma_f32_16x16x32_bf16 a[72:75], v[116:119], a[56:59], a[72:75]\n"
    "v_mul_f32 v97, v23, v97\n"
    "v_mul_f32 v98, v23, v98\n"
    "v_mul_f32 v99, v23, v99\n"
    "v_cvt_pk_bf16_f32 v96, v96, v97\n"
    "v_mul_f32 v127, s80, v51\n"
    "v_mfma_f32_16x16x32_bf16 a[76:79], v[116:119], a[60:63], a[76:79]\n"
    "s_add_u32 m0, s64, 32768\n"
    "s_waitcnt lgkmcnt(2)\n"
    "v_mfma_f32_16x16x32_bf16 a[80:83], v[120:123], a[48:51], a[80:83]\n"
    "ds_read_b64_tr_b16 v[116:117], v253 offset:8704\n"
    "s_add_u32 s65, s25, 73728\n"
    "v_mfma_f32_16x16x32_bf16 a[84:87], v[120:123], a[52:55], a[84:87]\n"
    "ds_read_b64_tr_b16 v[118:119], v253 offset:9728\n"
    "buffer_load_dwordx4 v240, s[48:51], s65 offen offset:0 lds\n"
    "v_rcp_f32 v124, v124\n"
    "s_add_u32 s61, s74, 16384\n"
    "buffer_load_dwordx4 v[210:213], v241, s[56:59], s61 offen offset:3072 sc1\n"
    "v_mfma_f32_16x16x32_bf16 a[88:91], v[120:123], a[56:59], a[88:91]\n"
    "v_cvt_pk_bf16_f32 v97, v98, v99\n"
    "v_mov_b32 v98, 0\n"
    "v_mov_b32 v99, 0\n"
    "v_mov_b32 v100, 0\n"
    "v_mfma_f32_16x16x32_bf16 a[92:95], v[120:123], a[60:63], a[92:95]\n"
    "v_mov_b32 v101, 0\n"
    "v_mov_b32 v102, 0\n"
    "v_fma_f32 v125, v125, v125, 1.0\n"
    "s_waitcnt lgkmcnt(2)\n"
    "v_mfma_f32_16x16x32_bf16 a[96:99], v[112:115], a[48:51], a[96:99]\n"
    "ds_read_b64_tr_b16 v[120:121], v252 offset:16384\n"
    "v_mov_b32 v103, 0\n"
    "v_mfma_f32_16x16x32_bf16 a[100:103], v[112:115], a[52:55], a[100:103]\n"
    "ds_read_b64_tr_b16 v[122:123], v252 offset:17408\n"
    "v_permlane32_swap_b32 v96, v100\n"
    "v_permlane32_swap_b32 v97, v101\n"
    "v_add_f32 v126, 1.0, v126\n"
    "v_mfma_f32_16x16x32_bf16 a[104:107], v[112:115], a[56:59], a[104:107]\n"
    "v_permlane32_swap_b32 v98, v102\n"
    "v_permlane32_swap_b32 v99, v103\n"
    "v_permlane16_swap_b32 v96, v100\n"
    "v_permlane16_swap_b32 v97, v101\n"
    "v_mfma_f32_16x16x32_bf16 a[108:111], v[112:115], a[60:63], a[108:111]\n"
    "v_permlane16_swap_b32 v98, v102\n"
    "s_waitcnt lgkmcnt(2)\n"
    "v_mfma_f32_16x16x32_bf16 a[112:115], v[116:119], a[48:51], a[112:115]\n"
    "ds_read_b64_tr_b16 v[112:113], v253 offset:16384\n"
    "v_permlane16_swap_b32 v99, v103\n"
    "v_exp_f32 v127, v127\n"
    "v_mfma_f32_16x16x32_bf16 a[116:119], v[116:119], a[52:55], a[116:119]\n"
    "ds_read_b64_tr_b16 v[114:115], v253 offset:17408\n"
    "s_add_u32 m0, s64, 36864\n"
    "v_mfma_f32_16x16x32_bf16 a[120:123], v[116:119], a[56:59], a[120:123]\n"
    "s_add_u32 s65, s25, 110592\n"
    "s_add_u32 s63, s85, 4096\n"
    "buffer_store_dwordx4 v[96:99], v241, s[56:59], s63 offen offset:0 sc1\n"
    "v_mfma_f32_16x16x32_bf16 a[124:127], v[116:119], a[60:63], a[124:127]\n"
    "buffer_load_dwordx4 v240, s[48:51], s65 offen offset:0 lds\n"
    "v_mul_f32 v128, s80, v52\n"
    "s_waitcnt lgkmcnt(2)\n"
    "v_mfma_f32_16x16x32_bf16 a[128:131], v[120:123], a[48:51], a[128:131]\n"
    "ds_read_b64_tr_b16 v[116:117], v252 offset:16896\n"
    "v_fma_f32 v124, v124, v248, v246\n"
    "v_rcp_f32 v125, v125\n"
    "v_mfma_f32_16x16x32_bf16 a[132:135], v[120:123], a[52:55], a[132:135]\n"
    "ds_read_b64_tr_b16 v[118:119], v252 offset:17920\n"
    "buffer_store_dwordx4 v[100:103], v241, s[56:59], s63 offen offset:1024 sc1\n"
    "v_mfma_f32_16x16x32_bf16 a[136:139], v[120:123], a[56:59], a[136:139]\n"
    "v_mul_f32 v64, v20, v64\n"
    "v_mul_f32 v65, v20, v65\n"
    "v_mul_f32 v66, v20, v66\n"
    "v_mul_f32 v67, v20, v67\n"
    "v_fma_f32 v126, v126, v126, 1.0\n"
    "v_mfma_f32_16x16x32_bf16 a[140:143], v[120:123], a[60:63], a[140:143]\n"
    "v_mul_f32 v68, v20, v68\n"
    "s_waitcnt lgkmcnt(2)\n"
    "v_mfma_f32_16x16x32_bf16 a[144:147], v[112:115], a[48:51], a[144:147]\n"
    "ds_read_b64_tr_b16 v[120:121], v253 offset:16896\n"
    "v_mul_f32 v69, v20, v69\n"
    "v_mul_f32 v70, v20, v70\n"
    "v_add_f32 v127, 1.0, v127\n"
    "v_mfma_f32_16x16x32_bf16 a[148:151], v[112:115], a[52:55], a[148:151]\n"
    "ds_read_b64_tr_b16 v[122:123], v253 offset:17920\n"
    "v_mul_f32 v71, v20, v71\n"
    "v_mul_f32 v72, v24, v72\n"
    "v_mfma_f32_16x16x32_bf16 a[152:155], v[112:115], a[56:59], a[152:155]\n"
    "v_mul_f32 v73, v24, v73\n"
    "v_mul_f32 v74, v21, v74\n"
    "v_mul_f32 v75, v21, v75\n"
    "s_add_u32 s61, s75, 0\n"
    "buffer_load_dwordx4 v[166:169], v241, s[56:59], s61 offen offset:0 sc1\n"
    "v_mfma_f32_16x16x32_bf16 a[156:159], v[112:115], a[60:63], a[156:159]\n"
    "v_mul_f32 v76, v21, v76\n"
    "v_mul_f32 v77, v21, v77\n"
    "v_exp_f32 v128, v128\n"
    "s_waitcnt lgkmcnt(2)\n"
    "v_mfma_f32_16x16x32_bf16 a[160:163], v[116:119], a[48:51], a[160:163]\n"
    "s_barrier\n"
    "ds_read_b64_tr_b16 v[112:113], v252 offset:24576\n"
    "v_mul_f32 v78, v21, v78\n"
    "v_mfma_f32_16x16x32_bf16 a[164:167], v[116:119], a[52:55], a[164:167]\n"
    "ds_read_b64_tr_b16 v[114:115], v252 offset:25600\n"
    "s_add_u32 m0, s64, 40960\n"
    "v_mfma_f32_16x16x32_bf16 a[168:171], v[116:119], a[56:59], a[168:171]\n"
    "s_add_u32 s65, s78, 147456\n"
    "v_mul_f32 v79, v21, v79\n"
    "v_cvt_pk_bf16_f32 v64, v64, v65\n"
    "v_mul_f32 v129, s80, v53\n"
    "v_mfma_f32_16x16x32_bf16 a[172:175], v[116:119], a[60:63], a[172:175]\n"
    "buffer_load_dwordx4 v240, s[48:51], s65 offen offset:0 lds\n"
    "s_add_u32 s61, s75, 0\n"
    "buffer_load_dwordx4 v[170:173], v241, s[56:59], s61 offen offset:1024 sc1\n"
    "s_waitcnt lgkmcnt(2)\n"
    "v_mfma_f32_16x16x32_bf16 a[176:179], v[120:123], a[48:51], a[176:179]\n"
    "ds_read_b64_tr_b16 v[116:117], v253 offset:24576\n"
    "v_cvt_pk_bf16_f32 v65, v66, v67\n"
    "v_cvt_pk_bf16_f32 v66, v68, v69\n"
    "v_mul_f32 v48, v124, v48\n"
    "v_mfma_f32_16x16x32_bf16 a[180:183], v[120:123], a[52:55], a[180:183]\n"
    "ds_read_b64_tr_b16 v[118:119], v253 offset:25600\n"
    "v_cvt_pk_bf16_f32 v67, v70, v71\n"
    "v_cvt_pk_bf16_f32 v68, v72, v73\n"
    "v_fma_f32 v125, v125, v248, v246\n"
    "v_mfma_f32_16x16x32_bf16 a[184:187], v[120:123], a[56:59], a[184:187]\n"
    "v_cvt_pk_bf16_f32 v69, v74, v75\n"
    "v_cvt_pk_bf16_f32 v70, v76, v77\n"
    "v_cvt_pk_bf16_f32 v71, v78, v79\n"
    "v_mfma_f32_16x16x32_bf16 a[188:191], v[120:123], a[60:63], a[188:191]\n"
    "v_permlane32_swap_b32 v64, v68\n"
    "v_permlane32_swap_b32 v65, v69\n"
    "s_waitcnt lgkmcnt(2)\n"
    "v_mfma_f32_16x16x32_bf16 a[64:67], v[112:115], a[32:35], a[64:67]\n"
    "ds_read_b64_tr_b16 v[120:121], v252 offset:25088\n"
    "s_add_u32 m0, s64, 45056\n"
    "v_rcp_f32 v126, v126\n"
    "s_add_u32 s61, s75, 0\n"
    "buffer_load_dwordx4 v[174:177], v241, s[56:59], s61 offen offset:2048 sc1\n"
    "v_mfma_f32_16x16x32_bf16 a[68:71], v[112:115], a[36:39], a[68:71]\n"
    "ds_read_b64_tr_b16 v[122:123], v252 offset:26112\n"
    "s_add_u32 s65, s78, 184320\n"
    "v_mfma_f32_16x16x32_bf16 a[72:75], v[112:115], a[40:43], a[72:75]\n"
    "buffer_load_dwordx4 v240, s[48:51], s65 offen offset:0 lds\n"
    "v_permlane32_swap_b32 v66, v70\n"
    "v_mfma_f32_16x16x32_bf16 a[76:79], v[112:115], a[44:47], a[76:79]\n"
    "v_permlane32_swap_b32 v67, v71\n"
    "v_fma_f32 v127, v127, v127, 1.0\n"
    "s_waitcnt lgkmcnt(2)\n"
    "v_mfma_f32_16x16x32_bf16 a[80:83], v[116:119], a[32:35], a[80:83]\n"
    "ds_read_b64_tr_b16 v[112:113], v253 offset:25088\n"
    "v_permlane16_swap_b32 v64, v68\n"
    "v_permlane16_swap_b32 v65, v69\n"
    "v_mfma_f32_16x16x32_bf16 a[84:87], v[116:119], a[36:39], a[84:87]\n"
    "ds_read_b64_tr_b16 v[114:115], v253 offset:26112\n"
    "v_permlane16_swap_b32 v66, v70\n"
    "v_permlane16_swap_b32 v67, v71\n"
    "v_add_f32 v128, 1.0, v128\n"
    "s_add_u32 s61, s75, 0\n"
    "buffer_load_dwordx4 v[178:181], v241, s[56:59], s61 offen offset:3072 sc1\n"
    "v_mfma_f32_16x16x32_bf16 a[88:91], v[116:119], a[40:43], a[88:91]\n"
    "s_add_u32 s63, s85, 0\n"
    "buffer_store_dwordx4 v[64:67], v241, s[56:59], s63 offen offset:0 sc1\n"
    "v_mfma_f32_16x16x32_bf16 a[92:95], v[116:119], a[44:47], a[92:95]\n"
    "v_exp_f32 v129, v129\n"
    "v_mul_f32 v130, s80, v54\n"
    "s_waitcnt lgkmcnt(2)\n"
    "v_mfma_f32_16x16x32_bf16 a[96:99], v[120:123], a[32:35], a[96:99]\n"
    "ds_read_b64_tr_b16 v[116:117], v252 offset:32768\n"
    "s_add_u32 m0, s64, 49152\n"
    "buffer_store_dwordx4 v[68:71], v241, s[56:59], s63 offen offset:1024 sc1\n"
    "v_mul_f32 v49, v125, v49\n"
    "v_mfma_f32_16x16x32_bf16 a[100:103], v[120:123], a[36:39], a[100:103]\n"
    "ds_read_b64_tr_b16 v[118:119], v252 offset:33792\n"
    "s_add_u32 s65, s26, 147456\n"
    "v_mfma_f32_16x16x32_bf16 a[104:107], v[120:123], a[40:43], a[104:107]\n"
    "buffer_load_dwordx4 v240, s[48:51], s65 offen offset:0 lds\n"
    "v_fma_f32 v126, v126, v248, v246\n"
    "v_mfma_f32_16x16x32_bf16 a[108:111], v[120:123], a[44:47], a[108:111]\n"
    "v_mul_f32 v80, v25, v80\n"
    "s_waitcnt lgkmcnt(2)\n"
    "v_mfma_f32_16x16x32_bf16 a[112:115], v[112:115], a[32:35], a[112:115]\n"
    "ds_read_b64_tr_b16 v[120:121], v253 offset:32768\n"
    "v_mul_f32 v81, v25, v81\n"
    "v_mul_f32 v82, v25, v82\n"
    "v_mfma_f32_16x16x32_bf16 a[116:119], v[112:115], a[36:39], a[116:119]\n"
    "ds_read_b64_tr_b16 v[122:123], v253 offset:33792\n"
    "v_mul_f32 v83, v25, v83\n"
    "v_mul_f32 v84, v22, v84\n"
    "v_rcp_f32 v127, v127\n"
    "v_mfma_f32_16x16x32_bf16 a[120:123], v[112:115], a[40:43], a[120:123]\n"
    "v_mul_f32 v85, v22, v85\n"
    "v_mul_f32 v86, v22, v86\n"
    "v_mul_f32 v87, v22, v87\n"
    "v_mfma_f32_16x16x32_bf16 a[124:127], v[112:115], a[44:47], a[124:127]\n"
    "v_mul_f32 v88, v22, v88\n"
    "v_mul_f32 v89, v22, v89\n"
    "s_waitcnt lgkmcnt(2)\n"
    "v_mfma_f32_16x16x32_bf16 a[128:131], v[116:119], a[32:35], a[128:131]\n"
    "ds_read_b64_tr_b16 v[112:113], v252 offset:33280\n"
    "s_add_u32 m0, s64, 53248\n"
    "v_fma_f32 v128, v128, v128, 1.0\n"
    "v_mfma_f32_16x16x32_bf16 a[132:135], v[116:119], a[36:39], a[132:135]\n"
    "ds_read_b64_tr_b16 v[114:115], v252 offset:34304\n"
    "s_add_u32 s65, s26, 184320\n"
    "v_mfma_f32_16x16x32_bf16 a[136:139], v[116:119], a[40:43], a[136:139]\n"
    "buffer_load_dwordx4 v240, s[48:51], s65 offen offset:0 lds\n"
    "v_mul_f32 v90, v26, v90\n"
    "v_add_f32 v129, 1.0, v129\n"
    "v_mfma_f32_16x16x32_bf16 a[140:143], v[116:119], a[44:47], a[140:143]\n"
    "v_mul_f32 v91, v26, v91\n"
    "s_waitcnt lgkmcnt(2)\n"
    "v_mfma_f32_16x16x32_bf16 a[144:147], v[120:123], a[32:35], a[144:147]\n"
    "ds_read_b64_tr_b16 v[116:117], v253 offset:33280\n"
    "v_mul_f32 v92, v23, v92\n"
    "v_mul_f32 v93, v23, v93\n"
    "v_exp_f32 v130, v130\n"
    "v_mfma_f32_16x16x32_bf16 a[148:151], v[120:123], a[36:39], a[148:151]\n"
    "ds_read_b64_tr_b16 v[118:119], v253 offset:34304\n"
    "v_mul_f32 v94, v23, v94\n"
    "v_mul_f32 v95, v23, v95\n"
    "v_mfma_f32_16x16x32_bf16 a[152:155], v[120:123], a[40:43], a[152:155]\n"
    "v_cvt_pk_bf16_f32 v80, v80, v81\n"
    "v_cvt_pk_bf16_f32 v81, v82, v83\n"
    "v_cvt_pk_bf16_f32 v82, v84, v85\n"
    "s_add_u32 s61, s73, 16384\n"
    "buffer_load_dwordx4 v[150:153], v241, s[56:59], s61 offen offset:0 sc1\n"
    "v_mfma_f32_16x16x32_bf16 a[156:159], v[120:123], a[44:47], a[156:159]\n"
    "v_cvt_pk_bf16_f32 v83, v86, v87\n"
    "v_cvt_pk_bf16_f32 v84, v88, v89\n"
    "v_mul_f32 v124, s80, v55\n"
    "s_waitcnt lgkmcnt(2)\n"
    "v_mfma_f32_16x16x32_bf16 a[160:163], v[112:115], a[32:35], a[160:163]\n"
    "ds_read_b64_tr_b16 v[120:121], v252 offset:40960\n"
    "s_add_u32 m0, s64, 57344\n"
    "v_mfma_f32_16x16x32_bf16 a[164:167], v[112:115], a[36:39], a[164:167]\n"
    "ds_read_b64_tr_b16 v[122:123], v252 offset:41984\n"
    "s_add_u32 s65, s79, 73728\n"
    "v_cvt_pk_bf16_f32 v85, v90, v91\n"
    "v_cvt_pk_bf16_f32 v8, v48, v49\n"
    "v_mfma_f32_16x16x32_bf16 a[168:171], v[112:115], a[40:43], a[168:171]\n"
    "buffer_load_dwordx4 v240, s[48:51], s65 offen offset:0 lds\n"
    "v_mfma_f32_16x16x32_bf16 a[172:175], v[112:115], a[44:47], a[172:175]\n"
    "v_cvt_pk_bf16_f32 v86, v92, v93\n"
    "v_cvt_pk_bf16_f32 v87, v94, v95\n"
    "v_mul_f32 v50, v126, v50\n"
    "s_add_u32 s61, s73, 16384\n"
    "buffer_load_dwordx4 v[154:157], v241, s[56:59], s61 offen offset:1024 sc1\n"
    "s_waitcnt lgkmcnt(2)\n"
    "v_mfma_f32_16x16x32_bf16 a[176:179], v[116:119], a[32:35], a[176:179]\n"
    "ds_read_b64_tr_b16 v[112:113], v253 offset:40960\n"
    "v_permlane32_swap_b32 v80, v84\n"
    "v_permlane32_swap_b32 v81, v85\n"
    "v_mfma_f32_16x16x32_bf16 a[180:183], v[116:119], a[36:39], a[180:183]\n"
    "ds_read_b64_tr_b16 v[114:115], v253 offset:41984\n"
    "v_permlane32_swap_b32 v82, v86\n"
    "v_permlane32_swap_b32 v83, v87\n"
    "v_fma_f32 v127, v127, v248, v246\n"
    "v_mfma_f32_16x16x32_bf16 a[184:187], v[116:119], a[40:43], a[184:187]\n"
    "v_permlane16_swap_b32 v80, v84\n"
    "v_permlane16_swap_b32 v81, v85\n"
    "v_permlane16_swap_b32 v82, v86\n"
    "v_mfma_f32_16x16x32_bf16 a[188:191], v[116:119], a[44:47], a[188:191]\n"
    "v_permlane16_swap_b32 v83, v87\n"
    "s_waitcnt lgkmcnt(2)\n"
    "v_mfma_f32_16x16x32_bf16 a[64:67], v[120:123], a[192:195], a[64:67]\n"
    "ds_read_b64_tr_b16 v[116:117], v252 offset:41472\n"
    "s_add_u32 m0, s64, 61440\n"
    "v_rcp_f32 v128, v128\n"
    "s_add_u32 s61, s73, 16384\n"
    "buffer_load_dwordx4 v[158:161], v241, s[56:59], s61 offen offset:2048 sc1\n"
    "v_mfma_f32_16x16x32_bf16 a[68:71], v[120:123], a[196:199], a[68:71]\n"
    "ds_read_b64_tr_b16 v[118:119], v252 offset:42496\n"
    "s_add_u32 s65, s79, 110592\n"
    "s_waitcnt vmcnt(36)\n"
    "v_mfma_f32_16x16x32_bf16 a[72:75], v[120:123], a[200:203], a[72:75]\n"
    "buffer_load_dwordx4 v240, s[48:51], s65 offen offset:0 lds\n"
    "s_waitcnt vmcnt(35)\n"
    "v_mfma_f32_16x16x32_bf16 a[76:79], v[120:123], a[204:207], a[76:79]\n"
    "s_add_u32 s63, s85, 2048\n"
    "buffer_store_dwordx4 v[80:83], v241, s[56:59], s63 offen offset:0 sc1\n"
    "v_fma_f32 v129, v129, v129, 1.0\n"
    "s_waitcnt lgkmcnt(2)\n"
    "v_mfma_f32_16x16x32_bf16 a[80:83], v[112:115], a[192:195], a[80:83]\n"
    "ds_read_b64_tr_b16 v[120:121], v253 offset:41472\n"
    "v_add_f32 v130, 1.0, v130\n"
    "v_exp_f32 v124, v124\n"
    "v_mfma_f32_16x16x32_bf16 a[84:87], v[112:115], a[196:199], a[84:87]\n"
    "ds_read_b64_tr_b16 v[122:123], v253 offset:42496\n"
    "buffer_store_dwordx4 v[84:87], v241, s[56:59], s63 offen offset:1024 sc1\n"
    "s_add_u32 s61, s73, 16384\n"
    "buffer_load_dwordx4 v[162:165], v241, s[56:59], s61 offen offset:3072 sc1\n"
    "v_mfma_f32_16x16x32_bf16 a[88:91], v[112:115], a[200:203], a[88:91]\n"
    "v_mul_f32 v125, s80, v56\n"
    "v_mul_f32 v51, v127, v51\n"
    "v_fma_f32 v128, v128, v248, v246\n"
    "v_rcp_f32 v129, v129\n"
    "v_fma_f32 v130, v130, v130, 1.0\n"
    "v_mfma_f32_16x16x32_bf16 a[92:95], v[112:115], a[204:207], a[92:95]\n"
    "v_add_f32 v124, 1.0, v124\n"
    "v_exp_f32 v125, v125\n"
    "v_mul_f32 v126, s80, v57\n"
    "s_waitcnt lgkmcnt(2)\n"
    "v_mfma_f32_16x16x32_bf16 a[96:99], v[116:119], a[192:195], a[96:99]\n"
    "s_barrier\n"
    "ds_read_b64_tr_b16 v[112:113], v252 offset:49152\n"
    "v_mfma_f32_16x16x32_bf16 a[100:103], v[116:119], a[196:199], a[100:103]\n"
    "ds_read_b64_tr_b16 v[114:115], v252 offset:50176\n"
    "s_add_u32 m0, s64, 65536\n"
    "v_cvt_pk_bf16_f32 v9, v50, v51\n"
    "v_mfma_f32_16x16x32_bf16 a[104:107], v[116:119], a[200:203], a[104:107]\n"
    "s_add_u32 s65, s27, 73728\n"
    "v_mul_f32 v52, v128, v52\n"
    "v_fma_f32 v129, v129, v248, v246\n"
    "v_mfma_f32_16x16x32_bf16 a[108:111], v[116:119], a[204:207], a[108:111]\n"
    "buffer_load_dwordx4 v240, s[48:51], s65 offen offset:0 lds\n"
    "v_rcp_f32 v130, v130\n"
    "s_waitcnt lgkmcnt(2)\n"
    "v_mfma_f32_16x16x32_bf16 a[112:115], v[120:123], a[192:195], a[112:115]\n"
    "ds_read_b64_tr_b16 v[116:117], v253 offset:49152\n"
    "v_fma_f32 v124, v124, v124, 1.0\n"
    "v_add_f32 v125, 1.0, v125\n"
    "v_mfma_f32_16x16x32_bf16 a[116:119], v[120:123], a[196:199], a[116:119]\n"
    "ds_read_b64_tr_b16 v[118:119], v253 offset:50176\n"
    "v_exp_f32 v126, v126\n"
    "v_mul_f32 v127, s80, v58\n"
    "v_mul_f32 v53, v129, v53\n"
    "v_mfma_f32_16x16x32_bf16 a[120:123], v[120:123], a[200:203], a[120:123]\n"
    "v_fma_f32 v130, v130, v248, v246\n"
    "v_rcp_f32 v124, v124\n"
    "v_fma_f32 v125, v125, v125, 1.0\n"
    "v_add_f32 v126, 1.0, v126\n"
    "v_mfma_f32_16x16x32_bf16 a[124:127], v[120:123], a[204:207], a[124:127]\n"
    "v_exp_f32 v127, v127\n"
    "v_mul_f32 v128, s80, v59\n"
    "v_cvt_pk_bf16_f32 v10, v52, v53\n"
    "s_waitcnt lgkmcnt(2)\n"
    "v_mfma_f32_16x16x32_bf16 a[128:131], v[112:115], a[192:195], a[128:131]\n"
    "ds_read_b64_tr_b16 v[120:121], v252 offset:49664\n"
    "s_add_u32 m0, s64, 69632\n"
    "v_mul_f32 v54, v130, v54\n"
    "v_mfma_f32_16x16x32_bf16 a[132:135], v[112:115], a[196:199], a[132:135]\n"
    "ds_read_b64_tr_b16 v[122:123], v252 offset:50688\n"
    "s_add_u32 s65, s27, 110592\n"
    "v_mfma_f32_16x16x32_bf16 a[136:139], v[112:115], a[200:203], a[136:139]\n"
    "buffer_load_dwordx4 v240, s[48:51], s65 offen offset:0 lds\n"
    "v_fma_f32 v124, v124, v248, v246\n"
    "v_mfma_f32_16x16x32_bf16 a[140:143], v[112:115], a[204:207], a[140:143]\n"
    "v_rcp_f32 v125, v125\n"
    "v_fma_f32 v126, v126, v126, 1.0\n"
    "s_waitcnt lgkmcnt(2)\n"
    "v_mfma_f32_16x16x32_bf16 a[144:147], v[116:119], a[192:195], a[144:147]\n"
    "ds_read_b64_tr_b16 v[112:113], v253 offset:49664\n"
    "v_add_f32 v127, 1.0, v127\n"
    "v_exp_f32 v128, v128\n"
    "v_mfma_f32_16x16x32_bf16 a[148:151], v[116:119], a[196:199], a[148:151]\n"
    "ds_read_b64_tr_b16 v[114:115], v253 offset:50688\n"
    "v_mul_f32 v129, s80, v60\n"
    "v_mul_f32 v55, v124, v55\n"
    "v_fma_f32 v125, v125, v248, v246\n"
    "v_mfma_f32_16x16x32_bf16 a[152:155], v[116:119], a[200:203], a[152:155]\n"
    "v_rcp_f32 v126, v126\n"
    "v_fma_f32 v127, v127, v127, 1.0\n"
    "v_add_f32 v128, 1.0, v128\n"
    "v_exp_f32 v129, v129\n"
    "s_add_u32 s61, s74, 32768\n"
    "buffer_load_dwordx4 v[16:19], v241, s[56:59], s61 offen offset:0 sc1\n"
    "v_mfma_f32_16x16x32_bf16 a[156:159], v[116:119], a[204:207], a[156:159]\n"
    "v_mul_f32 v130, s80, v61\n"
    "v_cvt_pk_bf16_f32 v11, v54, v55\n"
    "v_mul_f32 v56, v125, v56\n"
    "v_fma_f32 v126, v126, v248, v246\n"
    "s_waitcnt lgkmcnt(2)\n"
    "v_mfma_f32_16x16x32_bf16 a[160:163], v[120:123], a[192:195], a[160:163]\n"
    "ds_read_b64_tr_b16 v[116:117], v252 offset:57344\n"
    "s_add_u32 m0, s64, 73728\n"
    "v_mfma_f32_16x16x32_bf16 a[164:167], v[120:123], a[196:199], a[164:167]\n"
    "ds_read_b64_tr_b16 v[118:119], v252 offset:58368\n"
    "s_add_u32 s65, s76, 294912\n"
    "v_mfma_f32_16x16x32_bf16 a[168:171], v[120:123], a[200:203], a[168:171]\n"
    "buffer_load_dwordx4 v240, s[48:51], s65 offen offset:0 lds\n"
    "v_rcp_f32 v127, v127\n"
    "v_mfma_f32_16x16x32_bf16 a[172:175], v[120:123], a[204:207], a[172:175]\n"
    "v_fma_f32 v128, v128, v128, 1.0\n"
    "v_add_f32 v129, 1.0, v129\n"
    "s_add_u32 s61, s74, 32768\n"
    "buffer_load_dwordx4 v[20:23], v241, s[56:59], s61 offen offset:1024 sc1\n"
    "s_waitcnt lgkmcnt(2)\n"
    "v_mfma_f32_16x16x32_bf16 a[176:179], v[112:115], a[192:195], a[176:179]\n"
    "ds_read_b64_tr_b16 v[120:121], v253 offset:57344\n"
    "v_exp_f32 v130, v130\n"
    "v_mul_f32 v124, s80, v62\n"
    "v_mul_f32 v57, v126, v57\n"
    "v_bfe_u32 v243, v241, 4, 2\n"
    "v_max_u32 v243, 1, v243\n"
    "v_xor_b32 v243, s69, v243\n"
    "v_lshl_add_u32 v243, v243, 6, s22\n"
    "buffer_load_dwordx2 v[234:235], v243, s[56:59], 0 offen offset:0 sc1\n"
    "v_mfma_f32_16x16x32_bf16 a[180:183], v[112:115], a[196:199], a[180:183]\n"
    "ds_read_b64_tr_b16 v[122:123], v253 offset:58368\n"
    "v_fma_f32 v127, v127, v248, v246\n"
    "v_rcp_f32 v128, v128\n"
    "v_mfma_f32_16x16x32_bf16 a[184:187], v[112:115], a[200:203], a[184:187]\n"
    "v_fma_f32 v129, v129, v129, 1.0\n"
    "v_add_f32 v130, 1.0, v130\n"
    "v_exp_f32 v124, v124\n"
    "v_mul_f32 v125, s80, v63\n"
    "v_cvt_pk_bf16_f32 v12, v56, v57\n"
    "v_mfma_f32_16x16x32_bf16 a[188:191], v[112:115], a[204:207], a[188:191]\n"
    "v_mul_f32 v58, v127, v58\n"
    "v_fma_f32 v128, v128, v248, v246\n"
    "v_rcp_f32 v129, v129\n"
    "s_waitcnt vmcnt(41) lgkmcnt(2)\n"
    "v_mfma_f32_16x16x32_bf16 a[64:67], v[116:119], v[182:185], a[64:67]\n"
    "ds_read_b64_tr_b16 v[112:113], v252 offset:57856\n"
    "s_add_u32 m0, s64, 77824\n"
    "s_add_u32 s61, s74, 32768\n"
    "buffer_load_dwordx4 v[24:27], v241, s[56:59], s61 offen offset:2048 sc1\n"
    "s_waitcnt vmcnt(40)\n"
    "v_mfma_f32_16x16x32_bf16 a[68:71], v[116:119], v[186:189], a[68:71]\n"
    "ds_read_b64_tr_b16 v[114:115], v252 offset:58880\n"
    "s_add_u32 s65, s76, 331776\n"
    "s_waitcnt vmcnt(39)\n"
    "v_mfma_f32_16x16x32_bf16 a[72:75], v[116:119], v[190:193], a[72:75]\n"
    "buffer_load_dwordx4 v240, s[48:51], s65 offen offset:0 lds\n"
    "v_fma_f32 v130, v130, v130, 1.0\n"
    "s_waitcnt vmcnt(39)\n"
    "v_mfma_f32_16x16x32_bf16 a[76:79], v[116:119], v[194:197], a[76:79]\n"
    "v_add_f32 v124, 1.0, v124\n"
    "v_exp_f32 v125, v125\n"
    "s_waitcnt lgkmcnt(2)\n"
    "v_mfma_f32_16x16x32_bf16 a[80:83], v[120:123], v[182:185], a[80:83]\n"
    "ds_read_b64_tr_b16 v[116:117], v253 offset:57856\n"
    "v_mul_f32 v59, v128, v59\n"
    "v_fma_f32 v129, v129, v248, v246\n"
    "v_rcp_f32 v130, v130\n"
    "v_mfma_f32_16x16x32_bf16 a[84:87], v[120:123], v[186:189], a[84:87]\n"
    "ds_read_b64_tr_b16 v[118:119], v253 offset:58880\n"
    "v_fma_f32 v124, v124, v124, 1.0\n"
    "v_add_f32 v125, 1.0, v125\n"
    "s_add_u32 s61, s74, 32768\n"
    "buffer_load_dwordx4 v[28:31], v241, s[56:59], s61 offen offset:3072 sc1\n"
    "v_mfma_f32_16x16x32_bf16 a[88:91], v[120:123], v[190:193], a[88:91]\n"
    "v_cvt_pk_bf16_f32 v13, v58, v59\n"
    "v_mul_f32 v60, v129, v60\n"
    "v_fma_f32 v130, v130, v248, v246\n"
    "v_rcp_f32 v124, v124\n"
    "v_fma_f32 v125, v125, v125, 1.0\n"
    "v_mfma_f32_16x16x32_bf16 a[92:95], v[120:123], v[194:197], a[92:95]\n"
    "v_mul_f32 v61, v130, v61\n"
    "v_fma_f32 v124, v124, v248, v246\n"
    "v_rcp_f32 v125, v125\n"
    "s_waitcnt lgkmcnt(2)\n"
    "v_mfma_f32_16x16x32_bf16 a[96:99], v[112:115], v[182:185], a[96:99]\n"
    "ds_read_b64_tr_b16 v[120:121], v254 offset:0\n"
    "s_add_u32 m0, s64, 81920\n"
    "v_mfma_f32_16x16x32_bf16 a[100:103], v[112:115], v[186:189], a[100:103]\n"
    "ds_read_b64_tr_b16 v[122:123], v254 offset:1024\n"
    "s_add_u32 s65, s24, 294912\n"
    "v_cvt_pk_bf16_f32 v14, v60, v61\n"
    "v_mfma_f32_16x16x32_bf16 a[104:107], v[112:115], v[190:193], a[104:107]\n"
    "buffer_load_dwordx4 v240, s[48:51], s65 offen offset:0 lds\n"
    "v_mfma_f32_16x16x32_bf16 a[108:111], v[112:115], v[194:197], a[108:111]\n"
    "v_mul_f32 v62, v124, v62\n"
    "v_fma_f32 v125, v125, v248, v246\n"
    "v_mul_f32 v63, v125, v63\n"
    "s_waitcnt lgkmcnt(2)\n"
    "v_mfma_f32_16x16x32_bf16 a[112:115], v[116:119], v[182:185], a[112:115]\n"
    "ds_read_b64_tr_b16 v[112:113], v255 offset:0\n"
    "v_cvt_pk_bf16_f32 v15, v62, v63\n"
    "v_permlane32_swap_b32 v8, v12\n"
    "v_permlane32_swap_b32 v9, v13\n"
    "v_permlane32_swap_b32 v10, v14\n"
    "v_mfma_f32_16x16x32_bf16 a[116:119], v[116:119], v[186:189], a[116:119]\n"
    "ds_read_b64_tr_b16 v[114:115], v255 offset:1024\n"
    "v_permlane32_swap_b32 v11, v15\n"
    "v_permlane16_swap_b32 v8, v12\n"
    "v_permlane16_swap_b32 v9, v13\n"
    "v_mfma_f32_16x16x32_bf16 a[120:123], v[116:119], v[190:193], a[120:123]\n"
    "v_permlane16_swap_b32 v10, v14\n"
    "v_permlane16_swap_b32 v11, v15\n"
    "s_add_u32 s60, s72, 65536\n"
    "buffer_store_dwordx4 v[8:11], v241, s[56:59], s60 offen offset:2048 sc1\n"
    "v_mfma_f32_16x16x32_bf16 a[124:127], v[116:119], v[194:197], a[124:127]\n"
    "buffer_store_dwordx4 v[12:15], v241, s[56:59], s60 offen offset:3072 sc1\n"
    "s_waitcnt lgkmcnt(2)\n"
    "v_mfma_f32_16x16x32_bf16 a[128:131], v[120:123], v[182:185], a[128:131]\n"
    "ds_read_b64_tr_b16 v[116:117], v254 offset:512\n"
    "s_add_u32 m0, s64, 86016\n"
    "v_mfma_f32_16x16x32_bf16 a[132:135], v[120:123], v[186:189], a[132:135]\n"
    "ds_read_b64_tr_b16 v[118:119], v254 offset:1536\n"
    "s_add_u32 s65, s24, 331776\n"
    "v_mfma_f32_16x16x32_bf16 a[136:139], v[120:123], v[190:193], a[136:139]\n"
    "buffer_load_dwordx4 v240, s[48:51], s65 offen offset:0 lds\n"
    "v_mfma_f32_16x16x32_bf16 a[140:143], v[120:123], v[194:197], a[140:143]\n"
    "s_waitcnt lgkmcnt(2)\n"
    "v_mfma_f32_16x16x32_bf16 a[144:147], v[112:115], v[182:185], a[144:147]\n"
    "ds_read_b64_tr_b16 v[120:121], v255 offset:512\n"
    "v_mfma_f32_16x16x32_bf16 a[148:151], v[112:115], v[186:189], a[148:151]\n"
    "ds_read_b64_tr_b16 v[122:123], v255 offset:1536\n"
    "v_mfma_f32_16x16x32_bf16 a[152:155], v[112:115], v[190:193], a[152:155]\n"
    "s_add_u32 s61, s75, 16384\n"
    "buffer_load_dwordx4 v[64:67], v241, s[56:59], s61 offen offset:0 sc1\n"
    "v_mfma_f32_16x16x32_bf16 a[156:159], v[112:115], v[194:197], a[156:159]\n"
    "s_waitcnt lgkmcnt(2)\n"
    "v_mfma_f32_16x16x32_bf16 a[160:163], v[116:119], v[182:185], a[160:163]\n"
    "s_barrier\n"
    "ds_read_b64_tr_b16 v[112:113], v254 offset:8192\n"
    "v_mfma_f32_16x16x32_bf16 a[164:167], v[116:119], v[186:189], a[164:167]\n"
    "ds_read_b64_tr_b16 v[114:115], v254 offset:9216\n"
    "s_add_u32 m0, s64, 90112\n"
    "v_mfma_f32_16x16x32_bf16 a[168:171], v[116:119], v[190:193], a[168:171]\n"
    "s_add_u32 s65, s25, 147456\n"
    "v_mfma_f32_16x16x32_bf16 a[172:175], v[116:119], v[194:197], a[172:175]\n"
    "buffer_load_dwordx4 v240, s[48:51], s65 offen offset:0 lds\n"
    "s_waitcnt lgkmcnt(2)\n"
    "v_mfma_f32_16x16x32_bf16 a[176:179], v[120:123], v[182:185], a[176:179]\n"
    "ds_read_b64_tr_b16 v[116:117], v255 offset:8192\n"
    "s_add_u32 s61, s75, 16384\n"
    "buffer_load_dwordx4 v[68:71], v241, s[56:59], s61 offen offset:1024 sc1\n"
    "v_mfma_f32_16x16x32_bf16 a[180:183], v[120:123], v[186:189], a[180:183]\n"
    "ds_read_b64_tr_b16 v[118:119], v255 offset:9216\n"
    "v_mfma_f32_16x16x32_bf16 a[184:187], v[120:123], v[190:193], a[184:187]\n"
    "v_mfma_f32_16x16x32_bf16 a[188:191], v[120:123], v[194:197], a[188:191]\n"
    "s_add_u32 s61, s75, 16384\n"
    "buffer_load_dwordx4 v[72:75], v241, s[56:59], s61 offen offset:2048 sc1\n"
    "s_waitcnt vmcnt(43) lgkmcnt(2)\n"
    "v_mfma_f32_16x16x32_bf16 a[64:67], v[112:115], v[198:201], a[64:67]\n"
    "ds_read_b64_tr_b16 v[120:121], v254 offset:8704\n"
    "s_add_u32 m0, s64, 94208\n"
    "s_waitcnt vmcnt(41)\n"
    "v_mfma_f32_16x16x32_bf16 a[68:71], v[112:115], v[202:205], a[68:71]\n"
    "ds_read_b64_tr_b16 v[122:123], v254 offset:9728\n"
    "s_add_u32 s65, s25, 184320\n"
    "s_waitcnt vmcnt(40)\n"
    "v_mfma_f32_16x16x32_bf16 a[72:75], v[112:115], v[206:209], a[72:75]\n"
    "buffer_load_dwordx4 v240, s[48:51], s65 offen offset:0 lds\n"
    "s_waitcnt vmcnt(39)\n"
    "v_mfma_f32_16x16x32_bf16 a[76:79], v[112:115], v[210:213], a[76:79]\n"
    "s_waitcnt lgkmcnt(2)\n"
    "v_mfma_f32_16x16x32_bf16 a[80:83], v[116:119], v[198:201], a[80:83]\n"
    "ds_read_b64_tr_b16 v[112:113], v255 offset:8704\n"
    "v_mfma_f32_16x16x32_bf16 a[84:87], v[116:119], v[202:205], a[84:87]\n"
    "ds_read_b64_tr_b16 v[114:115], v255 offset:9728\n"
    "v_mfma_f32_16x16x32_bf16 a[88:91], v[116:119], v[206:209], a[88:91]\n"
    "s_add_u32 s61, s75, 16384\n"
    "buffer_load_dwordx4 v[76:79], v241, s[56:59], s61 offen offset:3072 sc1\n"
    "v_mfma_f32_16x16x32_bf16 a[92:95], v[116:119], v[210:213], a[92:95]\n"
    "s_waitcnt lgkmcnt(2)\n"
    "v_mfma_f32_16x16x32_bf16 a[96:99], v[120:123], v[198:201], a[96:99]\n"
    "ds_read_b64_tr_b16 v[116:117], v250 offset:0\n"
    "s_add_u32 m0, s64, 98304\n"
    "v_mfma_f32_16x16x32_bf16 a[100:103], v[120:123], v[202:205], a[100:103]\n"
    "ds_read_b64_tr_b16 v[118:119], v250 offset:1024\n"
    "s_add_u32 s65, s26, 221184\n"
    "v_mfma_f32_16x16x32_bf16 a[104:107], v[120:123], v[206:209], a[104:107]\n"
    "buffer_load_dwordx4 v240, s[48:51], s65 offen offset:0 lds\n"
    "v_mfma_f32_16x16x32_bf16 a[108:111], v[120:123], v[210:213], a[108:111]\n"
    "s_waitcnt lgkmcnt(2)\n"
    "v_mfma_f32_16x16x32_bf16 a[112:115], v[112:115], v[198:201], a[112:115]\n"
    "ds_read_b64_tr_b16 v[120:121], v251 offset:0\n"
    "v_mfma_f32_16x16x32_bf16 a[116:119], v[112:115], v[202:205], a[116:119]\n"
    "ds_read_b64_tr_b16 v[122:123], v251 offset:1024\n"
    "v_mfma_f32_16x16x32_bf16 a[120:123], v[112:115], v[206:209], a[120:123]\n"
    "v_mfma_f32_16x16x32_bf16 a[124:127], v[112:115], v[210:213], a[124:127]\n"
    "s_waitcnt lgkmcnt(2)\n"
    "v_mfma_f32_16x16x32_bf16 a[128:131], v[116:119], v[198:201], a[128:131]\n"
    "ds_read_b64_tr_b16 v[112:113], v250 offset:512\n"
    "s_add_u32 m0, s64, 102400\n"
    "v_mfma_f32_16x16x32_bf16 a[132:135], v[116:119], v[202:205], a[132:135]\n"
    "ds_read_b64_tr_b16 v[114:115], v250 offset:1536\n"
    "s_add_u32 s65, s26, 258048\n"
    "v_mfma_f32_16x16x32_bf16 a[136:139], v[116:119], v[206:209], a[136:139]\n"
    "buffer_load_dwordx4 v240, s[48:51], s65 offen offset:0 lds\n"
    "v_mfma_f32_16x16x32_bf16 a[140:143], v[116:119], v[210:213], a[140:143]\n"
    "s_waitcnt lgkmcnt(2)\n"
    "v_mfma_f32_16x16x32_bf16 a[144:147], v[120:123], v[198:201], a[144:147]\n"
    "ds_read_b64_tr_b16 v[116:117], v251 offset:512\n"
    "v_mfma_f32_16x16x32_bf16 a[148:151], v[120:123], v[202:205], a[148:151]\n"
    "ds_read_b64_tr_b16 v[118:119], v251 offset:1536\n"
    "s_waitcnt vmcnt(22)\n"
    "s_add_u32 s99, s70, 1\n"
    "v_mov_b32 v133, s21\n"
    "v_mov_b32 v132, s99\n"
    "buffer_store_dwordx2 v[132:133], v242, s[56:59], s89 offen offset:8 sc1\n"
    "v_mfma_f32_16x16x32_bf16 a[152:155], v[120:123], v[206:209], a[152:155]\n"
    "v_mfma_f32_16x16x32_bf16 a[156:159], v[120:123], v[210:213], a[156:159]\n"
    "s_waitcnt lgkmcnt(2)\n"
    "v_mfma_f32_16x16x32_bf16 a[160:163], v[112:115], v[198:201], a[160:163]\n"
    "ds_read_b64_tr_b16 v[120:121], v250 offset:8192\n"
    "s_add_u32 m0, s64, 106496\n"
    "s_waitcnt vmcnt(16)\n"
    "v_subrev_u32 v234, s70, v234\n"
    "v_cmp_eq_u32_e64 s[16:17], s21, v235\n"
    "v_subrev_u32 v234, 4, v234\n"
    "v_cmp_gt_u32 vcc, 16, v234\n"
    "s_nop 0\n"
    "s_and_b64 vcc, vcc, s[16:17]\n"
    "s_cmp_eq_u64 vcc, -1\n"
    "s_cbranch_scc0 LslowQ3_%=\n"
    "LbackQ3_%=:\n"
    "v_mfma_f32_16x16x32_bf16 a[164:167], v[112:115], v[202:205], a[164:167]\n"
    "ds_read_b64_tr_b16 v[122:123], v250 offset:9216\n"
    "s_add_u32 s65, s27, 147456\n"
    "v_mfma_f32_16x16x32_bf16 a[168:171], v[112:115], v[206:209], a[168:171]\n"
    "buffer_load_dwordx4 v240, s[48:51], s65 offen offset:0 lds\n"
    "v_mfma_f32_16x16x32_bf16 a[172:175], v[112:115], v[210:213], a[172:175]\n"
    "s_waitcnt lgkmcnt(2)\n"
    "v_mfma_f32_16x16x32_bf16 a[176:179], v[116:119], v[198:201], a[176:179]\n"
    "ds_read_b64_tr_b16 v[112:113], v251 offset:8192\n"
    "v_mfma_f32_16x16x32_bf16 a[180:183], v[116:119], v[202:205], a[180:183]\n"
    "ds_read_b64_tr_b16 v[114:115], v251 offset:9216\n"
    "v_mfma_f32_16x16x32_bf16 a[184:187], v[116:119], v[206:209], a[184:187]\n"
    "v_mfma_f32_16x16x32_bf16 a[188:191], v[116:119], v[210:213], a[188:191]\n"
    "s_waitcnt lgkmcnt(2)\n"
    "v_mfma_f32_16x16x32_bf16 a[64:67], v[120:123], v[166:169], a[64:67]\n"
    "ds_read_b64_tr_b16 v[116:117], v250 offset:8704\n"
    "s_add_u32 m0, s64, 110592\n"
    "v_mfma_f32_16x16x32_bf16 a[68:71], v[120:123], v[170:173], a[68:71]\n"
    "ds_read_b64_tr_b16 v[118:119], v250 offset:9728\n"
    "s_add_u32 s65, s27, 184320\n"
    "v_mfma_f32_16x16x32_bf16 a[72:75], v[120:123], v[174:177], a[72:75]\n"
    "buffer_load_dwordx4 v240, s[48:51], s65 offen offset:0 lds\n"
    "v_mfma_f32_16x16x32_bf16 a[76:79], v[120:123], v[178:181], a[76:79]\n"
    "s_waitcnt lgkmcnt(2)\n"
    "v_mfma_f32_16x16x32_bf16 a[80:83], v[112:115], v[166:169], a[80:83]\n"
    "ds_read_b64_tr_b16 v[120:121], v251 offset:8704\n"
    "v_mfma_f32_16x16x32_bf16 a[84:87], v[112:115], v[170:173], a[84:87]\n"
    "ds_read_b64_tr_b16 v[122:123], v251 offset:9728\n"
    "v_mfma_f32_16x16x32_bf16 a[88:91], v[112:115], v[174:177], a[88:91]\n"
    "v_mfma_f32_16x16x32_bf16 a[92:95], v[112:115], v[178:181], a[92:95]\n"
    "s_waitcnt lgkmcnt(2)\n"
    "v_mfma_f32_16x16x32_bf16 a[96:99], v[116:119], v[166:169], a[96:99]\n"
    "s_barrier\n"
    "ds_read_b64_tr_b16 v[112:113], v250 offset:16384\n"
    "v_mfma_f32_16x16x32_bf16 a[100:103], v[116:119], v[170:173], a[100:103]\n"
    "ds_read_b64_tr_b16 v[114:115], v250 offset:17408\n"
    "s_add_u32 m0, s64, 114688\n"
    "v_mfma_f32_16x16x32_bf16 a[104:107], v[116:119], v[174:177], a[104:107]\n"
    "s_add_u32 s65, s25, 221184\n"
    "v_mfma_f32_16x16x32_bf16 a[108:111], v[116:119], v[178:181], a[108:111]\n"
    "buffer_load_dwordx4 v240, s[48:51], s65 offen offset:0 lds\n"
    "s_waitcnt lgkmcnt(2)\n"
    "v_mfma_f32_16x16x32_bf16 a[112:115], v[120:123], v[166:169], a[112:115]\n"
    "ds_read_b64_tr_b16 v[116:117], v251 offset:16384\n"
    "v_mfma_f32_16x16x32_bf16 a[116:119], v[120:123], v[170:173], a[116:119]\n"
    "ds_read_b64_tr_b16 v[118:119], v251 offset:17408\n"
    "v_mfma_f32_16x16x32_bf16 a[120:123], v[120:123], v[174:177], a[120:123]\n"
    "v_mfma_f32_16x16x32_bf16 a[124:127], v[120:123], v[178:181], a[124:127]\n"
    "s_waitcnt lgkmcnt(2)\n"
    "v_mfma_f32_16x16x32_bf16 a[128:131], v[112:115], v[166:169], a[128:131]\n"
    "ds_read_b64_tr_b16 v[120:121], v250 offset:16896\n"
    "s_add_u32 m0, s64, 118784\n"
    "v_mfma_f32_16x16x32_bf16 a[132:135], v[112:115], v[170:173], a[132:135]\n"
    "ds_read_b64_tr_b16 v[122:123], v250 offset:17920\n"
    "s_add_u32 s65, s25, 258048\n"
    "v_mfma_f32_16x16x32_bf16 a[136:139], v[112:115], v[174:177], a[136:139]\n"
    "buffer_load_dwordx4 v240, s[48:51], s65 offen offset:0 lds\n"
    "v_mfma_f32_16x16x32_bf16 a[140:143], v[112:115], v[178:181], a[140:143]\n"
    "s_waitcnt lgkmcnt(2)\n"
    "v_mfma_f32_16x16x32_bf16 a[144:147], v[116:119], v[166:169], a[144:147]\n"
    "ds_read_b64_tr_b16 v[112:113], v251 offset:16896\n"
    "v_mfma_f32_16x16x32_bf16 a[148:151], v[116:119], v[170:173], a[148:151]\n"
    "ds_read_b64_tr_b16 v[114:115], v251 offset:17920\n"
    "v_mfma_f32_16x16x32_bf16 a[152:155], v[116:119], v[174:177], a[152:155]\n"
    "s_add_u32 s61, s73, 32768\n"
    "buffer_load_dwordx4 a[192:195], v241, s[56:59], s61 offen offset:0 sc1\n"
    "v_mfma_f32_16x16x32_bf16 a[156:159], v[116:119], v[178:181], a[156:159]\n"
    "s_waitcnt lgkmcnt(2)\n"
    "v_mfma_f32_16x16x32_bf16 a[160:163], v[120:123], v[166:169], a[160:163]\n"
    "ds_read_b64_tr_b16 v[116:117], v250 offset:24576\n"
    "s_add_u32 m0, s64, 122880\n"
    "v_mfma_f32_16x16x32_bf16 a[164:167], v[120:123], v[170:173], a[164:167]\n"
    "ds_read_b64_tr_b16 v[118:119], v250 offset:25600\n"
    "s_add_u32 s65, s26, 294912\n"
    "v_mfma_f32_16x16x32_bf16 a[168:171], v[120:123], v[174:177], a[168:171]\n"
    "buffer_load_dwordx4 v240, s[48:51], s65 offen offset:0 lds\n"
    "v_mfma_f32_16x16x32_bf16 a[172:175], v[120:123], v[178:181], a[172:175]\n"
    "s_add_u32 s61, s73, 32768\n"
    "buffer_load_dwordx4 a[196:199], v241, s[56:59], s61 offen offset:1024 sc1\n"
    "s_waitcnt lgkmcnt(2)\n"
    "v_mfma_f32_16x16x32_bf16 a[176:179], v[112:115], v[166:169], a[176:179]\n"
    "ds_read_b64_tr_b16 v[120:121], v251 offset:24576\n"
    "v_mfma_f32_16x16x32_bf16 a[180:183], v[112:115], v[170:173], a[180:183]\n"
    "ds_read_b64_tr_b16 v[122:123], v251 offset:25600\n"
    "v_mfma_f32_16x16x32_bf16 a[184:187], v[112:115], v[174:177], a[184:187]\n"
    "v_mfma_f32_16x16x32_bf16 a[188:191], v[112:115], v[178:181], a[188:191]\n"
    "s_waitcnt vmcnt(17)\n"
    "s_add_u32 s99, s70, 5\n"
    "v_mov_b32 v133, s21\n"
    "v_mov_b32 v132, s99\n"
    "buffer_store_dwordx2 v[132:133], v242, s[56:59], s89 offen offset:0 sc1\n"
    "s_waitcnt lgkmcnt(2)\n"
    "v_mfma_f32_16x16x32_bf16 a[64:67], v[116:119], v[150:153], a[64:67]\n"
    "ds_read_b64_tr_b16 v[112:113], v250 offset:25088\n"
    "s_add_u32 m0, s64, 126976\n"
    "s_and_b32 s96, s66, 1\n"
    "s_lshl_b32 s96, s96, 7\n"
    "s_add_u32 s96, s96, 72\n"
    "s_add_u32 s96, s96, s22\n"
    "v_and_b32 v243, 16, v241\n"
    "v_lshlrev_b32 v243, 4, v243\n"
    "v_xor_b32 v243, s96, v243\n"
    "buffer_load_dwordx2 v[234:235], v243, s[56:59], 0 offen offset:0 sc1\n"
    "s_add_u32 s61, s73, 32768\n"
    "buffer_load_dwordx4 a[200:203], v241, s[56:59], s61 offen offset:2048 sc1\n"
    "v_mfma_f32_16x16x32_bf16 a[68:71], v[116:119], v[154:157], a[68:71]\n"
    "ds_read_b64_tr_b16 v[114:115], v250 offset:26112\n"
    "s_add_u32 s65, s26, 331776\n"
    "v_mfma_f32_16x16x32_bf16 a[72:75], v[116:119], v[158:161], a[72:75]\n"
    "buffer_load_dwordx4 v240, s[48:51], s65 offen offset:0 lds\n"
    "v_mfma_f32_16x16x32_bf16 a[76:79], v[116:119], v[162:165], a[76:79]\n"
    "s_waitcnt lgkmcnt(2)\n"
    "v_mfma_f32_16x16x32_bf16 a[80:83], v[120:123], v[150:153], a[80:83]\n"
    "ds_read_b64_tr_b16 v[116:117], v251 offset:25088\n"
    "v_mfma_f32_16x16x32_bf16 a[84:87], v[120:123], v[154:157], a[84:87]\n"
    "ds_read_b64_tr_b16 v[118:119], v251 offset:26112\n"
    "s_add_u32 s61, s74, 49152\n"
    "buffer_load_dwordx4 a[32:35], v241, s[56:59], s61 offen offset:0 sc1\n"
    "v_mfma_f32_16x16x32_bf16 a[88:91], v[120:123], v[158:161], a[88:91]\n"
    "s_add_u32 s61, s73, 32768\n"
    "buffer_load_dwordx4 a[204:207], v241, s[56:59], s61 offen offset:3072 sc1\n"
    "v_mfma_f32_16x16x32_bf16 a[92:95], v[120:123], v[162:165], a[92:95]\n"
    "s_waitcnt lgkmcnt(2)\n"
    "v_mfma_f32_16x16x32_bf16 a[96:99], v[112:115], v[150:153], a[96:99]\n"
    "ds_read_b64_tr_b16 v[120:121], v250 offset:32768\n"
    "s_add_u32 m0, s64, 131072\n"
    "v_mfma_f32_16x16x32_bf16 a[100:103], v[112:115], v[154:157], a[100:103]\n"
    "ds_read_b64_tr_b16 v[122:123], v250 offset:33792\n"
    "s_add_u32 s65, s27, 221184\n"
    "v_mfma_f32_16x16x32_bf16 a[104:107], v[112:115], v[158:161], a[104:107]\n"
    "buffer_load_dwordx4 v240, s[48:51], s65 offen offset:0 lds\n"
    "v_mfma_f32_16x16x32_bf16 a[108:111], v[112:115], v[162:165], a[108:111]\n"
    "s_waitcnt lgkmcnt(2)\n"
    "v_mfma_f32_16x16x32_bf16 a[112:115], v[116:119], v[150:153], a[112:115]\n"
    "ds_read_b64_tr_b16 v[112:113], v251 offset:32768\n"
    "v_mfma_f32_16x16x32_bf16 a[116:119], v[116:119], v[154:157], a[116:119]\n"
    "ds_read_b64_tr_b16 v[114:115], v251 offset:33792\n"
    "s_add_u32 s61, s74, 49152\n"
    "buffer_load_dwordx4 a[36:39], v241, s[56:59], s61 offen offset:1024 sc1\n"
    "v_mfma_f32_16x16x32_bf16 a[120:123], v[116:119], v[158:161], a[120:123]\n"
    "v_mfma_f32_16x16x32_bf16 a[124:127], v[116:119], v[162:165], a[124:127]\n"
    "s_waitcnt lgkmcnt(2)\n"
    "v_mfma_f32_16x16x32_bf16 a[128:131], v[120:123], v[150:153], a[128:131]\n"
    "ds_read_b64_tr_b16 v[116:117], v250 offset:33280\n"
    "s_add_u32 m0, s64, 135168\n"
    "v_mfma_f32_16x16x32_bf16 a[132:135], v[120:123], v[154:157], a[132:135]\n"
    "ds_read_b64_tr_b16 v[118:119], v250 offset:34304\n"
    "s_add_u32 s65, s27, 258048\n"
    "v_mfma_f32_16x16x32_bf16 a[136:139], v[120:123], v[158:161], a[136:139]\n"
    "buffer_load_dwordx4 v240, s[48:51], s65 offen offset:0 lds\n"
    "s_add_u32 s61, s74, 49152\n"
    "buffer_load_dwordx4 a[40:43], v241, s[56:59], s61 offen offset:2048 sc1\n"
    "v_mfma_f32_16x16x32_bf16 a[140:143], v[120:123], v[162:165], a[140:143]\n"
    "s_waitcnt lgkmcnt(2)\n"
    "v_mfma_f32_16x16x32_bf16 a[144:147], v[112:115], v[150:153], a[144:147]\n"
    "ds_read_b64_tr_b16 v[120:121], v251 offset:33280\n"
    "v_mfma_f32_16x16x32_bf16 a[148:151], v[112:115], v[154:157], a[148:151]\n"
    "ds_read_b64_tr_b16 v[122:123], v251 offset:34304\n"
    "s_add_u32 s61, s75, 32768\n"
    "buffer_load_dwordx4 a[48:51], v241, s[56:59], s61 offen offset:0 sc1\n"
    "v_mfma_f32_16x16x32_bf16 a[152:155], v[112:115], v[158:161], a[152:155]\n"
    "s_add_u32 s61, s74, 49152\n"
    "buffer_load_dwordx4 a[44:47], v241, s[56:59], s61 offen offset:3072 sc1\n"
    "v_mfma_f32_16x16x32_bf16 a[156:159], v[112:115], v[162:165], a[156:159]\n"
    "s_waitcnt lgkmcnt(2)\n"
    "v_mfma_f32_16x16x32_bf16 a[160:163], v[116:119], v[150:153], a[160:163]\n"
    "s_barrier\n"
    "ds_read_b64_tr_b16 v[112:113], v250 offset:40960\n"
    "v_mfma_f32_16x16x32_bf16 a[164:167], v[116:119], v[154:157], a[164:167]\n"
    "ds_read_b64_tr_b16 v[114:115], v250 offset:41984\n"
    "s_add_u32 m0, s64, 139264\n"
    "v_mfma_f32_16x16x32_bf16 a[168:171], v[116:119], v[158:161], a[168:171]\n"
    "s_add_u32 s65, s28, 0\n"
    "v_mfma_f32_16x16x32_bf16 a[172:175], v[116:119], v[162:165], a[172:175]\n"
    "buffer_load_dwordx4 v240, s[52:55], s65 offen offset:0 lds\n"
    "s_waitcnt lgkmcnt(2)\n"
    "v_mfma_f32_16x16x32_bf16 a[176:179], v[120:123], v[150:153], a[176:179]\n"
    "ds_read_b64_tr_b16 v[116:117], v251 offset:40960\n"
    "v_mfma_f32_16x16x32_bf16 a[180:183], v[120:123], v[154:157], a[180:183]\n"
    "ds_read_b64_tr_b16 v[118:119], v251 offset:41984\n"
    "s_add_u32 s61, s75, 32768\n"
    "buffer_load_dwordx4 a[52:55], v241, s[56:59], s61 offen offset:1024 sc1\n"
    "v_mfma_f32_16x16x32_bf16 a[184:187], v[120:123], v[158:161], a[184:187]\n"
    "v_mfma_f32_16x16x32_bf16 a[188:191], v[120:123], v[162:165], a[188:191]\n"
    "s_waitcnt lgkmcnt(2)\n"
    "v_mfma_f32_16x16x32_bf16 a[64:67], v[112:115], v[16:19], a[64:67]\n"
    "ds_read_b64_tr_b16 v[120:121], v250 offset:41472\n"
    "s_add_u32 m0, s64, 143360\n"
    "v_mfma_f32_16x16x32_bf16 a[68:71], v[112:115], v[20:23], a[68:71]\n"
    "ds_read_b64_tr_b16 v[122:123], v250 offset:42496\n"
    "s_add_u32 s65, s28, 82944\n"
    "v_mfma_f32_16x16x32_bf16 a[72:75], v[112:115], v[24:27], a[72:75]\n"
    "buffer_load_dwordx4 v240, s[52:55], s65 offen offset:0 lds\n"
    "s_add_u32 s61, s75, 32768\n"
    "buffer_load_dwordx4 a[56:59], v241, s[56:59], s61 offen offset:2048 sc1\n"
    "v_mfma_f32_16x16x32_bf16 a[76:79], v[112:115], v[28:31], a[76:79]\n"
    "s_waitcnt lgkmcnt(2)\n"
    "v_mfma_f32_16x16x32_bf16 a[80:83], v[116:119], v[16:19], a[80:83]\n"
    "ds_read_b64_tr_b16 v[112:113], v251 offset:41472\n"
    "v_mfma_f32_16x16x32_bf16 a[84:87], v[116:119], v[20:23], a[84:87]\n"
    "ds_read_b64_tr_b16 v[114:115], v251 offset:42496\n"
    "v_mfma_f32_16x16x32_bf16 a[88:91], v[116:119], v[24:27], a[88:91]\n"
    "s_add_u32 s61, s75, 32768\n"
    "buffer_load_dwordx4 a[60:63], v241, s[56:59], s61 offen offset:3072 sc1\n"
    "v_mfma_f32_16x16x32_bf16 a[92:95], v[116:119], v[28:31], a[92:95]\n"
    "s_waitcnt lgkmcnt(2)\n"
    "v_mfma_f32_16x16x32_bf16 a[96:99], v[120:123], v[16:19], a[96:99]\n"
    "ds_read_b64_tr_b16 v[116:117], v250 offset:49152\n"
    "s_add_u32 m0, s64, 0\n"
    "v_mfma_f32_16x16x32_bf16 a[100:103], v[120:123], v[20:23], a[100:103]\n"
    "ds_read_b64_tr_b16 v[118:119], v250 offset:50176\n"
    "s_add_u32 s65, s28, 36864\n"
    "v_mfma_f32_16x16x32_bf16 a[104:107], v[120:123], v[24:27], a[104:107]\n"
    "buffer_load_dwordx4 v240, s[52:55], s65 offen offset:0 lds\n"
    "v_mfma_f32_16x16x32_bf16 a[108:111], v[120:123], v[28:31], a[108:111]\n"
    "s_waitcnt lgkmcnt(2)\n"
    "v_mfma_f32_16x16x32_bf16 a[112:115], v[112:115], v[16:19], a[112:115]\n"
    "ds_read_b64_tr_b16 v[120:121], v251 offset:49152\n"
    "v_mfma_f32_16x16x32_bf16 a[116:119], v[112:115], v[20:23], a[116:119]\n"
    "ds_read_b64_tr_b16 v[122:123], v251 offset:50176\n"
    "v_mfma_f32_16x16x32_bf16 a[120:123], v[112:115], v[24:27], a[120:123]\n"
    "v_mfma_f32_16x16x32_bf16 a[124:127], v[112:115], v[28:31], a[124:127]\n"
    "s_waitcnt lgkmcnt(2)\n"
    "v_mfma_f32_16x16x32_bf16 a[128:131], v[116:119], v[16:19], a[128:131]\n"
    "ds_read_b64_tr_b16 v[112:113], v250 offset:49664\n"
    "s_add_u32 m0, s64, 4096\n"
    "v_mfma_f32_16x16x32_bf16 a[132:135], v[116:119], v[20:23], a[132:135]\n"
    "ds_read_b64_tr_b16 v[114:115], v250 offset:50688\n"
    "s_add_u32 s65, s28, 119808\n"
    "v_mfma_f32_16x16x32_bf16 a[136:139], v[116:119], v[24:27], a[136:139]\n"
    "buffer_load_dwordx4 v240, s[52:55], s65 offen offset:0 lds\n"
    "v_mfma_f32_16x16x32_bf16 a[140:143], v[116:119], v[28:31], a[140:143]\n"
    "s_waitcnt lgkmcnt(2)\n"
    "v_mfma_f32_16x16x32_bf16 a[144:147], v[120:123], v[16:19], a[144:147]\n"
    "ds_read_b64_tr_b16 v[116:117], v251 offset:49664\n"
    "v_mfma_f32_16x16x32_bf16 a[148:151], v[120:123], v[20:23], a[148:151]\n"
    "ds_read_b64_tr_b16 v[118:119], v251 offset:50688\n"
    "v_mfma_f32_16x16x32_bf16 a[152:155], v[120:123], v[24:27], a[152:155]\n"
    "v_mfma_f32_16x16x32_bf16 a[156:159], v[120:123], v[28:31], a[156:159]\n"
    "s_waitcnt lgkmcnt(2)\n"
    "v_mfma_f32_16x16x32_bf16 a[160:163], v[112:115], v[16:19], a[160:163]\n"
    "ds_read_b64_tr_b16 v[120:121], v250 offset:57344\n"
    "s_add_u32 m0, s64, 8192\n"
    "v_mfma_f32_16x16x32_bf16 a[164:167], v[112:115], v[20:23], a[164:167]\n"
    "ds_read_b64_tr_b16 v[122:123], v250 offset:58368\n"
    "s_add_u32 s65, s28, 73728\n"
    "v_mfma_f32_16x16x32_bf16 a[168:171], v[112:115], v[24:27], a[168:171]\n"
    "buffer_load_dwordx4 v240, s[52:55], s65 offen offset:0 lds\n"
    "v_mfma_f32_16x16x32_bf16 a[172:175], v[112:115], v[28:31], a[172:175]\n"
    "s_waitcnt lgkmcnt(2)\n"
    "v_mfma_f32_16x16x32_bf16 a[176:179], v[116:119], v[16:19], a[176:179]\n"
    "ds_read_b64_tr_b16 v[112:113], v251 offset:57344\n"
    "s_waitcnt vmcnt(18)\n"
    "v_subrev_u32 v234, s70, v234\n"
    "v_cmp_eq_u32_e64 s[16:17], s21, v235\n"
    "v_subrev_u32 v234, 1, v234\n"
    "v_cmp_gt_u32 vcc, 16, v234\n"
    "s_nop 0\n"
    "s_and_b64 vcc, vcc, s[16:17]\n"
    "s_cmp_eq_u64 vcc, -1\n"
    "s_cbranch_scc0 LslowQ4_%=\n"
    "LbackQ4_%=:\n"
    "v_mfma_f32_16x16x32_bf16 a[180:183], v[116:119], v[20:23], a[180:183]\n"
    "ds_read_b64_tr_b16 v[114:115], v251 offset:58368\n"
    "v_bfe_u32 v243, v241, 4, 2\n"
    "v_max_u32 v243, 1, v243\n"
    "v_xor_b32 v243, s69, v243\n"
    "v_lshl_add_u32 v243, v243, 6, s22\n"
    "buffer_load_dwordx2 v[234:235], v243, s[56:59], 0 offen offset:0 sc1\n"
    "v_mfma_f32_16x16x32_bf16 a[184:187], v[116:119], v[24:27], a[184:187]\n"
    "v_mfma_f32_16x16x32_bf16 a[188:191], v[116:119], v[28:31], a[188:191]\n"
    "s_waitcnt lgkmcnt(2)\n"
    "v_mfma_f32_16x16x32_bf16 a[64:67], v[120:123], v[64:67], a[64:67]\n"
    "ds_read_b64_tr_b16 v[116:117], v250 offset:57856\n"
    "s_add_u32 m0, s64, 12288\n"
    "v_mfma_f32_16x16x32_bf16 a[68:71], v[120:123], v[68:71], a[68:71]\n"
    "ds_read_b64_tr_b16 v[118:119], v250 offset:58880\n"
    "s_add_u32 s65, s28, 156672\n"
    "v_mfma_f32_16x16x32_bf16 a[72:75], v[120:123], v[72:75], a[72:75]\n"
    "buffer_load_dwordx4 v240, s[52:55], s65 offen offset:0 lds\n"
    "v_mfma_f32_16x16x32_bf16 a[76:79], v[120:123], v[76:79], a[76:79]\n"
    "s_waitcnt lgkmcnt(2)\n"
    "v_mfma_f32_16x16x32_bf16 a[80:83], v[112:115], v[64:67], a[80:83]\n"
    "ds_read_b64_tr_b16 v[120:121], v251 offset:57856\n"
    "v_mfma_f32_16x16x32_bf16 a[84:87], v[112:115], v[68:71], a[84:87]\n"
    "ds_read_b64_tr_b16 v[122:123], v251 offset:58880\n"
    "v_mfma_f32_16x16x32_bf16 a[88:91], v[112:115], v[72:75], a[88:91]\n"
    "v_mfma_f32_16x16x32_bf16 a[92:95], v[112:115], v[76:79], a[92:95]\n"
    "s_waitcnt lgkmcnt(2)\n"
    "v_mfma_f32_16x16x32_bf16 a[96:99], v[116:119], v[64:67], a[96:99]\n"
    "s_barrier\n"
    "ds_read_b64_tr_b16 v[112:113], v252 offset:0\n"
    "v_mfma_f32_16x16x32_bf16 a[100:103], v[116:119], v[68:71], a[100:103]\n"
    "ds_read_b64_tr_b16 v[114:115], v252 offset:1024\n"
    "s_add_u32 m0, s64, 16384\n"
    "v_mfma_f32_16x16x32_bf16 a[104:107], v[116:119], v[72:75], a[104:107]\n"
    "s_add_u32 s65, s77, 147456\n"
    "v_mfma_f32_16x16x32_bf16 a[108:111], v[116:119], v[76:79], a[108:111]\n"
    "buffer_load_dwordx4 v240, s[48:51], s65 offen offset:0 lds\n"
    "s_waitcnt lgkmcnt(2)\n"
    "v_mfma_f32_16x16x32_bf16 a[112:115], v[120:123], v[64:67], a[112:115]\n"
    "ds_read_b64_tr_b16 v[116:117], v253 offset:0\n"
    "v_mfma_f32_16x16x32_bf16 a[116:119], v[120:123], v[68:71], a[116:119]\n"
    "ds_read_b64_tr_b16 v[118:119], v253 offset:1024\n"
    "v_mfma_f32_16x16x32_bf16 a[120:123], v[120:123], v[72:75], a[120:123]\n"
    "v_mfma_f32_16x16x32_bf16 a[124:127], v[120:123], v[76:79], a[124:127]\n"
    "s_waitcnt lgkmcnt(2)\n"
    "v_mfma_f32_16x16x32_bf16 a[128:131], v[112:115], v[64:67], a[128:131]\n"
    "ds_read_b64_tr_b16 v[120:121], v252 offset:512\n"
    "s_add_u32 m0, s64, 20480\n"
    "v_mfma_f32_16x16x32_bf16 a[132:135], v[112:115], v[68:71], a[132:135]\n"
    "ds_read_b64_tr_b16 v[122:123], v252 offset:1536\n"
    "s_add_u32 s65, s77, 184320\n"
    "v_mfma_f32_16x16x32_bf16 a[136:139], v[112:115], v[72:75], a[136:139]\n"
    "buffer_load_dwordx4 v240, s[48:51], s65 offen offset:0 lds\n"
    "v_mfma_f32_16x16x32_bf16 a[140:143], v[112:115], v[76:79], a[140:143]\n"
    "s_waitcnt lgkmcnt(2)\n"
    "v_mfma_f32_16x16x32_bf16 a[144:147], v[116:119], v[64:67], a[144:147]\n"
    "ds_read_b64_tr_b16 v[112:113], v253 offset:512\n"
    "v_mfma_f32_16x16x32_bf16 a[148:151], v[116:119], v[68:71], a[148:151]\n"
    "ds_read_b64_tr_b16 v[114:115], v253 offset:1536\n"
    "v_mfma_f32_16x16x32_bf16 a[152:155], v[116:119], v[72:75], a[152:155]\n"
    "s_add_u32 s61, s86, 0\n"
    "buffer_load_dwordx4 a[16:19], v241, s[56:59], s61 offen offset:0 sc1\n"
    "v_mfma_f32_16x16x32_bf16 a[156:159], v[116:119], v[76:79], a[156:159]\n"
    "s_waitcnt lgkmcnt(2)\n"
    "v_mfma_f32_16x16x32_bf16 a[160:163], v[120:123], v[64:67], a[160:163]\n"
    "ds_read_b64_tr_b16 v[116:117], v252 offset:8192\n"
    "s_add_u32 m0, s64, 24576\n"
    "v_mfma_f32_16x16x32_bf16 a[164:167], v[120:123], v[68:71], a[164:167]\n"
    "ds_read_b64_tr_b16 v[118:119], v252 offset:9216\n"
    "s_add_u32 s65, s78, 221184\n"
    "v_mfma_f32_16x16x32_bf16 a[168:171], v[120:123], v[72:75], a[168:171]\n"
    "buffer_load_dwordx4 v240, s[48:51], s65 offen offset:0 lds\n"
    "v_mfma_f32_16x16x32_bf16 a[172:175], v[120:123], v[76:79], a[172:175]\n"
    "s_waitcnt lgkmcnt(2)\n"
    "v_mfma_f32_16x16x32_bf16 a[176:179], v[112:115], v[64:67], a[176:179]\n"
    "ds_read_b64_tr_b16 v[120:121], v253 offset:8192\n"
    "s_add_u32 s61, s86, 0\n"
    "buffer_load_dwordx4 a[20:23], v241, s[56:59], s61 offen offset:1024 sc1\n"
    "v_mfma_f32_16x16x32_bf16 a[180:183], v[112:115], v[68:71], a[180:183]\n"
    "ds_read_b64_tr_b16 v[122:123], v253 offset:9216\n"
    "v_mfma_f32_16x16x32_bf16 a[184:187], v[112:115], v[72:75], a[184:187]\n"
    "v_mfma_f32_16x16x32_bf16 a[188:191], v[112:115], v[76:79], a[188:191]\n"
    "s_add_u32 s61, s87, 0\n"
    "buffer_load_dwordx4 a[24:27], v241, s[56:59], s61 offen offset:0 sc1\n"
    "s_waitcnt lgkmcnt(2)\n"
    "v_mfma_f32_16x16x32_bf16 a[64:67], v[116:119], v[0:3], a[64:67]\n"
    "ds_read_b64_tr_b16 v[112:113], v252 offset:8704\n"
    "s_add_u32 m0, s64, 28672\n"
    "v_mfma_f32_16x16x32_bf16 a[68:71], v[116:119], v[4:7], a[68:71]\n"
    "ds_read_b64_tr_b16 v[114:115], v252 offset:9728\n"
    "s_add_u32 s65, s78, 258048\n"
    "v_mfma_f32_16x16x32_bf16 a[72:75], v[116:119], v[8:11], a[72:75]\n"
    "buffer_load_dwordx4 v240, s[48:51], s65 offen offset:0 lds\n"
    "v_mfma_f32_16x16x32_bf16 a[76:79], v[116:119], v[12:15], a[76:79]\n"
    "s_waitcnt lgkmcnt(2)\n"
    "v_mfma_f32_16x16x32_bf16 a[80:83], v[120:123], v[0:3], a[80:83]\n"
    "ds_read_b64_tr_b16 v[116:117], v253 offset:8704\n"
    "v_mfma_f32_16x16x32_bf16 a[84:87], v[120:123], v[4:7], a[84:87]\n"
    "ds_read_b64_tr_b16 v[118:119], v253 offset:9728\n"
    "s_add_u32 s61, s86, 2048\n"
    "buffer_load_dwordx4 a[0:3], v241, s[56:59], s61 offen offset:0 sc1\n"
    "v_mfma_f32_16x16x32_bf16 a[88:91], v[120:123], v[8:11], a[88:91]\n"
    "s_add_u32 s61, s87, 0\n"
    "buffer_load_dwordx4 a[28:31], v241, s[56:59], s61 offen offset:1024 sc1\n"
    "v_mfma_f32_16x16x32_bf16 a[92:95], v[120:123], v[12:15], a[92:95]\n"
    "s_waitcnt lgkmcnt(2)\n"
    "v_mfma_f32_16x16x32_bf16 a[96:99], v[112:115], v[0:3], a[96:99]\n"
    "ds_read_b64_tr_b16 v[120:121], v252 offset:16384\n"
    "s_add_u32 m0, s64, 32768\n"
    "v_mfma_f32_16x16x32_bf16 a[100:103], v[112:115], v[4:7], a[100:103]\n"
    "ds_read_b64_tr_b16 v[122:123], v252 offset:17408\n"
    "s_add_u32 s65, s79, 147456\n"
    "v_mfma_f32_16x16x32_bf16 a[104:107], v[112:115], v[8:11], a[104:107]\n"
    "buffer_load_dwordx4 v240, s[48:51], s65 offen offset:0 lds\n"
    "v_mfma_f32_16x16x32_bf16 a[108:111], v[112:115], v[12:15], a[108:111]\n"
    "s_waitcnt lgkmcnt(2)\n"
    "v_mfma_f32_16x16x32_bf16 a[112:115], v[116:119], v[0:3], a[112:115]\n"
    "ds_read_b64_tr_b16 v[112:113], v253 offset:16384\n"
    "v_mfma_f32_16x16x32_bf16 a[116:119], v[116:119], v[4:7], a[116:119]\n"
    "ds_read_b64_tr_b16 v[114:115], v253 offset:17408\n"
    "s_add_u32 s61, s86, 2048\n"
    "buffer_load_dwordx4 a[4:7], v241, s[56:59], s61 offen offset:1024 sc1\n"
    "v_mfma_f32_16x16x32_bf16 a[120:123], v[116:119], v[8:11], a[120:123]\n"
    "v_mfma_f32_16x16x32_bf16 a[124:127], v[116:119], v[12:15], a[124:127]\n"
    "s_waitcnt lgkmcnt(2)\n"
    "v_mfma_f32_16x16x32_bf16 a[128:131], v[120:123], v[0:3], a[128:131]\n"
    "ds_read_b64_tr_b16 v[116:117], v252 offset:16896\n"
    "s_add_u32 m0, s64, 36864\n"
    "v_mfma_f32_16x16x32_bf16 a[132:135], v[120:123], v[4:7], a[132:135]\n"
    "ds_read_b64_tr_b16 v[118:119], v252 offset:17920\n"
    "s_add_u32 s65, s79, 184320\n"
    "v_mfma_f32_16x16x32_bf16 a[136:139], v[120:123], v[8:11], a[136:139]\n"
    "buffer_load_dwordx4 v240, s[48:51], s65 offen offset:0 lds\n"
    "s_add_u32 s61, s87, 2048\n"
    "buffer_load_dwordx4 a[8:11], v241, s[56:59], s61 offen offset:0 sc1\n"
    "v_mfma_f32_16x16x32_bf16 a[140:143], v[120:123], v[12:15], a[140:143]\n"
    "s_waitcnt lgkmcnt(2)\n"
    "v_mfma_f32_16x16x32_bf16 a[144:147], v[112:115], v[0:3], a[144:147]\n"
    "ds_read_b64_tr_b16 v[120:121], v253 offset:16896\n"
    "v_mfma_f32_16x16x32_bf16 a[148:151], v[112:115], v[4:7], a[148:151]\n"
    "ds_read_b64_tr_b16 v[122:123], v253 offset:17920\n"
    "s_add_u32 s61, s86, 4096\n"
    "buffer_load_dwordx4 v[64:67], v241, s[56:59], s61 offen offset:0 sc1\n"
    "v_mfma_f32_16x16x32_bf16 a[152:155], v[112:115], v[8:11], a[152:155]\n"
    "s_add_u32 s61, s87, 2048\n"
    "buffer_load_dwordx4 a[12:15], v241, s[56:59], s61 offen offset:1024 sc1\n"
    "v_mfma_f32_16x16x32_bf16 a[156:159], v[112:115], v[12:15], a[156:159]\n"
    "s_waitcnt lgkmcnt(2)\n"
    "v_mfma_f32_16x16x32_bf16 a[160:163], v[116:119], v[0:3], a[160:163]\n"
    "s_barrier\n"
    "ds_read_b64_tr_b16 v[112:113], v252 offset:24576\n"
    "v_mfma_f32_16x16x32_bf16 a[164:167], v[116:119], v[4:7], a[164:167]\n"
    "ds_read_b64_tr_b16 v[114:115], v252 offset:25600\n"
    "s_waitcnt vmcnt(16)\n"
    "v_subrev_u32 v234, s70, v234\n"
    "v_cmp_eq_u32_e64 s[16:17], s21, v235\n"
    "v_subrev_u32 v234, 5, v234\n"
    "v_cmp_gt_u32 vcc, 16, v234\n"
    "s_nop 0\n"
    "s_and_b64 vcc, vcc, s[16:17]\n"
    "s_cmp_eq_u64 vcc, -1\n"
    "s_cbranch_scc0 LslowQ5_%=\n"
    "LbackQ5_%=:\n"
    "v_mfma_f32_16x16x32_bf16 a[168:171], v[116:119], v[8:11], a[168:171]\n"
    "s_add_u32 s61, s86, 4096\n"
    "buffer_load_dwordx4 v[68:71], v241, s[56:59], s61 offen offset:1024 sc1\n"
    "v_mfma_f32_16x16x32_bf16 a[172:175], v[116:119], v[12:15], a[172:175]\n"
    "s_add_u32 s61, s74, 65536\n"
    "buffer_load_dwordx4 a[224:227], v241, s[56:59], s61 offen offset:0 sc1\n"
    "s_add_u32 s61, s75, 49152\n"
    "buffer_load_dwordx4 a[240:243], v241, s[56:59], s61 offen offset:0 sc1\n"
    "s_waitcnt lgkmcnt(2)\n"
    "v_mfma_f32_16x16x32_bf16 a[176:179], v[120:123], v[0:3], a[176:179]\n"
    "ds_read_b64_tr_b16 v[116:117], v253 offset:24576\n"
    "s_add_u32 s61, s73, 49152\n"
    "buffer_load_dwordx4 a[208:211], v241, s[56:59], s61 offen offset:0 sc1\n"
    "v_mfma_f32_16x16x32_bf16 a[180:183], v[120:123], v[4:7], a[180:183]\n"
    "ds_read_b64_tr_b16 v[118:119], v253 offset:25600\n"
    "v_mfma_f32_16x16x32_bf16 a[184:187], v[120:123], v[8:11], a[184:187]\n"
    "s_add_u32 s61, s87, 4096\n"
    "buffer_load_dwordx4 v[72:75], v241, s[56:59], s61 offen offset:0 sc1\n"
    "v_mfma_f32_16x16x32_bf16 a[188:191], v[120:123], v[12:15], a[188:191]\n"
    "s_add_u32 s61, s75, 49152\n"
    "buffer_load_dwordx4 a[244:247], v241, s[56:59], s61 offen offset:1024 sc1\n"
    "s_waitcnt lgkmcnt(2)\n"
    "v_mfma_f32_16x16x32_bf16 a[128:131], v[112:115], a[192:195], a[128:131]\n"
    "ds_read_b64_tr_b16 v[120:121], v252 offset:25088\n"
    "s_add_u32 s61, s74, 65536\n"
    "buffer_load_dwordx4 a[228:231], v241, s[56:59], s61 offen offset:1024 sc1\n"
    "v_mfma_f32_16x16x32_bf16 a[132:135], v[112:115], a[196:199], a[132:135]\n"
    "ds_read_b64_tr_b16 v[122:123], v252 offset:26112\n"
    "s_add_u32 s61, s73, 49152\n"
    "buffer_load_dwordx4 a[212:215], v241, s[56:59], s61 offen offset:1024 sc1\n"
    "v_mfma_f32_16x16x32_bf16 a[136:139], v[112:115], a[200:203], a[136:139]\n"
    "v_mfma_f32_16x16x32_bf16 a[140:143], v[112:115], a[204:207], a[140:143]\n"
    "s_add_u32 s61, s87, 4096\n"
    "buffer_load_dwordx4 v[76:79], v241, s[56:59], s61 offen offset:1024 sc1\n"
    "s_waitcnt lgkmcnt(2)\n"
    "v_mfma_f32_16x16x32_bf16 a[144:147], v[116:119], a[192:195], a[144:147]\n"
    "ds_read_b64_tr_b16 v[112:113], v253 offset:25088\n"
    "s_add_u32 s61, s75, 49152\n"
    "buffer_load_dwordx4 a[248:251], v241, s[56:59], s61 offen offset:2048 sc1\n"
    "v_mfma_f32_16x16x32_bf16 a[148:151], v[116:119], a[196:199], a[148:151]\n"
    "ds_read_b64_tr_b16 v[114:115], v253 offset:26112\n"
    "s_add_u32 s61, s74, 65536\n"
    "buffer_load_dwordx4 a[232:235], v241, s[56:59], s61 offen offset:2048 sc1\n"
    "v_mfma_f32_16x16x32_bf16 a[152:155], v[116:119], a[200:203], a[152:155]\n"
    "s_add_u32 s61, s73, 49152\n"
    "buffer_load_dwordx4 a[216:219], v241, s[56:59], s61 offen offset:2048 sc1\n"
    "v_mfma_f32_16x16x32_bf16 a[156:159], v[116:119], a[204:207], a[156:159]\n"
    "s_waitcnt lgkmcnt(2)\n"
    "v_mfma_f32_16x16x32_bf16 a[160:163], v[120:123], a[192:195], a[160:163]\n"
    "ds_read_b64_tr_b16 v[116:117], v252 offset:32768\n"
    "v_mfma_f32_16x16x32_bf16 a[164:167], v[120:123], a[196:199], a[164:167]\n"
    "ds_read_b64_tr_b16 v[118:119], v252 offset:33792\n"
    "v_mfma_f32_16x16x32_bf16 a[168:171], v[120:123], a[200:203], a[168:171]\n"
    "s_add_u32 s61, s73, 49152\n"
    "buffer_load_dwordx4 a[220:223], v241, s[56:59], s61 offen offset:3072 sc1\n"
    "v_mfma_f32_16x16x32_bf16 a[172:175], v[120:123], a[204:207], a[172:175]\n"
    "s_add_u32 s61, s74, 65536\n"
    "buffer_load_dwordx4 a[236:239], v241, s[56:59], s61 offen offset:3072 sc1\n"
    "s_waitcnt lgkmcnt(2)\n"
    "v_mfma_f32_16x16x32_bf16 a[176:179], v[112:115], a[192:195], a[176:179]\n"
    "ds_read_b64_tr_b16 v[120:121], v253 offset:32768\n"
    "s_add_u32 s61, s75, 49152\n"
    "buffer_load_dwordx4 a[252:255], v241, s[56:59], s61 offen offset:3072 sc1\n"
    "v_mfma_f32_16x16x32_bf16 a[180:183], v[112:115], a[196:199], a[180:183]\n"
    "ds_read_b64_tr_b16 v[122:123], v253 offset:33792\n"
    "v_mfma_f32_16x16x32_bf16 a[184:187], v[112:115], a[200:203], a[184:187]\n"
    "v_mfma_f32_16x16x32_bf16 a[188:191], v[112:115], a[204:207], a[188:191]\n"
    "s_waitcnt lgkmcnt(2)\n"
    "v_mfma_f32_16x16x32_bf16 a[128:131], v[116:119], a[32:35], a[128:131]\n"
    "ds_read_b64_tr_b16 v[112:113], v252 offset:33280\n"
    "v_mfma_f32_16x16x32_bf16 a[132:135], v[116:119], a[36:39], a[132:135]\n"
    "ds_read_b64_tr_b16 v[114:115], v252 offset:34304\n"
    "v_mfma_f32_16x16x32_bf16 a[136:139], v[116:119], a[40:43], a[136:139]\n"
    "v_mfma_f32_16x16x32_bf16 a[140:143], v[116:119], a[44:47], a[140:143]\n"
    "s_waitcnt lgkmcnt(2)\n"
    "v_mfma_f32_16x16x32_bf16 a[144:147], v[120:123], a[32:35], a[144:147]\n"
    "ds_read_b64_tr_b16 v[116:117], v253 offset:33280\n"
    "v_mfma_f32_16x16x32_bf16 a[148:151], v[120:123], a[36:39], a[148:151]\n"
    "ds_read_b64_tr_b16 v[118:119], v253 offset:34304\n"
    "v_mfma_f32_16x16x32_bf16 a[152:155], v[120:123], a[40:43], a[152:155]\n"
    "v_mfma_f32_16x16x32_bf16 a[156:159], v[120:123], a[44:47], a[156:159]\n"
    "s_waitcnt lgkmcnt(2)\n"
    "v_mfma_f32_16x16x32_bf16 a[160:163], v[112:115], a[32:35], a[160:163]\n"
    "ds_read_b64_tr_b16 v[120:121], v252 offset:40960\n"
    "v_mfma_f32_16x16x32_bf16 a[164:167], v[112:115], a[36:39], a[164:167]\n"
    "ds_read_b64_tr_b16 v[122:123], v252 offset:41984\n"
    "v_mfma_f32_16x16x32_bf16 a[168:171], v[112:115], a[40:43], a[168:171]\n"
    "v_mfma_f32_16x16x32_bf16 a[172:175], v[112:115], a[44:47], a[172:175]\n"
    "s_waitcnt lgkmcnt(2)\n"
    "v_mfma_f32_16x16x32_bf16 a[176:179], v[116:119], a[32:35], a[176:179]\n"
    "ds_read_b64_tr_b16 v[112:113], v253 offset:40960\n"
    "v_mfma_f32_16x16x32_bf16 a[180:183], v[116:119], a[36:39], a[180:183]\n"
    "ds_read_b64_tr_b16 v[114:115], v253 offset:41984\n"
    "v_mfma_f32_16x16x32_bf16 a[184:187], v[116:119], a[40:43], a[184:187]\n"
    "v_mfma_f32_16x16x32_bf16 a[188:191], v[116:119], a[44:47], a[188:191]\n"
    "s_waitcnt lgkmcnt(2)\n"
    "v_mfma_f32_16x16x32_bf16 a[128:131], v[120:123], a[48:51], a[128:131]\n"
    "ds_read_b64_tr_b16 v[116:117], v252 offset:41472\n"
    "v_mfma_f32_16x16x32_bf16 a[132:135], v[120:123], a[52:55], a[132:135]\n"
    "ds_read_b64_tr_b16 v[118:119], v252 offset:42496\n"
    "v_mfma_f32_16x16x32_bf16 a[136:139], v[120:123], a[56:59], a[136:139]\n"
    "v_mfma_f32_16x16x32_bf16 a[140:143], v[120:123], a[60:63], a[140:143]\n"
    "s_waitcnt lgkmcnt(2)\n"
    "v_mfma_f32_16x16x32_bf16 a[144:147], v[112:115], a[48:51], a[144:147]\n"
    "ds_read_b64_tr_b16 v[120:121], v253 offset:41472\n"
    "v_mfma_f32_16x16x32_bf16 a[148:151], v[112:115], a[52:55], a[148:151]\n"
    "ds_read_b64_tr_b16 v[122:123], v253 offset:42496\n"
    "v_mfma_f32_16x16x32_bf16 a[152:155], v[112:115], a[56:59], a[152:155]\n"
    "v_mfma_f32_16x16x32_bf16 a[156:159], v[112:115], a[60:63], a[156:159]\n"
    "s_waitcnt lgkmcnt(2)\n"
    "v_mfma_f32_16x16x32_bf16 a[160:163], v[116:119], a[48:51], a[160:163]\n"
    "s_barrier\n"
    "ds_read_b64_tr_b16 v[112:113], v252 offset:49152\n"
    "v_mfma_f32_16x16x32_bf16 a[164:167], v[116:119], a[52:55], a[164:167]\n"
    "ds_read_b64_tr_b16 v[114:115], v252 offset:50176\n"
    "s_add_u32 m0, s64, 73728\n"
    "v_mfma_f32_16x16x32_bf16 a[168:171], v[116:119], a[56:59], a[168:171]\n"
    "s_add_u32 s65, s77, 221184\n"
    "v_mfma_f32_16x16x32_bf16 a[172:175], v[116:119], a[60:63], a[172:175]\n"
    "buffer_load_dwordx4 v240, s[48:51], s65 offen offset:0 lds\n"
    "s_waitcnt lgkmcnt(2)\n"
    "v_mfma_f32_16x16x32_bf16 a[176:179], v[120:123], a[48:51], a[176:179]\n"
    "ds_read_b64_tr_b16 v[116:117], v253 offset:49152\n"
    "v_mfma_f32_16x16x32_bf16 a[180:183], v[120:123], a[52:55], a[180:183]\n"
    "ds_read_b64_tr_b16 v[118:119], v253 offset:50176\n"
    "v_mfma_f32_16x16x32_bf16 a[184:187], v[120:123], a[56:59], a[184:187]\n"
    "v_mfma_f32_16x16x32_bf16 a[188:191], v[120:123], a[60:63], a[188:191]\n"
    "s_waitcnt vmcnt(12) lgkmcnt(2)\n"
    "v_mfma_f32_16x16x32_bf16 a[128:131], v[112:115], a[208:211], a[128:131]\n"
    "ds_read_b64_tr_b16 v[120:121], v252 offset:49664\n"
    "s_waitcnt vmcnt(8)\n"
    "v_mfma_f32_16x16x32_bf16 a[132:135], v[112:115], a[212:215], a[132:135]\n"
    "ds_read_b64_tr_b16 v[122:123], v252 offset:50688\n"
    "s_waitcnt vmcnt(4)\n"
    "v_mfma_f32_16x16x32_bf16 a[136:139], v[112:115], a[216:219], a[136:139]\n"
    "s_waitcnt vmcnt(3)\n"
    "v_mfma_f32_16x16x32_bf16 a[140:143], v[112:115], a[220:223], a[140:143]\n"
    "s_waitcnt lgkmcnt(2)\n"
    "v_mfma_f32_16x16x32_bf16 a[144:147], v[116:119], a[208:211], a[144:147]\n"
    "ds_read_b64_tr_b16 v[112:113], v253 offset:49664\n"
    "s_add_u32 m0, s64, 77824\n"
    "v_mfma_f32_16x16x32_bf16 a[148:151], v[116:119], a[212:215], a[148:151]\n"
    "ds_read_b64_tr_b16 v[114:115], v253 offset:50688\n"
    "s_add_u32 s65, s77, 258048\n"
    "v_mfma_f32_16x16x32_bf16 a[152:155], v[116:119], a[216:219], a[152:155]\n"
    "buffer_load_dwordx4 v240, s[48:51], s65 offen offset:0 lds\n"
    "v_mfma_f32_16x16x32_bf16 a[156:159], v[116:119], a[220:223], a[156:159]\n"
    "s_waitcnt lgkmcnt(2)\n"
    "v_mfma_f32_16x16x32_bf16 a[160:163], v[120:123], a[208:211], a[160:163]\n"
    "ds_read_b64_tr_b16 v[116:117], v252 offset:57344\n"
    "v_mfma_f32_16x16x32_bf16 a[164:167], v[120:123], a[212:215], a[164:167]\n"
    "ds_read_b64_tr_b16 v[118:119], v252 offset:58368\n"
    "v_mfma_f32_16x16x32_bf16 a[168:171], v[120:123], a[216:219], a[168:171]\n"
    "v_mfma_f32_16x16x32_bf16 a[172:175], v[120:123], a[220:223], a[172:175]\n"
    "s_waitcnt lgkmcnt(2)\n"
    "v_mfma_f32_16x16x32_bf16 a[176:179], v[112:115], a[208:211], a[176:179]\n"
    "ds_read_b64_tr_b16 v[120:121], v253 offset:57344\n"
    "v_mfma_f32_16x16x32_bf16 a[180:183], v[112:115], a[212:215], a[180:183]\n"
    "ds_read_b64_tr_b16 v[122:123], v253 offset:58368\n"
    "v_mfma_f32_16x16x32_bf16 a[184:187], v[112:115], a[216:219], a[184:187]\n"
    "v_mfma_f32_16x16x32_bf16 a[188:191], v[112:115], a[220:223], a[188:191]\n"
    "s_waitcnt lgkmcnt(2)\n"
    "v_mfma_f32_16x16x32_bf16 a[128:131], v[116:119], a[224:227], a[128:131]\n"
    "ds_read_b64_tr_b16 v[112:113], v252 offset:57856\n"
    "s_add_u32 m0, s64, 81920\n"
    "v_mfma_f32_16x16x32_bf16 a[132:135], v[116:119], a[228:231], a[132:135]\n"
    "ds_read_b64_tr_b16 v[114:115], v252 offset:58880\n"
    "s_add_u32 s65, s78, 294912\n"
    "v_mfma_f32_16x16x32_bf16 a[136:139], v[116:119], a[232:235], a[136:139]\n"
    "buffer_load_dwordx4 v240, s[48:51], s65 offen offset:0 lds\n"
    "s_waitcnt vmcnt(4)\n"
    "v_mfma_f32_16x16x32_bf16 a[140:143], v[116:119], a[236:239], a[140:143]\n"
    "s_waitcnt lgkmcnt(2)\n"
    "v_mfma_f32_16x16x32_bf16 a[144:147], v[120:123], a[224:227], a[144:147]\n"
    "ds_read_b64_tr_b16 v[116:117], v253 offset:57856\n"
    "v_mfma_f32_16x16x32_bf16 a[148:151], v[120:123], a[228:231], a[148:151]\n"
    "ds_read_b64_tr_b16 v[118:119], v253 offset:58880\n"
    "v_mfma_f32_16x16x32_bf16 a[152:155], v[120:123], a[232:235], a[152:155]\n"
    "v_mfma_f32_16x16x32_bf16 a[156:159], v[120:123], a[236:239], a[156:159]\n"
    "s_waitcnt lgkmcnt(2)\n"
    "v_mfma_f32_16x16x32_bf16 a[160:163], v[112:115], a[224:227], a[160:163]\n"
    "ds_read_b64_tr_b16 v[120:121], v254 offset:0\n"
    "v_mfma_f32_16x16x32_bf16 a[164:167], v[112:115], a[228:231], a[164:167]\n"
    "ds_read_b64_tr_b16 v[122:123], v254 offset:1024\n"
    "v_mfma_f32_16x16x32_bf16 a[168:171], v[112:115], a[232:235], a[168:171]\n"
    "v_mfma_f32_16x16x32_bf16 a[172:175], v[112:115], a[236:239], a[172:175]\n"
    "s_waitcnt lgkmcnt(2)\n"
    "v_mfma_f32_16x16x32_bf16 a[176:179], v[116:119], a[224:227], a[176:179]\n"
    "ds_read_b64_tr_b16 v[112:113], v255 offset:0\n"
    "s_add_u32 m0, s64, 86016\n"
    "v_mfma_f32_16x16x32_bf16 a[180:183], v[116:119], a[228:231], a[180:183]\n"
    "ds_read_b64_tr_b16 v[114:115], v255 offset:1024\n"
    "s_add_u32 s65, s78, 331776\n"
    "v_mfma_f32_16x16x32_bf16 a[184:187], v[116:119], a[232:235], a[184:187]\n"
    "buffer_load_dwordx4 v240, s[48:51], s65 offen offset:0 lds\n"
    "v_mfma_f32_16x16x32_bf16 a[188:191], v[116:119], a[236:239], a[188:191]\n"
    "s_waitcnt lgkmcnt(2)\n"
    "v_mfma_f32_16x16x32_bf16 a[128:131], v[120:123], a[240:243], a[128:131]\n"
    "ds_read_b64_tr_b16 v[116:117], v254 offset:512\n"
    "v_mfma_f32_16x16x32_bf16 a[132:135], v[120:123], a[244:247], a[132:135]\n"
    "ds_read_b64_tr_b16 v[118:119], v254 offset:1536\n"
    "v_mfma_f32_16x16x32_bf16 a[136:139], v[120:123], a[248:251], a[136:139]\n"
    "s_waitcnt vmcnt(4)\n"
    "v_mfma_f32_16x16x32_bf16 a[140:143], v[120:123], a[252:255], a[140:143]\n"
    "s_waitcnt lgkmcnt(2)\n"
    "v_mfma_f32_16x16x32_bf16 a[144:147], v[112:115], a[240:243], a[144:147]\n"
    "ds_read_b64_tr_b16 v[120:121], v255 offset:512\n"
    "v_mfma_f32_16x16x32_bf16 a[148:151], v[112:115], a[244:247], a[148:151]\n"
    "ds_read_b64_tr_b16 v[122:123], v255 offset:1536\n"
    "v_mfma_f32_16x16x32_bf16 a[152:155], v[112:115], a[248:251], a[152:155]\n"
    "v_mfma_f32_16x16x32_bf16 a[156:159], v[112:115], a[252:255], a[156:159]\n"
    "s_waitcnt lgkmcnt(2)\n"
    "v_mfma_f32_16x16x32_bf16 a[160:163], v[116:119], a[240:243], a[160:163]\n"
    "s_barrier\n"
    "ds_read_b64_tr_b16 v[112:113], v254 offset:8192\n"
    "v_mfma_f32_16x16x32_bf16 a[164:167], v[116:119], a[244:247], a[164:167]\n"
    "ds_read_b64_tr_b16 v[114:115], v254 offset:9216\n"
    "s_add_u32 m0, s64, 90112\n"
    "v_mfma_f32_16x16x32_bf16 a[168:171], v[116:119], a[248:251], a[168:171]\n"
    "s_add_u32 s65, s79, 221184\n"
    "v_mfma_f32_16x16x32_bf16 a[172:175], v[116:119], a[252:255], a[172:175]\n"
    "buffer_load_dwordx4 v240, s[48:51], s65 offen offset:0 lds\n"
    "s_waitcnt lgkmcnt(2)\n"
    "v_mfma_f32_16x16x32_bf16 a[176:179], v[120:123], a[240:243], a[176:179]\n"
    "ds_read_b64_tr_b16 v[116:117], v255 offset:8192\n"
    "v_mfma_f32_16x16x32_bf16 a[180:183], v[120:123], a[244:247], a[180:183]\n"
    "ds_read_b64_tr_b16 v[118:119], v255 offset:9216\n"
    "v_mfma_f32_16x16x32_bf16 a[184:187], v[120:123], a[248:251], a[184:187]\n"
    "v_mfma_f32_16x16x32_bf16 a[188:191], v[120:123], a[252:255], a[188:191]\n"
    "s_waitcnt lgkmcnt(2)\n"
    "v_mfma_f32_16x16x32_bf16 a[128:131], v[112:115], a[16:19], a[128:131]\n"
    "ds_read_b64_tr_b16 v[120:121], v254 offset:8704\n"
    "s_add_u32 m0, s64, 94208\n"
    "v_mfma_f32_16x16x32_bf16 a[132:135], v[112:115], a[20:23], a[132:135]\n"
    "ds_read_b64_tr_b16 v[122:123], v254 offset:9728\n"
    "s_add_u32 s65, s79, 258048\n"
    "v_mfma_f32_16x16x32_bf16 a[136:139], v[112:115], a[24:27], a[136:139]\n"
    "buffer_load_dwordx4 v240, s[48:51], s65 offen offset:0 lds\n"
    "v_mfma_f32_16x16x32_bf16 a[140:143], v[112:115], a[28:31], a[140:143]\n"
    "s_waitcnt lgkmcnt(2)\n"
    "v_mfma_f32_16x16x32_bf16 a[144:147], v[116:119], a[16:19], a[144:147]\n"
    "ds_read_b64_tr_b16 v[112:113], v255 offset:8704\n"
    "v_mfma_f32_16x16x32_bf16 a[148:151], v[116:119], a[20:23], a[148:151]\n"
    "ds_read_b64_tr_b16 v[114:115], v255 offset:9728\n"
    "v_mfma_f32_16x16x32_bf16 a[152:155], v[116:119], a[24:27], a[152:155]\n"
    "v_mfma_f32_16x16x32_bf16 a[156:159], v[116:119], a[28:31], a[156:159]\n"
    "s_waitcnt lgkmcnt(2)\n"
    "v_mfma_f32_16x16x32_bf16 a[160:163], v[120:123], a[16:19], a[160:163]\n"
    "ds_read_b64_tr_b16 v[116:117], v250 offset:0\n"
    "s_add_u32 m0, s64, 98304\n"
    "v_mfma_f32_16x16x32_bf16 a[164:167], v[120:123], a[20:23], a[164:167]\n"
    "ds_read_b64_tr_b16 v[118:119], v250 offset:1024\n"
    "s_add_u32 s65, s83, 0\n"
    "v_mfma_f32_16x16x32_bf16 a[168:171], v[120:123], a[24:27], a[168:171]\n"
    "buffer_load_dwordx4 v240, s[52:55], s65 offen offset:0 lds\n"
    "v_mfma_f32_16x16x32_bf16 a[172:175], v[120:123], a[28:31], a[172:175]\n"
    "s_waitcnt lgkmcnt(2)\n"
    "v_mfma_f32_16x16x32_bf16 a[176:179], v[112:115], a[16:19], a[176:179]\n"
    "ds_read_b64_tr_b16 v[120:121], v251 offset:0\n"
    "v_mfma_f32_16x16x32_bf16 a[180:183], v[112:115], a[20:23], a[180:183]\n"
    "ds_read_b64_tr_b16 v[122:123], v251 offset:1024\n"
    "v_mfma_f32_16x16x32_bf16 a[184:187], v[112:115], a[24:27], a[184:187]\n"
    "v_mfma_f32_16x16x32_bf16 a[188:191], v[112:115], a[28:31], a[188:191]\n"
    "s_waitcnt lgkmcnt(2)\n"
    "v_mfma_f32_16x16x32_bf16 a[128:131], v[116:119], a[0:3], a[128:131]\n"
    "ds_read_b64_tr_b16 v[112:113], v250 offset:512\n"
    "s_add_u32 m0, s64, 102400\n"
    "v_mfma_f32_16x16x32_bf16 a[132:135], v[116:119], a[4:7], a[132:135]\n"
    "ds_read_b64_tr_b16 v[114:115], v250 offset:1536\n"
    "s_add_u32 s65, s83, 82944\n"
    "v_mfma_f32_16x16x32_bf16 a[136:139], v[116:119], a[8:11], a[136:139]\n"
    "buffer_load_dwordx4 v240, s[52:55], s65 offen offset:0 lds\n"
    "v_mfma_f32_16x16x32_bf16 a[140:143], v[116:119], a[12:15], a[140:143]\n"
    "s_waitcnt lgkmcnt(2)\n"
    "v_mfma_f32_16x16x32_bf16 a[144:147], v[120:123], a[0:3], a[144:147]\n"
    "ds_read_b64_tr_b16 v[116:117], v251 offset:512\n"
    "v_mfma_f32_16x16x32_bf16 a[148:151], v[120:123], a[4:7], a[148:151]\n"
    "ds_read_b64_tr_b16 v[118:119], v251 offset:1536\n"
    "v_mfma_f32_16x16x32_bf16 a[152:155], v[120:123], a[8:11], a[152:155]\n"
    "v_mfma_f32_16x16x32_bf16 a[156:159], v[120:123], a[12:15], a[156:159]\n"
    "s_waitcnt lgkmcnt(2)\n"
    "v_mfma_f32_16x16x32_bf16 a[160:163], v[112:115], a[0:3], a[160:163]\n"
    "ds_read_b64_tr_b16 v[120:121], v250 offset:8192\n"
    "s_add_u32 m0, s64, 106496\n"
    "v_mfma_f32_16x16x32_bf16 a[164:167], v[112:115], a[4:7], a[164:167]\n"
    "ds_read_b64_tr_b16 v[122:123], v250 offset:9216\n"
    "s_add_u32 s65, s83, 36864\n"
    "v_mfma_f32_16x16x32_bf16 a[168:171], v[112:115], a[8:11], a[168:171]\n"
    "buffer_load_dwordx4 v240, s[52:55], s65 offen offset:0 lds\n"
    "v_mfma_f32_16x16x32_bf16 a[172:175], v[112:115], a[12:15], a[172:175]\n"
    "s_waitcnt lgkmcnt(2)\n"
    "v_mfma_f32_16x16x32_bf16 a[176:179], v[116:119], a[0:3], a[176:179]\n"
    "ds_read_b64_tr_b16 v[112:113], v251 offset:8192\n"
    "v_mfma_f32_16x16x32_bf16 a[180:183], v[116:119], a[4:7], a[180:183]\n"
    "ds_read_b64_tr_b16 v[114:115], v251 offset:9216\n"
    "v_mfma_f32_16x16x32_bf16 a[184:187], v[116:119], a[8:11], a[184:187]\n"
    "v_mfma_f32_16x16x32_bf16 a[188:191], v[116:119], a[12:15], a[188:191]\n"
    "s_waitcnt lgkmcnt(2)\n"
    "v_mfma_f32_16x16x32_bf16 a[128:131], v[120:123], v[64:67], a[128:131]\n"
    "ds_read_b64_tr_b16 v[116:117], v250 offset:8704\n"
    "s_add_u32 m0, s64, 110592\n"
    "v_mfma_f32_16x16x32_bf16 a[132:135], v[120:123], v[68:71], a[132:135]\n"
    "ds_read_b64_tr_b16 v[118:119], v250 offset:9728\n"
    "s_add_u32 s65, s83, 119808\n"
    "v_mfma_f32_16x16x32_bf16 a[136:139], v[120:123], v[72:75], a[136:139]\n"
    "buffer_load_dwordx4 v240, s[52:55], s65 offen offset:0 lds\n"
    "v_mfma_f32_16x16x32_bf16 a[140:143], v[120:123], v[76:79], a[140:143]\n"
    "s_waitcnt lgkmcnt(2)\n"
    "v_mfma_f32_16x16x32_bf16 a[144:147], v[112:115], v[64:67], a[144:147]\n"
    "ds_read_b64_tr_b16 v[120:121], v251 offset:8704\n"
    "v_mfma_f32_16x16x32_bf16 a[148:151], v[112:115], v[68:71], a[148:151]\n"
    "ds_read_b64_tr_b16 v[122:123], v251 offset:9728\n"
    "v_mfma_f32_16x16x32_bf16 a[152:155], v[112:115], v[72:75], a[152:155]\n"
    "v_mfma_f32_16x16x32_bf16 a[156:159], v[112:115], v[76:79], a[156:159]\n"
    "s_waitcnt lgkmcnt(2)\n"
    "v_mfma_f32_16x16x32_bf16 a[160:163], v[116:119], v[64:67], a[160:163]\n"
    "s_barrier\n"
    "ds_read_b64_tr_b16 v[112:113], v250 offset:16384\n"
    "v_mfma_f32_16x16x32_bf16 a[164:167], v[116:119], v[68:71], a[164:167]\n"
    "ds_read_b64_tr_b16 v[114:115], v250 offset:17408\n"
    "s_add_u32 m0, s64, 114688\n"
    "v_mfma_f32_16x16x32_bf16 a[168:171], v[116:119], v[72:75], a[168:171]\n"
    "s_add_u32 s65, s83, 73728\n"
    "v_mfma_f32_16x16x32_bf16 a[172:175], v[116:119], v[76:79], a[172:175]\n"
    "buffer_load_dwordx4 v240, s[52:55], s65 offen offset:0 lds\n"
    "s_waitcnt lgkmcnt(2)\n"
    "v_mfma_f32_16x16x32_bf16 a[176:179], v[120:123], v[64:67], a[176:179]\n"
    "ds_read_b64_tr_b16 v[116:117], v251 offset:16384\n"
    "v_mfma_f32_16x16x32_bf16 a[180:183], v[120:123], v[68:71], a[180:183]\n"
    "ds_read_b64_tr_b16 v[118:119], v251 offset:17408\n"
    "v_mfma_f32_16x16x32_bf16 a[184:187], v[120:123], v[72:75], a[184:187]\n"
    "v_mfma_f32_16x16x32_bf16 a[188:191], v[120:123], v[76:79], a[188:191]\n"
    "s_waitcnt lgkmcnt(2)\n"
    "v_mfma_f32_16x16x32_bf16 a[64:67], v[112:115], a[192:195], a[64:67]\n"
    "ds_read_b64_tr_b16 v[120:121], v250 offset:16896\n"
    "v_mfma_f32_16x16x32_bf16 a[68:71], v[112:115], a[196:199], a[68:71]\n"
    "ds_read_b64_tr_b16 v[122:123], v250 offset:17920\n"
    "v_lshl_add_u32 v128, s66, 13, v241\n"
    "v_mfma_f32_16x16x32_bf16 a[72:75], v[112:115], a[200:203], a[72:75]\n"
    "v_xor_b32 v129, 0x2000, v128\n"
    "v_add_u32 v128, 40960, v128\n"
    "v_add_u32 v129, 40960, v129\n"
    "v_mfma_f32_16x16x32_bf16 a[76:79], v[112:115], a[204:207], a[76:79]\n"
    "s_waitcnt lgkmcnt(2)\n"
    "v_mfma_f32_16x16x32_bf16 a[80:83], v[116:119], a[192:195], a[80:83]\n"
    "ds_read_b64_tr_b16 v[112:113], v251 offset:16896\n"
    "ds_write_b128 v128, a[136:139] offset:0\n"
    "v_mfma_f32_16x16x32_bf16 a[84:87], v[116:119], a[196:199], a[84:87]\n"
    "ds_read_b64_tr_b16 v[114:115], v251 offset:17920\n"
    "v_mfma_f32_16x16x32_bf16 a[88:91], v[116:119], a[200:203], a[88:91]\n"
    "ds_write_b128 v128, a[152:155] offset:1024\n"
    "v_mfma_f32_16x16x32_bf16 a[92:95], v[116:119], a[204:207], a[92:95]\n"
    "s_waitcnt lgkmcnt(4)\n"
    "v_mfma_f32_16x16x32_bf16 a[96:99], v[120:123], a[192:195], a[96:99]\n"
    "ds_read_b64_tr_b16 v[116:117], v250 offset:24576\n"
    "v_mfma_f32_16x16x32_bf16 a[100:103], v[120:123], a[196:199], a[100:103]\n"
    "ds_read_b64_tr_b16 v[118:119], v250 offset:25600\n"
    "ds_write_b128 v128, a[168:171] offset:2048\n"
    "v_mfma_f32_16x16x32_bf16 a[104:107], v[120:123], a[200:203], a[104:107]\n"
    "v_mfma_f32_16x16x32_bf16 a[108:111], v[120:123], a[204:207], a[108:111]\n"
    "ds_write_b128 v128, a[184:187] offset:3072\n"
    "s_waitcnt lgkmcnt(5)\n"
    "v_mfma_f32_16x16x32_bf16 a[112:115], v[112:115], a[192:195], a[112:115]\n"
    "ds_read_b64_tr_b16 v[120:121], v251 offset:24576\n"
    "v_mfma_f32_16x16x32_bf16 a[116:119], v[112:115], a[196:199], a[116:119]\n"
    "ds_read_b64_tr_b16 v[122:123], v251 offset:25600\n"
    "v_mfma_f32_16x16x32_bf16 a[120:123], v[112:115], a[200:203], a[120:123]\n"
    "ds_write_b128 v128, a[140:143] offset:4096\n"
    "v_mfma_f32_16x16x32_bf16 a[124:127], v[112:115], a[204:207], a[124:127]\n"
    "s_waitcnt lgkmcnt(5)\n"
    "v_mfma_f32_16x16x32_bf16 a[64:67], v[116:119], a[32:35], a[64:67]\n"
    "ds_read_b64_tr_b16 v[112:113], v250 offset:25088\n"
    "s_add_u32 m0, s64, 118784\n"
    "v_mfma_f32_16x16x32_bf16 a[68:71], v[116:119], a[36:39], a[68:71]\n"
    "ds_read_b64_tr_b16 v[114:115], v250 offset:26112\n"
    "s_add_u32 s65, s83, 156672\n"
    "ds_write_b128 v128, a[156:159] offset:5120\n"
    "v_mfma_f32_16x16x32_bf16 a[72:75], v[116:119], a[40:43], a[72:75]\n"
    "buffer_load_dwordx4 v240, s[52:55], s65 offen offset:0 lds\n"
    "v_mfma_f32_16x16x32_bf16 a[76:79], v[116:119], a[44:47], a[76:79]\n"
    "s_waitcnt lgkmcnt(4)\n"
    "v_mfma_f32_16x16x32_bf16 a[80:83], v[120:123], a[32:35], a[80:83]\n"
    "ds_read_b64_tr_b16 v[116:117], v251 offset:25088\n"
    "v_mfma_f32_16x16x32_bf16 a[84:87], v[120:123], a[36:39], a[84:87]\n"
    "ds_read_b64_tr_b16 v[118:119], v251 offset:26112\n"
    "ds_write_b128 v128, a[172:175] offset:6144\n"
    "v_mfma_f32_16x16x32_bf16 a[88:91], v[120:123], a[40:43], a[88:91]\n"
    "v_mfma_f32_16x16x32_bf16 a[92:95], v[120:123], a[44:47], a[92:95]\n"
    "ds_write_b128 v128, a[188:191] offset:7168\n"
    "s_waitcnt lgkmcnt(5)\n"
    "v_mfma_f32_16x16x32_bf16 a[96:99], v[112:115], a[32:35], a[96:99]\n"
    "ds_read_b64_tr_b16 v[120:121], v250 offset:32768\n"
    "v_mfma_f32_16x16x32_bf16 a[100:103], v[112:115], a[36:39], a[100:103]\n"
    "ds_read_b64_tr_b16 v[122:123], v250 offset:33792\n"
    "v_mfma_f32_16x16x32_bf16 a[104:107], v[112:115], a[40:43], a[104:107]\n"
    "v_mfma_f32_16x16x32_bf16 a[108:111], v[112:115], a[44:47], a[108:111]\n"
    "s_waitcnt lgkmcnt(4)\n"
    "v_mfma_f32_16x16x32_bf16 a[112:115], v[116:119], a[32:35], a[112:115]\n"
    "ds_read_b64_tr_b16 v[112:113], v251 offset:32768\n"
    "v_mfma_f32_16x16x32_bf16 a[116:119], v[116:119], a[36:39], a[116:119]\n"
    "ds_read_b64_tr_b16 v[114:115], v251 offset:33792\n"
    "v_mfma_f32_16x16x32_bf16 a[120:123], v[116:119], a[40:43], a[120:123]\n"
    "v_mfma_f32_16x16x32_bf16 a[124:127], v[116:119], a[44:47], a[124:127]\n"
    "s_waitcnt lgkmcnt(2)\n"
    "v_mfma_f32_16x16x32_bf16 a[64:67], v[120:123], a[48:51], a[64:67]\n"
    "ds_read_b64_tr_b16 v[116:117], v250 offset:33280\n"
    "v_mfma_f32_16x16x32_bf16 a[68:71], v[120:123], a[52:55], a[68:71]\n"
    "ds_read_b64_tr_b16 v[118:119], v250 offset:34304\n"
    "v_mfma_f32_16x16x32_bf16 a[72:75], v[120:123], a[56:59], a[72:75]\n"
    "v_mfma_f32_16x16x32_bf16 a[76:79], v[120:123], a[60:63], a[76:79]\n"
    "s_waitcnt lgkmcnt(2)\n"
    "v_mfma_f32_16x16x32_bf16 a[80:83], v[112:115], a[48:51], a[80:83]\n"
    "ds_read_b64_tr_b16 v[120:121], v251 offset:33280\n"
    "v_mfma_f32_16x16x32_bf16 a[84:87], v[112:115], a[52:55], a[84:87]\n"
    "ds_read_b64_tr_b16 v[122:123], v251 offset:34304\n"
    "v_mfma_f32_16x16x32_bf16 a[88:91], v[112:115], a[56:59], a[88:91]\n"
    "v_mfma_f32_16x16x32_bf16 a[92:95], v[112:115], a[60:63], a[92:95]\n"
    "s_waitcnt lgkmcnt(2)\n"
    "v_mfma_f32_16x16x32_bf16 a[96:99], v[116:119], a[48:51], a[96:99]\n"
    "s_waitcnt vmcnt(6)\n"
    "s_barrier\n"
    "ds_read_b64_tr_b16 v[112:113], v252 offset:8192\n"
    "v_mfma_f32_16x16x32_bf16 a[100:103], v[116:119], a[52:55], a[100:103]\n"
    "ds_read_b64_tr_b16 v[114:115], v252 offset:9216\n"
    "ds_read_b128 v[80:83], v129 offset:0\n"
    "ds_read_b128 v[84:87], v129 offset:1024\n"
    "v_mfma_f32_16x16x32_bf16 a[104:107], v[116:119], a[56:59], a[104:107]\n"
    "ds_read_b128 v[88:91], v129 offset:2048\n"
    "ds_read_b128 v[92:95], v129 offset:3072\n"
    "ds_read_b128 v[96:99], v129 offset:4096\n"
    "v_mfma_f32_16x16x32_bf16 a[108:111], v[116:119], a[60:63], a[108:111]\n"
    "ds_read_b128 v[100:103], v129 offset:5120\n"
    "ds_read_b128 v[104:107], v129 offset:6144\n"
    "s_waitcnt lgkmcnt(9)\n"
    "v_mfma_f32_16x16x32_bf16 a[112:115], v[120:123], a[48:51], a[112:115]\n"
    "ds_read_b64_tr_b16 v[116:117], v253 offset:8192\n"
    "ds_read_b128 v[108:111], v129 offset:7168\n"
    "v_accvgpr_read_b32 v124, a128\n"
    "v_mfma_f32_16x16x32_bf16 a[116:119], v[120:123], a[52:55], a[116:119]\n"
    "ds_read_b64_tr_b16 v[118:119], v253 offset:9216\n"
    "v_accvgpr_read_b32 v125, a129\n"
    "v_accvgpr_read_b32 v126, a130\n"
    "v_mfma_f32_16x16x32_bf16 a[120:123], v[120:123], a[56:59], a[120:123]\n"
    "v_accvgpr_read_b32 v127, a131\n"
    "s_waitcnt lgkmcnt(9)\n"
    "v_add_f32 v80, v80, v124\n"
    "v_add_f32 v81, v81, v125\n"
    "v_mfma_f32_16x16x32_bf16 a[124:127], v[120:123], a[60:63], a[124:127]\n"
    "v_add_f32 v82, v82, v126\n"
    "v_add_f32 v83, v83, v127\n"
    "v_mfma_f32_16x16x32_bf16 a[64:67], v[112:115], a[208:211], a[64:67]\n"
    "ds_read_b64_tr_b16 v[120:121], v252 offset:8704\n"
    "v_accvgpr_read_b32 v124, a144\n"
    "v_accvgpr_read_b32 v125, a145\n"
    "v_mfma_f32_16x16x32_bf16 a[68:71], v[112:115], a[212:215], a[68:71]\n"
    "ds_read_b64_tr_b16 v[122:123], v252 offset:9728\n"
    "v_accvgpr_read_b32 v126, a146\n"
    "v_accvgpr_read_b32 v127, a147\n"
    "v_mfma_f32_16x16x32_bf16 a[72:75], v[112:115], a[216:219], a[72:75]\n"
    "s_waitcnt lgkmcnt(10)\n"
    "v_add_f32 v84, v84, v124\n"
    "v_add_f32 v85, v85, v125\n"
    "v_add_f32 v86, v86, v126\n"
    "v_mfma_f32_16x16x32_bf16 a[76:79], v[112:115], a[220:223], a[76:79]\n"
    "v_add_f32 v87, v87, v127\n"
    "v_accvgpr_read_b32 v124, a160\n"
    "s_waitcnt lgkmcnt(2)\n"
    "v_mfma_f32_16x16x32_bf16 a[80:83], v[116:119], a[208:211], a[80:83]\n"
    "ds_read_b64_tr_b16 v[112:113], v253 offset:8704\n"
    "v_accvgpr_read_b32 v125, a161\n"
    "v_accvgpr_read_b32 v126, a162\n"
    "v_mfma_f32_16x16x32_bf16 a[84:87], v[116:119], a[212:215], a[84:87]\n"
    "ds_read_b64_tr_b16 v[114:115], v253 offset:9728\n"
    "v_accvgpr_read_b32 v127, a163\n"
    "v_add_f32 v88, v88, v124\n"
    "v_mfma_f32_16x16x32_bf16 a[88:91], v[116:119], a[216:219], a[88:91]\n"
    "v_add_f32 v89, v89, v125\n"
    "v_add_f32 v90, v90, v126\n"
    "v_add_f32 v91, v91, v127\n"
    "v_mfma_f32_16x16x32_bf16 a[92:95], v[116:119], a[220:223], a[92:95]\n"
    "v_accvgpr_read_b32 v124, a176\n"
    "v_accvgpr_read_b32 v125, a177\n"
    "s_waitcnt lgkmcnt(2)\n"
    "v_mfma_f32_16x16x32_bf16 a[96:99], v[120:123], a[208:211], a[96:99]\n"
    "ds_read_b64_tr_b16 v[116:117], v252 offset:16384\n"
    "v_accvgpr_read_b32 v126, a178\n"
    "v_accvgpr_read_b32 v127, a179\n"
    "v_mfma_f32_16x16x32_bf16 a[100:103], v[120:123], a[212:215], a[100:103]\n"
    "ds_read_b64_tr_b16 v[118:119], v252 offset:17408\n"
    "v_add_f32 v92, v92, v124\n"
    "v_add_f32 v93, v93, v125\n"
    "v_mfma_f32_16x16x32_bf16 a[104:107], v[120:123], a[216:219], a[104:107]\n"
    "v_add_f32 v94, v94, v126\n"
    "v_add_f32 v95, v95, v127\n"
    "v_cvt_pk_bf16_f32 v80, v80, v81\n"
    "v_mfma_f32_16x16x32_bf16 a[108:111], v[120:123], a[220:223], a[108:111]\n"
    "v_cvt_pk_bf16_f32 v81, v82, v83\n"
    "v_cvt_pk_bf16_f32 v82, v84, v85\n"
    "s_waitcnt lgkmcnt(2)\n"
    "v_mfma_f32_16x16x32_bf16 a[112:115], v[112:115], a[208:211], a[112:115]\n"
    "ds_read_b64_tr_b16 v[120:121], v253 offset:16384\n"
    "v_cvt_pk_bf16_f32 v83, v86, v87\n"
    "v_cvt_pk_bf16_f32 v84, v88, v89\n"
    "v_mfma_f32_16x16x32_bf16 a[116:119], v[112:115], a[212:215], a[116:119]\n"
    "ds_read_b64_tr_b16 v[122:123], v253 offset:17408\n"
    "v_cvt_pk_bf16_f32 v85, v90, v91\n"
    "v_cvt_pk_bf16_f32 v86, v92, v93\n"
    "v_mfma_f32_16x16x32_bf16 a[120:123], v[112:115], a[216:219], a[120:123]\n"
    "v_cvt_pk_bf16_f32 v87, v94, v95\n"
    "s_xor_b32 s20, s91, 69632\n"
    "buffer_store_dwordx4 v[80:83], v241, s[56:59], s20 offen offset:0 sc1\n"
    "v_mfma_f32_16x16x32_bf16 a[124:127], v[112:115], a[220:223], a[124:127]\n"
    "s_waitcnt lgkmcnt(2)\n"
    "v_mfma_f32_16x16x32_bf16 a[64:67], v[116:119], a[224:227], a[64:67]\n"
    "ds_read_b64_tr_b16 v[112:113], v252 offset:16896\n"
    "v_mfma_f32_16x16x32_bf16 a[68:71], v[116:119], a[228:231], a[68:71]\n"
    "ds_read_b64_tr_b16 v[114:115], v252 offset:17920\n"
    "s_xor_b32 s20, s91, 69632\n"
    "buffer_store_dwordx4 v[84:87], v241, s[56:59], s20 offen offset:1024 sc1\n"
    "v_mfma_f32_16x16x32_bf16 a[72:75], v[116:119], a[232:235], a[72:75]\n"
    "v_accvgpr_read_b32 v124, a132\n"
    "v_accvgpr_read_b32 v125, a133\n"
    "v_accvgpr_read_b32 v126, a134\n"
    "v_mfma_f32_16x16x32_bf16 a[76:79], v[116:119], a[236:239], a[76:79]\n"
    "v_accvgpr_read_b32 v127, a135\n"
    "v_add_f32 v96, v96, v124\n"
    "s_waitcnt lgkmcnt(2)\n"
    "v_mfma_f32_16x16x32_bf16 a[80:83], v[120:123], a[224:227], a[80:83]\n"
    "ds_read_b64_tr_b16 v[116:117], v253 offset:16896\n"
    "v_add_f32 v97, v97, v125\n"
    "v_add_f32 v98, v98, v126\n"
    "v_mfma_f32_16x16x32_bf16 a[84:87], v[120:123], a[228:231], a[84:87]\n"
    "ds_read_b64_tr_b16 v[118:119], v253 offset:17920\n"
    "v_add_f32 v99, v99, v127\n"
    "v_accvgpr_read_b32 v124, a148\n"
    "v_mfma_f32_16x16x32_bf16 a[88:91], v[120:123], a[232:235], a[88:91]\n"
    "v_accvgpr_read_b32 v125, a149\n"
    "v_accvgpr_read_b32 v126, a150\n"
    "v_accvgpr_read_b32 v127, a151\n"
    "v_mfma_f32_16x16x32_bf16 a[92:95], v[120:123], a[236:239], a[92:95]\n"
    "v_add_f32 v100, v100, v124\n"
    "v_add_f32 v101, v101, v125\n"
    "s_waitcnt lgkmcnt(2)\n"
    "v_mfma_f32_16x16x32_bf16 a[96:99], v[112:115], a[224:227], a[96:99]\n"
    "ds_read_b64_tr_b16 v[120:121], v252 offset:24576\n"
    "v_add_f32 v102, v102, v126\n"
    "v_add_f32 v103, v103, v127\n"
    "v_mfma_f32_16x16x32_bf16 a[100:103], v[112:115], a[228:231], a[100:103]\n"
    "ds_read_b64_tr_b16 v[122:123], v252 offset:25600\n"
    "v_accvgpr_read_b32 v124, a164\n"
    "v_accvgpr_read_b32 v125, a165\n"
    "v_mfma_f32_16x16x32_bf16 a[104:107], v[112:115], a[232:235], a[104:107]\n"
    "v_accvgpr_read_b32 v126, a166\n"
    "v_accvgpr_read_b32 v127, a167\n"
    "v_add_f32 v104, v104, v124\n"
    "v_mfma_f32_16x16x32_bf16 a[108:111], v[112:115], a[236:239], a[108:111]\n"
    "v_add_f32 v105, v105, v125\n"
    "v_add_f32 v106, v106, v126\n"
    "s_waitcnt lgkmcnt(2)\n"
    "v_mfma_f32_16x16x32_bf16 a[112:115], v[116:119], a[224:227], a[112:115]\n"
    "ds_read_b64_tr_b16 v[112:113], v253 offset:24576\n"
    "v_add_f32 v107, v107, v127\n"
    "v_accvgpr_read_b32 v124, a180\n"
    "v_mfma_f32_16x16x32_bf16 a[116:119], v[116:119], a[228:231], a[116:119]\n"
    "ds_read_b64_tr_b16 v[114:115], v253 offset:25600\n"
    "v_accvgpr_read_b32 v125, a181\n"
    "v_accvgpr_read_b32 v126, a182\n"
    "v_mfma_f32_16x16x32_bf16 a[120:123], v[116:119], a[232:235], a[120:123]\n"
    "v_accvgpr_read_b32 v127, a183\n"
    "v_add_f32 v108, v108, v124\n"
    "v_add_f32 v109, v109, v125\n"
    "v_mfma_f32_16x16x32_bf16 a[124:127], v[116:119], a[236:239], a[124:127]\n"
    "v_add_f32 v110, v110, v126\n"
    "v_add_f32 v111, v111, v127\n"
    "s_waitcnt lgkmcnt(2)\n"
    "v_mfma_f32_16x16x32_bf16 a[64:67], v[120:123], a[240:243], a[64:67]\n"
    "ds_read_b64_tr_b16 v[116:117], v252 offset:25088\n"
    "v_cvt_pk_bf16_f32 v96, v96, v97\n"
    "v_cvt_pk_bf16_f32 v97, v98, v99\n"
    "v_mfma_f32_16x16x32_bf16 a[68:71], v[120:123], a[244:247], a[68:71]\n"
    "ds_read_b64_tr_b16 v[118:119], v252 offset:26112\n"
    "v_cvt_pk_bf16_f32 v98, v100, v101\n"
    "v_cvt_pk_bf16_f32 v99, v102, v103\n"
    "v_mfma_f32_16x16x32_bf16 a[72:75], v[120:123], a[248:251], a[72:75]\n"
    "v_cvt_pk_bf16_f32 v100, v104, v105\n"
    "v_cvt_pk_bf16_f32 v101, v106, v107\n"
    "v_cvt_pk_bf16_f32 v102, v108, v109\n"
    "v_mfma_f32_16x16x32_bf16 a[76:79], v[120:123], a[252:255], a[76:79]\n"
    "v_cvt_pk_bf16_f32 v103, v110, v111\n"
    "s_waitcnt lgkmcnt(2)\n"
    "v_mfma_f32_16x16x32_bf16 a[80:83], v[112:115], a[240:243], a[80:83]\n"
    "ds_read_b64_tr_b16 v[120:121], v253 offset:25088\n"
    "s_xor_b32 s20, s91, 104448\n"
    "buffer_store_dwordx4 v[96:99], v241, s[56:59], s20 offen offset:0 sc1\n"
    "v_mfma_f32_16x16x32_bf16 a[84:87], v[112:115], a[244:247], a[84:87]\n"
    "ds_read_b64_tr_b16 v[122:123], v253 offset:26112\n"
    "v_mfma_f32_16x16x32_bf16 a[88:91], v[112:115], a[248:251], a[88:91]\n"
    "s_xor_b32 s20, s91, 104448\n"
    "buffer_store_dwordx4 v[100:103], v241, s[56:59], s20 offen offset:1024 sc1\n"
    "v_mfma_f32_16x16x32_bf16 a[92:95], v[112:115], a[252:255], a[92:95]\n"
    "s_waitcnt lgkmcnt(2)\n"
    "v_mfma_f32_16x16x32_bf16 a[96:99], v[116:119], a[240:243], a[96:99]\n"
    "s_waitcnt vmcnt(4)\n"
    "s_barrier\n"
    "ds_read_b64_tr_b16 v[112:113], v252 offset:32768\n"
    "v_mfma_f32_16x16x32_bf16 a[100:103], v[116:119], a[244:247], a[100:103]\n"
    "ds_read_b64_tr_b16 v[114:115], v252 offset:33792\n"
    "v_mfma_f32_16x16x32_bf16 a[104:107], v[116:119], a[248:251], a[104:107]\n"
    "v_mfma_f32_16x16x32_bf16 a[108:111], v[116:119], a[252:255], a[108:111]\n"
    "s_waitcnt lgkmcnt(2)\n"
    "v_mfma_f32_16x16x32_bf16 a[112:115], v[120:123], a[240:243], a[112:115]\n"
    "ds_read_b64_tr_b16 v[116:117], v253 offset:32768\n"
    "v_mfma_f32_16x16x32_bf16 a[116:119], v[120:123], a[244:247], a[116:119]\n"
    "ds_read_b64_tr_b16 v[118:119], v253 offset:33792\n"
    "v_mfma_f32_16x16x32_bf16 a[120:123], v[120:123], a[248:251], a[120:123]\n"
    "v_mfma_f32_16x16x32_bf16 a[124:127], v[120:123], a[252:255], a[124:127]\n"
    "s_waitcnt lgkmcnt(2)\n"
    "v_mfma_f32_16x16x32_bf16 a[64:67], v[112:115], a[16:19], a[64:67]\n"
    "ds_read_b64_tr_b16 v[120:121], v252 offset:33280\n"
    "v_mfma_f32_16x16x32_bf16 a[68:71], v[112:115], a[20:23], a[68:71]\n"
    "ds_read_b64_tr_b16 v[122:123], v252 offset:34304\n"
    "v_mfma_f32_16x16x32_bf16 a[72:75], v[112:115], a[24:27], a[72:75]\n"
    "v_mfma_f32_16x16x32_bf16 a[76:79], v[112:115], a[28:31], a[76:79]\n"
    "s_waitcnt lgkmcnt(2)\n"
    "v_mfma_f32_16x16x32_bf16 a[80:83], v[116:119], a[16:19], a[80:83]\n"
    "ds_read_b64_tr_b16 v[112:113], v253 offset:33280\n"
    "v_mfma_f32_16x16x32_bf16 a[84:87], v[116:119], a[20:23], a[84:87]\n"
    "ds_read_b64_tr_b16 v[114:115], v253 offset:34304\n"
    "v_mfma_f32_16x16x32_bf16 a[88:91], v[116:119], a[24:27], a[88:91]\n"
    "v_mfma_f32_16x16x32_bf16 a[92:95], v[116:119], a[28:31], a[92:95]\n"
    "s_waitcnt lgkmcnt(2)\n"
    "v_mfma_f32_16x16x32_bf16 a[96:99], v[120:123], a[16:19], a[96:99]\n"
    "ds_read_b64_tr_b16 v[116:117], v252 offset:40960\n"
    "v_mfma_f32_16x16x32_bf16 a[100:103], v[120:123], a[20:23], a[100:103]\n"
    "ds_read_b64_tr_b16 v[118:119], v252 offset:41984\n"
    "v_mfma_f32_16x16x32_bf16 a[104:107], v[120:123], a[24:27], a[104:107]\n"
    "v_mfma_f32_16x16x32_bf16 a[108:111], v[120:123], a[28:31], a[108:111]\n"
    "s_waitcnt lgkmcnt(2)\n"
    "v_mfma_f32_16x16x32_bf16 a[112:115], v[112:115], a[16:19], a[112:115]\n"
    "ds_read_b64_tr_b16 v[120:121], v253 offset:40960\n"
    "v_mfma_f32_16x16x32_bf16 a[116:119], v[112:115], a[20:23], a[116:119]\n"
    "ds_read_b64_tr_b16 v[122:123], v253 offset:41984\n"
    "v_mfma_f32_16x16x32_bf16 a[120:123], v[112:115], a[24:27], a[120:123]\n"
    "v_mfma_f32_16x16x32_bf16 a[124:127], v[112:115], a[28:31], a[124:127]\n"
    "s_waitcnt lgkmcnt(2)\n"
    "v_mfma_f32_16x16x32_bf16 a[64:67], v[116:119], a[0:3], a[64:67]\n"
    "ds_read_b64_tr_b16 v[112:113], v252 offset:41472\n"
    "v_mfma_f32_16x16x32_bf16 a[68:71], v[116:119], a[4:7], a[68:71]\n"
    "ds_read_b64_tr_b16 v[114:115], v252 offset:42496\n"
    "v_mfma_f32_16x16x32_bf16 a[72:75], v[116:119], a[8:11], a[72:75]\n"
    "v_mfma_f32_16x16x32_bf16 a[76:79], v[116:119], a[12:15], a[76:79]\n"
    "s_waitcnt lgkmcnt(2)\n"
    "v_mfma_f32_16x16x32_bf16 a[80:83], v[120:123], a[0:3], a[80:83]\n"
    "ds_read_b64_tr_b16 v[116:117], v253 offset:41472\n"
    "v_mfma_f32_16x16x32_bf16 a[84:87], v[120:123], a[4:7], a[84:87]\n"
    "ds_read_b64_tr_b16 v[118:119], v253 offset:42496\n"
    "v_mfma_f32_16x16x32_bf16 a[88:91], v[120:123], a[8:11], a[88:91]\n"
    "v_mfma_f32_16x16x32_bf16 a[92:95], v[120:123], a[12:15], a[92:95]\n"
    "s_waitcnt lgkmcnt(2)\n"
    "v_mfma_f32_16x16x32_bf16 a[96:99], v[112:115], a[0:3], a[96:99]\n"
    "ds_read_b64_tr_b16 v[120:121], v252 offset:49152\n"
    "v_mfma_f32_16x16x32_bf16 a[100:103], v[112:115], a[4:7], a[100:103]\n"
    "ds_read_b64_tr_b16 v[122:123], v252 offset:50176\n"
    "v_mfma_f32_16x16x32_bf16 a[104:107], v[112:115], a[8:11], a[104:107]\n"
    "v_mfma_f32_16x16x32_bf16 a[108:111], v[112:115], a[12:15], a[108:111]\n"
    "s_waitcnt lgkmcnt(2)\n"
    "v_mfma_f32_16x16x32_bf16 a[112:115], v[116:119], a[0:3], a[112:115]\n"
    "ds_read_b64_tr_b16 v[112:113], v253 offset:49152\n"
    "v_mfma_f32_16x16x32_bf16 a[116:119], v[116:119], a[4:7], a[116:119]\n"
    "ds_read_b64_tr_b16 v[114:115], v253 offset:50176\n"
    "v_mfma_f32_16x16x32_bf16 a[120:123], v[116:119], a[8:11], a[120:123]\n"
    "v_mfma_f32_16x16x32_bf16 a[124:127], v[116:119], a[12:15], a[124:127]\n"
    "s_waitcnt lgkmcnt(2)\n"
    "v_mfma_f32_16x16x32_bf16 a[64:67], v[120:123], v[64:67], a[64:67]\n"
    "ds_read_b64_tr_b16 v[116:117], v252 offset:49664\n"
    "v_mfma_f32_16x16x32_bf16 a[68:71], v[120:123], v[68:71], a[68:71]\n"
    "ds_read_b64_tr_b16 v[118:119], v252 offset:50688\n"
    "v_mfma_f32_16x16x32_bf16 a[72:75], v[120:123], v[72:75], a[72:75]\n"
    "v_mfma_f32_16x16x32_bf16 a[76:79], v[120:123], v[76:79], a[76:79]\n"
    "s_waitcnt lgkmcnt(2)\n"
    "v_mfma_f32_16x16x32_bf16 a[80:83], v[112:115], v[64:67], a[80:83]\n"
    "ds_read_b64_tr_b16 v[120:121], v253 offset:49664\n"
    "v_lshl_add_u32 v128, s66, 13, v241\n"
    "v_mfma_f32_16x16x32_bf16 a[84:87], v[112:115], v[68:71], a[84:87]\n"
    "ds_read_b64_tr_b16 v[122:123], v253 offset:50688\n"
    "v_mfma_f32_16x16x32_bf16 a[88:91], v[112:115], v[72:75], a[88:91]\n"
    "v_mfma_f32_16x16x32_bf16 a[92:95], v[112:115], v[76:79], a[92:95]\n"
    "v_xor_b32 v129, 0x2000, v128\n"
    "s_waitcnt lgkmcnt(2)\n"
    "v_mfma_f32_16x16x32_bf16 a[96:99], v[116:119], v[64:67], a[96:99]\n"
    "v_mfma_f32_16x16x32_bf16 a[100:103], v[116:119], v[68:71], a[100:103]\n"
    "v_add_u32 v128, 0, v128\n"
    "v_mfma_f32_16x16x32_bf16 a[104:107], v[116:119], v[72:75], a[104:107]\n"
    "v_mfma_f32_16x16x32_bf16 a[108:111], v[116:119], v[76:79], a[108:111]\n"
    "s_waitcnt lgkmcnt(0)\n"
    "v_mfma_f32_16x16x32_bf16 a[112:115], v[120:123], v[64:67], a[112:115]\n"
    "v_add_u32 v129, 0, v129\n"
    "v_mfma_f32_16x16x32_bf16 a[116:119], v[120:123], v[68:71], a[116:119]\n"
    "v_mfma_f32_16x16x32_bf16 a[120:123], v[120:123], v[72:75], a[120:123]\n"
    "v_mfma_f32_16x16x32_bf16 a[124:127], v[120:123], v[76:79], a[124:127]\n"
    "ds_write_b128 v128, a[76:79] offset:4096\n"
    "ds_write_b128 v128, a[92:95] offset:5120\n"
    "ds_write_b128 v128, a[108:111] offset:6144\n"
    "s_nop 4\n"
    "ds_write_b128 v128, a[124:127] offset:7168\n"
    "ds_write_b128 v128, a[72:75] offset:0\n"
    "ds_write_b128 v128, a[88:91] offset:1024\n"
    "ds_write_b128 v128, a[104:107] offset:2048\n"
    "ds_write_b128 v128, a[120:123] offset:3072\n"
    "v_accvgpr_read_b32 v134, a68\n"
    "v_accvgpr_read_b32 v135, a69\n"
    "v_accvgpr_read_b32 v136, a70\n"
    "v_accvgpr_read_b32 v137, a71\n"
    "v_accvgpr_read_b32 v138, a84\n"
    "v_accvgpr_read_b32 v139, a85\n"
    "v_accvgpr_read_b32 v140, a86\n"
    "v_accvgpr_read_b32 v141, a87\n"
    "v_accvgpr_read_b32 v142, a100\n"
    "v_accvgpr_read_b32 v143, a101\n"
    "v_accvgpr_read_b32 v144, a102\n"
    "v_accvgpr_read_b32 v145, a103\n"
    "v_accvgpr_read_b32 v146, a116\n"
    "v_accvgpr_read_b32 v147, a117\n"
    "v_accvgpr_read_b32 v148, a118\n"
    "v_accvgpr_read_b32 v149, a119\n"
    "v_accvgpr_read_b32 v32, a64\n"
    "v_accvgpr_read_b32 v33, a65\n"
    "v_accvgpr_read_b32 v34, a66\n"
    "v_accvgpr_read_b32 v35, a67\n"
    "v_accvgpr_read_b32 v36, a80\n"
    "v_accvgpr_read_b32 v37, a81\n"
    "v_accvgpr_read_b32 v38, a82\n"
    "v_accvgpr_read_b32 v39, a83\n"
    "v_accvgpr_read_b32 v40, a96\n"
    "v_accvgpr_read_b32 v41, a97\n"
    "v_accvgpr_read_b32 v42, a98\n"
    "v_accvgpr_read_b32 v43, a99\n"
    "v_accvgpr_read_b32 v44, a112\n"
    "v_accvgpr_read_b32 v45, a113\n"
    "v_accvgpr_read_b32 v46, a114\n"
    "v_accvgpr_read_b32 v47, a115\n"
    "s_waitcnt lgkmcnt(0)\n"
    "s_barrier\n"
    "ds_read_b128 v[96:99], v129 offset:4096\n"
    "ds_read_b128 v[100:103], v129 offset:5120\n"
    "ds_read_b128 v[104:107], v129 offset:6144\n"
    "ds_read_b128 v[108:111], v129 offset:7168\n"
    "ds_read_b128 v[80:83], v129 offset:0\n"
    "ds_read_b128 v[84:87], v129 offset:1024\n"
    "ds_read_b128 v[88:91], v129 offset:2048\n"
    "ds_read_b128 v[92:95], v129 offset:3072\n"
    "s_waitcnt lgkmcnt(7)\n"
    "v_add_f32 v96, v96, v134\n"
    "v_add_f32 v97, v97, v135\n"
    "v_add_f32 v98, v98, v136\n"
    "v_add_f32 v99, v99, v137\n"
    "s_waitcnt lgkmcnt(6)\n"
    "v_add_f32 v100, v100, v138\n"
    "v_add_f32 v101, v101, v139\n"
    "v_add_f32 v102, v102, v140\n"
    "v_add_f32 v103, v103, v141\n"
    "s_waitcnt lgkmcnt(5)\n"
    "v_add_f32 v104, v104, v142\n"
    "v_add_f32 v105, v105, v143\n"
    "v_add_f32 v106, v106, v144\n"
    "v_add_f32 v107, v107, v145\n"
    "s_waitcnt lgkmcnt(4)\n"
    "v_add_f32 v108, v108, v146\n"
    "v_add_f32 v109, v109, v147\n"
    "v_add_f32 v110, v110, v148\n"
    "v_add_f32 v111, v111, v149\n"
    "v_cvt_pk_bf16_f32 v96, v96, v97\n"
    "v_cvt_pk_bf16_f32 v97, v98, v99\n"
    "v_cvt_pk_bf16_f32 v98, v100, v101\n"
    "v_cvt_pk_bf16_f32 v99, v102, v103\n"
    "v_cvt_pk_bf16_f32 v100, v104, v105\n"
    "v_cvt_pk_bf16_f32 v101, v106, v107\n"
    "v_cvt_pk_bf16_f32 v102, v108, v109\n"
    "v_cvt_pk_bf16_f32 v103, v110, v111\n"
    "s_xor_b32 s20, s91, 34816\n"
    "buffer_store_dwordx4 v[96:99], v241, s[56:59], s20 offen offset:0 sc1\n"
    "s_xor_b32 s20, s91, 34816\n"
    "buffer_store_dwordx4 v[100:103], v241, s[56:59], s20 offen offset:1024 sc1\n"
    "s_waitcnt lgkmcnt(3)\n"
    "v_add_f32 v80, v80, v32\n"
    "v_add_f32 v81, v81, v33\n"
    "v_add_f32 v82, v82, v34\n"
    "v_add_f32 v83, v83, v35\n"
    "s_waitcnt lgkmcnt(2)\n"
    "v_add_f32 v84, v84, v36\n"
    "v_add_f32 v85, v85, v37\n"
    "v_add_f32 v86, v86, v38\n"
    "v_add_f32 v87, v87, v39\n"
    "s_waitcnt lgkmcnt(1)\n"
    "v_add_f32 v88, v88, v40\n"
    "v_add_f32 v89, v89, v41\n"
    "v_add_f32 v90, v90, v42\n"
    "v_add_f32 v91, v91, v43\n"
    "s_waitcnt lgkmcnt(0)\n"
    "v_add_f32 v92, v92, v44\n"
    "v_add_f32 v93, v93, v45\n"
    "v_add_f32 v94, v94, v46\n"
    "v_add_f32 v95, v95, v47\n"
    "v_bfe_u32 v130, v241, 4, 4\n"
    "v_mul_u32_u24 v130, 1152, v130\n"
    "v_bfe_u32 v131, v241, 8, 1\n"
    "v_lshl_add_u32 v130, v131, 5, v130\n"
    "v_bfe_u32 v131, v241, 9, 1\n"
    "v_lshl_add_u32 v130, v131, 4, v130\n"
    "s_lshl_b32 s96, s67, 2\n"
    "s_add_u32 s96, s96, s66\n"
    "s_lshl_b32 s96, s96, 1\n"
    "s_and_b32 s97, s68, 1\n"
    "s_add_u32 s96, s96, s97\n"
    "s_mul_i32 s96, s96, 18432\n"
    "s_lshl_b32 s97, s69, 1\n"
    "s_lshr_b32 s60, s68, 1\n"
    "s_add_u32 s97, s97, s60\n"
    "s_lshl_b32 s97, s97, 7\n"
    "s_add_u32 s96, s96, s97\n"
    "s_lshr_b32 s97, s69, 1\n"
    "s_lshl_b32 s97, s97, 6\n"
    "s_add_u32 s60, s96, s97\n"
    "s_xor_b32 s97, s97, 64\n"
    "s_add_u32 s61, s96, s97\n"
    "s_waitcnt vmcnt(0)\n"
    "s_add_u32 s99, s70, 2\n"
    "v_mov_b32 v133, s21\n"
    "v_mov_b32 v132, s99\n"
    "buffer_store_dwordx2 v[132:133], v242, s[56:59], s89 offen offset:16 sc1\n"
    "s_lshl_b32 s96, s67, 12\n"
    "s_lshl_b32 s97, s68, 8\n"
    "s_add_u32 s96, s96, s97\n"
    "s_add_u32 s96, s96, 32784\n"
    "v_bfe_u32 v243, v241, 5, 2\n"
    "v_lshl_add_u32 v243, v243, 10, s96\n"
    "v_bfe_u32 v132, v241, 4, 1\n"
    "v_lshl_add_u32 v243, v132, 7, v243\n"
    "buffer_load_dwordx2 v[234:235], v243, s[56:59], 0 offen offset:0 sc1\n"
    "s_waitcnt vmcnt(0)\n"
    "v_subrev_u32 v234, s70, v234\n"
    "v_cmp_eq_u32_e64 s[16:17], s21, v235\n"
    "v_subrev_u32 v234, 1, v234\n"
    "v_cmp_gt_u32 vcc, 16, v234\n"
    "s_nop 0\n"
    "s_and_b64 vcc, vcc, s[16:17]\n"
    "s_cmp_eq_u64 vcc, -1\n"
    "s_cbranch_scc0 LslowQ6_%=\n"
    "LbackQ6_%=:\n"
    "s_lshl_b32 s96, s67, 2\n"
    "s_add_u32 s96, s96, s68\n"
    "s_lshl_b32 s96, s96, 15\n"
    "s_lshr_b32 s97, s69, 1\n"
    "s_lshl_b32 s97, s97, 11\n"
    "s_add_u32 s96, s96, s97\n"
    "s_lshr_b32 s97, s66, 1\n"
    "s_lshl_b32 s97, s97, 10\n"
    "s_add_u32 s96, s96, s97\n"
    "s_and_b32 s97, s66, 1\n"
    "s_lshl_b32 s97, s97, 3\n"
    "s_add_u32 s96, s96, s97\n"
    "s_add_u32 s96, s96, 37748736\n"
    "s_xor_b32 s62, s96, 0\n"
    "buffer_load_dwordx2 v[134:135], v241, s[56:59], s62 offen offset:0 sc1\n"
    "s_xor_b32 s62, s96, 5120\n"
    "buffer_load_dwordx2 v[136:137], v241, s[56:59], s62 offen offset:0 sc1\n"
    "s_xor_b32 s62, s96, 10240\n"
    "buffer_load_dwordx2 v[138:139], v241, s[56:59], s62 offen offset:0 sc1\n"
    "s_xor_b32 s62, s96, 15360\n"
    "buffer_load_dwordx2 v[140:141], v241, s[56:59], s62 offen offset:0 sc1\n"
    "s_xor_b32 s62, s96, 16384\n"
    "buffer_load_dwordx2 v[142:143], v241, s[56:59], s62 offen offset:0 sc1\n"
    "s_xor_b32 s62, s96, 21504\n"
    "buffer_load_dwordx2 v[144:145], v241, s[56:59], s62 offen offset:0 sc1\n"
    "s_xor_b32 s62, s96, 26624\n"
    "buffer_load_dwordx2 v[146:147], v241, s[56:59], s62 offen offset:0 sc1\n"
    "s_xor_b32 s62, s96, 31744\n"
    "buffer_load_dwordx2 v[148:149], v241, s[56:59], s62 offen offset:0 sc1\n"
    "s_add_u32 s97, s89, 16\n"
    "v_bfe_u32 v243, v241, 4, 2\n"
    "v_max_u32 v243, 1, v243\n"
    "v_lshlrev_b32 v243, 10, v243\n"
    "v_xor_b32 v243, s97, v243\n"
    "buffer_load_dwordx2 v[234:235], v243, s[56:59], 0 offen offset:0 sc1\n"
    "s_waitcnt vmcnt(8)\n"
    "v_and_b32 v33, 0xffff0000, v134\n"
    "v_lshlrev_b32 v32, 16, v134\n"
    "v_and_b32 v35, 0xffff0000, v135\n"
    "v_lshlrev_b32 v34, 16, v135\n"
    "s_waitcnt vmcnt(7)\n"
    "v_lshlrev_b32 v124, 16, v136\n"
    "v_and_b32 v125, 0xffff0000, v136\n"
    "v_add_f32 v32, v32, v124\n"
    "v_add_f32 v33, v33, v125\n"
    "v_lshlrev_b32 v126, 16, v137\n"
    "v_and_b32 v127, 0xffff0000, v137\n"
    "v_add_f32 v34, v34, v126\n"
    "v_add_f32 v35, v35, v127\n"
    "s_waitcnt vmcnt(6)\n"
    "v_lshlrev_b32 v124, 16, v138\n"
    "v_and_b32 v125, 0xffff0000, v138\n"
    "v_add_f32 v32, v32, v124\n"
    "v_add_f32 v33, v33, v125\n"
    "v_lshlrev_b32 v126, 16, v139\n"
    "v_and_b32 v127, 0xffff0000, v139\n"
    "v_add_f32 v34, v34, v126\n"
    "v_add_f32 v35, v35, v127\n"
    "s_waitcnt vmcnt(5)\n"
    "v_lshlrev_b32 v124, 16, v140\n"
    "v_and_b32 v125, 0xffff0000, v140\n"
    "v_add_f32 v32, v32, v124\n"
    "v_add_f32 v33, v33, v125\n"
    "v_lshlrev_b32 v126, 16, v141\n"
    "v_and_b32 v127, 0xffff0000, v141\n"
    "v_add_f32 v34, v34, v126\n"
    "v_add_f32 v35, v35, v127\n"
    "s_waitcnt vmcnt(4)\n"
    "v_lshlrev_b32 v124, 16, v142\n"
    "v_and_b32 v125, 0xffff0000, v142\n"
    "v_add_f32 v32, v32, v124\n"
    "v_add_f32 v33, v33, v125\n"
    "v_lshlrev_b32 v126, 16, v143\n"
    "v_and_b32 v127, 0xffff0000, v143\n"
    "v_add_f32 v34, v34, v126\n"
    "v_add_f32 v35, v35, v127\n"
    "s_waitcnt vmcnt(3)\n"
    "v_lshlrev_b32 v124, 16, v144\n"
    "v_and_b32 v125, 0xffff0000, v144\n"
    "v_add_f32 v32, v32, v124\n"
    "v_add_f32 v33, v33, v125\n"
    "v_lshlrev_b32 v126, 16, v145\n"
    "v_and_b32 v127, 0xffff0000, v145\n"
    "v_add_f32 v34, v34, v126\n"
    "v_add_f32 v35, v35, v127\n"
    "s_waitcnt vmcnt(2)\n"
    "v_lshlrev_b32 v124, 16, v146\n"
    "v_and_b32 v125, 0xffff0000, v146\n"
    "v_add_f32 v32, v32, v124\n"
    "v_add_f32 v33, v33, v125\n"
    "v_lshlrev_b32 v126, 16, v147\n"
    "v_and_b32 v127, 0xffff0000, v147\n"
    "v_add_f32 v34, v34, v126\n"
    "v_add_f32 v35, v35, v127\n"
    "s_waitcnt vmcnt(1)\n"
    "v_lshlrev_b32 v124, 16, v148\n"
    "v_and_b32 v125, 0xffff0000, v148\n"
    "v_add_f32 v32, v32, v124\n"
    "v_add_f32 v33, v33, v125\n"
    "v_lshlrev_b32 v126, 16, v149\n"
    "v_and_b32 v127, 0xffff0000, v149\n"
    "v_add_f32 v34, v34, v126\n"
    "v_add_f32 v35, v35, v127\n"
    "v_cvt_pk_bf16_f32 v32, v32, v33\n"
    "v_cvt_pk_bf16_f32 v33, v34, v35\n"
    "v_bfe_u32 v40, v241, 4, 4\n"
    "v_mul_u32_u24 v40, 1152, v40\n"
    "v_lshrrev_b32 v41, 8, v241\n"
    "v_lshl_add_u32 v40, v41, 3, v40\n"
    "s_lshl_b32 s96, s67, 2\n"
    "s_add_u32 s96, s96, s68\n"
    "s_lshl_b32 s96, s96, 1\n"
    "s_lshr_b32 s97, s69, 1\n"
    "s_add_u32 s96, s96, s97\n"
    "s_mul_i32 s96, s96, 18432\n"
    "s_lshl_b32 s97, s66, 5\n"
    "s_add_u32 s96, s96, s97\n"
    "s_add_u32 s96, s96, 1024\n"
    "buffer_store_dwordx2 v[32:33], v40, s[12:15], s96 offen offset:0\n"
    "s_waitcnt vmcnt(1)\n"
    "v_subrev_u32 v234, s70, v234\n"
    "v_cmp_eq_u32_e64 s[16:17], s21, v235\n"
    "v_subrev_u32 v234, 2, v234\n"
    "v_cmp_gt_u32 vcc, 16, v234\n"
    "s_nop 0\n"
    "s_and_b64 vcc, vcc, s[16:17]\n"
    "s_cmp_eq_u64 vcc, -1\n"
    "s_cbranch_scc0 LslowQ7_%=\n"
    "LbackQ7_%=:\n"
    "s_add_u32 s62, s91, 2048\n"
    "buffer_load_dwordx4 v[134:137], v241, s[56:59], s62 offen offset:0 sc1\n"
    "buffer_load_dwordx4 v[138:141], v241, s[56:59], s62 offen offset:1024 sc1\n"
    "s_add_u32 s62, s91, 4096\n"
    "buffer_load_dwordx4 v[142:145], v241, s[56:59], s62 offen offset:0 sc1\n"
    "buffer_load_dwordx4 v[146:149], v241, s[56:59], s62 offen offset:1024 sc1\n"
    "s_add_u32 s62, s91, 6144\n"
    "buffer_load_dwordx4 v[48:51], v241, s[56:59], s62 offen offset:0 sc1\n"
    "buffer_load_dwordx4 v[52:55], v241, s[56:59], s62 offen offset:1024 sc1\n"
    "s_or_b32 s96, %[bid], s66\n"
    "s_mov_b32 s97, 268435456\n"
    "s_cmp_eq_u32 s96, 0\n"
    "s_cselect_b32 s96, 98304, s97\n"
    "v_mov_b32 v132, s70\n"
    "buffer_store_dword v132, v242, s[56:59], s96 offen offset:0 sc1\n"
    "s_waitcnt vmcnt(6)\n"
    "v_lshlrev_b32 v124, 16, v134\n"
    "v_and_b32 v125, 0xffff0000, v134\n"
    "v_add_f32 v80, v80, v124\n"
    "v_add_f32 v81, v81, v125\n"
    "v_lshlrev_b32 v126, 16, v135\n"
    "v_and_b32 v127, 0xffff0000, v135\n"
    "v_add_f32 v82, v82, v126\n"
    "v_add_f32 v83, v83, v127\n"
    "v_lshlrev_b32 v124, 16, v136\n"
    "v_and_b32 v125, 0xffff0000, v136\n"
    "v_add_f32 v84, v84, v124\n"
    "v_add_f32 v85, v85, v125\n"
    "v_lshlrev_b32 v126, 16, v137\n"
    "v_and_b32 v127, 0xffff0000, v137\n"
    "v_add_f32 v86, v86, v126\n"
    "v_add_f32 v87, v87, v127\n"
    "s_waitcnt vmcnt(5)\n"
    "v_lshlrev_b32 v124, 16, v138\n"
    "v_and_b32 v125, 0xffff0000, v138\n"
    "v_add_f32 v88, v88, v124\n"
    "v_add_f32 v89, v89, v125\n"
    "v_lshlrev_b32 v126, 16, v139\n"
    "v_and_b32 v127, 0xffff0000, v139\n"
    "v_add_f32 v90, v90, v126\n"
    "v_add_f32 v91, v91, v127\n"
    "v_lshlrev_b32 v124, 16, v140\n"
    "v_and_b32 v125, 0xffff0000, v140\n"
    "v_add_f32 v92, v92, v124\n"
    "v_add_f32 v93, v93, v125\n"
    "v_lshlrev_b32 v126, 16, v141\n"
    "v_and_b32 v127, 0xffff0000, v141\n"
    "v_add_f32 v94, v94, v126\n"
    "v_add_f32 v95, v95, v127\n"
    "s_waitcnt vmcnt(4)\n"
    "v_lshlrev_b32 v124, 16, v142\n"
    "v_and_b32 v125, 0xffff0000, v142\n"
    "v_add_f32 v80, v80, v124\n"
    "v_add_f32 v81, v81, v125\n"
    "v_lshlrev_b32 v126, 16, v143\n"
    "v_and_b32 v127, 0xffff0000, v143\n"
    "v_add_f32 v82, v82, v126\n"
    "v_add_f32 v83, v83, v127\n"
    "v_lshlrev_b32 v124, 16, v144\n"
    "v_and_b32 v125, 0xffff0000, v144\n"
    "v_add_f32 v84, v84, v124\n"
    "v_add_f32 v85, v85, v125\n"
    "v_lshlrev_b32 v126, 16, v145\n"
    "v_and_b32 v127, 0xffff0000, v145\n"
    "v_add_f32 v86, v86, v126\n"
    "v_add_f32 v87, v87, v127\n"
    "s_waitcnt vmcnt(3)\n"
    "v_lshlrev_b32 v124, 16, v146\n"
    "v_and_b32 v125, 0xffff0000, v146\n"
    "v_add_f32 v88, v88, v124\n"
    "v_add_f32 v89, v89, v125\n"
    "v_lshlrev_b32 v126, 16, v147\n"
    "v_and_b32 v127, 0xffff0000, v147\n"
    "v_add_f32 v90, v90, v126\n"
    "v_add_f32 v91, v91, v127\n"
    "v_lshlrev_b32 v124, 16, v148\n"
    "v_and_b32 v125, 0xffff0000, v148\n"
    "v_add_f32 v92, v92, v124\n"
    "v_add_f32 v93, v93, v125\n"
    "v_lshlrev_b32 v126, 16, v149\n"
    "v_and_b32 v127, 0xffff0000, v149\n"
    "v_add_f32 v94, v94, v126\n"
    "v_add_f32 v95, v95, v127\n"
    "s_waitcnt vmcnt(2)\n"
    "v_lshlrev_b32 v124, 16, v48\n"
    "v_and_b32 v125, 0xffff0000, v48\n"
    "v_add_f32 v80, v80, v124\n"
    "v_add_f32 v81, v81, v125\n"
    "v_lshlrev_b32 v126, 16, v49\n"
    "v_and_b32 v127, 0xffff0000, v49\n"
    "v_add_f32 v82, v82, v126\n"
    "v_add_f32 v83, v83, v127\n"
    "v_lshlrev_b32 v124, 16, v50\n"
    "v_and_b32 v125, 0xffff0000, v50\n"
    "v_add_f32 v84, v84, v124\n"
    "v_add_f32 v85, v85, v125\n"
    "v_lshlrev_b32 v126, 16, v51\n"
    "v_and_b32 v127, 0xffff0000, v51\n"
    "v_add_f32 v86, v86, v126\n"
    "v_add_f32 v87, v87, v127\n"
    "s_waitcnt vmcnt(1)\n"
    "v_lshlrev_b32 v124, 16, v52\n"
    "v_and_b32 v125, 0xffff0000, v52\n"
    "v_add_f32 v88, v88, v124\n"
    "v_add_f32 v89, v89, v125\n"
    "v_lshlrev_b32 v126, 16, v53\n"
    "v_and_b32 v127, 0xffff0000, v53\n"
    "v_add_f32 v90, v90, v126\n"
    "v_add_f32 v91, v91, v127\n"
    "v_lshlrev_b32 v124, 16, v54\n"
    "v_and_b32 v125, 0xffff0000, v54\n"
    "v_add_f32 v92, v92, v124\n"
    "v_add_f32 v93, v93, v125\n"
    "v_lshlrev_b32 v126, 16, v55\n"
    "v_and_b32 v127, 0xffff0000, v55\n"
    "v_add_f32 v94, v94, v126\n"
    "v_add_f32 v95, v95, v127\n"
    "v_cvt_pk_bf16_f32 v80, v80, v81\n"
    "v_cvt_pk_bf16_f32 v81, v82, v83\n"
    "v_cvt_pk_bf16_f32 v82, v84, v85\n"
    "v_cvt_pk_bf16_f32 v83, v86, v87\n"
    "v_cvt_pk_bf16_f32 v84, v88, v89\n"
    "v_cvt_pk_bf16_f32 v85, v90, v91\n"
    "v_cvt_pk_bf16_f32 v86, v92, v93\n"
    "v_cvt_pk_bf16_f32 v87, v94, v95\n"
    "v_permlane16_swap_b32 v80, v82\n"
    "v_permlane16_swap_b32 v81, v83\n"
    "v_permlane16_swap_b32 v84, v86\n"
    "v_permlane16_swap_b32 v85, v87\n"
    "buffer_store_dwordx4 v[80:83], v130, s[12:15], s60 offen offset:0\n"
    "buffer_store_dwordx4 v[84:87], v130, s[12:15], s61 offen offset:0\n"
    "s_waitcnt vmcnt(0) lgkmcnt(0)\n"
    "s_branch Lend_%=\n"
    "LslowQ0_%=:\n"
    "s_mov_b32 s98, 100000\n"
    "LspQ0_%=:\n"
    "buffer_load_dwordx2 v[234:235], v243, s[56:59], 0 offen offset:0 sc1\n"
    "s_waitcnt vmcnt(0)\n"
    "v_subrev_u32 v234, s70, v234\n"
    "v_cmp_eq_u32_e64 s[16:17], s21, v235\n"
    "v_subrev_u32 v234, 1, v234\n"
    "v_cmp_gt_u32 vcc, 16, v234\n"
    "s_nop 4\n"
    "s_and_b64 vcc, vcc, s[16:17]\n"
    "s_cmp_eq_u64 vcc, -1\n"
    "s_cbranch_scc1 LbackQ0_%=\n"
    "s_sub_u32 s98, s98, 1\n"
    "s_cmp_eq_u32 s98, 0\n"
    "s_cbranch_scc1 LbackQ0_%=\n"
    "s_sleep 1\n"
    "s_branch LspQ0_%=\n"
    "LslowQ1_%=:\n"
    "s_mov_b32 s98, 100000\n"
    "LspQ1_%=:\n"
    "buffer_load_dwordx2 v[234:235], v243, s[56:59], 0 offen offset:0 sc1\n"
    "s_waitcnt vmcnt(0)\n"
    "v_subrev_u32 v234, s70, v234\n"
    "v_cmp_eq_u32_e64 s[16:17], s21, v235\n"
    "v_subrev_u32 v234, 2, v234\n"
    "v_cmp_gt_u32 vcc, 16, v234\n"
    "s_nop 4\n"
    "s_and_b64 vcc, vcc, s[16:17]\n"
    "s_cmp_eq_u64 vcc, -1\n"
    "s_cbranch_scc1 LbackQ1_%=\n"
    "s_sub_u32 s98, s98, 1\n"
    "s_cmp_eq_u32 s98, 0\n"
    "s_cbranch_scc1 LbackQ1_%=\n"
    "s_sleep 1\n"
    "s_branch LspQ1_%=\n"
    "LslowQ2_%=:\n"
    "s_mov_b32 s98, 100000\n"
    "LspQ2_%=:\n"
    "buffer_load_dwordx2 v[234:235], v243, s[56:59], 0 offen offset:0 sc1\n"
    "s_waitcnt vmcnt(0)\n"
    "v_subrev_u32 v234, s70, v234\n"
    "v_cmp_eq_u32_e64 s[16:17], s21, v235\n"
    "v_subrev_u32 v234, 3, v234\n"
    "v_cmp_gt_u32 vcc, 16, v234\n"
    "s_nop 4\n"
    "s_and_b64 vcc, vcc, s[16:17]\n"
    "s_cmp_eq_u64 vcc, -1\n"
    "s_cbranch_scc1 LbackQ2_%=\n"
    "s_sub_u32 s98, s98, 1\n"
    "s_cmp_eq_u32 s98, 0\n"
    "s_cbranch_scc1 LbackQ2_%=\n"
    "s_sleep 1\n"
    "s_branch LspQ2_%=\n"
    "LslowQ3_%=:\n"
    "s_mov_b32 s98, 100000\n"
    "LspQ3_%=:\n"
    "buffer_load_dwordx2 v[234:235], v243, s[56:59], 0 offen offset:0 sc1\n"
    "s_waitcnt vmcnt(0)\n"
    "v_subrev_u32 v234, s70, v234\n"
    "v_cmp_eq_u32_e64 s[16:17], s21, v235\n"
    "v_subrev_u32 v234, 4, v234\n"
    "v_cmp_gt_u32 vcc, 16, v234\n"
    "s_nop 4\n"
    "s_and_b64 vcc, vcc, s[16:17]\n"
    "s_cmp_eq_u64 vcc, -1\n"
    "s_cbranch_scc1 LbackQ3_%=\n"
    "s_sub_u32 s98, s98, 1\n"
    "s_cmp_eq_u32 s98, 0\n"
    "s_cbranch_scc1 LbackQ3_%=\n"
    "s_sleep 1\n"
    "s_branch LspQ3_%=\n"
    "LslowQ4_%=:\n"
    "s_mov_b32 s98, 100000\n"
    "LspQ4_%=:\n"
    "buffer_load_dwordx2 v[234:235], v243, s[56:59], 0 offen offset:0 sc1\n"
    "s_waitcnt vmcnt(0)\n"
    "v_subrev_u32 v234, s70, v234\n"
    "v_cmp_eq_u32_e64 s[16:17], s21, v235\n"
    "v_subrev_u32 v234, 1, v234\n"
    "v_cmp_gt_u32 vcc, 16, v234\n"
    "s_nop 4\n"
    "s_and_b64 vcc, vcc, s[16:17]\n"
    "s_cmp_eq_u64 vcc, -1\n"
    "s_cbranch_scc1 LbackQ4_%=\n"
    "s_sub_u32 s98, s98, 1\n"
    "s_cmp_eq_u32 s98, 0\n"
    "s_cbranch_scc1 LbackQ4_%=\n"
    "s_sleep 1\n"
    "s_branch LspQ4_%=\n"
    "LslowQ5_%=:\n"
    "s_mov_b32 s98, 100000\n"
    "LspQ5_%=:\n"
    "buffer_load_dwordx2 v[234:235], v243, s[56:59], 0 offen offset:0 sc1\n"
    "s_waitcnt vmcnt(0)\n"
    "v_subrev_u32 v234, s70, v234\n"
    "v_cmp_eq_u32_e64 s[16:17], s21, v235\n"
    "v_subrev_u32 v234, 5, v234\n"
    "v_cmp_gt_u32 vcc, 16, v234\n"
    "s_nop 4\n"
    "s_and_b64 vcc, vcc, s[16:17]\n"
    "s_cmp_eq_u64 vcc, -1\n"
    "s_cbranch_scc1 LbackQ5_%=\n"
    "s_sub_u32 s98, s98, 1\n"
    "s_cmp_eq_u32 s98, 0\n"
    "s_cbranch_scc1 LbackQ5_%=\n"
    "s_sleep 1\n"
    "s_branch LspQ5_%=\n"
    "LslowQ6_%=:\n"
    "s_mov_b32 s98, 100000\n"
    "LspQ6_%=:\n"
    "buffer_load_dwordx2 v[234:235], v243, s[56:59], 0 offen offset:0 sc1\n"
    "s_waitcnt vmcnt(0)\n"
    "v_subrev_u32 v234, s70, v234\n"
    "v_cmp_eq_u32_e64 s[16:17], s21, v235\n"
    "v_subrev_u32 v234, 1, v234\n"
    "v_cmp_gt_u32 vcc, 16, v234\n"
    "s_nop 4\n"
    "s_and_b64 vcc, vcc, s[16:17]\n"
    "s_cmp_eq_u64 vcc, -1\n"
    "s_cbranch_scc1 LbackQ6_%=\n"
    "s_sub_u32 s98, s98, 1\n"
    "s_cmp_eq_u32 s98, 0\n"
    "s_cbranch_scc1 LbackQ6_%=\n"
    "s_sleep 1\n"
    "s_branch LspQ6_%=\n"
    "LslowQ7_%=:\n"
    "s_mov_b32 s98, 100000\n"
    "LspQ7_%=:\n"
    "buffer_load_dwordx2 v[234:235], v243, s[56:59], 0 offen offset:0 sc1\n"
    "s_waitcnt vmcnt(0)\n"
    "v_subrev_u32 v234, s70, v234\n"
    "v_cmp_eq_u32_e64 s[16:17], s21, v235\n"
    "v_subrev_u32 v234, 2, v234\n"
    "v_cmp_gt_u32 vcc, 16, v234\n"
    "s_nop 4\n"
    "s_and_b64 vcc, vcc, s[16:17]\n"
    "s_cmp_eq_u64 vcc, -1\n"
    "s_cbranch_scc1 LbackQ7_%=\n"
    "s_sub_u32 s98, s98, 1\n"
    "s_cmp_eq_u32 s98, 0\n"
    "s_cbranch_scc1 LbackQ7_%=\n"
    "s_sleep 1\n"
    "s_branch LspQ7_%=\n"
    "LceQ_%=:\n"
    "Lend_%=:\n"
    :
    : [ka] "s"(ka), [bid] "s"(bid), [wid] "s"(wid)
    : "v0", "v1", "v2", "v3", "v4", "v5", "v6", "v7", "v8", "v9", "v10", "v11", "v12", "v13", "v14", "v15", "v16", "v17", "v18", "v19", "v20", "v21", "v22", "v23", "v24", "v25", "v26", "v27", "v28", "v29", "v30", "v31", "v32", "v33", "v34", "v35", "v36", "v37", "v38", "v39", "v40", "v41", "v42", "v43", "v44", "v45", "v46", "v47", "v48", "v49", "v50", "v51", "v52", "v53", "v54", "v55", "v56", "v57", "v58", "v59", "v60", "v61", "v62", "v63", "v64", "v65", "v66", "v67", "v68", "v69", "v70", "v71", "v72", "v73", "v74", "v75", "v76", "v77", "v78", "v79", "v80", "v81", "v82", "v83", "v84", "v85", "v86", "v87", "v88", "v89", "v90", "v91", "v92", "v93", "v94", "v95", "v96", "v97", "v98", "v99", "v100", "v101", "v102", "v103", "v104", "v105", "v106", "v107", "v108", "v109", "v110", "v111", "v112", "v113", "v114", "v115", "v116", "v117", "v118", "v119", "v120", "v121", "v122", "v123", "v124", "v125", "v126", "v127", "v128", "v129", "v130", "v131", "v132", "v133", "v134", "v135", "v136", "v137", "v138", "v139", "v140", "v141", "v142", "v143", "v144", "v145", "v146", "v147", "v148", "v149", "v150", "v151", "v152", "v153", "v154", "v155", "v156", "v157", "v158", "v159", "v160", "v161", "v162", "v163", "v164", "v165", "v166", "v167", "v168", "v169", "v170", "v171", "v172", "v173", "v174", "v175", "v176", "v177", "v178", "v179", "v180", "v181", "v182", "v183", "v184", "v185", "v186", "v187", "v188", "v189", "v190", "v191", "v192", "v193", "v194", "v195", "v196", "v197", "v198", "v199", "v200", "v201", "v202", "v203", "v204", "v205", "v206", "v207", "v208", "v209", "v210", "v211", "v212", "v213", "v214", "v215", "v216", "v217", "v218", "v219", "v220", "v221", "v222", "v223", "v224", "v225", "v226", "v227", "v228", "v229", "v230", "v231", "v232", "v233", "v234", "v235", "v236", "v237", "v238", "v239", "v240", "v241", "v242", "v243", "v244", "v245", "v246", "v247", "v248", "v249", "v250", "v251", "v252", "v253", "v254", "v255", "a0", "a1", "a2", "a3", "a4", "a5", "a6", "a7", "a8", "a9", "a10", "a11", "a12", "a13", "a14", "a15", "a16", "a17", "a18", "a19", "a20", "a21", "a22", "a23", "a24", "a25", "a26", "a27", "a28", "a29", "a30", "a31", "a32", "a33", "a34", "a35", "a36", "a37", "a38", "a39", "a40", "a41", "a42", "a43", "a44", "a45", "a46", "a47", "a48", "a49", "a50", "a51", "a52", "a53", "a54", "a55", "a56", "a57", "a58", "a59", "a60", "a61", "a62", "a63", "a64", "a65", "a66", "a67", "a68", "a69", "a70", "a71", "a72", "a73", "a74", "a75", "a76", "a77", "a78", "a79", "a80", "a81", "a82", "a83", "a84", "a85", "a86", "a87", "a88", "a89", "a90", "a91", "a92", "a93", "a94", "a95", "a96", "a97", "a98", "a99", "a100", "a101", "a102", "a103", "a104", "a105", "a106", "a107", "a108", "a109", "a110", "a111", "a112", "a113", "a114", "a115", "a116", "a117", "a118", "a119", "a120", "a121", "a122", "a123", "a124", "a125", "a126", "a127", "a128", "a129", "a130", "a131", "a132", "a133", "a134", "a135", "a136", "a137", "a138", "a139", "a140", "a141", "a142", "a143", "a144", "a145", "a146", "a147", "a148", "a149", "a150", "a151", "a152", "a153", "a154", "a155", "a156", "a157", "a158", "a159", "a160", "a161", "a162", "a163", "a164", "a165", "a166", "a167", "a168", "a169", "a170", "a171", "a172", "a173", "a174", "a175", "a176", "a177", "a178", "a179", "a180", "a181", "a182", "a183", "a184", "a185", "a186", "a187", "a188", "a189", "a190", "a191", "a192", "a193", "a194", "a195", "a196", "a197", "a198", "a199", "a200", "a201", "a202", "a203", "a204", "a205", "a206", "a207", "a208", "a209", "a210", "a211", "a212", "a213", "a214", "a215", "a216", "a217", "a218", "a219", "a220", "a221", "a222", "a223", "a224", "a225", "a226", "a227", "a228", "a229", "a230", "a231", "a232", "a233", "a234", "a235", "a236", "a237", "a238", "a239", "a240", "a241", "a242", "a243", "a244", "a245", "a246", "a247", "a248", "a249", "a250", "a251", "a252", "a253", "a254", "a255", "s10", "s11", "s12", "s13", "s14", "s15", "s16", "s17", "s18", "s19", "s20", "s21", "s22", "s23", "s24", "s25", "s26", "s27", "s28", "s29", "s30", "s31", "s33", "s34", "s35", "s36", "s37", "s38", "s39", "s40", "s41", "s42", "s43", "s44", "s45", "s46", "s47", "s48", "s49", "s50", "s51", "s52", "s53", "s54", "s55", "s56", "s57", "s58", "s59", "s60", "s61", "s62", "s63", "s64", "s65", "s66", "s67", "s68", "s69", "s70", "s71", "s72", "s73", "s74", "s75", "s76", "s77", "s78", "s79", "s80", "s81", "s82", "s83", "s84", "s85", "s86", "s87", "s88", "s89", "s90", "s91", "s92", "s93", "s94", "s95", "s96", "s97", "s98", "s99", "vcc", "scc", "memory");
}

extern "C" void kernel_launch(void* const* d_in, const int* in_sizes, int n_in, void* d_out, int out_size, void* d_ws, size_t ws_size,
                              hipStream_t stream) {
  (void)hipFuncSetAttribute((const void*)gm_main, hipFuncAttributeMaxDynamicSharedMemorySize, 163840);
  gm_main<<<256, 256, 163840, stream>>>(d_in[0], d_in[1], d_in[2], d_in[4], d_in[5], d_in[6], d_in[7], d_ws, d_out);
}
```
